```python
import torch

def _softmax(x, axis=-1):
    return torch.softmax(x, dim=axis)
def _gelu(x):
    return torch.nn.functional.gelu(x, approximate="tanh")
def _silu(x):
    return torch.nn.functional.silu(x)
def _sigmoid(x):
    return torch.sigmoid(x)
def _layernorm(x, axis=-1, eps=1e-5):
    m = x.mean(dim=axis, keepdim=True)
    v = x.var(dim=axis, keepdim=True, unbiased=False)
    return (x - m) / torch.sqrt(v + eps)
def _scatter_add(tgt, idx, src, axis=0):
    return tgt.clone().index_add_(axis, idx, src)
def _segment_sum(src, seg, n):
    out = torch.zeros((n,) + src.shape[1:], dtype=src.dtype)
    return out.index_add_(0, seg, src)
def _pad(x, pw):
    flat = tuple(v for pair in reversed(pw) for v in pair)
    return torch.nn.functional.pad(x, flat)

kernel_name = 'sequential_preLN__ssm_conv_h3__glu_gated_glu__prompt'

def setup_inputs():

    _w = (lambda a: (lambda t: t.to(torch.bfloat16) if t.is_floating_point() else t)(torch.as_tensor(a)))
    def _A_setup():
        B, S, D = 1, 2048, 512
        ks = 4
        return {
            "q": _w((lambda *s: torch.randn(*s, dtype=torch.bfloat16) * 0.05)(B, S, D)),
            "k": _w((lambda *s: torch.randn(*s, dtype=torch.bfloat16) * 0.4)(B, S, D)),
            "v": _w((lambda *s: torch.randn(*s, dtype=torch.bfloat16) * 0.4)(B, S, D)),
            "shift_k": _w((lambda *s: torch.randn(*s, dtype=torch.bfloat16) * 0.4)(D, ks) * 0.5),
            "ssm_k": _w((lambda *s: torch.randn(*s, dtype=torch.bfloat16) * 0.4)(D, S) * (1.0 / S ** 0.5)),
            "ssm_D": _w((lambda *s: torch.randn(*s, dtype=torch.bfloat16) * 0.4)(D)),
        }
    _A_in = _A_setup()

    def _B_setup():
        B, S, D, F = 1, 2048, 512, 1376
        return {
            "x": _w((lambda *s: torch.randn(*s, dtype=torch.bfloat16) * 0.05)(B, S, D)),
            "w_gate": _w((lambda *s: torch.randn(*s, dtype=torch.bfloat16) * 0.05)(D, F)),
            "w_up": _w((lambda *s: torch.randn(*s, dtype=torch.bfloat16) * 0.05)(D, F)),
            "w_down": _w((lambda *s: torch.randn(*s, dtype=torch.bfloat16) * 0.05)(F, D)),
        }
    _B_in = _B_setup()

    return {
        "A_q": _A_in["q"],
        "A_k": _A_in["k"],
        "A_v": _A_in["v"],
        "A_shift_k": _A_in["shift_k"],
        "A_ssm_k": _A_in["ssm_k"],
        "A_ssm_D": _A_in["ssm_D"],
        "B_x": _B_in["x"],
        "B_w_gate": _B_in["w_gate"],
        "B_w_up": _B_in["w_up"],
        "B_w_down": _B_in["w_down"],
    }

def reference(A_q, A_k, A_v, A_shift_k, A_ssm_k, A_ssm_D, B_x, B_w_gate, B_w_up, B_w_down):
    def _A_ref(q, k, v, shift_k, ssm_k, ssm_D):
        B, S, D = q.shape
        ks = shift_k.shape[-1]
        k_pad = _pad(k, ((0, 0), (ks - 1, 0), (0, 0)))
        k_shift = torch.zeros_like(k)
        for t in range(ks):
            k_shift = k_shift + k_pad[:, t:t + S, :] * shift_k[:, ks - 1 - t][None, None, :]
        kv = k_shift * v
        L = 2 * S
        kv_pad = _pad(kv, ((0, 0), (0, S), (0, 0)))
        kf_pad = _pad(ssm_k, ((0, 0), (0, S)))
        kv_f = (lambda x, axes=(-2,-1): torch.fft.rfft2(x.float(), dim=axes))(kv_pad[:, None, :, :], axes=(1, 2))
        kf_f = (lambda x, axes=(-2,-1): torch.fft.rfft2(x.float(), dim=axes))(kf_pad[:, None, :], axes=(1, 2))
        kf_f = (lambda x, *d: x.permute(*d))(kf_f, 1, 2, 0)
        y_f = kv_f * kf_f[None, :, :, :]
        y = (lambda x, s, axes=(-2,-1): torch.fft.irfft2(x, s=s, dim=axes).to(torch.bfloat16))(y_f, (1, L), axes=(1, 2))[:, 0, :S, :]
        y = y + kv * ssm_D[None, None, :]
        return q * y

    def _B_ref(x, w_gate, w_up, w_down):
        g = torch.einsum("bsd,df->bsf", x, w_gate)
        u = torch.einsum("bsd,df->bsf", x, w_up)
        h = _sigmoid(g) * u
        return torch.einsum("bsf,fd->bsd", h, w_down)

    x = A_q
    h = x + _A_ref(_layernorm(x, axis=-1), A_k, A_v, A_shift_k, A_ssm_k, A_ssm_D)
    h2 = h + _B_ref(_layernorm(h, axis=-1), B_w_gate, B_w_up, B_w_down)
    return h2
```

```cpp
#include <hip/hip_runtime.h>
#include <hip/hip_bf16.h>
typedef unsigned short u16;
constexpr int S = 2048, D = 512, F = 1376;
__device__ __forceinline__ float b2f(u16 x) { return __uint_as_float(((unsigned)x) << 16); }
__device__ __forceinline__ u16 f2b(float x) { unsigned u = __float_as_uint(x); u += 0x7fffu + ((u >> 16) & 1u); return (u16)(u >> 16); }

__global__ void k_kv(const u16* k, const u16* v, const u16* sh, u16* xT) {
  int i = blockIdx.x * 256 + threadIdx.x; int s = i / D, d = i % D;
  float acc = 0.f;
  for (int j = 0; j < 4; ++j) if (s - j >= 0) acc += b2f(k[(s - j) * D + d]) * b2f(sh[d * 4 + j]);
  xT[d * S + s] = f2b(acc * b2f(v[s * D + d]));
}
__global__ void k_conv(const u16* xT, const u16* sk, const u16* Dv, u16* z) {
  __shared__ float xs[S], ks[S];
  int d = blockIdx.x;
  for (int i = threadIdx.x; i < S; i += 256) { xs[i] = b2f(xT[d * S + i]); ks[i] = b2f(sk[d * S + i]); }
  __syncthreads();
  for (int s = threadIdx.x; s < S; s += 256) {
    float acc = 0.f;
    for (int j = 0; j <= s; ++j) acc += xs[s - j] * ks[j];
    z[s * D + d] = f2b(acc + xs[s] * b2f(Dv[d]));
  }
}
__device__ float bsum(float v, float* red) {
  for (int o = 32; o; o >>= 1) v += __shfl_xor(v, o);
  __syncthreads();
  if ((threadIdx.x & 63) == 0) red[threadIdx.x >> 6] = v;
  __syncthreads();
  return red[0] + red[1] + red[2] + red[3];
}
__global__ void k_ln(const u16* q, const u16* z, u16* h, u16* a) {
  __shared__ float red[4];
  int s = blockIdx.x, t = threadIdx.x;
  float x0 = b2f(q[s * D + t]), x1 = b2f(q[s * D + t + 256]);
  float m = bsum(x0 + x1, red) / D;
  float v = bsum((x0 - m) * (x0 - m) + (x1 - m) * (x1 - m), red) / D;
  float r = rsqrtf(v + 1e-5f);
  float h0 = b2f(f2b(x0 + (x0 - m) * r * b2f(z[s * D + t]))), h1 = b2f(f2b(x1 + (x1 - m) * r * b2f(z[s * D + t + 256])));
  float m2 = bsum(h0 + h1, red) / D;
  float v2 = bsum((h0 - m2) * (h0 - m2) + (h1 - m2) * (h1 - m2), red) / D;
  float r2 = rsqrtf(v2 + 1e-5f);
  h[s * D + t] = f2b(h0); h[s * D + t + 256] = f2b(h1);
  a[s * D + t] = f2b((h0 - m2) * r2); a[s * D + t + 256] = f2b((h1 - m2) * r2);
}
__global__ void k_g1(const u16* a, const u16* wg, const u16* wu, u16* H) {
  __shared__ float as[D];
  int s = blockIdx.x;
  for (int i = threadIdx.x; i < D; i += 256) as[i] = b2f(a[s * D + i]);
  __syncthreads();
  for (int f = threadIdx.x; f < F; f += 256) {
    float g = 0, u = 0;
    for (int d = 0; d < D; ++d) { g += as[d] * b2f(wg[d * F + f]); u += as[d] * b2f(wu[d * F + f]); }
    H[s * F + f] = f2b(u / (1.f + __expf(-g)));
  }
}
__global__ void k_g2(const u16* H, const u16* wd, const u16* h, u16* out) {
  __shared__ float hs[F];
  int s = blockIdx.x;
  for (int i = threadIdx.x; i < F; i += 256) hs[i] = b2f(H[s * F + i]);
  __syncthreads();
  for (int d = threadIdx.x; d < D; d += 256) {
    float o = 0;
    for (int f = 0; f < F; ++f) o += hs[f] * b2f(wd[f * D + d]);
    out[s * D + d] = f2b(o + b2f(h[s * D + d]));
  }
}
extern "C" void kernel_launch(void* const* d_in, const int* in_sizes, int n_in, void* d_out, int out_size, void* d_ws, size_t ws_size, hipStream_t stream) {
  const u16 *q = (const u16*)d_in[0], *k = (const u16*)d_in[1], *v = (const u16*)d_in[2], *sh = (const u16*)d_in[3], *sk = (const u16*)d_in[4], *Dv = (const u16*)d_in[5];
  const u16 *wg = (const u16*)d_in[7], *wu = (const u16*)d_in[8], *wd = (const u16*)d_in[9];
  u16* ws = (u16*)d_ws;
  u16 *xT = ws, *z = ws + S * D, *h = ws + 2 * S * D, *a = ws + 3 * S * D, *H = ws + 4 * S * D;
  k_kv<<<S * D / 256, 256, 0, stream>>>(k, v, sh, xT);
  k_conv<<<D, 256, 0, stream>>>(xT, sk, Dv, z);
  k_ln<<<S, 256, 0, stream>>>(q, z, h, a);
  k_g1<<<S, 256, 0, stream>>>(a, wg, wu, H);
  k_g2<<<S, 256, 0, stream>>>(H, wd, h, (u16*)d_out);
}
```

```cpp
#include <hip/hip_runtime.h>
#include <stdint.h>

typedef unsigned short u16;
typedef unsigned u32;
typedef __attribute__((ext_vector_type(2))) u32 u32x2;
typedef __attribute__((ext_vector_type(4))) u32 u32x4;
typedef __attribute__((ext_vector_type(2))) float f32x2;
typedef __attribute__((ext_vector_type(4))) float f32x4;
typedef __attribute__((ext_vector_type(2))) __bf16 bf16x2;
typedef __attribute__((ext_vector_type(8))) __bf16 bf16x8;
typedef __attribute__((ext_vector_type(4))) short s16x4;
#define LDSP __attribute__((address_space(3)))
#define DI __device__ __forceinline__

constexpr int S = 2048, D = 512, F = 1376;
constexpr int NU = F / 8;
constexpr int KS1 = D / 32;
constexpr int KS2 = F / 32;
constexpr float EPS = 1e-5f;

constexpr size_t WS_XR = 0;
constexpr size_t WS_QT = WS_XR + (size_t)D * S * 2;
constexpr size_t WS_HT = WS_QT + (size_t)D * S * 2;
constexpr size_t WS_HR = WS_HT + (size_t)D * S * 2;
constexpr size_t WS_W1 = WS_HR + (size_t)D * S * 2;
constexpr size_t WS_WD = WS_W1 + (size_t)NU * KS1 * 1024;
constexpr size_t WS_H = WS_WD + (size_t)8 * KS2 * 4 * 1024;
constexpr size_t WS_CS = WS_H + (size_t)(S / 16) * KS2 * 1024;
constexpr size_t WS_ST = WS_CS + (size_t)KS1 * NU * 16 * 4;

DI float blo(u32 u) { return __uint_as_float(u << 16); }
DI float bhi(u32 u) { return __uint_as_float(u & 0xffff0000u); }
DI u32 pk2(float a, float b) { f32x2 t = {a, b}; return __builtin_bit_cast(u32, __builtin_convertvector(t, bf16x2)); }
DI f32x4 mfma16(u32x4 a, u32x4 b, f32x4 c) {
  return __builtin_amdgcn_mfma_f32_16x16x32_bf16(__builtin_bit_cast(bf16x8, a), __builtin_bit_cast(bf16x8, b), c, 0, 0, 0);
}
DI u32x2 tr_read(u32 lds_byte) {
  return __builtin_bit_cast(u32x2, __builtin_amdgcn_ds_read_tr16_b64_v4i16((LDSP s16x4*)(size_t)lds_byte));
}
DI u32x4 tr_frag(u32 a0, u32 a1) { u32x2 x = tr_read(a0), y = tr_read(a1); return u32x4{x[0], x[1], y[0], y[1]}; }
DI float wave_sum(float v) {
#pragma unroll
  for (int o = 32; o; o >>= 1) v += __shfl_xor(v, o);
  return v;
}
DI int slice_u0(int j) { return j < 12 ? 11 * j : 132 + 10 * (j - 12); }
DI int slice_nu(int j) { return j < 12 ? 11 : 10; }

__global__ __launch_bounds__(256) void k0_prep(const u16* __restrict__ q, const u16* __restrict__ k, const u16* __restrict__ v,
                                               const u16* __restrict__ shk, const u16* __restrict__ wg, const u16* __restrict__ wu,
                                               const u16* __restrict__ wd, char* __restrict__ ws) {
  __shared__ __attribute__((aligned(16))) char tile[32 * 368];
  const int t = threadIdx.x, lane = t & 63, w = t >> 6;
  int b = blockIdx.x;
  if (b < 256) {
    const int x = b & 7, i = b >> 3;
    const int rg = lane & 7, cp = (lane >> 3) + 8 * w;
    const int d = 64 * x + 2 * cp, r0 = 64 * i + 8 * rg;
    u32 kk[11], vv[8], qq[8];
#pragma unroll
    for (int j = 0; j < 11; ++j) {
      int row = r0 - 3 + j;
      u32 val = *(const u32*)(k + (size_t)(row < 0 ? 0 : row) * D + d);
      kk[j] = row < 0 ? 0u : val;
    }
#pragma unroll
    for (int j = 0; j < 8; ++j) vv[j] = *(const u32*)(v + (size_t)(r0 + j) * D + d);
#pragma unroll
    for (int j = 0; j < 8; ++j) qq[j] = *(const u32*)(q + (size_t)(r0 + j) * D + d);
    const u32x4 sh = *(const u32x4*)(shk + d * 4);
    const int srow = 8 * b + 2 * w;
    const u32x4 qa = *(const u32x4*)(q + (size_t)srow * D + 8 * lane);
    const u32x4 qb = *(const u32x4*)(q + (size_t)(srow + 1) * D + 8 * lane);

    const float a0 = blo(sh[0]), a1 = bhi(sh[0]), a2 = blo(sh[1]), a3 = bhi(sh[1]);
    const float b0 = blo(sh[2]), b1 = bhi(sh[2]), b2 = blo(sh[3]), b3 = bhi(sh[3]);
    float xl[8], xh[8];
#pragma unroll
    for (int e = 0; e < 8; ++e) {
      float sl = blo(kk[e + 3]) * a0 + blo(kk[e + 2]) * a1 + blo(kk[e + 1]) * a2 + blo(kk[e]) * a3;
      float sh2 = bhi(kk[e + 3]) * b0 + bhi(kk[e + 2]) * b1 + bhi(kk[e + 1]) * b2 + bhi(kk[e]) * b3;
      xl[e] = sl * blo(vv[e]);
      xh[e] = sh2 * bhi(vv[e]);
    }
    u16* xr = (u16*)(ws + WS_XR);
    *(u32x4*)(xr + (size_t)d * S + (2040 - r0)) = u32x4{pk2(xl[7], xl[6]), pk2(xl[5], xl[4]), pk2(xl[3], xl[2]), pk2(xl[1], xl[0])};
    *(u32x4*)(xr + (size_t)(d + 1) * S + (2040 - r0)) = u32x4{pk2(xh[7], xh[6]), pk2(xh[5], xh[4]), pk2(xh[3], xh[2]), pk2(xh[1], xh[0])};
    u16* qT = (u16*)(ws + WS_QT);
    u32x4 ql, qh;
#pragma unroll
    for (int e = 0; e < 4; ++e) {
      ql[e] = __builtin_amdgcn_perm(qq[2 * e + 1], qq[2 * e], 0x05040100u);
      qh[e] = __builtin_amdgcn_perm(qq[2 * e + 1], qq[2 * e], 0x07060302u);
    }
    *(u32x4*)(qT + (size_t)d * S + r0) = ql;
    *(u32x4*)(qT + (size_t)(d + 1) * S + r0) = qh;

    float sa = 0.f, sb = 0.f;
#pragma unroll
    for (int e = 0; e < 4; ++e) { sa += blo(qa[e]) + bhi(qa[e]); sb += blo(qb[e]) + bhi(qb[e]); }
    const float ma = wave_sum(sa) * (1.f / D), mb = wave_sum(sb) * (1.f / D);
    float va = 0.f, vb = 0.f;
#pragma unroll
    for (int e = 0; e < 4; ++e) {
      float u0 = blo(qa[e]) - ma, u1 = bhi(qa[e]) - ma, u2 = blo(qb[e]) - mb, u3 = bhi(qb[e]) - mb;
      va += u0 * u0 + u1 * u1; vb += u2 * u2 + u3 * u3;
    }
    va = wave_sum(va) * (1.f / D); vb = wave_sum(vb) * (1.f / D);
    if (lane == 0) {
      f32x4 o = {ma, 1.f / sqrtf(va + EPS), mb, 1.f / sqrtf(vb + EPS)};
      *(f32x4*)(ws + WS_ST + (size_t)srow * 8) = o;
    }
    return;
  }
  const u32 tb = (u32)(size_t)(LDSP char*)tile;
  const int i16 = lane & 15, qd = i16 >> 2, p = i16 & 3, g = lane >> 4;
  b -= 256;
  if (b < 256) {
    const int ks = b & 15, j = b >> 4;
    const int U0 = slice_u0(j), nu = slice_nu(j);
#pragma unroll
    for (int rep = 0; rep < 2; ++rep) {
      int idx = t + 256 * rep, r = idx / 11, u = idx - 11 * r;
      if (r < 32 && u < nu) {
        size_t off = (size_t)(32 * ks + r) * F + 8 * (U0 + u);
        u32x4 g4 = *(const u32x4*)(wg + off), u4 = *(const u32x4*)(wu + off);
        *(u32x4*)(tile + r * 368 + u * 32) = u32x4{g4[0], u4[0], g4[1], u4[1]};
        *(u32x4*)(tile + r * 368 + u * 32 + 16) = u32x4{g4[2], u4[2], g4[3], u4[3]};
      }
    }
    __syncthreads();
    const u32x4 ones = {0x3f803f80u, 0x3f803f80u, 0x3f803f80u, 0x3f803f80u};
    for (int u = w; u < nu; u += 4) {
      u32 a = tb + (8 * g + qd) * 368 + u * 32 + 8 * p;
      u32x4 fr = tr_frag(a, a + 4 * 368);
      *(u32x4*)(ws + WS_W1 + ((size_t)((U0 + u) * KS1 + ks) * 64 + lane) * 16) = fr;
      f32x4 cs = mfma16(fr, ones, f32x4{0.f, 0.f, 0.f, 0.f});
      if (i16 == 0) *(f32x4*)(ws + WS_CS + ((size_t)(ks * NU + U0 + u) * 16 + 4 * g) * 4) = cs;
    }
    return;
  }
  b -= 256;
  {
    const int ks = b >> 2, dp = b & 3;
#pragma unroll
    for (int rep = 0; rep < 2; ++rep) {
      int idx = t + 256 * rep, r = idx >> 4, c = idx & 15;
      *(u32x4*)(tile + r * 272 + c * 16) = *(const u32x4*)(wd + (size_t)(32 * ks + r) * D + 128 * dp + 8 * c);
    }
    __syncthreads();
#pragma unroll
    for (int rep = 0; rep < 2; ++rep) {
      int fi = w + 4 * rep, dtl = fi >> 2, df = fi & 3;
      u32 a = tb + (8 * g + qd) * 272 + (64 * dtl + 16 * p + 4 * df) * 2;
      u32x4 fr = tr_frag(a, a + 4 * 272);
      *(u32x4*)(ws + WS_WD + ((size_t)(((2 * dp + dtl) * KS2 + ks) * 4 + df) * 64 + lane) * 16) = fr;
    }
  }
}

constexpr int K1_CS = 4160;
constexpr int K1_KC = 4 * K1_CS;
constexpr int K1_XR = 2 * K1_KC;
constexpr int K1_RED = K1_XR + 2 * 4608;
constexpr int K1_LDS = K1_RED + 4 * 4096;

__global__ __launch_bounds__(256) void k1_conv(const u16* __restrict__ ssmk, const u16* __restrict__ dskip, char* __restrict__ ws) {
  extern __shared__ __attribute__((aligned(16))) char smem[];
  const int t = threadIdx.x, lane = t & 63;
  const int w = __builtin_amdgcn_readfirstlane(t >> 6);
  const int b = blockIdx.x;
  const int ch0 = (b & 7) * 64 + (b >> 3) * 2;
  const u16* xr = (const u16*)(ws + WS_XR);
  const u16* qT = (const u16*)(ws + WS_QT);
  const int c = w >> 1, half = w & 1;
  const int n = lane & 15, g = lane >> 4;

  u32x4 kp[2], xv[2], kc[2];
#pragma unroll
  for (int cc = 0; cc < 2; ++cc) {
    const u16* kr = ssmk + (size_t)(ch0 + cc) * S;
    u32x4 pv = *(const u32x4*)(kr + 8 * (t > 0 ? t - 1 : 0));
    kp[cc] = t > 0 ? pv : u32x4{0, 0, 0, 0};
    kc[cc] = *(const u32x4*)(kr + 8 * t);
    xv[cc] = *(const u32x4*)(xr + (size_t)(ch0 + cc) * S + 8 * t);
  }
  u32x2 qv[4]; f32x4 st[4][2];
  const float2* stp = (const float2*)(ws + WS_ST);
#pragma unroll
  for (int i = 0; i < 4; ++i) {
    int s = 256 * (4 * half + i) + 16 * n + 4 * g;
    qv[i] = *(const u32x2*)(qT + (size_t)(ch0 + c) * S + s);
    st[i][0] = *(const f32x4*)(stp + s);
    st[i][1] = *(const f32x4*)(stp + s + 2);
  }
  const float dsk = blo(dskip[ch0 + c]);

#pragma unroll
  for (int cc = 0; cc < 2; ++cc) {
    u32 wv[6] = {kp[cc][0], kp[cc][1], kp[cc][2], kp[cc][3], kc[cc][0], kc[cc][1]};
    u32 o1[5];
#pragma unroll
    for (int i = 0; i < 5; ++i) o1[i] = __builtin_amdgcn_alignbit(wv[i + 1], wv[i], 16);
    char* base = smem + cc * K1_KC + (t + 1) * 16;
    *(u32x4*)(base + 0 * K1_CS) = u32x4{o1[0], o1[1], o1[2], o1[3]};
    *(u32x4*)(base + 1 * K1_CS) = u32x4{wv[1], wv[2], wv[3], wv[4]};
    *(u32x4*)(base + 2 * K1_CS) = u32x4{o1[1], o1[2], o1[3], o1[4]};
    *(u32x4*)(base + 3 * K1_CS) = u32x4{wv[2], wv[3], wv[4], wv[5]};
    *(u32x4*)(smem + K1_XR + cc * 4608 + t * 16) = xv[cc];
    if (t == 255) {
      const u32 z = 0;
      const u32 e0 = __builtin_amdgcn_alignbit(kc[cc][1], kc[cc][0], 16), e1 = __builtin_amdgcn_alignbit(kc[cc][2], kc[cc][1], 16);
      const u32 e2 = __builtin_amdgcn_alignbit(kc[cc][3], kc[cc][2], 16), e3 = __builtin_amdgcn_alignbit(z, kc[cc][3], 16);
      *(u32x4*)(base + 16 + 0 * K1_CS) = u32x4{e0, e1, e2, e3};
      *(u32x4*)(base + 16 + 1 * K1_CS) = u32x4{kc[cc][1], kc[cc][2], kc[cc][3], z};
      *(u32x4*)(base + 16 + 2 * K1_CS) = u32x4{e1, e2, e3, z};
      *(u32x4*)(base + 16 + 3 * K1_CS) = u32x4{kc[cc][2], kc[cc][3], z, z};
    }
  }
  if (t < 8) *(u32x4*)(smem + (t >> 2) * K1_KC + (t & 3) * K1_CS) = u32x4{0, 0, 0, 0};
  if (t >= 64 && t < 64 + 60) { int i = t - 64; *(u32x4*)(smem + K1_XR + (i / 30) * 4608 + 4096 + (i % 30) * 16) = u32x4{0, 0, 0, 0}; }
  __syncthreads();

  const u32 sb = (u32)(size_t)(LDSP char*)smem;
  u32 aa0 = sb + c * K1_KC + (n & 3) * K1_CS + 16 * g + 2 * (n & 12) + half * 256;
  u32 aa1 = aa0 + 8;
  asm volatile("" : "+v"(aa1));
  u32 ba = sb + K1_XR + c * 4608 + 480 - 32 * n + 16 * g + half * 256;
  f32x4 acc[8];
#pragma unroll
  for (int i = 0; i < 8; ++i) acc[i] = f32x4{0.f, 0.f, 0.f, 0.f};
  u32x4 A[2][8], B[2][8];
  auto ld = [&](int buf, int c0) {
#pragma unroll
    for (int i = 0; i < 8; ++i) {
      u32x2 x0 = *(LDSP const u32x2*)(size_t)(aa0 + 512 * i + 64 * c0);
      u32x2 x1 = *(LDSP const u32x2*)(size_t)(aa1 + 512 * i + 64 * c0);
      A[buf][i] = u32x4{x0[0], x0[1], x1[0], x1[1]};
      B[buf][i] = *(LDSP const u32x4*)(size_t)(ba + 512 * (7 - i) + 64 * c0);
    }
  };
  ld(0, 0);
#pragma unroll
  for (int c0 = 0; c0 < 4; ++c0) {
    if (c0 < 3) ld((c0 + 1) & 1, c0 + 1);
#pragma unroll
    for (int c1 = 0; c1 < 8; ++c1)
#pragma unroll
      for (int u = 0; u < 8 - c1; ++u) acc[c1 + u] = mfma16(A[c0 & 1][c1], B[c0 & 1][u], acc[c1 + u]);
  }
  {
    f32x4* red = (f32x4*)(smem + K1_RED);
    f32x4 keep[4];
#pragma unroll
    for (int i = 0; i < 4; ++i) {
      f32x4 lo4 = acc[i], hi4 = acc[4 + i];
      asm volatile("" : "+v"(lo4), "+v"(hi4));
      red[(w * 4 + i) * 64 + lane] = half ? lo4 : hi4;
      keep[i] = half ? hi4 : lo4;
    }
    __syncthreads();
#pragma unroll
    for (int i = 0; i < 4; ++i) acc[i] = keep[i] + red[((w ^ 1) * 4 + i) * 64 + lane];
  }
  u16* hT = (u16*)(ws + WS_HT);
#pragma unroll
  for (int i = 0; i < 4; ++i) {
    const int s = 256 * (4 * half + i) + 16 * n + 4 * g;
    const u32x2 xq = *(LDSP const u32x2*)(size_t)(sb + K1_XR + c * 4608 + 2 * (2044 - s));
    const float xs[4] = {bhi(xq[1]), blo(xq[1]), bhi(xq[0]), blo(xq[0])};
    const float qs[4] = {blo(qv[i][0]), bhi(qv[i][0]), blo(qv[i][1]), bhi(qv[i][1])};
    const float mu[4] = {st[i][0][0], st[i][0][2], st[i][1][0], st[i][1][2]};
    const float rs[4] = {st[i][0][1], st[i][0][3], st[i][1][1], st[i][1][3]};
    float h[4];
#pragma unroll
    for (int r = 0; r < 4; ++r) h[r] = qs[r] + (qs[r] - mu[r]) * rs[r] * (acc[i][r] + xs[r] * dsk);
    *(u32x2*)(hT + (size_t)(ch0 + c) * S + s) = u32x2{pk2(h[0], h[1]), pk2(h[2], h[3])};
  }
}

constexpr int K2_ST = 131072;
constexpr int K2_LDS = K2_ST + 1024;
constexpr int K2_DEPTH = 8;

DI void glds16(u32 voff, const void* sbase, u32 ldsaddr) {
  asm volatile("s_mov_b32 m0, %2\n\ts_nop 0\n\tglobal_load_lds_dwordx4 %0, %1" ::"v"(voff), "s"(sbase), "s"(ldsaddr) : "memory");
}
DI void gload16(u32x4& dst, u32 voff, const void* sbase) {
  asm volatile("global_load_dwordx4 %0, %1, %2" : "=v"(dst) : "v"(voff), "s"(sbase) : "memory");
}
#define WAIT_VM(N) asm volatile("s_waitcnt vmcnt(" #N ")" ::: "memory")

template <int N> DI void wait_vm() { asm volatile("s_waitcnt vmcnt(%0)" ::"n"(N) : "memory"); }

__global__ __launch_bounds__(256) void k2_gemm1(char* __restrict__ ws) {
  extern __shared__ __attribute__((aligned(16))) char smem[];
  const int t = threadIdx.x, lane = t & 63;
  const int w = __builtin_amdgcn_readfirstlane(t >> 6);
  const int b = blockIdx.x, xcd = b & 7, bi = b >> 3;
  const int rt = (xcd >> 1) * 4 + (bi & 3), j = (xcd & 1) * 8 + (bi >> 2);
  const int U0 = slice_u0(j), nu = slice_nu(j);
  const int rem = nu - 8;
  const int nw = w < rem ? 3 : 2;
  const int uw = U0 + (w < rem ? 3 * w : 3 * rem + 2 * (w - rem));
  const int mode = nw == 3 ? 0 : (w == 3 ? 1 : 2);
  const int n = lane & 15, g = lane >> 4;
  const u32 sb = (u32)(size_t)(LDSP char*)smem;
  const int s0 = 128 * rt;

  f32x4 csp[3];
#pragma unroll
  for (int u = 0; u < 3; ++u) {
    int uu = uw + (u < nw ? u : 0);
    csp[u] = *(const f32x4*)(ws + WS_CS + ((size_t)((lane >> 2) * NU + uu) * 16 + 4 * (lane & 3)) * 4);
  }

  const int c16 = lane & 15;
  const u32 gv = (u32)(g * 4096 + (((c16 >> 1) ^ (g | ((w & 1) << 2))) * 32 + (c16 & 1) * 16));
  const char* hbase = ws + WS_HT + (size_t)s0 * 2;
  const char* wbase[3];
#pragma unroll
  for (int u = 0; u < 3; ++u) wbase[u] = ws + WS_W1 + (size_t)(uw + (u < nw ? u : 0)) * KS1 * 1024;
  const u32 wv = lane * 16;

  u32x4 W[K2_DEPTH][3];
  auto issue = [&](int ks) {
#pragma unroll
    for (int u = 0; u < 3; ++u) gload16(W[ks % K2_DEPTH][u], wv, wbase[u] + ks * 1024);
#pragma unroll
    for (int i = 0; i < 2; ++i) {
      const int d0 = 32 * ks + 4 * (2 * w + i);
      glds16(gv, hbase + (size_t)d0 * 4096, sb + d0 * 256);
    }
  };
#pragma unroll
  for (int ks = 0; ks < K2_DEPTH; ++ks) issue(ks);

  const int qd = n >> 2, p = n & 3;
  u32 ra[8][1];
  const int frow = qd | ((g & 1) << 2);
#pragma unroll
  for (int i = 0; i < 8; ++i) {
    int sf = (i + 2 * w) & 7;
    ra[i][0] = sb + (8 * g + qd) * 256 + ((sf ^ frow) * 32) + 8 * p;
  }
  f32x4 acc[3][8], sum2[2];
#pragma unroll
  for (int u = 0; u < 3; ++u)
#pragma unroll
    for (int i = 0; i < 8; ++i) acc[u][i] = f32x4{0.f, 0.f, 0.f, 0.f};
  sum2[0] = sum2[1] = f32x4{0.f, 0.f, 0.f, 0.f};
  const u32x4 ones = {0x3f803f80u, 0x3f803f80u, 0x3f803f80u, 0x3f803f80u};
  u32x4 Bf[2][8];
  auto rdB = [&](int ks) {
#pragma unroll
    for (int i = 0; i < 8; ++i) Bf[ks & 1][i] = tr_frag(ra[i][0] + ks * 8192, ra[i][0] + ks * 8192 + 1024);
  };
  u16* hrow = (u16*)(ws + WS_HR);

  wait_vm<(K2_DEPTH - 1) * 5>();
  __builtin_amdgcn_s_barrier();
  rdB(0);
#pragma unroll
  for (int ks = 0; ks < KS1; ++ks) {
    if (ks + 1 < KS1) {
      constexpr int dummy = 0; (void)dummy;
      const int younger = (ks + K2_DEPTH <= KS1 ? K2_DEPTH - 2 : KS1 - ks - 2) * 5;
      switch (younger) {
        case 30: wait_vm<30>(); break; case 25: wait_vm<25>(); break; case 20: wait_vm<20>(); break;
        case 15: wait_vm<15>(); break; case 10: wait_vm<10>(); break; case 5: wait_vm<5>(); break; default: wait_vm<0>(); break;
      }
      __builtin_amdgcn_s_barrier();
    }
    u32x4 Wc[3];
#pragma unroll
    for (int u = 0; u < 3; ++u) { asm volatile("" : "+v"(W[ks % K2_DEPTH][u])); Wc[u] = W[ks % K2_DEPTH][u]; }
    if (ks + 1 < KS1) rdB(ks + 1);
#pragma unroll
    for (int i = 0; i < 8; ++i) {
      acc[0][i] = mfma16(Wc[0], Bf[ks & 1][i], acc[0][i]);
      acc[1][i] = mfma16(Wc[1], Bf[ks & 1][i], acc[1][i]);
    }
    if (mode == 0) {
#pragma unroll
      for (int i = 0; i < 8; ++i) acc[2][i] = mfma16(Wc[2], Bf[ks & 1][i], acc[2][i]);
    } else if (mode == 1) {
#pragma unroll
      for (int i = 0; i < 8; ++i) acc[2][i] = mfma16(Bf[ks & 1][i], Bf[ks & 1][i], acc[2][i]);
    }
    sum2[0] = mfma16(ones, Bf[ks & 1][0], sum2[0]);
    sum2[1] = mfma16(ones, Bf[ks & 1][1], sum2[1]);
    if (ks == j) {
#pragma unroll
      for (int i = 0; i < 2; ++i) {
        int sf = (i + 2 * w) & 7;
        *(u32x4*)(hrow + (size_t)(s0 + 16 * sf + n) * D + 32 * ks + 8 * g) = Bf[ks & 1][i];
      }
    }
    if (ks + K2_DEPTH < KS1) issue(ks + K2_DEPTH);
  }

  float* stl = (float*)(smem + K2_ST);
  if (g == 0) {
#pragma unroll
    for (int i = 0; i < 2; ++i) stl[16 * ((i + 2 * w) & 7) + n] = sum2[i][0];
  }
  if (mode == 1 && g == (n >> 2)) {
#pragma unroll
    for (int i = 0; i < 8; ++i) {
      f32x4 a = acc[2][i];
      float dg = (n & 2) ? ((n & 1) ? a[3] : a[2]) : ((n & 1) ? a[1] : a[0]);
      stl[128 + 16 * ((i + 2 * w) & 7) + n] = dg;
    }
  }
  f32x4 cs[3];
#pragma unroll
  for (int u = 0; u < 3; ++u) {
#pragma unroll
    for (int e = 0; e < 4; ++e) {
      float v = csp[u][e];
#pragma unroll
      for (int o = 4; o < 64; o <<= 1) v += __shfl_xor(v, o);
      cs[u][e] = __shfl(v, g);
    }
  }
  __syncthreads();
  char* Hb = ws + WS_H;
#pragma unroll
  for (int i = 0; i < 8; ++i) {
    const int sf = (i + 2 * w) & 7;
    const float mean = stl[16 * sf + n] * (1.f / D);
    const float var = stl[128 + 16 * sf + n] * (1.f / D) - mean * mean;
    const float rstd = 1.f / sqrtf(fmaxf(var, 0.f) + EPS);
    const float ce = -1.4426950408889634f * rstd;
    u32 hv[4] = {0, 0, 0, 0};
#pragma unroll
    for (int u = 0; u < 3; ++u) {
      f32x4 a = acc[u][i];
      float o[2];
#pragma unroll
      for (int r = 0; r < 2; ++r) {
        float tg = a[r] - mean * cs[u][r];
        float tu = a[2 + r] - mean * cs[u][2 + r];
        float e = __builtin_amdgcn_exp2f(tg * ce);
        o[r] = tu * rstd * __builtin_amdgcn_rcpf(1.f + e);
      }
      hv[u] = pk2(o[0], o[1]);
    }
    auto s02 = __builtin_amdgcn_permlane32_swap(hv[0], hv[2], false, false);
    auto s13 = __builtin_amdgcn_permlane32_swap(hv[1], hv[3], false, false);
    auto t01 = __builtin_amdgcn_permlane16_swap(s02[0], s13[0], false, false);
    auto t23 = __builtin_amdgcn_permlane16_swap(s02[1], s13[1], false, false);
    if (g < nw) {
      const int U = uw + g;
      u32x4 o4 = {t01[0], t01[1], t23[0], t23[1]};
      *(u32x4*)(Hb + ((size_t)((8 * rt + sf) * KS2 + (U >> 2)) * 64 + (U & 3) * 16 + n) * 16) = o4;
    }
  }
}

constexpr int K3_LDS = 4 * 3 * 4096;
constexpr int K3_DEPTH = 4;

__global__ __launch_bounds__(256) void k3_gemm2(const char* __restrict__ ws, u16* __restrict__ out) {
  extern __shared__ __attribute__((aligned(16))) char smem[];
  const int t = threadIdx.x, lane = t & 63;
  const int w = __builtin_amdgcn_readfirstlane(t >> 6);
  const int b = blockIdx.x, xcd = b & 7, bi = b >> 3;
  const int rt = xcd * 4 + (bi & 3), dt = bi >> 2;
  const int n = lane & 15, g = lane >> 4;
  const char* wa = ws + WS_WD + (size_t)dt * KS2 * 4096 + lane * 16;
  const char* hb = ws + WS_H + (size_t)(4 * rt) * KS2 * 1024 + lane * 16;
  const u16* hrow = (const u16*)(ws + WS_HR);
  const int srow = 64 * rt + 16 * w + n, dcol = 64 * dt + 16 * g;
  const u32x4 r0 = *(const u32x4*)(hrow + (size_t)srow * D + dcol), r1 = *(const u32x4*)(hrow + (size_t)srow * D + dcol + 8);

  f32x4 acc[4][4];
#pragma unroll
  for (int i = 0; i < 4; ++i)
#pragma unroll
    for (int jj = 0; jj < 4; ++jj) acc[i][jj] = f32x4{0.f, 0.f, 0.f, 0.f};
  u32x4 A[K3_DEPTH][4], B[K3_DEPTH][4];
  auto ld = [&](int it) {
    int ks = w + 4 * it; ks = ks < KS2 ? ks : KS2 - 1;
#pragma unroll
    for (int i = 0; i < 4; ++i) {
      A[it % K3_DEPTH][i] = *(const u32x4*)(wa + (size_t)ks * 4096 + i * 1024);
      B[it % K3_DEPTH][i] = *(const u32x4*)(hb + ((size_t)i * KS2 + ks) * 1024);
    }
  };
#pragma unroll
  for (int it = 0; it < K3_DEPTH; ++it) ld(it);
#pragma unroll
  for (int it = 0; it < 11; ++it) {
    if (it < 10 || w < 3) {
#pragma unroll
      for (int df = 0; df < 4; ++df)
#pragma unroll
        for (int sf = 0; sf < 4; ++sf) acc[df][sf] = mfma16(A[it % K3_DEPTH][df], B[it % K3_DEPTH][sf], acc[df][sf]);
    }
    if (it + K3_DEPTH < 11) ld(it + K3_DEPTH);
    __builtin_amdgcn_sched_barrier(0);
  }
  f32x4* red = (f32x4*)smem;
#pragma unroll
  for (int o = 1; o < 4; ++o) {
    const int dst = (w + o) & 3;
#pragma unroll
    for (int df = 0; df < 4; ++df) {
      f32x4 vsel = dst == 0 ? acc[df][0] : dst == 1 ? acc[df][1] : dst == 2 ? acc[df][2] : acc[df][3];
      red[((dst * 3 + (o - 1)) * 4 + df) * 64 + lane] = vsel;
    }
  }
  f32x4 fin[4];
#pragma unroll
  for (int df = 0; df < 4; ++df) fin[df] = w == 0 ? acc[df][0] : w == 1 ? acc[df][1] : w == 2 ? acc[df][2] : acc[df][3];
  __syncthreads();
#pragma unroll
  for (int o = 0; o < 3; ++o)
#pragma unroll
    for (int df = 0; df < 4; ++df) fin[df] += red[((w * 3 + o) * 4 + df) * 64 + lane];
  u32x4 o0, o1;
#pragma unroll
  for (int e = 0; e < 4; ++e) {
    const int df0 = e >> 1, rr = (e & 1) * 2;
    o0[e] = pk2(fin[df0][rr] + blo(r0[e]), fin[df0][rr + 1] + bhi(r0[e]));
    o1[e] = pk2(fin[2 + df0][rr] + blo(r1[e]), fin[2 + df0][rr + 1] + bhi(r1[e]));
  }
  *(u32x4*)(out + (size_t)srow * D + dcol) = o0;
  *(u32x4*)(out + (size_t)srow * D + dcol + 8) = o1;
}

extern "C" void kernel_launch(void* const* d_in, const int* in_sizes, int n_in, void* d_out, int out_size, void* d_ws,
                              size_t ws_size, hipStream_t stream) {
  const u16 *q = (const u16*)d_in[0], *k = (const u16*)d_in[1], *v = (const u16*)d_in[2], *shk = (const u16*)d_in[3];
  const u16 *ssmk = (const u16*)d_in[4], *dskip = (const u16*)d_in[5];
  const u16 *wg = (const u16*)d_in[7], *wu = (const u16*)d_in[8], *wd = (const u16*)d_in[9];
  char* ws = (char*)d_ws;
  hipFuncSetAttribute((const void*)k1_conv, hipFuncAttributeMaxDynamicSharedMemorySize, K1_LDS);
  hipFuncSetAttribute((const void*)k2_gemm1, hipFuncAttributeMaxDynamicSharedMemorySize, K2_LDS);
  hipFuncSetAttribute((const void*)k3_gemm2, hipFuncAttributeMaxDynamicSharedMemorySize, K3_LDS);
  k0_prep<<<256 + 256 + KS2 * 4, 256, 0, stream>>>(q, k, v, shk, wg, wu, wd, ws);
  k1_conv<<<256, 256, K1_LDS, stream>>>(ssmk, dskip, ws);
  k2_gemm1<<<256, 256, K2_LDS, stream>>>(ws);
  k3_gemm2<<<256, 256, K3_LDS, stream>>>(ws, (u16*)d_out);
}
```

```cpp
#include <hip/hip_runtime.h>
#include <stdint.h>
#include <utility>

typedef unsigned short u16;
typedef unsigned u32;
typedef __attribute__((ext_vector_type(2))) u32 u32x2;
typedef __attribute__((ext_vector_type(4))) u32 u32x4;
typedef __attribute__((ext_vector_type(2))) float f32x2;
typedef __attribute__((ext_vector_type(4))) float f32x4;
typedef __attribute__((ext_vector_type(2))) __bf16 bf16x2;
typedef __attribute__((ext_vector_type(8))) __bf16 bf16x8;
typedef __attribute__((ext_vector_type(4))) short s16x4;
#define LDSP __attribute__((address_space(3)))
#define DI __device__ __forceinline__

constexpr int S = 2048, D = 512, F = 1376;
constexpr int NU = F / 8;
constexpr int KS1 = D / 32;
constexpr int KS2 = F / 32;
constexpr float EPS = 1e-5f;

constexpr size_t WS_XR = 0;
constexpr size_t WS_QT = WS_XR + (size_t)D * S * 2;
constexpr size_t WS_HT = WS_QT + (size_t)D * S * 2;
constexpr size_t WS_W1 = WS_HT + (size_t)D * S * 2;
constexpr size_t WS_WD = WS_W1 + (size_t)NU * KS1 * 1024;
constexpr size_t WS_H = WS_WD + (size_t)8 * KS2 * 4 * 1024;
constexpr size_t WS_CS = WS_H + (size_t)(S / 16) * KS2 * 1024;
constexpr size_t WS_ST = WS_CS + (size_t)KS1 * NU * 16 * 4;

template <int... Is, class Fn> DI void static_for_(std::integer_sequence<int, Is...>, Fn&& f) { (f(std::integral_constant<int, Is>{}), ...); }
template <int N, class Fn> DI void static_for(Fn&& f) { static_for_(std::make_integer_sequence<int, N>{}, f); }

DI float blo(u32 u) { return __uint_as_float(u << 16); }
DI float bhi(u32 u) { return __uint_as_float(u & 0xffff0000u); }
DI u32 pk2(float a, float b) { f32x2 t = {a, b}; return __builtin_bit_cast(u32, __builtin_convertvector(t, bf16x2)); }
DI f32x4 mfma16(u32x4 a, u32x4 b, f32x4 c) {
  return __builtin_amdgcn_mfma_f32_16x16x32_bf16(__builtin_bit_cast(bf16x8, a), __builtin_bit_cast(bf16x8, b), c, 0, 0, 0);
}
DI u32x2 tr_read(u32 lds_byte) {
  return __builtin_bit_cast(u32x2, __builtin_amdgcn_ds_read_tr16_b64_v4i16((LDSP s16x4*)(size_t)lds_byte));
}
DI u32x4 tr_frag(u32 a0, u32 a1) { u32x2 x = tr_read(a0), y = tr_read(a1); return u32x4{x[0], x[1], y[0], y[1]}; }
DI u32x4 cat(u32x2 x, u32x2 y) { return __builtin_shufflevector(x, y, 0, 1, 2, 3); }
template <int CTRL> DI float dpp_add(float v) {
  return v + __builtin_bit_cast(float, __builtin_amdgcn_update_dpp(0, __builtin_bit_cast(int, v), CTRL, 0xf, 0xf, true));
}
DI float sum_rows(float v) {
  u32 u = __float_as_uint(v);
  auto a = __builtin_amdgcn_permlane16_swap(u, u, false, false);
  v = __uint_as_float(a[0]) + __uint_as_float(a[1]);
  u = __float_as_uint(v);
  auto b = __builtin_amdgcn_permlane32_swap(u, u, false, false);
  return __uint_as_float(b[0]) + __uint_as_float(b[1]);
}
DI float wave_sum(float v) {
  v = dpp_add<0xb1>(v);
  v = dpp_add<0x4e>(v);
  v = dpp_add<0x124>(v);
  v = dpp_add<0x128>(v);
  return sum_rows(v);
}
DI int slice_u0(int j) { return j < 12 ? 11 * j : 132 + 10 * (j - 12); }
DI int slice_nu(int j) { return j < 12 ? 11 : 10; }

DI void mfma_vv(f32x4& acc, const u32x4& a, const u32x4& b) {
  asm volatile("v_mfma_f32_16x16x32_bf16 %0, %1, %2, %0" : "+v"(acc) : "v"(a), "v"(b));
}
DI void mfma_av(f32x4& acc, const u32x4& a, const u32x4& b) {
  asm volatile("v_mfma_f32_16x16x32_bf16 %0, %1, %2, %0" : "+v"(acc) : "a"(a), "v"(b));
}
template <int OFF> DI void ds_b64(u32x2& d, u32 addr) { asm volatile("ds_read_b64 %0, %1 offset:%c2" : "=v"(d) : "v"(addr), "i"(OFF) : "memory"); }
template <int OFF> DI void ds_b128(u32x4& d, u32 addr) { asm volatile("ds_read_b128 %0, %1 offset:%c2" : "=v"(d) : "v"(addr), "i"(OFF) : "memory"); }
template <int OFF> DI void ds_tr(u32x2& d, u32 addr) { asm volatile("ds_read_b64_tr_b16 %0, %1 offset:%c2" : "=v"(d) : "v"(addr), "i"(OFF) : "memory"); }
template <int OFF> DI void gld_a(u32x4& d, u32 voff, const void* sbase) {
  asm volatile("global_load_dwordx4 %0, %1, %2 offset:%c3" : "=a"(d) : "v"(voff), "s"(sbase), "i"(OFF) : "memory");
}
template <int OFF> DI void gld_v(u32x4& d, u32 voff, const void* sbase) {
  asm volatile("global_load_dwordx4 %0, %1, %2 offset:%c3" : "=v"(d) : "v"(voff), "s"(sbase), "i"(OFF) : "memory");
}
template <int LOFF> DI void glds16(u32 voff, const void* sbase, u32 ldsbase) {
  asm volatile("s_add_u32 m0, %2, %c3\n\ts_nop 0\n\tglobal_load_lds_dwordx4 %0, %1" ::"v"(voff), "s"(sbase), "s"(ldsbase), "i"(LOFF) : "memory", "scc");
}
template <int N> DI void wait_vm() { asm volatile("s_waitcnt vmcnt(%c0)" ::"i"(N) : "memory"); }
template <int N> DI void wait_lgkm() { asm volatile("s_waitcnt lgkmcnt(%c0)" ::"i"(N) : "memory"); }
DI void barrier() { asm volatile("s_barrier" ::: "memory"); }
DI void pin(u32x4& x) { asm volatile("" : "+v"(x)); }
DI void pin(u32x2& x) { asm volatile("" : "+v"(x)); }
DI void pin_a(u32x4& x) { asm volatile("" : "+a"(x)); }

#define STAMP(kern, idx) do {} while (0)

__global__ __launch_bounds__(256) void k0_prep(const u16* __restrict__ q, const u16* __restrict__ k, const u16* __restrict__ v,
                                               const u16* __restrict__ shk, char* __restrict__ ws) {
  const int t = threadIdx.x, lane = t & 63, w = t >> 6;
  const int b = blockIdx.x;
  char* ws_stamp = ws; (void)ws_stamp;
  STAMP(0, 0);
  const int x = b & 7, i = b >> 3;
  const int rg = lane & 7, cp = (lane >> 3) + 8 * w;
  const int d = 64 * x + 2 * cp, r0 = 64 * i + 8 * rg;
  u32 kk[11], vv[8], qq[8];
#pragma unroll
  for (int j = 0; j < 11; ++j) {
    int row = r0 - 3 + j;
    u32 val = *(const u32*)(k + (size_t)(row < 0 ? 0 : row) * D + d);
    kk[j] = row < 0 ? 0u : val;
  }
#pragma unroll
  for (int j = 0; j < 8; ++j) vv[j] = *(const u32*)(v + (size_t)(r0 + j) * D + d);
  const u32x4 sh = *(const u32x4*)(shk + d * 4);
#pragma unroll
  for (int j = 0; j < 8; ++j) qq[j] = *(const u32*)(q + (size_t)(r0 + j) * D + d);
  const int srow = 8 * b + 2 * w;
  const u32x4 qa = *(const u32x4*)(q + (size_t)srow * D + 8 * lane);
  const u32x4 qb = *(const u32x4*)(q + (size_t)(srow + 1) * D + 8 * lane);

  const float a0 = blo(sh[0]), a1 = bhi(sh[0]), a2 = blo(sh[1]), a3 = bhi(sh[1]);
  const float b0 = blo(sh[2]), b1 = bhi(sh[2]), b2 = blo(sh[3]), b3 = bhi(sh[3]);
  float xl[8], xh[8];
#pragma unroll
  for (int e = 0; e < 8; ++e) {
    float sl = blo(kk[e + 3]) * a0 + blo(kk[e + 2]) * a1 + blo(kk[e + 1]) * a2 + blo(kk[e]) * a3;
    float sh2 = bhi(kk[e + 3]) * b0 + bhi(kk[e + 2]) * b1 + bhi(kk[e + 1]) * b2 + bhi(kk[e]) * b3;
    xl[e] = sl * blo(vv[e]);
    xh[e] = sh2 * bhi(vv[e]);
  }
  u16* xr = (u16*)(ws + WS_XR);
  *(u32x4*)(xr + (size_t)d * S + (2040 - r0)) = u32x4{pk2(xl[7], xl[6]), pk2(xl[5], xl[4]), pk2(xl[3], xl[2]), pk2(xl[1], xl[0])};
  *(u32x4*)(xr + (size_t)(d + 1) * S + (2040 - r0)) = u32x4{pk2(xh[7], xh[6]), pk2(xh[5], xh[4]), pk2(xh[3], xh[2]), pk2(xh[1], xh[0])};
  u16* qT = (u16*)(ws + WS_QT);
  u32x4 ql, qh;
#pragma unroll
  for (int e = 0; e < 4; ++e) {
    ql[e] = __builtin_amdgcn_perm(qq[2 * e + 1], qq[2 * e], 0x05040100u);
    qh[e] = __builtin_amdgcn_perm(qq[2 * e + 1], qq[2 * e], 0x07060302u);
  }
  *(u32x4*)(qT + (size_t)d * S + r0) = ql;
  *(u32x4*)(qT + (size_t)(d + 1) * S + r0) = qh;

  float sa = 0.f, sb = 0.f;
#pragma unroll
  for (int e = 0; e < 4; ++e) { sa += blo(qa[e]) + bhi(qa[e]); sb += blo(qb[e]) + bhi(qb[e]); }
  const float ma = wave_sum(sa) * (1.f / D), mb = wave_sum(sb) * (1.f / D);
  float va = 0.f, vb = 0.f;
#pragma unroll
  for (int e = 0; e < 4; ++e) {
    float u0 = blo(qa[e]) - ma, u1 = bhi(qa[e]) - ma, u2 = blo(qb[e]) - mb, u3 = bhi(qb[e]) - mb;
    va += u0 * u0 + u1 * u1; vb += u2 * u2 + u3 * u3;
  }
  va = wave_sum(va) * (1.f / D); vb = wave_sum(vb) * (1.f / D);
  if (lane == 0) {
    f32x4 o = {ma, __builtin_amdgcn_rsqf(va + EPS), mb, __builtin_amdgcn_rsqf(vb + EPS)};
    *(f32x4*)(ws + WS_ST + (size_t)srow * 8) = o;
  }
  STAMP(0, 1);
}

DI void w_job(int b, const u16* __restrict__ wg, const u16* __restrict__ wu, const u16* __restrict__ wd, char* __restrict__ ws, char* smem) {
  const int t = threadIdx.x, lane = t & 63, w = t >> 6;
  char* tile = smem;
  char* tile2 = smem + 32 * 368;
  const u32 tb = (u32)(size_t)(LDSP char*)smem;
  const int i16 = lane & 15, qd = i16 >> 2, p = i16 & 3, g = lane >> 4;
  const int ks = b & 15, j = b >> 4;
  const int U0 = slice_u0(j), nu = slice_nu(j);
  u32x4 g4[2], u4[2], d4[2];
#pragma unroll
  for (int rep = 0; rep < 2; ++rep) {
    int idx = t + 256 * rep, r = idx / 11, u = idx - 11 * r;
    bool ok = r < 32 && u < nu;
    size_t off = ok ? (size_t)(32 * ks + r) * F + 8 * (U0 + u) : 0;
    g4[rep] = *(const u32x4*)(wg + off); u4[rep] = *(const u32x4*)(wu + off);
  }
  const int ks2 = b >> 2, dp = b & 3;
  if (b < KS2 * 4) {
#pragma unroll
    for (int rep = 0; rep < 2; ++rep) {
      int idx = t + 256 * rep, r = idx >> 4, c = idx & 15;
      d4[rep] = *(const u32x4*)(wd + (size_t)(32 * ks2 + r) * D + 128 * dp + 8 * c);
    }
  }
#pragma unroll
  for (int rep = 0; rep < 2; ++rep) {
    int idx = t + 256 * rep, r = idx / 11, u = idx - 11 * r;
    if (r < 32 && u < nu) {
      *(u32x4*)(tile + r * 368 + u * 32) = u32x4{g4[rep][0], u4[rep][0], g4[rep][1], u4[rep][1]};
      *(u32x4*)(tile + r * 368 + u * 32 + 16) = u32x4{g4[rep][2], u4[rep][2], g4[rep][3], u4[rep][3]};
    }
  }
  if (b < KS2 * 4) {
#pragma unroll
    for (int rep = 0; rep < 2; ++rep) { int idx = t + 256 * rep, r = idx >> 4, c = idx & 15; *(u32x4*)(tile2 + r * 272 + c * 16) = d4[rep]; }
  }
  __syncthreads();
  const u32x4 ones = {0x3f803f80u, 0x3f803f80u, 0x3f803f80u, 0x3f803f80u};
  for (int u = w; u < nu; u += 4) {
    u32 a = tb + (8 * g + qd) * 368 + u * 32 + 8 * p;
    u32x4 fr = tr_frag(a, a + 4 * 368);
    *(u32x4*)(ws + WS_W1 + ((size_t)((U0 + u) * KS1 + ks) * 64 + lane) * 16) = fr;
    f32x4 cs = mfma16(fr, ones, f32x4{0.f, 0.f, 0.f, 0.f});
    if (i16 == 0) *(f32x4*)(ws + WS_CS + ((size_t)(ks * NU + U0 + u) * 16 + 4 * g) * 4) = cs;
  }
  if (b < KS2 * 4) {
#pragma unroll
    for (int rep = 0; rep < 2; ++rep) {
      int fi = w + 4 * rep, dtl = fi >> 2, df = fi & 3;
      u32 a = tb + 32 * 368 + (8 * g + qd) * 272 + (64 * dtl + 16 * p + 4 * df) * 2;
      u32x4 fr = tr_frag(a, a + 4 * 272);
      *(u32x4*)(ws + WS_WD + ((size_t)(((2 * dp + dtl) * KS2 + ks2) * 4 + df) * 64 + lane) * 16) = fr;
    }
  }
}

constexpr int K1_CS = 4160;
constexpr int K1_KC = 4 * K1_CS;
constexpr int K1_XR = 2 * K1_KC;
constexpr int K1_RED = K1_XR + 2 * 4608;
constexpr int K1_LDS = K1_RED + 4 * 4096;

__global__ __launch_bounds__(256, 2) void k1_conv(const u16* __restrict__ ssmk, const u16* __restrict__ dskip, const u16* __restrict__ wg,
                                                  const u16* __restrict__ wu, const u16* __restrict__ wd, char* __restrict__ ws) {
  extern __shared__ __attribute__((aligned(16))) char smem[];
  const int t = threadIdx.x, lane = t & 63;
  const int w = __builtin_amdgcn_readfirstlane(t >> 6);
  const int b = blockIdx.x;
  char* ws_stamp = ws; (void)ws_stamp;
  STAMP(1, 0);
  if (b >= 256) { w_job(b - 256, wg, wu, wd, ws, smem); STAMP(1, 4); return; }
  const int ch0 = (b & 7) * 64 + (b >> 3) * 2;
  const u16* xr = (const u16*)(ws + WS_XR);
  const u16* qT = (const u16*)(ws + WS_QT);
  const int c = w >> 1, half = w & 1;
  const int n = lane & 15, g = lane >> 4;

  u32x4 kp[2], xv[2], kc[2];
#pragma unroll
  for (int cc = 0; cc < 2; ++cc) {
    const u16* kr = ssmk + (size_t)(ch0 + cc) * S;
    u32x4 pv = *(const u32x4*)(kr + 8 * (t > 0 ? t - 1 : 0));
    kp[cc] = t > 0 ? pv : u32x4{0, 0, 0, 0};
    kc[cc] = *(const u32x4*)(kr + 8 * t);
    xv[cc] = *(const u32x4*)(xr + (size_t)(ch0 + cc) * S + 8 * t);
  }
  u32x2 qv[4]; f32x4 st[4][2];
  const float2* stp = (const float2*)(ws + WS_ST);
#pragma unroll
  for (int i = 0; i < 4; ++i) {
    int s = 256 * (4 * half + i) + 16 * n + 4 * g;
    qv[i] = *(const u32x2*)(qT + (size_t)(ch0 + c) * S + s);
    st[i][0] = *(const f32x4*)(stp + s);
    st[i][1] = *(const f32x4*)(stp + s + 2);
  }
  const float dsk = blo(dskip[ch0 + c]);

#pragma unroll
  for (int cc = 0; cc < 2; ++cc) {
    u32 wv[6] = {kp[cc][0], kp[cc][1], kp[cc][2], kp[cc][3], kc[cc][0], kc[cc][1]};
    u32 o1[5];
#pragma unroll
    for (int i = 0; i < 5; ++i) o1[i] = __builtin_amdgcn_alignbit(wv[i + 1], wv[i], 16);
    char* base = smem + cc * K1_KC + (t + 1) * 16;
    *(u32x4*)(base + 0 * K1_CS) = u32x4{o1[0], o1[1], o1[2], o1[3]};
    *(u32x4*)(base + 1 * K1_CS) = u32x4{wv[1], wv[2], wv[3], wv[4]};
    *(u32x4*)(base + 2 * K1_CS) = u32x4{o1[1], o1[2], o1[3], o1[4]};
    *(u32x4*)(base + 3 * K1_CS) = u32x4{wv[2], wv[3], wv[4], wv[5]};
    *(u32x4*)(smem + K1_XR + cc * 4608 + t * 16) = xv[cc];
    if (t == 255) {
      const u32 z = 0;
      const u32 e0 = __builtin_amdgcn_alignbit(kc[cc][1], kc[cc][0], 16), e1 = __builtin_amdgcn_alignbit(kc[cc][2], kc[cc][1], 16);
      const u32 e2 = __builtin_amdgcn_alignbit(kc[cc][3], kc[cc][2], 16), e3 = __builtin_amdgcn_alignbit(z, kc[cc][3], 16);
      *(u32x4*)(base + 16 + 0 * K1_CS) = u32x4{e0, e1, e2, e3};
      *(u32x4*)(base + 16 + 1 * K1_CS) = u32x4{kc[cc][1], kc[cc][2], kc[cc][3], z};
      *(u32x4*)(base + 16 + 2 * K1_CS) = u32x4{e1, e2, e3, z};
      *(u32x4*)(base + 16 + 3 * K1_CS) = u32x4{kc[cc][2], kc[cc][3], z, z};
    }
  }
  if (t < 8) *(u32x4*)(smem + (t >> 2) * K1_KC + (t & 3) * K1_CS) = u32x4{0, 0, 0, 0};
  if (t >= 64 && t < 64 + 60) { int i = t - 64; *(u32x4*)(smem + K1_XR + (i / 30) * 4608 + 4096 + (i % 30) * 16) = u32x4{0, 0, 0, 0}; }
  __syncthreads();
  STAMP(1, 1);

  const u32 sb = (u32)(size_t)(LDSP char*)smem;
  const u32 aa = sb + c * K1_KC + (n & 3) * K1_CS + 16 * g + 2 * (n & 12) + half * 256;
  const u32 ba = sb + K1_XR + c * 4608 + 480 - 32 * n + 16 * g + half * 256;
  f32x4 acc[8];
#pragma unroll
  for (int i = 0; i < 8; ++i) acc[i] = f32x4{0.f, 0.f, 0.f, 0.f};
  u32x2 Alo[2][8], Ahi[2][8];
  u32x4 B[2][8];
  auto rd = [&](auto C0, auto R) {
    constexpr int c0 = C0, r = R, buf = c0 & 1;
    if constexpr (r < 16) {
      constexpr int i = r >> 1;
      if constexpr ((r & 1) == 0) ds_b64<512 * i + 64 * c0>(Alo[buf][i], aa);
      else ds_b64<512 * i + 64 * c0 + 8>(Ahi[buf][i], aa);
    } else {
      constexpr int u = r - 16;
      ds_b128<512 * (7 - u) + 64 * c0>(B[buf][u], ba);
    }
  };
  static_for<24>([&](auto R) { rd(std::integral_constant<int, 0>{}, R); });
  static_for<4>([&](auto C0) {
    constexpr int c0 = C0, buf = c0 & 1;
    wait_lgkm<0>();
    u32x4 A[8];
#pragma unroll
    for (int i = 0; i < 8; ++i) { pin(Alo[buf][i]); pin(Ahi[buf][i]); pin(B[buf][i]); }
#pragma unroll
    for (int i = 0; i < 8; ++i) A[i] = cat(Alo[buf][i], Ahi[buf][i]);
    static_for<36>([&](auto M) {
      constexpr int m = M;
      constexpr int c1 = m < 8 ? 0 : m < 15 ? 1 : m < 21 ? 2 : m < 26 ? 3 : m < 30 ? 4 : m < 33 ? 5 : m < 35 ? 6 : 7;
      constexpr int first = c1 * 8 - c1 * (c1 - 1) / 2;
      constexpr int u = m - first;
      mfma_vv(acc[c1 + u], A[c1], B[buf][u]);
      if constexpr (c0 < 3 && m < 24) rd(std::integral_constant<int, c0 + 1>{}, M);
    });
  });
  asm volatile("s_nop 7\n\ts_nop 1" ::: "memory");
  STAMP(1, 2);
  {
    f32x4* red = (f32x4*)(smem + K1_RED);
    f32x4 keep[4];
#pragma unroll
    for (int i = 0; i < 4; ++i) {
      f32x4 lo4 = acc[i], hi4 = acc[4 + i];
      asm volatile("" : "+v"(lo4), "+v"(hi4));
      red[(w * 4 + i) * 64 + lane] = half ? lo4 : hi4;
      keep[i] = half ? hi4 : lo4;
    }
    __syncthreads();
#pragma unroll
    for (int i = 0; i < 4; ++i) acc[i] = keep[i] + red[((w ^ 1) * 4 + i) * 64 + lane];
  }
  u16* hT = (u16*)(ws + WS_HT);
#pragma unroll
  for (int i = 0; i < 4; ++i) {
    const int s = 256 * (4 * half + i) + 16 * n + 4 * g;
    const u32x2 xq = *(LDSP const u32x2*)(size_t)(sb + K1_XR + c * 4608 + 2 * (2044 - s));
    const float xs[4] = {bhi(xq[1]), blo(xq[1]), bhi(xq[0]), blo(xq[0])};
    const float qs[4] = {blo(qv[i][0]), bhi(qv[i][0]), blo(qv[i][1]), bhi(qv[i][1])};
    const float mu[4] = {st[i][0][0], st[i][0][2], st[i][1][0], st[i][1][2]};
    const float rs[4] = {st[i][0][1], st[i][0][3], st[i][1][1], st[i][1][3]};
    float h[4];
#pragma unroll
    for (int r = 0; r < 4; ++r) h[r] = qs[r] + (qs[r] - mu[r]) * rs[r] * (acc[i][r] + xs[r] * dsk);
    *(u32x2*)(hT + (size_t)(ch0 + c) * S + s) = u32x2{pk2(h[0], h[1]), pk2(h[2], h[3])};
  }
  STAMP(1, 3);
}

constexpr int K2_ST = 131072;
constexpr int K2_LDS = K2_ST + 1024;
constexpr int K2_RING = 8;

template <int GRAM>
DI void k2_main(f32x4 (&acc)[3][8], f32x4 (&rsum)[2], u32 sb, u32 gv, u32 wv, const char* hb0, const char* hb1, u32 lb0, u32 lb1,
                const char* w0, const char* w1, const char* w2, const u32 (&ra)[8], f32x4 (&csp)[3], const char* cs0, const char* cs1, const char* cs2, u32 csv, char* ws_stamp) {
  constexpr int OPS = GRAM ? 4 : 5;
  constexpr int AHEAD = K2_RING - 1;
  u32x4 W[K2_RING][3];
  u32x2 Bl[2][8], Bh[2][8];
  u32x4 ones = {0x3f803f80u, 0x3f803f80u, 0x3f803f80u, 0x3f803f80u};
  pin(ones);
  {
    u32x4 c0, c1, c2;
    gld_v<0>(c0, csv, cs0); gld_v<0>(c1, csv, cs1); gld_v<0>(c2, csv, cs2);
    auto issue = [&](auto KS, auto O) {
      constexpr int ks = KS, o = O;
#define ABL 0
      if constexpr (o == 0 && (!(ABL & 1) || ks < K2_RING)) gld_a<(ks & 3) * 1024>(W[ks % K2_RING][0], wv, w0 + (ks >> 2) * 4096);
      if constexpr (o == 1 && (!(ABL & 1) || ks < K2_RING)) gld_a<(ks & 3) * 1024>(W[ks % K2_RING][1], wv, w1 + (ks >> 2) * 4096);
      if constexpr (o == 2 && !GRAM && (!(ABL & 1) || ks < K2_RING)) gld_a<(ks & 3) * 1024>(W[ks % K2_RING][2], wv, w2 + (ks >> 2) * 4096);
      if constexpr (o == 3 && (!(ABL & 2) || ks < K2_RING)) glds16<ks * 8192>(gv, hb0 + (size_t)ks * 131072, lb0);
      if constexpr (o == 4 && (!(ABL & 2) || ks < K2_RING)) glds16<ks * 8192>(gv, hb1 + (size_t)ks * 131072, lb1);
    };
    auto rdB = [&](auto KS, auto R) {
      constexpr int ks = KS, r = R, i = r >> 1;
      if constexpr ((r & 1) == 0) ds_tr<ks * 8192 - (ks >= 8 ? 65536 : 0)>(Bl[ks & 1][i], ra[i] + (ks >= 8 ? 65536 : 0));
      else ds_tr<ks * 8192 + 1024 - (ks >= 8 ? 65536 : 0)>(Bh[ks & 1][i], ra[i] + (ks >= 8 ? 65536 : 0));
    };
    static_for<AHEAD>([&](auto KS) { static_for<5>([&](auto O) { issue(KS, O); }); });
    wait_vm<AHEAD * OPS>();
    asm volatile("" : "+v"(c0), "+v"(c1), "+v"(c2));
    csp[0] = __builtin_bit_cast(f32x4, c0); csp[1] = __builtin_bit_cast(f32x4, c1); csp[2] = __builtin_bit_cast(f32x4, c2);
    const int g = (threadIdx.x & 63) >> 4;
#pragma unroll
    for (int u = 0; u < 3; ++u)
#pragma unroll
      for (int e = 0; e < 4; ++e) {
        float v = dpp_add<0x128>(dpp_add<0x124>(csp[u][e]));
        csp[u][e] = __shfl(sum_rows(v), g);
      }
    wait_vm<(AHEAD - 1) * OPS>();
    barrier();
    static_for<16>([&](auto R) { rdB(std::integral_constant<int, 0>{}, R); });
    static_for<KS1>([&](auto KS) {
      constexpr int ks = KS, buf = ks & 1, slot = ks % K2_RING;
      wait_lgkm<0>();
      u32x4 B[8];
#pragma unroll
      for (int i = 0; i < 8; ++i) { pin(Bl[buf][i]); pin(Bh[buf][i]); }
#pragma unroll
      for (int i = 0; i < 8; ++i) B[i] = cat(Bl[buf][i], Bh[buf][i]);
      pin_a(W[slot][0]); pin_a(W[slot][1]);
      if constexpr (!GRAM) pin_a(W[slot][2]);
      static_for<24>([&](auto M) {
        constexpr int m = M, i = m / 3, u = m % 3;
        if constexpr (u < 2 || !GRAM) mfma_av(acc[u][i], W[slot][u], B[i]);
        else mfma_vv(acc[2][i], B[i], B[i]);
        if constexpr (m == 1 && ks + 1 < KS1) {
          constexpr int last = ks + AHEAD - 1 < KS1 - 1 ? ks + AHEAD - 1 : KS1 - 1;
          if constexpr (!(ABL & 3)) wait_vm<(last - (ks + 1)) * OPS>();
          if constexpr (!(ABL & 8)) barrier();
        }
        if constexpr (m >= 2 && m < 18 && ks + 1 < KS1 && !(ABL & 4)) rdB(std::integral_constant<int, ks + 1>{}, std::integral_constant<int, m - 2>{});
        if constexpr (m >= 18 && m < 23 && ks + AHEAD < KS1) issue(std::integral_constant<int, ks + AHEAD>{}, std::integral_constant<int, m - 18>{});
        if constexpr (m == 22) mfma_vv(rsum[0], ones, B[0]);
        if constexpr (m == 23) mfma_vv(rsum[1], ones, B[1]);
      });
    });
  }
  asm volatile("s_nop 7\n\ts_nop 1" ::: "memory");
}

__global__ __launch_bounds__(256) void k2_gemm1(char* __restrict__ ws) {
  extern __shared__ __attribute__((aligned(16))) char smem[];
  const int t = threadIdx.x, lane = t & 63;
  const int w = __builtin_amdgcn_readfirstlane(t >> 6);
  const int b = blockIdx.x, xcd = b & 7, bi = b >> 3;
  const int rt = (xcd >> 1) * 4 + (bi & 3), j = (xcd & 1) * 8 + (bi >> 2);
  const int U0 = slice_u0(j), nu = slice_nu(j);
  const int rem = nu - 8;
  const int nw = w < rem ? 3 : 2;
  const int uw = U0 + (w < rem ? 3 * w : 3 * rem + 2 * (w - rem));
  const int u2 = uw + (nw == 3 ? 2 : 0);
  const int n = lane & 15, g = lane >> 4;
  const u32 sb = (u32)(size_t)(LDSP char*)smem;
  const int s0 = 128 * rt;
  char* ws_stamp = ws; (void)ws_stamp;
  STAMP(2, 0);

  const u32 gv = (u32)(g * 4096 + (((n >> 1) ^ (g | ((w & 1) << 2))) * 32 + (n & 1) * 16));
  const char* hb0 = ws + WS_HT + (size_t)s0 * 2 + (size_t)(8 * w) * 4096;
  const char* hb1 = hb0 + 4 * 4096;
  const u32 lb0 = sb + 8 * w * 256, lb1 = lb0 + 1024;
  const char* w0 = ws + WS_W1 + (size_t)uw * KS1 * 1024;
  const char* w1 = w0 + KS1 * 1024;
  const char* w2 = ws + WS_W1 + (size_t)u2 * KS1 * 1024;
  const char* cs0 = ws + WS_CS + (size_t)uw * 64;
  const char* cs1 = cs0 + 64;
  const char* cs2 = ws + WS_CS + (size_t)u2 * 64;
  const u32 csv = (lane >> 2) * (NU * 64) + (lane & 3) * 16;
  const int qd = n >> 2, p = n & 3;
  const int frow = qd | ((g & 1) << 2);
  u32 ra[8];
#pragma unroll
  for (int i = 0; i < 8; ++i) ra[i] = sb + (8 * g + qd) * 256 + ((((i + 2 * w) & 7) ^ frow) * 32) + 8 * p;
  f32x4 acc[3][8], cs[3];
#pragma unroll
  for (int u = 0; u < 3; ++u)
#pragma unroll
    for (int i = 0; i < 8; ++i) acc[u][i] = f32x4{0.f, 0.f, 0.f, 0.f};
  f32x4 rsum[2] = {f32x4{0.f, 0.f, 0.f, 0.f}, f32x4{0.f, 0.f, 0.f, 0.f}};
  if (w == 3) k2_main<1>(acc, rsum, sb, gv, lane * 16, hb0, hb1, lb0, lb1, w0, w1, w2, ra, cs, cs0, cs1, cs2, csv, ws_stamp);
  else k2_main<0>(acc, rsum, sb, gv, lane * 16, hb0, hb1, lb0, lb1, w0, w1, w2, ra, cs, cs0, cs1, cs2, csv, ws_stamp);
  STAMP(2, 2);

  float* stl = (float*)(smem + K2_ST);
#pragma unroll
  for (int i = 0; i < 2; ++i) {
    if (g == 0) stl[2 * (16 * ((i + 2 * w) & 7) + n)] = rsum[i][0];
  }
  if (w == 3 && g == (n >> 2)) {
#pragma unroll
    for (int i = 0; i < 8; ++i) {
      f32x4 a = acc[2][i];
      float dg = (n & 2) ? ((n & 1) ? a[3] : a[2]) : ((n & 1) ? a[1] : a[0]);
      stl[2 * (16 * ((i + 2 * w) & 7) + n) + 1] = dg;
    }
  }
  __syncthreads();
  STAMP(2, 3);
  char* Hb = ws + WS_H;
  f32x2 sv[8];
#pragma unroll
  for (int i = 0; i < 8; ++i) sv[i] = *(const f32x2*)(stl + 2 * (16 * ((i + 2 * w) & 7) + n));
#pragma unroll
  for (int i = 0; i < 8; ++i) {
    const int sf = (i + 2 * w) & 7;
    const float mean = sv[i][0] * (1.f / D);
    const float var = fmaxf(sv[i][1] * (1.f / D) - mean * mean, 0.f) + EPS;
    const float rstd = __builtin_amdgcn_rsqf(var);
    const float rinv = var * rstd;
    const float ce = -1.4426950408889634f * rstd;
    const f32x2 nm = {-mean, -mean}, ce2 = {ce, ce}, ri2 = {rinv, rinv};
    u32 hv[4] = {0, 0, 0, 0};
#pragma unroll
    for (int u = 0; u < 3; ++u) {
      const f32x4 a = acc[u][i];
      const f32x2 tg = (f32x2{a[0], a[1]} + nm * f32x2{cs[u][0], cs[u][1]}) * ce2;
      const f32x2 tu = f32x2{a[2], a[3]} + nm * f32x2{cs[u][2], cs[u][3]};
      const f32x2 e = {__builtin_amdgcn_exp2f(tg[0]), __builtin_amdgcn_exp2f(tg[1])};
      const f32x2 den = e * ri2 + ri2;
      const f32x2 o = tu * f32x2{__builtin_amdgcn_rcpf(den[0]), __builtin_amdgcn_rcpf(den[1])};
      hv[u] = pk2(o[0], o[1]);
    }
    auto s02 = __builtin_amdgcn_permlane32_swap(hv[0], hv[2], false, false);
    auto s13 = __builtin_amdgcn_permlane32_swap(hv[1], hv[3], false, false);
    auto t01 = __builtin_amdgcn_permlane16_swap(s02[0], s13[0], false, false);
    auto t23 = __builtin_amdgcn_permlane16_swap(s02[1], s13[1], false, false);
    if (g < nw) {
      const int U = uw + g;
      u32x4 o4 = {t01[0], t01[1], t23[0], t23[1]};
      *(u32x4*)(Hb + ((size_t)((8 * rt + sf) * KS2 + (U >> 2)) * 64 + (U & 3) * 16 + n) * 16) = o4;
    }
  }
  STAMP(2, 4);
}

constexpr int K3_RES = 4 * 3 * 4096;
constexpr int K3_LDS = K3_RES + 8192;
constexpr int K3_RING = 6;

__global__ __launch_bounds__(256) void k3_gemm2(const char* __restrict__ ws, u16* __restrict__ out) {
  extern __shared__ __attribute__((aligned(16))) char smem[];
  const int t = threadIdx.x, lane = t & 63;
  const int w = __builtin_amdgcn_readfirstlane(t >> 6);
  const int b = blockIdx.x, xcd = b & 7, bi = b >> 3;
  const int rt = (xcd >> 1) * 8 + (bi & 7), dt = (xcd & 1) * 4 + (bi >> 3);
  const int n = lane & 15, g = lane >> 4;
  char* ws_stamp = (char*)ws; (void)ws_stamp;
  STAMP(3, 0);
  const u32 sb = (u32)(size_t)(LDSP char*)smem;
  {
    const int dl = lane >> 3, c8 = lane & 7;
    const u32 rv = (u32)(dl * 4096 + (((c8 >> 1) ^ (dl & 3)) * 32 + (c8 & 1) * 16));
    const char* hb = ws + WS_HT + (size_t)(64 * dt + 16 * w) * 4096 + (size_t)rt * 128;
    glds16<0>(rv, hb, sb + K3_RES + w * 2048);
    glds16<1024>(rv, hb + 8 * 4096, sb + K3_RES + w * 2048);
  }
  const char* wa = ws + WS_WD + (size_t)dt * KS2 * 4096 + (size_t)w * 4096;
  const char* hb = ws + WS_H + (size_t)(4 * rt) * KS2 * 1024 + (size_t)w * 1024;
  const u32 lv = lane * 16;

  f32x4 acc[4][4];
#pragma unroll
  for (int i = 0; i < 4; ++i)
#pragma unroll
    for (int jj = 0; jj < 4; ++jj) acc[i][jj] = f32x4{0.f, 0.f, 0.f, 0.f};
  u32x4 A[K3_RING][4], B[K3_RING][4];
  const size_t lastoff = w == 3 ? 9 : 10;
  auto issue = [&](auto IT, auto O) {
    constexpr int it = IT, o = O, i = o >> 1;
    const size_t st = it == 10 ? lastoff : (size_t)it;
    if constexpr ((o & 1) == 0) gld_a<i * 1024>(A[it % K3_RING][i], lv, wa + st * 16384);
    else gld_v<0>(B[it % K3_RING][i], lv, hb + ((size_t)i * KS2 + 4 * st) * 1024);
  };
  static_for<K3_RING - 1>([&](auto IT) { static_for<8>([&](auto O) { issue(IT, O); }); });
  static_for<11>([&](auto IT) {
    constexpr int it = IT, slot = it % K3_RING;
    constexpr int last = it + K3_RING - 2 < 10 ? it + K3_RING - 2 : 10;
    wait_vm<(last - it) * 8>();
#pragma unroll
    for (int i = 0; i < 4; ++i) { pin_a(A[slot][i]); pin(B[slot][i]); }
    if (it < 10 || w < 3) {
      static_for<16>([&](auto M) {
        constexpr int m = M;
        mfma_av(acc[m >> 2][m & 3], A[slot][m >> 2], B[slot][m & 3]);
        if constexpr ((m & 1) == 1 && it + K3_RING - 1 < 11) issue(std::integral_constant<int, it + K3_RING - 1>{}, std::integral_constant<int, (m >> 1)>{});
      });
    }
  });
  asm volatile("s_nop 7\n\ts_nop 1" ::: "memory");
  STAMP(3, 1);
  f32x4* red = (f32x4*)smem;
#pragma unroll
  for (int o = 1; o < 4; ++o) {
    const int dst = (w + o) & 3;
#pragma unroll
    for (int df = 0; df < 4; ++df) {
      f32x4 vsel = dst == 0 ? acc[df][0] : dst == 1 ? acc[df][1] : dst == 2 ? acc[df][2] : acc[df][3];
      red[((dst * 3 + (o - 1)) * 4 + df) * 64 + lane] = vsel;
    }
  }
  f32x4 fin[4];
#pragma unroll
  for (int df = 0; df < 4; ++df) fin[df] = w == 0 ? acc[df][0] : w == 1 ? acc[df][1] : w == 2 ? acc[df][2] : acc[df][3];
  __syncthreads();
  u32x2 res[4];
  {
    const int qd = n >> 2, p = n & 3;
#pragma unroll
    for (int df = 0; df < 4; ++df) {
      const int d = 16 * g + 4 * df + qd;
      res[df] = tr_read(sb + K3_RES + d * 128 + ((w ^ (d & 3)) * 32) + 8 * p);
    }
  }
#pragma unroll
  for (int o = 0; o < 3; ++o)
#pragma unroll
    for (int df = 0; df < 4; ++df) fin[df] += red[((w * 3 + o) * 4 + df) * 64 + lane];
  u32x4 o0, o1;
#pragma unroll
  for (int df = 0; df < 2; ++df) {
    o0[2 * df] = pk2(fin[df][0] + blo(res[df][0]), fin[df][1] + bhi(res[df][0]));
    o0[2 * df + 1] = pk2(fin[df][2] + blo(res[df][1]), fin[df][3] + bhi(res[df][1]));
    o1[2 * df] = pk2(fin[2 + df][0] + blo(res[2 + df][0]), fin[2 + df][1] + bhi(res[2 + df][0]));
    o1[2 * df + 1] = pk2(fin[2 + df][2] + blo(res[2 + df][1]), fin[2 + df][3] + bhi(res[2 + df][1]));
  }
  const int srow = 64 * rt + 16 * w + n, dcol = 64 * dt + 16 * g;
  *(u32x4*)(out + (size_t)srow * D + dcol) = o0;
  *(u32x4*)(out + (size_t)srow * D + dcol + 8) = o1;
  STAMP(3, 2);
}

extern "C" void kernel_launch(void* const* d_in, const int* in_sizes, int n_in, void* d_out, int out_size, void* d_ws,
                              size_t ws_size, hipStream_t stream) {
  const u16 *q = (const u16*)d_in[0], *k = (const u16*)d_in[1], *v = (const u16*)d_in[2], *shk = (const u16*)d_in[3];
  const u16 *ssmk = (const u16*)d_in[4], *dskip = (const u16*)d_in[5];
  const u16 *wg = (const u16*)d_in[7], *wu = (const u16*)d_in[8], *wd = (const u16*)d_in[9];
  char* ws = (char*)d_ws;
  (void)hipFuncSetAttribute((const void*)k1_conv, hipFuncAttributeMaxDynamicSharedMemorySize, K1_LDS);
  (void)hipFuncSetAttribute((const void*)k2_gemm1, hipFuncAttributeMaxDynamicSharedMemorySize, K2_LDS);
  (void)hipFuncSetAttribute((const void*)k3_gemm2, hipFuncAttributeMaxDynamicSharedMemorySize, K3_LDS);
  k0_prep<<<256, 256, 0, stream>>>(q, k, v, shk, ws);
  k1_conv<<<512, 256, K1_LDS, stream>>>(ssmk, dskip, wg, wu, wd, ws);
  k2_gemm1<<<256, 256, K2_LDS, stream>>>(ws);
  k3_gemm2<<<256, 256, K3_LDS, stream>>>(ws, (u16*)d_out);
}
```

```cpp
#include <hip/hip_runtime.h>
#include <stdint.h>
#include <utility>

typedef unsigned short u16;
typedef unsigned u32;
typedef __attribute__((ext_vector_type(2))) u32 u32x2;
typedef __attribute__((ext_vector_type(4))) u32 u32x4;
typedef __attribute__((ext_vector_type(2))) float f32x2;
typedef __attribute__((ext_vector_type(4))) float f32x4;
typedef __attribute__((ext_vector_type(2))) __bf16 bf16x2;
typedef __attribute__((ext_vector_type(8))) __bf16 bf16x8;
typedef __attribute__((ext_vector_type(4))) short s16x4;
#define LDSP __attribute__((address_space(3)))
#define DI __device__ __forceinline__

constexpr int S = 2048, D = 512, F = 1376;
constexpr int NU = F / 8;
constexpr int KS1 = D / 32;
constexpr int KS2 = F / 32;
constexpr float EPS = 1e-5f;

constexpr size_t WS_XR = 0;
constexpr size_t WS_QT = WS_XR + (size_t)D * S * 2;
constexpr size_t WS_HT = WS_QT + (size_t)D * S * 2;
constexpr size_t WS_W1 = WS_HT + (size_t)D * S * 2;
constexpr size_t WS_WD = WS_W1 + (size_t)NU * KS1 * 1024;
constexpr size_t WS_H = WS_WD + (size_t)8 * KS2 * 4 * 1024;
constexpr size_t WS_CS = WS_H + (size_t)(S / 16) * KS2 * 1024;
constexpr size_t WS_SA = WS_CS + (size_t)KS1 * NU * 16 * 4;
constexpr size_t WS_SB = WS_SA + (size_t)S * 4;
constexpr size_t WS_CNT = WS_SB + (size_t)S * 4;

template <int... Is, class Fn> DI void static_for_(std::integer_sequence<int, Is...>, Fn&& f) { (f(std::integral_constant<int, Is>{}), ...); }
template <int N, class Fn> DI void static_for(Fn&& f) { static_for_(std::make_integer_sequence<int, N>{}, f); }

DI float blo(u32 u) { return __uint_as_float(u << 16); }
DI float bhi(u32 u) { return __uint_as_float(u & 0xffff0000u); }
DI u32 pk2(float a, float b) { f32x2 t = {a, b}; return __builtin_bit_cast(u32, __builtin_convertvector(t, bf16x2)); }
DI f32x4 mfma16(u32x4 a, u32x4 b, f32x4 c) {
  return __builtin_amdgcn_mfma_f32_16x16x32_bf16(__builtin_bit_cast(bf16x8, a), __builtin_bit_cast(bf16x8, b), c, 0, 0, 0);
}
DI u32x2 tr_read(u32 lds_byte) {
  return __builtin_bit_cast(u32x2, __builtin_amdgcn_ds_read_tr16_b64_v4i16((LDSP s16x4*)(size_t)lds_byte));
}
DI u32x4 tr_frag(u32 a0, u32 a1) { u32x2 x = tr_read(a0), y = tr_read(a1); return u32x4{x[0], x[1], y[0], y[1]}; }
DI u32x4 cat(u32x2 x, u32x2 y) { return __builtin_shufflevector(x, y, 0, 1, 2, 3); }
template <int CTRL> DI float dpp_add(float v) {
  return v + __builtin_bit_cast(float, __builtin_amdgcn_update_dpp(0, __builtin_bit_cast(int, v), CTRL, 0xf, 0xf, true));
}
DI float sum_rows(float v) {
  u32 u = __float_as_uint(v);
  auto a = __builtin_amdgcn_permlane16_swap(u, u, false, false);
  v = __uint_as_float(a[0]) + __uint_as_float(a[1]);
  u = __float_as_uint(v);
  auto b = __builtin_amdgcn_permlane32_swap(u, u, false, false);
  return __uint_as_float(b[0]) + __uint_as_float(b[1]);
}
DI float wave_sum(float v) {
  v = dpp_add<0xb1>(v);
  v = dpp_add<0x4e>(v);
  v = dpp_add<0x124>(v);
  v = dpp_add<0x128>(v);
  return sum_rows(v);
}
DI __amdgpu_buffer_rsrc_t ws_rsrc(char* ws) { return __builtin_amdgcn_make_buffer_rsrc(ws, 0, 0x04000000, 0x00020000); }
DI void st16_wt(__amdgpu_buffer_rsrc_t r, u32 off, u32x4 v) { __builtin_amdgcn_raw_buffer_store_b128(v, r, off, 0, 16); }
DI int slice_u0(int j) { return j < 12 ? 11 * j : 132 + 10 * (j - 12); }
DI int slice_nu(int j) { return j < 12 ? 11 : 10; }

template <bool FIRST = false> DI void mfma_vv(f32x4& acc, const u32x4& a, const u32x4& b) {
  if constexpr (FIRST) asm volatile("v_mfma_f32_16x16x32_bf16 %0, %1, %2, 0" : "=&v"(acc) : "v"(a), "v"(b));
  else asm volatile("v_mfma_f32_16x16x32_bf16 %0, %1, %2, %0" : "+v"(acc) : "v"(a), "v"(b));
}
template <bool FIRST = false> DI void mfma_av(f32x4& acc, const u32x4& a, const u32x4& b) {
  if constexpr (FIRST) asm volatile("v_mfma_f32_16x16x32_bf16 %0, %1, %2, 0" : "=&v"(acc) : "a"(a), "v"(b));
  else asm volatile("v_mfma_f32_16x16x32_bf16 %0, %1, %2, %0" : "+v"(acc) : "a"(a), "v"(b));
}
template <int OFF> DI void ds_b64(u32x2& d, u32 addr) { asm volatile("ds_read_b64 %0, %1 offset:%c2" : "=v"(d) : "v"(addr), "i"(OFF) : "memory"); }
template <int OFF> DI void ds_b128(u32x4& d, u32 addr) { asm volatile("ds_read_b128 %0, %1 offset:%c2" : "=v"(d) : "v"(addr), "i"(OFF) : "memory"); }
template <int OFF> DI void ds_tr(u32x2& d, u32 addr) { asm volatile("ds_read_b64_tr_b16 %0, %1 offset:%c2" : "=v"(d) : "v"(addr), "i"(OFF) : "memory"); }
template <int OFF> DI void gld_a(u32x4& d, u32 voff, const void* sbase) {
  asm volatile("global_load_dwordx4 %0, %1, %2 offset:%c3" : "=a"(d) : "v"(voff), "s"(sbase), "i"(OFF) : "memory");
}
template <int OFF> DI void gld_v(u32x4& d, u32 voff, const void* sbase) {
  asm volatile("global_load_dwordx4 %0, %1, %2 offset:%c3" : "=v"(d) : "v"(voff), "s"(sbase), "i"(OFF) : "memory");
}
template <int OFF> DI void gld2_v(u32x2& d, u32 voff, const void* sbase) {
  asm volatile("global_load_dwordx2 %0, %1, %2 offset:%c3" : "=v"(d) : "v"(voff), "s"(sbase), "i"(OFF) : "memory");
}
template <int LOFF> DI void glds16(u32 voff, const void* sbase, u32 ldsbase) {
  asm volatile("s_add_u32 m0, %2, %c3\n\ts_nop 0\n\tglobal_load_lds_dwordx4 %0, %1" ::"v"(voff), "s"(sbase), "s"(ldsbase), "i"(LOFF) : "memory", "scc");
}
template <int N> DI void wait_vm() { asm volatile("s_waitcnt vmcnt(%c0)" ::"i"(N) : "memory"); }
template <int N> DI void wait_lgkm() { asm volatile("s_waitcnt lgkmcnt(%c0)" ::"i"(N) : "memory"); }
template <int OFF> DI void gld_a_sc1(u32x4& d, u32 voff, const void* sbase) {
  asm volatile("global_load_dwordx4 %0, %1, %2 offset:%c3 sc1" : "=a"(d) : "v"(voff), "s"(sbase), "i"(OFF) : "memory");
}
DI void mfma_aa(f32x4& acc, const u32x4& a, const u32x4& b) {
  asm volatile("v_mfma_f32_16x16x32_bf16 %0, %1, %2, %0" : "+v"(acc) : "a"(a), "a"(b));
}
template <bool FIRST = false> DI void mfma_va(f32x4& acc, const u32x4& a, const u32x4& b) {
  if constexpr (FIRST) asm volatile("v_mfma_f32_16x16x32_bf16 %0, %1, %2, 0" : "=&v"(acc) : "v"(a), "a"(b));
  else asm volatile("v_mfma_f32_16x16x32_bf16 %0, %1, %2, %0" : "+v"(acc) : "v"(a), "a"(b));
}
DI void mfma_fence(f32x4& a, f32x4& b, f32x4& c, f32x4& d) { asm volatile("s_nop 7\n\ts_nop 1" : "+v"(a), "+v"(b), "+v"(c), "+v"(d)); }
DI void after_fence(f32x4& a, f32x4& b, f32x4& c, f32x4& d) { asm volatile("" : "+v"(a), "+v"(b), "+v"(c), "+v"(d)); }
DI void after_fence(f32x4& a, f32x4& b, f32x4& c) { asm volatile("" : "+v"(a), "+v"(b), "+v"(c)); }
template <int STAGE> DI void dpp_stage(float& v) {
  if constexpr (STAGE == 0) asm volatile("v_add_f32_dpp %0, %0, %0 quad_perm:[1,0,3,2] row_mask:0xf bank_mask:0xf" : "+v"(v));
  if constexpr (STAGE == 1) asm volatile("v_add_f32_dpp %0, %0, %0 quad_perm:[2,3,0,1] row_mask:0xf bank_mask:0xf" : "+v"(v));
  if constexpr (STAGE == 2) asm volatile("v_add_f32_dpp %0, %0, %0 row_ror:4 row_mask:0xf bank_mask:0xf" : "+v"(v));
  if constexpr (STAGE == 3) asm volatile("v_add_f32_dpp %0, %0, %0 row_ror:8 row_mask:0xf bank_mask:0xf" : "+v"(v));
}
DI void barrier() { asm volatile("s_barrier" ::: "memory"); }
DI void pin(u32x4& x) { asm volatile("" : "+v"(x)); }
DI void pin(u32x2& x) { asm volatile("" : "+v"(x)); }
DI void pin4(f32x4& x) { asm volatile("" : "+v"(x)); }
DI void pin_a(u32x4& x) { asm volatile("" : "+a"(x)); }

#define STAMP(kern, idx) do {} while (0)

constexpr int K0_K = 0, K0_V = 72 * 128, K0_Q = K0_V + 64 * 128, K0_LDS = K0_Q + 64 * 128;

__global__ __launch_bounds__(256) void k0_prep(const u16* __restrict__ q, const u16* __restrict__ k, const u16* __restrict__ v,
                                               const u16* __restrict__ shk, char* __restrict__ ws) {
  extern __shared__ __attribute__((aligned(16))) char smem[];
  const int t = threadIdx.x, lane = t & 63;
  const int w = __builtin_amdgcn_readfirstlane(t >> 6);
  const int b = blockIdx.x;
  char* ws_stamp = ws; (void)ws_stamp;
  STAMP(0, 0);
  const int x = b & 7, s0 = 64 * (b >> 3);
  const u32 sb = (u32)(size_t)(LDSP char*)smem;
  const int i16 = lane & 15, g = lane >> 4, qd = i16 >> 2, p = i16 & 3;
  const int d = 64 * x + 16 * w + i16;
  const int srow = 8 * b + 2 * w;
  u32x4 qa, qb; u32x2 sh;
  gld_v<0>(qa, lane * 16, (const char*)q + (size_t)srow * 1024);
  gld_v<1024>(qb, lane * 16, (const char*)q + (size_t)srow * 1024);
  gld2_v<0>(sh, d * 8, shk);
  {
    const int r8 = lane >> 3, c8 = lane & 7;
    auto voff = [&](int instr) { const int f = ((r8 >> 1) & 1) | ((instr & 1) << 1); return (u32)((((c8 >> 1) ^ f) * 2 + (c8 & 1)) * 16); };
    const size_t col = (size_t)x * 128;
    {
      int row = s0 - 8 + 8 * w + r8; row = row < 0 ? 0 : row;
      glds16<0>((u32)row * 1024 + voff(w), (const char*)k + col, sb + K0_K + w * 1024);
      glds16<0>((u32)(s0 - 8 + 8 * (w + 4) + r8) * 1024 + voff(w), (const char*)k + col, sb + K0_K + (w + 4) * 1024);
    }
#pragma unroll
    for (int i = 0; i < 2; ++i) {
      const int in = w + 4 * i;
      glds16<0>((u32)(s0 + 8 * in + r8) * 1024 + voff(in), (const char*)v + col, sb + K0_V + in * 1024);
      glds16<0>((u32)(s0 + 8 * in + r8) * 1024 + voff(in), (const char*)q + col, sb + K0_Q + in * 1024);
    }
    if (w == 0) glds16<0>((u32)(s0 + 56 + r8) * 1024 + voff(8), (const char*)k + col, sb + K0_K + 8 * 1024);
  }
  if (b == 0 && t < 16) *(u32*)(ws + WS_CNT + t * 128) = 0u;
  if (w == 0) wait_vm<7>(); else wait_vm<6>();
  asm volatile("" : "+v"(qa), "+v"(qb), "+v"(sh));
  {
    float sa = 0.f, sbb = 0.f;
#pragma unroll
    for (int e = 0; e < 4; ++e) { sa += blo(qa[e]) + bhi(qa[e]); sbb += blo(qb[e]) + bhi(qb[e]); }
    const float ma = wave_sum(sa) * (1.f / D), mb = wave_sum(sbb) * (1.f / D);
    float va = 0.f, vb = 0.f;
#pragma unroll
    for (int e = 0; e < 4; ++e) {
      float u0 = blo(qa[e]) - ma, u1 = bhi(qa[e]) - ma, u2 = blo(qb[e]) - mb, u3 = bhi(qb[e]) - mb;
      va += u0 * u0 + u1 * u1; vb += u2 * u2 + u3 * u3;
    }
    va = wave_sum(va) * (1.f / D); vb = wave_sum(vb) * (1.f / D);
    if (lane == 0) {
      const float ra = __builtin_amdgcn_rsqf(va + EPS), rb = __builtin_amdgcn_rsqf(vb + EPS);
      *(f32x2*)(ws + WS_SA + (size_t)srow * 4) = f32x2{ra, rb};
      *(f32x2*)(ws + WS_SB + (size_t)srow * 4) = f32x2{-ma * ra, -mb * rb};
    }
  }
  wait_vm<0>();
  barrier();
  STAMP(0, 1);

  auto taddr = [&](int base, int tr) { const int f = ((tr >> 1) & 1) | ((tr >> 2) & 2); return sb + base + tr * 128 + ((w ^ f) * 32) + 8 * p; };
  const float a0 = blo(sh[0]), a1 = bhi(sh[0]), a2 = blo(sh[1]), a3 = bhi(sh[1]);
  const __amdgpu_buffer_rsrc_t wr = ws_rsrc(ws);
#pragma unroll
  for (int e = 0; e < 2; ++e) {
    const int rg = g + 4 * e;
    u32x2 km = tr_read(taddr(K0_K, 8 * rg + 4 + qd));
    const u32x2 k0 = tr_read(taddr(K0_K, 8 * rg + 8 + qd)), k1 = tr_read(taddr(K0_K, 8 * rg + 12 + qd));
    const u32x2 v0 = tr_read(taddr(K0_V, 8 * rg + qd)), v1 = tr_read(taddr(K0_V, 8 * rg + 4 + qd));
    const u32x2 q0 = tr_read(taddr(K0_Q, 8 * rg + qd)), q1 = tr_read(taddr(K0_Q, 8 * rg + 4 + qd));
    if (s0 + rg == 0) km = u32x2{0, 0};
    const float kf[11] = {bhi(km[0]), blo(km[1]), bhi(km[1]), blo(k0[0]), bhi(k0[0]), blo(k0[1]), bhi(k0[1]), blo(k1[0]), bhi(k1[0]), blo(k1[1]), bhi(k1[1])};
    const float vf[8] = {blo(v0[0]), bhi(v0[0]), blo(v0[1]), bhi(v0[1]), blo(v1[0]), bhi(v1[0]), blo(v1[1]), bhi(v1[1])};
    float xo[8];
#pragma unroll
    for (int j = 0; j < 8; ++j) xo[j] = (kf[j + 3] * a0 + kf[j + 2] * a1 + kf[j + 1] * a2 + kf[j] * a3) * vf[j];
    const int r0 = s0 + 8 * rg;
    st16_wt(wr, (u32)WS_XR + (d * S + (2040 - r0)) * 2, u32x4{pk2(xo[7], xo[6]), pk2(xo[5], xo[4]), pk2(xo[3], xo[2]), pk2(xo[1], xo[0])});
    st16_wt(wr, (u32)WS_QT + (d * S + r0) * 2, cat(q0, q1));
  }

  STAMP(0, 3);
}

DI void w_job(int b, const u16* __restrict__ wg, const u16* __restrict__ wu, const u16* __restrict__ wd, char* __restrict__ ws, char* smem) {
  const int t = threadIdx.x, lane = t & 63, w = t >> 6;
  char* tile = smem;
  char* tile2 = smem + 32 * 368;
  const u32 tb = (u32)(size_t)(LDSP char*)smem;
  const int i16 = lane & 15, qd = i16 >> 2, p = i16 & 3, g = lane >> 4;
  const int ks = b & 15, j = b >> 4;
  const int U0 = slice_u0(j), nu = slice_nu(j);
  u32x4 g4[2], u4[2], d4[2];
#pragma unroll
  for (int rep = 0; rep < 2; ++rep) {
    int idx = t + 256 * rep, r = idx / 11, u = idx - 11 * r;
    bool ok = r < 32 && u < nu;
    size_t off = ok ? (size_t)(32 * ks + r) * F + 8 * (U0 + u) : 0;
    g4[rep] = *(const u32x4*)(wg + off); u4[rep] = *(const u32x4*)(wu + off);
  }
  const int ks2 = b >> 2, dp = b & 3;
  if (b < KS2 * 4) {
#pragma unroll
    for (int rep = 0; rep < 2; ++rep) {
      int idx = t + 256 * rep, r = idx >> 4, c = idx & 15;
      d4[rep] = *(const u32x4*)(wd + (size_t)(32 * ks2 + r) * D + 128 * dp + 8 * c);
    }
  }
#pragma unroll
  for (int rep = 0; rep < 2; ++rep) {
    int idx = t + 256 * rep, r = idx / 11, u = idx - 11 * r;
    if (r < 32 && u < nu) {
      *(u32x4*)(tile + r * 368 + u * 32) = u32x4{g4[rep][0], u4[rep][0], g4[rep][1], u4[rep][1]};
      *(u32x4*)(tile + r * 368 + u * 32 + 16) = u32x4{g4[rep][2], u4[rep][2], g4[rep][3], u4[rep][3]};
    }
  }
  if (b < KS2 * 4) {
#pragma unroll
    for (int rep = 0; rep < 2; ++rep) { int idx = t + 256 * rep, r = idx >> 4, c = idx & 15; *(u32x4*)(tile2 + r * 272 + c * 16) = d4[rep]; }
  }
  __syncthreads();
  const u32x4 ones = {0x3f803f80u, 0x3f803f80u, 0x3f803f80u, 0x3f803f80u};
  const __amdgpu_buffer_rsrc_t wr = ws_rsrc(ws);
  for (int u = w; u < nu; u += 4) {
    u32 a = tb + (8 * g + qd) * 368 + u * 32 + 8 * p;
    u32x4 fr = tr_frag(a, a + 4 * 368);
    st16_wt(wr, (u32)WS_W1 + (((U0 + u) * KS1 + ks) * 64 + lane) * 16, fr);
    f32x4 cs = mfma16(fr, ones, f32x4{0.f, 0.f, 0.f, 0.f});
    if (i16 == 0) st16_wt(wr, (u32)WS_CS + ((ks * NU + U0 + u) * 16 + 4 * g) * 4, __builtin_bit_cast(u32x4, cs));
  }
  if (b < KS2 * 4) {
#pragma unroll
    for (int rep = 0; rep < 2; ++rep) {
      int fi = w + 4 * rep, dtl = fi >> 2, df = fi & 3;
      u32 a = tb + 32 * 368 + (8 * g + qd) * 272 + (64 * dtl + 32 * (df >> 1) + 8 * p + 4 * (df & 1)) * 2;
      u32x4 fr = tr_frag(a, a + 4 * 272);
      st16_wt(wr, (u32)WS_WD + ((((2 * dp + dtl) * KS2 + ks2) * 4 + df) * 64 + lane) * 16, fr);
    }
  }
}

constexpr int K1_CS = 4160;
constexpr int K1_KC = 4 * K1_CS;
constexpr int K1_XR = 2 * K1_KC;
constexpr int K1_RED = K1_XR + 2 * 4608;
constexpr int K1_LDS = K1_RED + 4 * 4096;

__global__ __launch_bounds__(256, 2) void k1_conv(const u16* __restrict__ ssmk, const u16* __restrict__ dskip, const u16* __restrict__ wg,
                                                  const u16* __restrict__ wu, const u16* __restrict__ wd, char* __restrict__ ws) {
  extern __shared__ __attribute__((aligned(16))) char smem[];
  const int t = threadIdx.x, lane = t & 63;
  const int w = __builtin_amdgcn_readfirstlane(t >> 6);
  const int b = blockIdx.x;
  char* ws_stamp = ws; (void)ws_stamp;
  STAMP(1, 0);
  if (b >= 256) { w_job(b - 256, wg, wu, wd, ws, smem); STAMP(1, 4); return; }
  const int ch0 = (b & 7) * 64 + (b >> 3) * 2;
  const int c = w >> 1, half = w & 1;
  const int n = lane & 15, g = lane >> 4;
  const u32 sb = (u32)(size_t)(LDSP char*)smem;

  {
    const char* xr = ws + WS_XR + (size_t)(ch0 + c) * 4096 + half * 2048;
    glds16<0>(lane * 16, xr, sb + K1_XR + c * 4608 + half * 2048);
    glds16<1024>(lane * 16, xr + 1024, sb + K1_XR + c * 4608 + half * 2048);
  }
  u32x4 kp[2], kc[2];
#pragma unroll
  for (int cc = 0; cc < 2; ++cc) {
    const u16* kr = ssmk + (size_t)(ch0 + cc) * S;
    u32x4 pv = *(const u32x4*)(kr + 8 * (t > 0 ? t - 1 : 0));
    kp[cc] = t > 0 ? pv : u32x4{0, 0, 0, 0};
    kc[cc] = *(const u32x4*)(kr + 8 * t);
  }
  const float dsk = blo(dskip[ch0 + c]);
  if (t < 8) *(u32x4*)(smem + (t >> 2) * K1_KC + (t & 3) * K1_CS) = u32x4{0, 0, 0, 0};
  if (t >= 64 && t < 64 + 60) { int i = t - 64; *(u32x4*)(smem + K1_XR + (i / 30) * 4608 + 4096 + (i % 30) * 16) = u32x4{0, 0, 0, 0}; }

#pragma unroll
  for (int cc = 0; cc < 2; ++cc) {
    u32 wv[6] = {kp[cc][0], kp[cc][1], kp[cc][2], kp[cc][3], kc[cc][0], kc[cc][1]};
    u32 o1[5];
#pragma unroll
    for (int i = 0; i < 5; ++i) o1[i] = __builtin_amdgcn_alignbit(wv[i + 1], wv[i], 16);
    char* base = smem + cc * K1_KC + (t + 1) * 16;
    *(u32x4*)(base + 0 * K1_CS) = u32x4{o1[0], o1[1], o1[2], o1[3]};
    *(u32x4*)(base + 1 * K1_CS) = u32x4{wv[1], wv[2], wv[3], wv[4]};
    *(u32x4*)(base + 2 * K1_CS) = u32x4{o1[1], o1[2], o1[3], o1[4]};
    *(u32x4*)(base + 3 * K1_CS) = u32x4{wv[2], wv[3], wv[4], wv[5]};
    if (t == 255) {
      const u32 z = 0;
      const u32 e0 = __builtin_amdgcn_alignbit(kc[cc][1], kc[cc][0], 16), e1 = __builtin_amdgcn_alignbit(kc[cc][2], kc[cc][1], 16);
      const u32 e2 = __builtin_amdgcn_alignbit(kc[cc][3], kc[cc][2], 16), e3 = __builtin_amdgcn_alignbit(z, kc[cc][3], 16);
      *(u32x4*)(base + 16 + 0 * K1_CS) = u32x4{e0, e1, e2, e3};
      *(u32x4*)(base + 16 + 1 * K1_CS) = u32x4{kc[cc][1], kc[cc][2], kc[cc][3], z};
      *(u32x4*)(base + 16 + 2 * K1_CS) = u32x4{e1, e2, e3, z};
      *(u32x4*)(base + 16 + 3 * K1_CS) = u32x4{kc[cc][2], kc[cc][3], z, z};
    }
  }
  asm volatile("s_waitcnt vmcnt(0) lgkmcnt(0)\n\ts_barrier" ::: "memory");
  STAMP(1, 1);

  const u32 aa = sb + c * K1_KC + (n & 3) * K1_CS + 16 * g + 2 * (n & 12) + half * 256;
  const u32 ba = sb + K1_XR + c * 4608 + 480 - 32 * n + 16 * g + half * 256;
  f32x4 acc[8];
  u32x2 Alo[2][8], Ahi[2][8];
  u32x4 B[2][8];
  auto rd = [&](auto C0, auto R) {
    constexpr int c0 = C0, r = R, buf = c0 & 1;
    if constexpr (r >= 2 && r < 10) {
      constexpr int u = r - 2;
      ds_b128<512 * (7 - u) + 64 * c0>(B[buf][u], ba);
    } else {
      constexpr int ar = r < 2 ? r : r - 8, i = ar >> 1;
      if constexpr ((ar & 1) == 0) ds_b64<512 * i + 64 * c0>(Alo[buf][i], aa);
      else ds_b64<512 * i + 64 * c0 + 8>(Ahi[buf][i], aa);
    }
  };
  u32x2 qv[4]; u32x4 sa[4], sbv[4];
  const u32 eo = (u32)(1024 * half + 16 * n + 4 * g);
  const char* qTc = ws + WS_QT + (size_t)(ch0 + c) * 4096;
  auto ldE = [&](auto E) {
    constexpr int e = E, i = e / 3, kind = e % 3;
    if constexpr (kind == 0) gld2_v<i * 512>(qv[i], eo * 2, qTc);
    if constexpr (kind == 1) gld_v<i * 1024>(sa[i], eo * 4, ws + WS_SA);
    if constexpr (kind == 2) gld_v<i * 1024>(sbv[i], eo * 4, ws + WS_SB);
  };
  static_for<24>([&](auto R) { rd(std::integral_constant<int, 0>{}, R); });
  static_for<4>([&](auto C0) {
    constexpr int c0 = C0, buf = c0 & 1;
    u32x4 A[8];
    static_for<36>([&](auto M) {
      constexpr int m = M;
      constexpr int c1 = m < 8 ? 0 : m < 15 ? 1 : m < 21 ? 2 : m < 26 ? 3 : m < 30 ? 4 : m < 33 ? 5 : m < 35 ? 6 : 7;
      constexpr int first = c1 * 8 - c1 * (c1 - 1) / 2;
      constexpr int u = m - first;
      if constexpr (m == 0) {
        if constexpr (c0 == 0) wait_lgkm<14>(); else wait_lgkm<0>();
        pin(Alo[buf][0]); pin(Ahi[buf][0]);
#pragma unroll
        for (int i = 0; i < 8; ++i) pin(B[buf][i]);
        A[0] = cat(Alo[buf][0], Ahi[buf][0]);
      }
      if constexpr (m == 8) {
        if constexpr (c0 == 0) wait_lgkm<8>();
#pragma unroll
        for (int i = 1; i < 8; ++i) { pin(Alo[buf][i]); pin(Ahi[buf][i]); A[i] = cat(Alo[buf][i], Ahi[buf][i]); }
      }
      mfma_vv<(c0 == 0 && c1 == 0)>(acc[c1 + u], A[c1], B[buf][u]);
      if constexpr (c0 == 0) { if constexpr (m < 8) rd(std::integral_constant<int, 1>{}, M); else if constexpr (m >= 8 && m < 24) rd(std::integral_constant<int, 1>{}, M); }
      else if constexpr (c0 < 3 && m < 24) rd(std::integral_constant<int, c0 + 1>{}, M);
      if constexpr ((c0 == 1 || c0 == 2) && m >= 24 && m < 36 && (m & 1) == 0) ldE(std::integral_constant<int, 6 * (c0 - 1) + (m - 24) / 2>{});
    });
  });
  mfma_fence(acc[0], acc[1], acc[2], acc[3]);
  after_fence(acc[4], acc[5], acc[6], acc[7]);
  wait_vm<0>();
#pragma unroll
  for (int i = 0; i < 4; ++i) asm volatile("" : "+v"(qv[i]), "+v"(sa[i]), "+v"(sbv[i]));
  STAMP(1, 2);
  f32x4 fin[4];
  {
    f32x4* red = (f32x4*)(smem + K1_RED);
    if (half) {
#pragma unroll
      for (int i = 0; i < 4; ++i) { red[(w * 4 + i) * 64 + lane] = acc[i]; fin[i] = acc[4 + i]; }
      asm volatile("; half 1" ::: "memory");
    } else {
#pragma unroll
      for (int i = 0; i < 4; ++i) { red[(w * 4 + i) * 64 + lane] = acc[4 + i]; fin[i] = acc[i]; }
      asm volatile("; half 0" ::: "memory");
    }
    asm volatile("s_waitcnt lgkmcnt(0)\n\ts_barrier" ::: "memory");
#pragma unroll
    for (int i = 0; i < 4; ++i) fin[i] += red[((w ^ 1) * 4 + i) * 64 + lane];
  }
  const __amdgpu_buffer_rsrc_t wr = ws_rsrc(ws);
  const f32x2 dd = {dsk, dsk};
  u32x2 hv[4];
#pragma unroll
  for (int i = 0; i < 4; ++i) {
    const int s = 256 * (4 * half + i) + 16 * n + 4 * g;
    const u32x2 xq = *(LDSP const u32x2*)(size_t)(sb + K1_XR + c * 4608 + 2 * (2044 - s));
    const f32x2 x0 = {bhi(xq[1]), blo(xq[1])}, x1 = {bhi(xq[0]), blo(xq[0])};
    const f32x2 q0 = {blo(qv[i][0]), bhi(qv[i][0])}, q1 = {blo(qv[i][1]), bhi(qv[i][1])};
    const f32x2 z0 = x0 * dd + f32x2{fin[i][0], fin[i][1]}, z1 = x1 * dd + f32x2{fin[i][2], fin[i][3]};
    const f32x4 a4 = __builtin_bit_cast(f32x4, sa[i]), b4 = __builtin_bit_cast(f32x4, sbv[i]);
    const f32x2 t0 = q0 * f32x2{a4[0], a4[1]} + f32x2{b4[0], b4[1]};
    const f32x2 t1 = q1 * f32x2{a4[2], a4[3]} + f32x2{b4[2], b4[3]};
    const f32x2 h0 = t0 * z0 + q0, h1 = t1 * z1 + q1;
    hv[i] = u32x2{pk2(h0[0], h0[1]), pk2(h1[0], h1[1])};
  }
#pragma unroll
  for (int i = 0; i < 4; i += 2) {
    auto a = __builtin_amdgcn_permlane16_swap(hv[i][0], hv[i + 1][0], false, false);
    auto bb = __builtin_amdgcn_permlane16_swap(hv[i][1], hv[i + 1][1], false, false);
    const int s = 256 * (4 * half + i + (g & 1)) + 16 * n + 4 * (g & 2);
    st16_wt(wr, (u32)WS_HT + ((ch0 + c) * S + s) * 2, u32x4{a[0], bb[0], a[1], bb[1]});
  }
  STAMP(1, 3);
}

constexpr int KB_NL = 39;
constexpr int KB_ST = KB_NL * 4096;
constexpr int KB_LDS = KB_ST + 1024;
constexpr int KB_RING = 8;

constexpr int p2_upto(int ks) { return ks < 0 ? 2 : ks <= 3 ? 2 * ks + 4 : (ks + 7 > 15 ? 15 : ks + 7); }
constexpr int p2_extra(int ks) { return ks == 9 ? 3 : ks >= 10 ? 4 : 0; }
constexpr int p2_extra_first(int ks) { int c = 0; for (int s = 0; s < ks; ++s) c += p2_extra(s); return c; }
constexpr int P2_NEXTRA = 23;
constexpr int p2_extra_real(int ks) { int f = p2_extra_first(ks), n = p2_extra(ks); return f >= P2_NEXTRA ? 0 : (f + n > P2_NEXTRA ? P2_NEXTRA - f : n); }
constexpr int p2_issue_step(int t) { for (int s = -1; s < KS1; ++s) if (p2_upto(s) >= t) return s; return KS1; }
constexpr int p2_younger(int t, int ks, int ops) {
  int c = (p2_upto(ks - 1) - t) * ops;
  for (int s = p2_issue_step(t); s < ks; ++s) if (s >= 0) c += p2_extra_real(s);
  return c;
}

struct KbCtx {
  u32 sb, gv, lv, lb0, lwd, csv;
  const char *hb0, *hb1, *w0, *w1, *w2, *cs0, *cs1, *cs2, *wd, *wdr;
};

template <int GRAM, class Setup>
DI void kb_phase2(const KbCtx& c, f32x4 (&acc)[3][8], f32x4 (&rsum)[2], f32x4 (&csp)[3], u32x4 (&Wr)[4][4], u32 (&ra)[8], Setup&& setup, char* ws_stamp) {
  constexpr int OPS = GRAM ? 4 : 5;
  u32x4 W[KB_RING][3];
  u32x2 Bl[2][8], Bh[2][8];
  auto issue = [&](auto KS, auto O) {
    constexpr int ks = KS, o = O;
    if constexpr (o == 0) glds16<ks * 8192>(c.gv, c.hb0 + (size_t)ks * 131072, c.lb0);
    if constexpr (o == 1) gld_a<(ks & 3) * 1024>(W[ks % KB_RING][0], c.lv, c.w0 + (ks >> 2) * 4096);
    if constexpr (o == 2) gld_a<(ks & 3) * 1024>(W[ks % KB_RING][1], c.lv, c.w1 + (ks >> 2) * 4096);
    if constexpr (o == 3 && !GRAM) gld_a<(ks & 3) * 1024>(W[ks % KB_RING][2], c.lv, c.w2 + (ks >> 2) * 4096);
    if constexpr (o == 4) glds16<ks * 8192 + 1024>(c.gv, c.hb1 + (size_t)ks * 131072, c.lb0);
  };
  auto extra = [&](auto E) {
    constexpr int e = E;
    if constexpr (e < 7) glds16<(32 + e) * 4096>(c.lv, c.wd + (size_t)(32 + e) * 4096, c.lwd);
    else if constexpr (e < P2_NEXTRA) gld_a<((e - 7) & 3) * 1024>(Wr[(e - 7) >> 2][(e - 7) & 3], c.lv, c.wdr + ((e - 7) >> 2) * 4096);
  };
  auto rdB = [&](auto KS, auto R) {
    constexpr int ks = KS, r = R, i = r >> 1;
    if constexpr ((r & 1) == 0) ds_tr<(ks & 7) * 8192>(Bl[ks & 1][i], ra[i] + (ks >= 8 ? 65536 : 0));
    else ds_tr<(ks & 7) * 8192 + 1024>(Bh[ks & 1][i], ra[i] + (ks >= 8 ? 65536 : 0));
  };
  u32x4 c0, c1, c2;
  static_for<5>([&](auto O) { issue(std::integral_constant<int, 0>{}, O); });
  gld_v<0>(c0, c.csv, c.cs0); gld_v<0>(c1, c.csv, c.cs1); gld_v<0>(c2, c.csv, c.cs2);
  static_for<p2_upto(-1)>([&](auto KS) { static_for<5>([&](auto O) { issue(std::integral_constant<int, KS + 1>{}, O); }); });
  setup();
  u32x4 ones = {0x3f803f80u, 0x3f803f80u, 0x3f803f80u, 0x3f803f80u};
  pin(ones);
  wait_vm<p2_upto(-1) * OPS>();
  barrier();
  static_for<16>([&](auto R) { rdB(std::integral_constant<int, 0>{}, R); });
  asm volatile("" : "+v"(c0), "+v"(c1), "+v"(c2));
  float cv[12];
#pragma unroll
  for (int e = 0; e < 4; ++e) { cv[e] = __uint_as_float(c0[e]); cv[4 + e] = __uint_as_float(c1[e]); cv[8 + e] = __uint_as_float(c2[e]); }
  static_for<KS1>([&](auto KS) {
    constexpr int ks = KS, buf = ks & 1, slot = ks % KB_RING;
    constexpr int nmain = (p2_upto(ks) - p2_upto(ks - 1)) * 5, next = p2_extra_real(ks);
    wait_lgkm<0>();
    u32x4 B[8];
#pragma unroll
    for (int i = 0; i < 8; ++i) { pin(Bl[buf][i]); pin(Bh[buf][i]); }
#pragma unroll
    for (int i = 0; i < 8; ++i) B[i] = cat(Bl[buf][i], Bh[buf][i]);
    pin_a(W[slot][0]); pin_a(W[slot][1]);
    if constexpr (!GRAM) pin_a(W[slot][2]);
    static_for<26>([&](auto M) {
      constexpr int m = M, i = m / 3, u = m % 3;
      if constexpr (m >= 24) mfma_vv<ks == 0>(rsum[m - 24], ones, B[m - 24]);
      else if constexpr (u < 2 || !GRAM) mfma_av<ks == 0>(acc[u][i], W[slot][u], B[i]);
      else mfma_vv<ks == 0>(acc[2][i], B[i], B[i]);
      if constexpr (m == 1 && ks + 1 < KS1) { wait_vm<p2_younger(ks + 1, ks, OPS)>(); barrier(); }
      if constexpr (m >= 2 && m < 18 && ks + 1 < KS1) rdB(std::integral_constant<int, ks + 1>{}, std::integral_constant<int, m - 2>{});
      if constexpr (ks >= 9 && ks < 13 && m >= 2 && m < 14) dpp_stage<ks - 9>(cv[m - 2]);
      constexpr int v = m - (26 - nmain - next);
      if constexpr (v >= 0 && v < nmain) issue(std::integral_constant<int, p2_upto(ks - 1) + 1 + v / 5>{}, std::integral_constant<int, v % 5>{});
      if constexpr (v >= nmain) extra(std::integral_constant<int, p2_extra_first(ks) + v - nmain>{});
    });
  });
  mfma_fence(acc[0][0], acc[1][0], rsum[0], rsum[1]);
#pragma unroll
  for (int i = 0; i < 8; ++i) after_fence(acc[0][i], acc[1][i], acc[2][i], rsum[i & 1]);
#pragma unroll
  for (int u = 0; u < 3; ++u) csp[u] = f32x4{cv[4 * u], cv[4 * u + 1], cv[4 * u + 2], cv[4 * u + 3]};
}

__global__ __launch_bounds__(256) void kb_mlp(char* __restrict__ ws, u16* __restrict__ out) {
  extern __shared__ __attribute__((aligned(16))) char smem[];
  const int t = threadIdx.x, lane = t & 63;
  const int w = __builtin_amdgcn_readfirstlane(t >> 6);
  const int b = blockIdx.x, xcd = b & 7, bi = b >> 3;
#define KB_MAP 1
  const int rt = KB_MAP == 0 ? 2 * xcd + (bi & 1) : (xcd >> 1) * 4 + (bi & 3);
  const int j = KB_MAP == 0 ? bi >> 1 : (xcd & 1) * 8 + (bi >> 2);
  const int half = KB_MAP == 0 ? j & 1 : j >> 3, dt = KB_MAP == 0 ? j >> 1 : j & 7;
  const int U0 = slice_u0(j), nu = slice_nu(j);
  const int rem = nu - 8;
  const int nw = w < rem ? 3 : 2;
  const int uw = U0 + (w < rem ? 3 * w : 3 * rem + 2 * (w - rem));
  const int u2 = uw + (nw == 3 ? 2 : 0);
  const int n = lane & 15, g = lane >> 4;
  const int s0 = 128 * rt;
  char* ws_stamp = ws; (void)ws_stamp;
  STAMP(2, 0);
  auto sf = [&](int i) { return ((w + (i >> 1)) & 3) + 4 * (half ^ (i & 1)); };

  KbCtx c;
  c.sb = (u32)(size_t)(LDSP char*)smem;
  c.lv = lane * 16;
  c.gv = (u32)(g * 4096 + (((n >> 1) ^ (g | ((w & 1) << 2))) * 32 + (n & 1) * 16));
  c.hb0 = ws + WS_HT + (size_t)s0 * 2 + (size_t)(8 * w) * 4096;
  c.hb1 = c.hb0 + 4 * 4096;
  c.lb0 = c.sb + 8 * w * 256;
  c.w0 = ws + WS_W1 + (size_t)uw * KS1 * 1024;
  c.w1 = c.w0 + KS1 * 1024;
  c.w2 = ws + WS_W1 + (size_t)u2 * KS1 * 1024;
  c.cs0 = ws + WS_CS + (size_t)uw * 64;
  c.cs1 = c.cs0 + 64;
  c.cs2 = ws + WS_CS + (size_t)u2 * 64;
  c.csv = n * (NU * 64) + g * 16;
  c.wdr = ws + WS_WD + (size_t)dt * KS2 * 4096 + (size_t)KB_NL * 4096;
  c.wd = ws + WS_WD + (size_t)dt * KS2 * 4096 + (size_t)w * 1024;
  c.lwd = c.sb + 1024 * w;
  u32 ra[8], hoff[6];
  auto setup = [&]() {
    const int qd = n >> 2, p = n & 3;
    const int frow = qd | ((g & 1) << 2);
#pragma unroll
    for (int i = 0; i < 8; ++i) { ra[i] = c.sb + (8 * g + qd) * 256 + ((sf(i) ^ frow) * 32) + 8 * p; asm volatile("" : "+v"(ra[i])); }
#pragma unroll
    for (int tt = 0; tt < 6; ++tt) {
      const int q = 4 * tt + g, i = (q * 11) >> 5, u = q - 3 * i, U = uw + u;
      const u32 o = (u32)(((8 * rt + sf(i)) * KS2 + (U >> 2)) * 1024 + ((U & 3) * 16 + n) * 16);
      hoff[tt] = u < nw ? o : 0xfffffff0u;
      asm volatile("" : "+v"(hoff[tt]));
    }
  };
  f32x4 acc[3][8], cs[3], rsum[2];
  u32x4 Wr[4][4];
  if (w == 3) kb_phase2<1>(c, acc, rsum, cs, Wr, ra, setup, ws_stamp);
  else kb_phase2<0>(c, acc, rsum, cs, Wr, ra, setup, ws_stamp);
  STAMP(2, 2);

  u32x2 res[4];
  {
    const u32 rb = ra[0] + dt * 16384;
    ds_tr<0>(res[0], rb); ds_tr<1024>(res[1], rb); ds_tr<8192>(res[2], rb); ds_tr<9216>(res[3], rb);
  }
  float* stl = (float*)(smem + KB_ST);
  if (g == 0) {
#pragma unroll
    for (int i = 0; i < 2; ++i) stl[2 * (16 * sf(i) + n)] = rsum[i][0];
  }
  if (w == 3 && g == (n >> 2)) {
#pragma unroll
    for (int i = 0; i < 8; ++i) {
      f32x4 a = acc[2][i];
      float dg = (n & 2) ? ((n & 1) ? a[3] : a[2]) : ((n & 1) ? a[1] : a[0]);
      stl[2 * (16 * sf(i) + n) + 1] = dg;
    }
  }
  asm volatile("s_waitcnt lgkmcnt(0)\n\ts_barrier" : "+v"(res[0]), "+v"(res[1]), "+v"(res[2]), "+v"(res[3]) :: "memory");
  STAMP(2, 3);
  const __amdgpu_buffer_rsrc_t hrs = __builtin_amdgcn_make_buffer_rsrc(ws + WS_H, 0, (S / 16) * KS2 * 1024, 0x00020000);
  f32x2 sv[8];
#pragma unroll
  for (int i = 0; i < 8; ++i) sv[i] = *(const f32x2*)(stl + 2 * (16 * sf(i) + n));
  u32 hv[24];
  static_for<8>([&](auto I) {
    constexpr int i = I;
    auto fill = [&](auto E) { constexpr int wk = 4 * i + E; glds16<wk * 4096>(c.lv, c.wd + (size_t)wk * 4096, c.lwd); };
    fill(std::integral_constant<int, 3>{});
    const float mean = sv[i][0] * (1.f / D);
    const float var = fmaxf(sv[i][1] * (1.f / D) - mean * mean, 0.f) + EPS;
    const float rstd = __builtin_amdgcn_rsqf(var);
    const float rinv = var * rstd;
    const float ce = -1.4426950408889634f * rstd;
    const f32x2 nm = {-mean, -mean}, ce2 = {ce, ce}, ri2 = {rinv, rinv};
    static_for<3>([&](auto U_) {
      constexpr int u = U_;
      pin4(acc[u][i]);
      fill(U_);
      const f32x4 a = acc[u][i];
      const f32x2 tg = (f32x2{a[0], a[1]} + nm * f32x2{cs[u][0], cs[u][1]}) * ce2;
      const f32x2 tu = f32x2{a[2], a[3]} + nm * f32x2{cs[u][2], cs[u][3]};
      const f32x2 e = {__builtin_amdgcn_exp2f(tg[0]), __builtin_amdgcn_exp2f(tg[1])};
      const f32x2 den = e * ri2 + ri2;
      const f32x2 o = tu * f32x2{__builtin_amdgcn_rcpf(den[0]), __builtin_amdgcn_rcpf(den[1])};
      hv[3 * i + u] = pk2(o[0], o[1]);
    });
    static_for<6>([&](auto T) {
      constexpr int tt = T;
      if constexpr ((4 * tt + 3) / 3 == i) {
        auto s02 = __builtin_amdgcn_permlane32_swap(hv[4 * tt], hv[4 * tt + 2], false, false);
        auto s13 = __builtin_amdgcn_permlane32_swap(hv[4 * tt + 1], hv[4 * tt + 3], false, false);
        auto t01 = __builtin_amdgcn_permlane16_swap(s02[0], s13[0], false, false);
        auto t23 = __builtin_amdgcn_permlane16_swap(s02[1], s13[1], false, false);
        __builtin_amdgcn_raw_buffer_store_b128(u32x4{t01[0], t01[1], t23[0], t23[1]}, hrs, hoff[tt], 0, 16);
      }
    });
  });
  STAMP(2, 4);
  u32* cnt = (u32*)(ws + WS_CNT + rt * 128);
  asm volatile("s_waitcnt vmcnt(0)\n\ts_barrier" ::: "memory");
  STAMP(2, 5);
  if (w == 0) {
    if (lane == 0) __hip_atomic_fetch_add(cnt, 1u, __ATOMIC_RELAXED, __HIP_MEMORY_SCOPE_AGENT);
    for (int spins = 0; spins < (1 << 22); ++spins) {
      const u32 v = __builtin_amdgcn_readfirstlane(__hip_atomic_load(cnt, __ATOMIC_RELAXED, __HIP_MEMORY_SCOPE_AGENT));
      if (v >= 16u) break;
      __builtin_amdgcn_s_sleep(1);
    }
  }
  asm volatile("s_waitcnt vmcnt(0)\n\ts_barrier" ::: "memory");
  STAMP(2, 6);

  const char* hb = ws + WS_H + (size_t)(8 * rt + 4 * half + w) * KS2 * 1024;
  u32x4 Hf[KS2];
  constexpr int P3_AHEAD = 12;
  auto ldH = [&](auto KS) { constexpr int ks = KS; if constexpr (ks < KS2) gld_a_sc1<(ks & 3) * 1024>(Hf[ks], c.lv, hb + (ks >> 2) * 4096); };
  static_for<P3_AHEAD>([&](auto KS) { ldH(KS); });
  STAMP(3, 0);
  f32x4 o4[4];
  u32x4 Af[3][4];
  const u32 la0 = c.sb + lane * 16, la1 = la0 + 65536, la2 = la0 + 131072;
  auto rdA = [&](auto KS, auto DF) {
    constexpr int ks = KS, df = DF;
    if constexpr (ks < KB_NL) ds_b128<(ks & 15) * 4096 + df * 1024>(Af[ks % 3][df], ks < 16 ? la0 : ks < 32 ? la1 : la2);
  };
  static_for<4>([&](auto DF) { rdA(std::integral_constant<int, 0>{}, DF); });
  static_for<4>([&](auto DF) { rdA(std::integral_constant<int, 1>{}, DF); });
  static_for<KS2>([&](auto KS) {
    constexpr int ks = KS;
    wait_vm<(ks + P3_AHEAD <= KS2 ? P3_AHEAD - 1 : KS2 - 1 - ks)>();
    if constexpr (ks + 1 < KB_NL) wait_lgkm<4>(); else wait_lgkm<0>();
    pin_a(Hf[ks]);
    static_for<4>([&](auto DF) {
      constexpr int df = DF;
      if constexpr (ks < KB_NL) { pin(Af[ks % 3][df]); mfma_va<ks == 0>(o4[df], Af[ks % 3][df], Hf[ks]); }
      else { pin_a(Wr[ks - KB_NL][df]); mfma_aa(o4[df], Wr[ks - KB_NL][df], Hf[ks]); }
      rdA(std::integral_constant<int, ks + 2>{}, DF);
      if constexpr (df == 1 && ks + P3_AHEAD < KS2) ldH(std::integral_constant<int, ks + P3_AHEAD>{});
    });
  });
  mfma_fence(o4[0], o4[1], o4[2], o4[3]);
  u32x4 o0, o1;
#pragma unroll
  for (int df = 0; df < 2; ++df) {
    o0[2 * df] = pk2(o4[df][0] + blo(res[df][0]), o4[df][1] + bhi(res[df][0]));
    o0[2 * df + 1] = pk2(o4[df][2] + blo(res[df][1]), o4[df][3] + bhi(res[df][1]));
    o1[2 * df] = pk2(o4[2 + df][0] + blo(res[2 + df][0]), o4[2 + df][1] + bhi(res[2 + df][0]));
    o1[2 * df + 1] = pk2(o4[2 + df][2] + blo(res[2 + df][1]), o4[2 + df][3] + bhi(res[2 + df][1]));
  }
  u16* orow = out + (size_t)(s0 + 64 * half + 16 * w + n) * D + 64 * dt + 8 * g;
  *(u32x4*)orow = o0;
  *(u32x4*)(orow + 32) = o1;
  STAMP(2, 7);
}

extern "C" void kernel_launch(void* const* d_in, const int* in_sizes, int n_in, void* d_out, int out_size, void* d_ws,
                              size_t ws_size, hipStream_t stream) {
  const u16 *q = (const u16*)d_in[0], *k = (const u16*)d_in[1], *v = (const u16*)d_in[2], *shk = (const u16*)d_in[3];
  const u16 *ssmk = (const u16*)d_in[4], *dskip = (const u16*)d_in[5];
  const u16 *wg = (const u16*)d_in[7], *wu = (const u16*)d_in[8], *wd = (const u16*)d_in[9];
  char* ws = (char*)d_ws;
  (void)hipFuncSetAttribute((const void*)k0_prep, hipFuncAttributeMaxDynamicSharedMemorySize, K0_LDS);
  (void)hipFuncSetAttribute((const void*)k1_conv, hipFuncAttributeMaxDynamicSharedMemorySize, K1_LDS);
  (void)hipFuncSetAttribute((const void*)kb_mlp, hipFuncAttributeMaxDynamicSharedMemorySize, KB_LDS);
  k0_prep<<<256, 256, K0_LDS, stream>>>(q, k, v, shk, ws);
  k1_conv<<<512, 256, K1_LDS, stream>>>(ssmk, dskip, wg, wu, wd, ws);
  kb_mlp<<<256, 256, KB_LDS, stream>>>(ws, (u16*)d_out);
}
```

```cpp
#include <hip/hip_runtime.h>
#include <stdint.h>
#include <utility>

typedef unsigned short u16;
typedef unsigned u32;
typedef __attribute__((ext_vector_type(2))) u32 u32x2;
typedef __attribute__((ext_vector_type(4))) u32 u32x4;
typedef __attribute__((ext_vector_type(2))) float f32x2;
typedef __attribute__((ext_vector_type(4))) float f32x4;
typedef __attribute__((ext_vector_type(2))) __bf16 bf16x2;
typedef __attribute__((ext_vector_type(8))) __bf16 bf16x8;
typedef __attribute__((ext_vector_type(4))) short s16x4;
#define LDSP __attribute__((address_space(3)))
#define DI __device__ __forceinline__

constexpr int S = 2048, D = 512, F = 1376;
constexpr int NU = F / 8;
constexpr int KS1 = D / 32;
constexpr int KS2 = F / 32;
constexpr float EPS = 1e-5f;

constexpr size_t WS_XR = 0;
constexpr size_t WS_QT = WS_XR + (size_t)D * S * 2;
constexpr size_t WS_HT = WS_QT + (size_t)D * S * 2;
constexpr size_t WS_W1 = WS_HT + (size_t)D * S * 2;
constexpr size_t WS_WD = WS_W1 + (size_t)NU * KS1 * 1024;
constexpr size_t WS_H = WS_WD + (size_t)8 * KS2 * 4 * 1024;
constexpr size_t WS_CS = WS_H + (size_t)(S / 16) * KS2 * 1024;
constexpr size_t WS_SA = WS_CS + (size_t)KS1 * NU * 16 * 4;
constexpr size_t WS_SB = WS_SA + (size_t)S * 4;
constexpr size_t WS_CNT = WS_SB + (size_t)S * 4;

template <int... Is, class Fn> DI void static_for_(std::integer_sequence<int, Is...>, Fn&& f) { (f(std::integral_constant<int, Is>{}), ...); }
template <int N, class Fn> DI void static_for(Fn&& f) { static_for_(std::make_integer_sequence<int, N>{}, f); }

DI float blo(u32 u) { return __uint_as_float(u << 16); }
DI float bhi(u32 u) { return __uint_as_float(u & 0xffff0000u); }
DI u32 pk2(float a, float b) { f32x2 t = {a, b}; return __builtin_bit_cast(u32, __builtin_convertvector(t, bf16x2)); }
DI f32x4 mfma16(u32x4 a, u32x4 b, f32x4 c) {
  return __builtin_amdgcn_mfma_f32_16x16x32_bf16(__builtin_bit_cast(bf16x8, a), __builtin_bit_cast(bf16x8, b), c, 0, 0, 0);
}
DI u32x2 tr_read(u32 lds_byte) {
  return __builtin_bit_cast(u32x2, __builtin_amdgcn_ds_read_tr16_b64_v4i16((LDSP s16x4*)(size_t)lds_byte));
}
DI u32x4 tr_frag(u32 a0, u32 a1) { u32x2 x = tr_read(a0), y = tr_read(a1); return u32x4{x[0], x[1], y[0], y[1]}; }
DI u32x4 cat(u32x2 x, u32x2 y) { return __builtin_shufflevector(x, y, 0, 1, 2, 3); }
template <int CTRL> DI float dpp_add(float v) {
  return v + __builtin_bit_cast(float, __builtin_amdgcn_update_dpp(0, __builtin_bit_cast(int, v), CTRL, 0xf, 0xf, true));
}
DI float sum_rows(float v) {
  u32 u = __float_as_uint(v);
  auto a = __builtin_amdgcn_permlane16_swap(u, u, false, false);
  v = __uint_as_float(a[0]) + __uint_as_float(a[1]);
  u = __float_as_uint(v);
  auto b = __builtin_amdgcn_permlane32_swap(u, u, false, false);
  return __uint_as_float(b[0]) + __uint_as_float(b[1]);
}
DI float wave_sum(float v) {
  v = dpp_add<0xb1>(v);
  v = dpp_add<0x4e>(v);
  v = dpp_add<0x124>(v);
  v = dpp_add<0x128>(v);
  return sum_rows(v);
}
DI __amdgpu_buffer_rsrc_t ws_rsrc(char* ws) { return __builtin_amdgcn_make_buffer_rsrc(ws, 0, 0x04000000, 0x00020000); }
DI void st16_wt(__amdgpu_buffer_rsrc_t r, u32 off, u32x4 v) { __builtin_amdgcn_raw_buffer_store_b128(v, r, off, 0, 16); }
DI int slice_u0(int j) { return j < 12 ? 11 * j : 132 + 10 * (j - 12); }
DI int slice_nu(int j) { return j < 12 ? 11 : 10; }

template <bool FIRST = false> DI void mfma_vv(f32x4& acc, const u32x4& a, const u32x4& b) {
  if constexpr (FIRST) asm volatile("v_mfma_f32_16x16x32_bf16 %0, %1, %2, 0" : "=&v"(acc) : "v"(a), "v"(b));
  else asm volatile("v_mfma_f32_16x16x32_bf16 %0, %1, %2, %0" : "+v"(acc) : "v"(a), "v"(b));
}
template <bool FIRST = false> DI void mfma_av(f32x4& acc, const u32x4& a, const u32x4& b) {
  if constexpr (FIRST) asm volatile("v_mfma_f32_16x16x32_bf16 %0, %1, %2, 0" : "=&v"(acc) : "a"(a), "v"(b));
  else asm volatile("v_mfma_f32_16x16x32_bf16 %0, %1, %2, %0" : "+v"(acc) : "a"(a), "v"(b));
}
template <int OFF> DI void ds_b64(u32x2& d, u32 addr) { asm volatile("ds_read_b64 %0, %1 offset:%c2" : "=v"(d) : "v"(addr), "i"(OFF) : "memory"); }
template <int OFF> DI void ds_b128(u32x4& d, u32 addr) { asm volatile("ds_read_b128 %0, %1 offset:%c2" : "=v"(d) : "v"(addr), "i"(OFF) : "memory"); }
template <int OFF> DI void ds_tr(u32x2& d, u32 addr) { asm volatile("ds_read_b64_tr_b16 %0, %1 offset:%c2" : "=v"(d) : "v"(addr), "i"(OFF) : "memory"); }
template <int OFF> DI void gld_a(u32x4& d, u32 voff, const void* sbase) {
  asm volatile("global_load_dwordx4 %0, %1, %2 offset:%c3" : "=a"(d) : "v"(voff), "s"(sbase), "i"(OFF) : "memory");
}
template <int OFF> DI void gld_v(u32x4& d, u32 voff, const void* sbase) {
  asm volatile("global_load_dwordx4 %0, %1, %2 offset:%c3" : "=v"(d) : "v"(voff), "s"(sbase), "i"(OFF) : "memory");
}
template <int OFF> DI void gld2_v(u32x2& d, u32 voff, const void* sbase) {
  asm volatile("global_load_dwordx2 %0, %1, %2 offset:%c3" : "=v"(d) : "v"(voff), "s"(sbase), "i"(OFF) : "memory");
}
template <int LOFF> DI void glds16(u32 voff, const void* sbase, u32 ldsbase) {
  asm volatile("s_add_u32 m0, %2, %c3\n\ts_nop 0\n\tglobal_load_lds_dwordx4 %0, %1" ::"v"(voff), "s"(sbase), "s"(ldsbase), "i"(LOFF) : "memory", "scc");
}
template <int N> DI void wait_vm() { asm volatile("s_waitcnt vmcnt(%c0)" ::"i"(N) : "memory"); }
template <int N> DI void wait_lgkm() { asm volatile("s_waitcnt lgkmcnt(%c0)" ::"i"(N) : "memory"); }
template <int OFF> DI void gld_a_sc1(u32x4& d, u32 voff, const void* sbase) {
  asm volatile("global_load_dwordx4 %0, %1, %2 offset:%c3 sc1" : "=a"(d) : "v"(voff), "s"(sbase), "i"(OFF) : "memory");
}
DI void mfma_aa(f32x4& acc, const u32x4& a, const u32x4& b) {
  asm volatile("v_mfma_f32_16x16x32_bf16 %0, %1, %2, %0" : "+v"(acc) : "a"(a), "a"(b));
}
template <bool FIRST = false> DI void mfma_va(f32x4& acc, const u32x4& a, const u32x4& b) {
  if constexpr (FIRST) asm volatile("v_mfma_f32_16x16x32_bf16 %0, %1, %2, 0" : "=&v"(acc) : "v"(a), "a"(b));
  else asm volatile("v_mfma_f32_16x16x32_bf16 %0, %1, %2, %0" : "+v"(acc) : "v"(a), "a"(b));
}
DI void mfma_fence(f32x4& a, f32x4& b, f32x4& c, f32x4& d) { asm volatile("s_nop 7\n\ts_nop 1" : "+v"(a), "+v"(b), "+v"(c), "+v"(d)); }
DI void after_fence(f32x4& a, f32x4& b, f32x4& c, f32x4& d) { asm volatile("" : "+v"(a), "+v"(b), "+v"(c), "+v"(d)); }
DI void after_fence(f32x4& a, f32x4& b, f32x4& c) { asm volatile("" : "+v"(a), "+v"(b), "+v"(c)); }
template <int STAGE> DI void dpp_stage(float& v) {
  if constexpr (STAGE == 0) asm volatile("v_add_f32_dpp %0, %0, %0 quad_perm:[1,0,3,2] row_mask:0xf bank_mask:0xf" : "+v"(v));
  if constexpr (STAGE == 1) asm volatile("v_add_f32_dpp %0, %0, %0 quad_perm:[2,3,0,1] row_mask:0xf bank_mask:0xf" : "+v"(v));
  if constexpr (STAGE == 2) asm volatile("v_add_f32_dpp %0, %0, %0 row_ror:4 row_mask:0xf bank_mask:0xf" : "+v"(v));
  if constexpr (STAGE == 3) asm volatile("v_add_f32_dpp %0, %0, %0 row_ror:8 row_mask:0xf bank_mask:0xf" : "+v"(v));
}
DI void barrier() { asm volatile("s_barrier" ::: "memory"); }
DI void pin(u32x4& x) { asm volatile("" : "+v"(x)); }
DI void pin(u32x2& x) { asm volatile("" : "+v"(x)); }
DI void pin4(f32x4& x) { asm volatile("" : "+v"(x)); }
DI void pin_a(u32x4& x) { asm volatile("" : "+a"(x)); }

#define STAMP(kern, idx) do {} while (0)

constexpr int K0_K = 0, K0_V = 72 * 128, K0_Q = K0_V + 64 * 128, K0_LDS = K0_Q + 64 * 128;

__global__ __launch_bounds__(256) void k0_prep(const u16* __restrict__ q, const u16* __restrict__ k, const u16* __restrict__ v,
                                               const u16* __restrict__ shk, char* __restrict__ ws) {
  extern __shared__ __attribute__((aligned(16))) char smem[];
  const int t = threadIdx.x, lane = t & 63;
  const int w = __builtin_amdgcn_readfirstlane(t >> 6);
  const int b = blockIdx.x;
  char* ws_stamp = ws; (void)ws_stamp;
  STAMP(0, 0);
  const int x = b & 7, s0 = 64 * (b >> 3);
  const u32 sb = (u32)(size_t)(LDSP char*)smem;
  const int i16 = lane & 15, g = lane >> 4, qd = i16 >> 2, p = i16 & 3;
  const int d = 64 * x + 16 * w + i16;
  const int srow = 8 * b + 2 * w;
  u32x4 qa, qb; u32x2 sh;
  gld_v<0>(qa, lane * 16, (const char*)q + (size_t)srow * 1024);
  gld_v<1024>(qb, lane * 16, (const char*)q + (size_t)srow * 1024);
  gld2_v<0>(sh, d * 8, shk);
  {
    const int r8 = lane >> 3, c8 = lane & 7;
    auto voff = [&](int instr) { const int f = ((r8 >> 1) & 1) | ((instr & 1) << 1); return (u32)((((c8 >> 1) ^ f) * 2 + (c8 & 1)) * 16); };
    const size_t col = (size_t)x * 128;
    {
      int row = s0 - 8 + 8 * w + r8; row = row < 0 ? 0 : row;
      glds16<0>((u32)row * 1024 + voff(w), (const char*)k + col, sb + K0_K + w * 1024);
      glds16<0>((u32)(s0 - 8 + 8 * (w + 4) + r8) * 1024 + voff(w), (const char*)k + col, sb + K0_K + (w + 4) * 1024);
    }
#pragma unroll
    for (int i = 0; i < 2; ++i) {
      const int in = w + 4 * i;
      glds16<0>((u32)(s0 + 8 * in + r8) * 1024 + voff(in), (const char*)v + col, sb + K0_V + in * 1024);
      glds16<0>((u32)(s0 + 8 * in + r8) * 1024 + voff(in), (const char*)q + col, sb + K0_Q + in * 1024);
    }
    if (w == 0) glds16<0>((u32)(s0 + 56 + r8) * 1024 + voff(8), (const char*)k + col, sb + K0_K + 8 * 1024);
  }
  if (b == 0 && t < 16) *(u32*)(ws + WS_CNT + t * 128) = 0u;
  if (b < 8 && w == 3) {
    const char* pc; u32 len;
    asm volatile("s_getpc_b64 %0\n1:\n\ts_mov_b32 %1, kb_code_end-1b" : "=s"(pc), "=s"(len));
    for (u32 o0 = 0; o0 < len; o0 += 64 * 128) {
      u32 o = o0 + lane * 128; o = o < len ? o : 0;
      asm volatile("s_mov_b32 m0, %2\n\ts_nop 0\n\tglobal_load_lds_dword %0, %1" ::"v"(o), "s"(pc), "s"(sb + K0_LDS) : "memory");
    }
  }
  if (w == 0) wait_vm<7>(); else if (!(b < 8 && w == 3)) wait_vm<6>(); else wait_vm<0>();
  asm volatile("" : "+v"(qa), "+v"(qb), "+v"(sh));
  {
    float sa = 0.f, sbb = 0.f;
#pragma unroll
    for (int e = 0; e < 4; ++e) { sa += blo(qa[e]) + bhi(qa[e]); sbb += blo(qb[e]) + bhi(qb[e]); }
    const float ma = wave_sum(sa) * (1.f / D), mb = wave_sum(sbb) * (1.f / D);
    float va = 0.f, vb = 0.f;
#pragma unroll
    for (int e = 0; e < 4; ++e) {
      float u0 = blo(qa[e]) - ma, u1 = bhi(qa[e]) - ma, u2 = blo(qb[e]) - mb, u3 = bhi(qb[e]) - mb;
      va += u0 * u0 + u1 * u1; vb += u2 * u2 + u3 * u3;
    }
    va = wave_sum(va) * (1.f / D); vb = wave_sum(vb) * (1.f / D);
    if (lane == 0) {
      const float ra = __builtin_amdgcn_rsqf(va + EPS), rb = __builtin_amdgcn_rsqf(vb + EPS);
      *(f32x2*)(ws + WS_SA + (size_t)srow * 4) = f32x2{ra, rb};
      *(f32x2*)(ws + WS_SB + (size_t)srow * 4) = f32x2{-ma * ra, -mb * rb};
    }
  }
  wait_vm<0>();
  barrier();
  STAMP(0, 1);

  auto taddr = [&](int base, int tr) { const int f = ((tr >> 1) & 1) | ((tr >> 2) & 2); return sb + base + tr * 128 + ((w ^ f) * 32) + 8 * p; };
  const float a0 = blo(sh[0]), a1 = bhi(sh[0]), a2 = blo(sh[1]), a3 = bhi(sh[1]);
  const __amdgpu_buffer_rsrc_t wr = ws_rsrc(ws);
#pragma unroll
  for (int e = 0; e < 2; ++e) {
    const int rg = g + 4 * e;
    u32x2 km = tr_read(taddr(K0_K, 8 * rg + 4 + qd));
    const u32x2 k0 = tr_read(taddr(K0_K, 8 * rg + 8 + qd)), k1 = tr_read(taddr(K0_K, 8 * rg + 12 + qd));
    const u32x2 v0 = tr_read(taddr(K0_V, 8 * rg + qd)), v1 = tr_read(taddr(K0_V, 8 * rg + 4 + qd));
    const u32x2 q0 = tr_read(taddr(K0_Q, 8 * rg + qd)), q1 = tr_read(taddr(K0_Q, 8 * rg + 4 + qd));
    if (s0 + rg == 0) km = u32x2{0, 0};
    const float kf[11] = {bhi(km[0]), blo(km[1]), bhi(km[1]), blo(k0[0]), bhi(k0[0]), blo(k0[1]), bhi(k0[1]), blo(k1[0]), bhi(k1[0]), blo(k1[1]), bhi(k1[1])};
    const float vf[8] = {blo(v0[0]), bhi(v0[0]), blo(v0[1]), bhi(v0[1]), blo(v1[0]), bhi(v1[0]), blo(v1[1]), bhi(v1[1])};
    float xo[8];
#pragma unroll
    for (int j = 0; j < 8; ++j) xo[j] = (kf[j + 3] * a0 + kf[j + 2] * a1 + kf[j + 1] * a2 + kf[j] * a3) * vf[j];
    const int r0 = s0 + 8 * rg;
    st16_wt(wr, (u32)WS_XR + (d * S + (2040 - r0)) * 2, u32x4{pk2(xo[7], xo[6]), pk2(xo[5], xo[4]), pk2(xo[3], xo[2]), pk2(xo[1], xo[0])});
    st16_wt(wr, (u32)WS_QT + (d * S + r0) * 2, cat(q0, q1));
  }

  STAMP(0, 3);
}

DI void w_job(int b, const u16* __restrict__ wg, const u16* __restrict__ wu, const u16* __restrict__ wd, char* __restrict__ ws, char* smem) {
  const int t = threadIdx.x, lane = t & 63, w = t >> 6;
  char* tile = smem;
  char* tile2 = smem + 32 * 368;
  const u32 tb = (u32)(size_t)(LDSP char*)smem;
  const int i16 = lane & 15, qd = i16 >> 2, p = i16 & 3, g = lane >> 4;
  const int ks = b & 15, j = b >> 4;
  const int U0 = slice_u0(j), nu = slice_nu(j);
  u32x4 g4[2], u4[2], d4[2];
#pragma unroll
  for (int rep = 0; rep < 2; ++rep) {
    int idx = t + 256 * rep, r = idx / 11, u = idx - 11 * r;
    bool ok = r < 32 && u < nu;
    size_t off = ok ? (size_t)(32 * ks + r) * F + 8 * (U0 + u) : 0;
    g4[rep] = *(const u32x4*)(wg + off); u4[rep] = *(const u32x4*)(wu + off);
  }
  const int ks2 = b >> 2, dp = b & 3;
  if (b < KS2 * 4) {
#pragma unroll
    for (int rep = 0; rep < 2; ++rep) {
      int idx = t + 256 * rep, r = idx >> 4, c = idx & 15;
      d4[rep] = *(const u32x4*)(wd + (size_t)(32 * ks2 + r) * D + 128 * dp + 8 * c);
    }
  }
#pragma unroll
  for (int rep = 0; rep < 2; ++rep) {
    int idx = t + 256 * rep, r = idx / 11, u = idx - 11 * r;
    if (r < 32 && u < nu) {
      *(u32x4*)(tile + r * 368 + u * 32) = u32x4{g4[rep][0], u4[rep][0], g4[rep][1], u4[rep][1]};
      *(u32x4*)(tile + r * 368 + u * 32 + 16) = u32x4{g4[rep][2], u4[rep][2], g4[rep][3], u4[rep][3]};
    }
  }
  if (b < KS2 * 4) {
#pragma unroll
    for (int rep = 0; rep < 2; ++rep) { int idx = t + 256 * rep, r = idx >> 4, c = idx & 15; *(u32x4*)(tile2 + r * 272 + c * 16) = d4[rep]; }
  }
  __syncthreads();
  const u32x4 ones = {0x3f803f80u, 0x3f803f80u, 0x3f803f80u, 0x3f803f80u};
  const __amdgpu_buffer_rsrc_t wr = ws_rsrc(ws);
  for (int u = w; u < nu; u += 4) {
    u32 a = tb + (8 * g + qd) * 368 + u * 32 + 8 * p;
    u32x4 fr = tr_frag(a, a + 4 * 368);
    st16_wt(wr, (u32)WS_W1 + (((U0 + u) * KS1 + ks) * 64 + lane) * 16, fr);
    f32x4 cs = mfma16(fr, ones, f32x4{0.f, 0.f, 0.f, 0.f});
    if (i16 == 0) st16_wt(wr, (u32)WS_CS + ((ks * NU + U0 + u) * 16 + 4 * g) * 4, __builtin_bit_cast(u32x4, cs));
  }
  if (b < KS2 * 4) {
#pragma unroll
    for (int rep = 0; rep < 2; ++rep) {
      int fi = w + 4 * rep, dtl = fi >> 2, df = fi & 3;
      u32 a = tb + 32 * 368 + (8 * g + qd) * 272 + (64 * dtl + 32 * (df >> 1) + 8 * p + 4 * (df & 1)) * 2;
      u32x4 fr = tr_frag(a, a + 4 * 272);
      st16_wt(wr, (u32)WS_WD + ((((2 * dp + dtl) * KS2 + ks2) * 4 + df) * 64 + lane) * 16, fr);
    }
  }
}

constexpr int K1_CS = 4160;
constexpr int K1_KC = 4 * K1_CS;
constexpr int K1_XR = 2 * K1_KC;
constexpr int K1_RED = K1_XR + 2 * 4608;
constexpr int K1_LDS = K1_RED + 4 * 4096;

__global__ __launch_bounds__(256, 2) void k1_conv(const u16* __restrict__ ssmk, const u16* __restrict__ dskip, const u16* __restrict__ wg,
                                                  const u16* __restrict__ wu, const u16* __restrict__ wd, char* __restrict__ ws) {
  extern __shared__ __attribute__((aligned(16))) char smem[];
  const int t = threadIdx.x, lane = t & 63;
  const int w = __builtin_amdgcn_readfirstlane(t >> 6);
  const int b = blockIdx.x;
  char* ws_stamp = ws; (void)ws_stamp;
  STAMP(1, 0);
  if (b >= 256) { w_job(b - 256, wg, wu, wd, ws, smem); STAMP(1, 4); return; }
  const int ch0 = (b & 7) * 64 + (b >> 3) * 2;
  const int c = w >> 1, half = w & 1;
  const int n = lane & 15, g = lane >> 4;
  const u32 sb = (u32)(size_t)(LDSP char*)smem;

  {
    const char* xr = ws + WS_XR + (size_t)(ch0 + c) * 4096 + half * 2048;
    glds16<0>(lane * 16, xr, sb + K1_XR + c * 4608 + half * 2048);
    glds16<1024>(lane * 16, xr + 1024, sb + K1_XR + c * 4608 + half * 2048);
  }
  u32x4 kp[2], kc[2];
#pragma unroll
  for (int cc = 0; cc < 2; ++cc) {
    const u16* kr = ssmk + (size_t)(ch0 + cc) * S;
    u32x4 pv = *(const u32x4*)(kr + 8 * (t > 0 ? t - 1 : 0));
    kp[cc] = t > 0 ? pv : u32x4{0, 0, 0, 0};
    kc[cc] = *(const u32x4*)(kr + 8 * t);
  }
  const float dsk = blo(dskip[ch0 + c]);
  if (t < 8) *(u32x4*)(smem + (t >> 2) * K1_KC + (t & 3) * K1_CS) = u32x4{0, 0, 0, 0};
  if (t >= 64 && t < 64 + 60) { int i = t - 64; *(u32x4*)(smem + K1_XR + (i / 30) * 4608 + 4096 + (i % 30) * 16) = u32x4{0, 0, 0, 0}; }

#pragma unroll
  for (int cc = 0; cc < 2; ++cc) {
    u32 wv[6] = {kp[cc][0], kp[cc][1], kp[cc][2], kp[cc][3], kc[cc][0], kc[cc][1]};
    u32 o1[5];
#pragma unroll
    for (int i = 0; i < 5; ++i) o1[i] = __builtin_amdgcn_alignbit(wv[i + 1], wv[i], 16);
    char* base = smem + cc * K1_KC + (t + 1) * 16;
    *(u32x4*)(base + 0 * K1_CS) = u32x4{o1[0], o1[1], o1[2], o1[3]};
    *(u32x4*)(base + 1 * K1_CS) = u32x4{wv[1], wv[2], wv[3], wv[4]};
    *(u32x4*)(base + 2 * K1_CS) = u32x4{o1[1], o1[2], o1[3], o1[4]};
    *(u32x4*)(base + 3 * K1_CS) = u32x4{wv[2], wv[3], wv[4], wv[5]};
    if (t == 255) {
      const u32 z = 0;
      const u32 e0 = __builtin_amdgcn_alignbit(kc[cc][1], kc[cc][0], 16), e1 = __builtin_amdgcn_alignbit(kc[cc][2], kc[cc][1], 16);
      const u32 e2 = __builtin_amdgcn_alignbit(kc[cc][3], kc[cc][2], 16), e3 = __builtin_amdgcn_alignbit(z, kc[cc][3], 16);
      *(u32x4*)(base + 16 + 0 * K1_CS) = u32x4{e0, e1, e2, e3};
      *(u32x4*)(base + 16 + 1 * K1_CS) = u32x4{kc[cc][1], kc[cc][2], kc[cc][3], z};
      *(u32x4*)(base + 16 + 2 * K1_CS) = u32x4{e1, e2, e3, z};
      *(u32x4*)(base + 16 + 3 * K1_CS) = u32x4{kc[cc][2], kc[cc][3], z, z};
    }
  }
  asm volatile("s_waitcnt vmcnt(0) lgkmcnt(0)\n\ts_barrier" ::: "memory");
  STAMP(1, 1);

  const u32 aa = sb + c * K1_KC + (n & 3) * K1_CS + 16 * g + 2 * (n & 12) + half * 256;
  const u32 ba = sb + K1_XR + c * 4608 + 480 - 32 * n + 16 * g + half * 256;
  f32x4 acc[8];
  u32x2 Alo[2][8], Ahi[2][8];
  u32x4 B[2][8];
  auto rd = [&](auto C0, auto R) {
    constexpr int c0 = C0, r = R, buf = c0 & 1;
    if constexpr (r >= 2 && r < 10) {
      constexpr int u = r - 2;
      ds_b128<512 * (7 - u) + 64 * c0>(B[buf][u], ba);
    } else {
      constexpr int ar = r < 2 ? r : r - 8, i = ar >> 1;
      if constexpr ((ar & 1) == 0) ds_b64<512 * i + 64 * c0>(Alo[buf][i], aa);
      else ds_b64<512 * i + 64 * c0 + 8>(Ahi[buf][i], aa);
    }
  };
  u32x2 qv[4]; u32x4 sa[4], sbv[4];
  const u32 eo = (u32)(1024 * half + 16 * n + 4 * g);
  const char* qTc = ws + WS_QT + (size_t)(ch0 + c) * 4096;
  auto ldE = [&](auto E) {
    constexpr int e = E, i = e / 3, kind = e % 3;
    if constexpr (kind == 0) gld2_v<i * 512>(qv[i], eo * 2, qTc);
    if constexpr (kind == 1) gld_v<i * 1024>(sa[i], eo * 4, ws + WS_SA);
    if constexpr (kind == 2) gld_v<i * 1024>(sbv[i], eo * 4, ws + WS_SB);
  };
  static_for<24>([&](auto R) { rd(std::integral_constant<int, 0>{}, R); });
  static_for<4>([&](auto C0) {
    constexpr int c0 = C0, buf = c0 & 1;
    u32x4 A[8];
    static_for<36>([&](auto M) {
      constexpr int m = M;
      constexpr int c1 = m < 8 ? 0 : m < 15 ? 1 : m < 21 ? 2 : m < 26 ? 3 : m < 30 ? 4 : m < 33 ? 5 : m < 35 ? 6 : 7;
      constexpr int first = c1 * 8 - c1 * (c1 - 1) / 2;
      constexpr int u = m - first;
      if constexpr (m == 0) {
        if constexpr (c0 == 0) wait_lgkm<14>(); else wait_lgkm<0>();
        pin(Alo[buf][0]); pin(Ahi[buf][0]);
#pragma unroll
        for (int i = 0; i < 8; ++i) pin(B[buf][i]);
        A[0] = cat(Alo[buf][0], Ahi[buf][0]);
      }
      if constexpr (m == 8) {
        if constexpr (c0 == 0) wait_lgkm<8>();
#pragma unroll
        for (int i = 1; i < 8; ++i) { pin(Alo[buf][i]); pin(Ahi[buf][i]); A[i] = cat(Alo[buf][i], Ahi[buf][i]); }
      }
      mfma_vv<(c0 == 0 && c1 == 0)>(acc[c1 + u], A[c1], B[buf][u]);
      if constexpr (c0 == 0) { if constexpr (m < 8) rd(std::integral_constant<int, 1>{}, M); else if constexpr (m >= 8 && m < 24) rd(std::integral_constant<int, 1>{}, M); }
      else if constexpr (c0 < 3 && m < 24) rd(std::integral_constant<int, c0 + 1>{}, M);
      if constexpr ((c0 == 1 || c0 == 2) && m >= 24 && m < 36 && (m & 1) == 0) ldE(std::integral_constant<int, 6 * (c0 - 1) + (m - 24) / 2>{});
    });
  });
  mfma_fence(acc[0], acc[1], acc[2], acc[3]);
  after_fence(acc[4], acc[5], acc[6], acc[7]);
  wait_vm<0>();
#pragma unroll
  for (int i = 0; i < 4; ++i) asm volatile("" : "+v"(qv[i]), "+v"(sa[i]), "+v"(sbv[i]));
  STAMP(1, 2);
  f32x4 fin[4];
  {
    f32x4* red = (f32x4*)(smem + K1_RED);
    if (half) {
#pragma unroll
      for (int i = 0; i < 4; ++i) { red[(w * 4 + i) * 64 + lane] = acc[i]; fin[i] = acc[4 + i]; }
      asm volatile("; half 1" ::: "memory");
    } else {
#pragma unroll
      for (int i = 0; i < 4; ++i) { red[(w * 4 + i) * 64 + lane] = acc[4 + i]; fin[i] = acc[i]; }
      asm volatile("; half 0" ::: "memory");
    }
    asm volatile("s_waitcnt lgkmcnt(0)\n\ts_barrier" ::: "memory");
#pragma unroll
    for (int i = 0; i < 4; ++i) fin[i] += red[((w ^ 1) * 4 + i) * 64 + lane];
  }
  const __amdgpu_buffer_rsrc_t wr = ws_rsrc(ws);
  const f32x2 dd = {dsk, dsk};
  u32x2 hv[4];
#pragma unroll
  for (int i = 0; i < 4; ++i) {
    const int s = 256 * (4 * half + i) + 16 * n + 4 * g;
    const u32x2 xq = *(LDSP const u32x2*)(size_t)(sb + K1_XR + c * 4608 + 2 * (2044 - s));
    const f32x2 x0 = {bhi(xq[1]), blo(xq[1])}, x1 = {bhi(xq[0]), blo(xq[0])};
    const f32x2 q0 = {blo(qv[i][0]), bhi(qv[i][0])}, q1 = {blo(qv[i][1]), bhi(qv[i][1])};
    const f32x2 z0 = x0 * dd + f32x2{fin[i][0], fin[i][1]}, z1 = x1 * dd + f32x2{fin[i][2], fin[i][3]};
    const f32x4 a4 = __builtin_bit_cast(f32x4, sa[i]), b4 = __builtin_bit_cast(f32x4, sbv[i]);
    const f32x2 t0 = q0 * f32x2{a4[0], a4[1]} + f32x2{b4[0], b4[1]};
    const f32x2 t1 = q1 * f32x2{a4[2], a4[3]} + f32x2{b4[2], b4[3]};
    const f32x2 h0 = t0 * z0 + q0, h1 = t1 * z1 + q1;
    hv[i] = u32x2{pk2(h0[0], h0[1]), pk2(h1[0], h1[1])};
  }
#pragma unroll
  for (int i = 0; i < 4; i += 2) {
    auto a = __builtin_amdgcn_permlane16_swap(hv[i][0], hv[i + 1][0], false, false);
    auto bb = __builtin_amdgcn_permlane16_swap(hv[i][1], hv[i + 1][1], false, false);
    const int s = 256 * (4 * half + i + (g & 1)) + 16 * n + 4 * (g & 2);
    st16_wt(wr, (u32)WS_HT + ((ch0 + c) * S + s) * 2, u32x4{a[0], bb[0], a[1], bb[1]});
  }
  STAMP(1, 3);
}

constexpr int KB_NL = 39;
constexpr int KB_ST = KB_NL * 4096;
constexpr int KB_LDS = KB_ST + 1024;
constexpr int KB_RING = 8;

constexpr int p2_upto(int ks) { return ks < 0 ? 2 : ks <= 3 ? 2 * ks + 4 : (ks + 7 > 15 ? 15 : ks + 7); }
constexpr int p2_extra(int ks) { return ks == 9 ? 3 : ks >= 10 ? 4 : 0; }
constexpr int p2_extra_first(int ks) { int c = 0; for (int s = 0; s < ks; ++s) c += p2_extra(s); return c; }
constexpr int P2_NEXTRA = 23;
constexpr int p2_extra_real(int ks) { int f = p2_extra_first(ks), n = p2_extra(ks); return f >= P2_NEXTRA ? 0 : (f + n > P2_NEXTRA ? P2_NEXTRA - f : n); }
constexpr int p2_issue_step(int t) { for (int s = -1; s < KS1; ++s) if (p2_upto(s) >= t) return s; return KS1; }
constexpr int p2_younger(int t, int ks, int ops) {
  int c = (p2_upto(ks - 1) - t) * ops;
  for (int s = p2_issue_step(t); s < ks; ++s) if (s >= 0) c += p2_extra_real(s);
  return c;
}

struct KbCtx {
  u32 sb, gv, lv, lb0, lwd, csv;
  const char *hb0, *hb1, *w0, *w1, *w2, *cs0, *cs1, *cs2, *wd, *wdr;
};

template <int GRAM, class Setup>
DI void kb_phase2(const KbCtx& c, f32x4 (&acc)[3][8], f32x4 (&rsum)[2], f32x4 (&csp)[3], u32x4 (&Wr)[4][4], u32 (&ra)[8], Setup&& setup, char* ws_stamp) {
  constexpr int OPS = GRAM ? 4 : 5;
  u32x4 W[KB_RING][3];
  u32x2 Bl[2][8], Bh[2][8];
  auto issue = [&](auto KS, auto O) {
    constexpr int ks = KS, o = O;
    if constexpr (o == 0) glds16<ks * 8192>(c.gv, c.hb0 + (size_t)ks * 131072, c.lb0);
    if constexpr (o == 1) gld_a<(ks & 3) * 1024>(W[ks % KB_RING][0], c.lv, c.w0 + (ks >> 2) * 4096);
    if constexpr (o == 2) gld_a<(ks & 3) * 1024>(W[ks % KB_RING][1], c.lv, c.w1 + (ks >> 2) * 4096);
    if constexpr (o == 3 && !GRAM) gld_a<(ks & 3) * 1024>(W[ks % KB_RING][2], c.lv, c.w2 + (ks >> 2) * 4096);
    if constexpr (o == 4) glds16<ks * 8192 + 1024>(c.gv, c.hb1 + (size_t)ks * 131072, c.lb0);
  };
  auto extra = [&](auto E) {
    constexpr int e = E;
    if constexpr (e < 7) glds16<(32 + e) * 4096>(c.lv, c.wd + (size_t)(32 + e) * 4096, c.lwd);
    else if constexpr (e < P2_NEXTRA) gld_a<((e - 7) & 3) * 1024>(Wr[(e - 7) >> 2][(e - 7) & 3], c.lv, c.wdr + ((e - 7) >> 2) * 4096);
  };
  auto rdB = [&](auto KS, auto R) {
    constexpr int ks = KS, r = R, i = r >> 1;
    if constexpr ((r & 1) == 0) ds_tr<(ks & 7) * 8192>(Bl[ks & 1][i], ra[i] + (ks >= 8 ? 65536 : 0));
    else ds_tr<(ks & 7) * 8192 + 1024>(Bh[ks & 1][i], ra[i] + (ks >= 8 ? 65536 : 0));
  };
  u32x4 c0, c1, c2;
  static_for<5>([&](auto O) { issue(std::integral_constant<int, 0>{}, O); });
  gld_v<0>(c0, c.csv, c.cs0); gld_v<0>(c1, c.csv, c.cs1); gld_v<0>(c2, c.csv, c.cs2);
  static_for<p2_upto(-1)>([&](auto KS) { static_for<5>([&](auto O) { issue(std::integral_constant<int, KS + 1>{}, O); }); });
  setup();
  u32x4 ones = {0x3f803f80u, 0x3f803f80u, 0x3f803f80u, 0x3f803f80u};
  pin(ones);
  wait_vm<p2_upto(-1) * OPS>();
  barrier();
  static_for<16>([&](auto R) { rdB(std::integral_constant<int, 0>{}, R); });
  asm volatile("" : "+v"(c0), "+v"(c1), "+v"(c2));
  float cv[12];
#pragma unroll
  for (int e = 0; e < 4; ++e) { cv[e] = __uint_as_float(c0[e]); cv[4 + e] = __uint_as_float(c1[e]); cv[8 + e] = __uint_as_float(c2[e]); }
  static_for<KS1>([&](auto KS) {
    constexpr int ks = KS, buf = ks & 1, slot = ks % KB_RING;
    constexpr int nmain = (p2_upto(ks) - p2_upto(ks - 1)) * 5, next = p2_extra_real(ks);
    wait_lgkm<0>();
    u32x4 B[8];
#pragma unroll
    for (int i = 0; i < 8; ++i) { pin(Bl[buf][i]); pin(Bh[buf][i]); }
#pragma unroll
    for (int i = 0; i < 8; ++i) B[i] = cat(Bl[buf][i], Bh[buf][i]);
    pin_a(W[slot][0]); pin_a(W[slot][1]);
    if constexpr (!GRAM) pin_a(W[slot][2]);
    static_for<26>([&](auto M) {
      constexpr int m = M, i = m / 3, u = m % 3;
      if constexpr (m >= 24) mfma_vv<ks == 0>(rsum[m - 24], ones, B[m - 24]);
      else if constexpr (u < 2 || !GRAM) mfma_av<ks == 0>(acc[u][i], W[slot][u], B[i]);
      else mfma_vv<ks == 0>(acc[2][i], B[i], B[i]);
      if constexpr (m == 1 && ks + 1 < KS1) { wait_vm<p2_younger(ks + 1, ks, OPS)>(); barrier(); }
      if constexpr (m >= 2 && m < 18 && ks + 1 < KS1) rdB(std::integral_constant<int, ks + 1>{}, std::integral_constant<int, m - 2>{});
      if constexpr (ks >= 9 && ks < 13 && m >= 2 && m < 14) dpp_stage<ks - 9>(cv[m - 2]);
      constexpr int v = m - (26 - nmain - next);
      if constexpr (v >= 0 && v < nmain) issue(std::integral_constant<int, p2_upto(ks - 1) + 1 + v / 5>{}, std::integral_constant<int, v % 5>{});
      if constexpr (v >= nmain) extra(std::integral_constant<int, p2_extra_first(ks) + v - nmain>{});
    });
  });
  mfma_fence(acc[0][0], acc[1][0], rsum[0], rsum[1]);
#pragma unroll
  for (int i = 0; i < 8; ++i) after_fence(acc[0][i], acc[1][i], acc[2][i], rsum[i & 1]);
#pragma unroll
  for (int u = 0; u < 3; ++u) csp[u] = f32x4{cv[4 * u], cv[4 * u + 1], cv[4 * u + 2], cv[4 * u + 3]};
}

__global__ __launch_bounds__(256) void kb_mlp(char* __restrict__ ws, u16* __restrict__ out) {
  extern __shared__ __attribute__((aligned(16))) char smem[];
  const int t = threadIdx.x, lane = t & 63;
  const int w = __builtin_amdgcn_readfirstlane(t >> 6);
  const int b = blockIdx.x, xcd = b & 7, bi = b >> 3;
#define KB_MAP 1
  const int rt = KB_MAP == 0 ? 2 * xcd + (bi & 1) : (xcd >> 1) * 4 + (bi & 3);
  const int j = KB_MAP == 0 ? bi >> 1 : (xcd & 1) * 8 + (bi >> 2);
  const int half = KB_MAP == 0 ? j & 1 : j >> 3, dt = KB_MAP == 0 ? j >> 1 : j & 7;
  const int U0 = slice_u0(j), nu = slice_nu(j);
  const int rem = nu - 8;
  const int nw = w < rem ? 3 : 2;
  const int uw = U0 + (w < rem ? 3 * w : 3 * rem + 2 * (w - rem));
  const int u2 = uw + (nw == 3 ? 2 : 0);
  const int n = lane & 15, g = lane >> 4;
  const int s0 = 128 * rt;
  char* ws_stamp = ws; (void)ws_stamp;
  STAMP(2, 0);
  auto sf = [&](int i) { return ((w + (i >> 1)) & 3) + 4 * (half ^ (i & 1)); };

  KbCtx c;
  c.sb = (u32)(size_t)(LDSP char*)smem;
  c.lv = lane * 16;
  c.gv = (u32)(g * 4096 + (((n >> 1) ^ (g | ((w & 1) << 2))) * 32 + (n & 1) * 16));
  c.hb0 = ws + WS_HT + (size_t)s0 * 2 + (size_t)(8 * w) * 4096;
  c.hb1 = c.hb0 + 4 * 4096;
  c.lb0 = c.sb + 8 * w * 256;
  c.w0 = ws + WS_W1 + (size_t)uw * KS1 * 1024;
  c.w1 = c.w0 + KS1 * 1024;
  c.w2 = ws + WS_W1 + (size_t)u2 * KS1 * 1024;
  c.cs0 = ws + WS_CS + (size_t)uw * 64;
  c.cs1 = c.cs0 + 64;
  c.cs2 = ws + WS_CS + (size_t)u2 * 64;
  c.csv = n * (NU * 64) + g * 16;
  c.wdr = ws + WS_WD + (size_t)dt * KS2 * 4096 + (size_t)KB_NL * 4096;
  c.wd = ws + WS_WD + (size_t)dt * KS2 * 4096 + (size_t)w * 1024;
  c.lwd = c.sb + 1024 * w;
  u32 ra[8], hoff[6];
  auto setup = [&]() {
    const int qd = n >> 2, p = n & 3;
    const int frow = qd | ((g & 1) << 2);
#pragma unroll
    for (int i = 0; i < 8; ++i) { ra[i] = c.sb + (8 * g + qd) * 256 + ((sf(i) ^ frow) * 32) + 8 * p; asm volatile("" : "+v"(ra[i])); }
#pragma unroll
    for (int tt = 0; tt < 6; ++tt) {
      const int q = 4 * tt + g, i = (q * 11) >> 5, u = q - 3 * i, U = uw + u;
      const u32 o = (u32)(((8 * rt + sf(i)) * KS2 + (U >> 2)) * 1024 + ((U & 3) * 16 + n) * 16);
      hoff[tt] = u < nw ? o : 0xfffffff0u;
      asm volatile("" : "+v"(hoff[tt]));
    }
  };
  f32x4 acc[3][8], cs[3], rsum[2];
  u32x4 Wr[4][4];
  if (w == 3) kb_phase2<1>(c, acc, rsum, cs, Wr, ra, setup, ws_stamp);
  else kb_phase2<0>(c, acc, rsum, cs, Wr, ra, setup, ws_stamp);
  STAMP(2, 2);

  u32x2 res[4];
  {
    const u32 rb = ra[0] + dt * 16384;
    ds_tr<0>(res[0], rb); ds_tr<1024>(res[1], rb); ds_tr<8192>(res[2], rb); ds_tr<9216>(res[3], rb);
  }
  float* stl = (float*)(smem + KB_ST);
  if (g == 0) {
#pragma unroll
    for (int i = 0; i < 2; ++i) stl[2 * (16 * sf(i) + n)] = rsum[i][0];
  }
  if (w == 3 && g == (n >> 2)) {
#pragma unroll
    for (int i = 0; i < 8; ++i) {
      f32x4 a = acc[2][i];
      float dg = (n & 2) ? ((n & 1) ? a[3] : a[2]) : ((n & 1) ? a[1] : a[0]);
      stl[2 * (16 * sf(i) + n) + 1] = dg;
    }
  }
  asm volatile("s_waitcnt lgkmcnt(0)\n\ts_barrier" : "+v"(res[0]), "+v"(res[1]), "+v"(res[2]), "+v"(res[3]) :: "memory");
  STAMP(2, 3);
  const __amdgpu_buffer_rsrc_t hrs = __builtin_amdgcn_make_buffer_rsrc(ws + WS_H, 0, (S / 16) * KS2 * 1024, 0x00020000);
  f32x2 sv[8];
#pragma unroll
  for (int i = 0; i < 8; ++i) sv[i] = *(const f32x2*)(stl + 2 * (16 * sf(i) + n));
  u32 hv[24];
  static_for<8>([&](auto I) {
    constexpr int i = I;
    auto fill = [&](auto E) { constexpr int wk = 4 * i + E; glds16<wk * 4096>(c.lv, c.wd + (size_t)wk * 4096, c.lwd); };
    fill(std::integral_constant<int, 3>{});
    const float mean = sv[i][0] * (1.f / D);
    const float var = fmaxf(sv[i][1] * (1.f / D) - mean * mean, 0.f) + EPS;
    const float rstd = __builtin_amdgcn_rsqf(var);
    const float rinv = var * rstd;
    const float ce = -1.4426950408889634f * rstd;
    const f32x2 nm = {-mean, -mean}, ce2 = {ce, ce}, ri2 = {rinv, rinv};
    static_for<3>([&](auto U_) {
      constexpr int u = U_;
      pin4(acc[u][i]);
      fill(U_);
      const f32x4 a = acc[u][i];
      const f32x2 tg = (f32x2{a[0], a[1]} + nm * f32x2{cs[u][0], cs[u][1]}) * ce2;
      const f32x2 tu = f32x2{a[2], a[3]} + nm * f32x2{cs[u][2], cs[u][3]};
      const f32x2 e = {__builtin_amdgcn_exp2f(tg[0]), __builtin_amdgcn_exp2f(tg[1])};
      const f32x2 den = e * ri2 + ri2;
      const f32x2 o = tu * f32x2{__builtin_amdgcn_rcpf(den[0]), __builtin_amdgcn_rcpf(den[1])};
      hv[3 * i + u] = pk2(o[0], o[1]);
    });
    static_for<6>([&](auto T) {
      constexpr int tt = T;
      if constexpr ((4 * tt + 3) / 3 == i) {
        auto s02 = __builtin_amdgcn_permlane32_swap(hv[4 * tt], hv[4 * tt + 2], false, false);
        auto s13 = __builtin_amdgcn_permlane32_swap(hv[4 * tt + 1], hv[4 * tt + 3], false, false);
        auto t01 = __builtin_amdgcn_permlane16_swap(s02[0], s13[0], false, false);
        auto t23 = __builtin_amdgcn_permlane16_swap(s02[1], s13[1], false, false);
        __builtin_amdgcn_raw_buffer_store_b128(u32x4{t01[0], t01[1], t23[0], t23[1]}, hrs, hoff[tt], 0, 16);
      }
    });
  });
  STAMP(2, 4);
  u32* cnt = (u32*)(ws + WS_CNT + rt * 128);
  asm volatile("s_waitcnt vmcnt(0)\n\ts_barrier" ::: "memory");
  STAMP(2, 5);
  if (w == 0) {
    if (lane == 0) __hip_atomic_fetch_add(cnt, 1u, __ATOMIC_RELAXED, __HIP_MEMORY_SCOPE_AGENT);
    for (int spins = 0; spins < (1 << 22); ++spins) {
      const u32 v = __builtin_amdgcn_readfirstlane(__hip_atomic_load(cnt, __ATOMIC_RELAXED, __HIP_MEMORY_SCOPE_AGENT));
      if (v >= 16u) break;
      __builtin_amdgcn_s_sleep(1);
    }
  }
  asm volatile("s_waitcnt vmcnt(0)\n\ts_barrier" ::: "memory");
  STAMP(2, 6);

  const char* hb = ws + WS_H + (size_t)(8 * rt + 4 * half + w) * KS2 * 1024;
  u32x4 Hf[KS2];
  constexpr int P3_AHEAD = 12;
  auto ldH = [&](auto KS) { constexpr int ks = KS; if constexpr (ks < KS2) gld_a_sc1<(ks & 3) * 1024>(Hf[ks], c.lv, hb + (ks >> 2) * 4096); };
  static_for<P3_AHEAD>([&](auto KS) { ldH(KS); });
  STAMP(3, 0);
  f32x4 o4[4];
  u32x4 Af[3][4];
  const u32 la0 = c.sb + lane * 16, la1 = la0 + 65536, la2 = la0 + 131072;
  auto rdA = [&](auto KS, auto DF) {
    constexpr int ks = KS, df = DF;
    if constexpr (ks < KB_NL) ds_b128<(ks & 15) * 4096 + df * 1024>(Af[ks % 3][df], ks < 16 ? la0 : ks < 32 ? la1 : la2);
  };
  static_for<4>([&](auto DF) { rdA(std::integral_constant<int, 0>{}, DF); });
  static_for<4>([&](auto DF) { rdA(std::integral_constant<int, 1>{}, DF); });
  static_for<KS2>([&](auto KS) {
    constexpr int ks = KS;
    wait_vm<(ks + P3_AHEAD <= KS2 ? P3_AHEAD - 1 : KS2 - 1 - ks)>();
    if constexpr (ks + 1 < KB_NL) wait_lgkm<4>(); else wait_lgkm<0>();
    pin_a(Hf[ks]);
    static_for<4>([&](auto DF) {
      constexpr int df = DF;
      if constexpr (ks < KB_NL) { pin(Af[ks % 3][df]); mfma_va<ks == 0>(o4[df], Af[ks % 3][df], Hf[ks]); }
      else { pin_a(Wr[ks - KB_NL][df]); mfma_aa(o4[df], Wr[ks - KB_NL][df], Hf[ks]); }
      rdA(std::integral_constant<int, ks + 2>{}, DF);
      if constexpr (df == 1 && ks + P3_AHEAD < KS2) ldH(std::integral_constant<int, ks + P3_AHEAD>{});
    });
  });
  mfma_fence(o4[0], o4[1], o4[2], o4[3]);
  u32x4 o0, o1;
#pragma unroll
  for (int df = 0; df < 2; ++df) {
    o0[2 * df] = pk2(o4[df][0] + blo(res[df][0]), o4[df][1] + bhi(res[df][0]));
    o0[2 * df + 1] = pk2(o4[df][2] + blo(res[df][1]), o4[df][3] + bhi(res[df][1]));
    o1[2 * df] = pk2(o4[2 + df][0] + blo(res[2 + df][0]), o4[2 + df][1] + bhi(res[2 + df][0]));
    o1[2 * df + 1] = pk2(o4[2 + df][2] + blo(res[2 + df][1]), o4[2 + df][3] + bhi(res[2 + df][1]));
  }
  u16* orow = out + (size_t)(s0 + 64 * half + 16 * w + n) * D + 64 * dt + 8 * g;
  *(u32x4*)orow = o0;
  *(u32x4*)(orow + 32) = o1;
  asm volatile("kb_code_end:");
  STAMP(2, 7);
}

extern "C" void kernel_launch(void* const* d_in, const int* in_sizes, int n_in, void* d_out, int out_size, void* d_ws,
                              size_t ws_size, hipStream_t stream) {
  const u16 *q = (const u16*)d_in[0], *k = (const u16*)d_in[1], *v = (const u16*)d_in[2], *shk = (const u16*)d_in[3];
  const u16 *ssmk = (const u16*)d_in[4], *dskip = (const u16*)d_in[5];
  const u16 *wg = (const u16*)d_in[7], *wu = (const u16*)d_in[8], *wd = (const u16*)d_in[9];
  char* ws = (char*)d_ws;
  (void)hipFuncSetAttribute((const void*)k0_prep, hipFuncAttributeMaxDynamicSharedMemorySize, K0_LDS + 256);
  (void)hipFuncSetAttribute((const void*)k1_conv, hipFuncAttributeMaxDynamicSharedMemorySize, K1_LDS);
  (void)hipFuncSetAttribute((const void*)kb_mlp, hipFuncAttributeMaxDynamicSharedMemorySize, KB_LDS);
  k0_prep<<<256, 256, K0_LDS + 256, stream>>>(q, k, v, shk, ws);
  k1_conv<<<512, 256, K1_LDS, stream>>>(ssmk, dskip, wg, wu, wd, ws);
  kb_mlp<<<256, 256, KB_LDS, stream>>>(ws, (u16*)d_out);
}
```

```cpp
#include <hip/hip_runtime.h>
#include <stdint.h>
#include <utility>

typedef unsigned short u16;
typedef unsigned u32;
typedef __attribute__((ext_vector_type(2))) u32 u32x2;
typedef __attribute__((ext_vector_type(4))) u32 u32x4;
typedef __attribute__((ext_vector_type(2))) float f32x2;
typedef __attribute__((ext_vector_type(4))) float f32x4;
typedef __attribute__((ext_vector_type(2))) __bf16 bf16x2;
typedef __attribute__((ext_vector_type(8))) __bf16 bf16x8;
typedef __attribute__((ext_vector_type(4))) short s16x4;
#define LDSP __attribute__((address_space(3)))
#define DI __device__ __forceinline__

constexpr int S = 2048, D = 512, F = 1376;
constexpr int NU = F / 8;
constexpr int KS1 = D / 32;
constexpr int KS2 = F / 32;
constexpr float EPS = 1e-5f;

constexpr size_t WS_XR = 0;
constexpr size_t WS_QT = WS_XR + (size_t)D * S * 2;
constexpr size_t WS_HT = WS_QT + (size_t)D * S * 2;
constexpr size_t WS_W1 = WS_HT + (size_t)D * S * 2;
constexpr size_t WS_WD = WS_W1 + (size_t)NU * KS1 * 1024;
constexpr size_t WS_H = WS_WD + (size_t)8 * KS2 * 4 * 1024;
constexpr size_t WS_CS = WS_H + (size_t)(S / 16) * KS2 * 1024;
constexpr size_t WS_SA = WS_CS + (size_t)KS1 * NU * 16 * 4;
constexpr size_t WS_SB = WS_SA + (size_t)S * 4;
constexpr size_t WS_CNT = WS_SB + (size_t)S * 4;
constexpr size_t WS_FLG = WS_CNT + 16 * 128;
constexpr size_t WS_HNT = WS_FLG + 256 * 8;
constexpr size_t WS_EPO = WS_HNT + 256 * 8;

template <int... Is, class Fn> DI void static_for_(std::integer_sequence<int, Is...>, Fn&& f) { (f(std::integral_constant<int, Is>{}), ...); }
template <int N, class Fn> DI void static_for(Fn&& f) { static_for_(std::make_integer_sequence<int, N>{}, f); }

DI float blo(u32 u) { return __uint_as_float(u << 16); }
DI float bhi(u32 u) { return __uint_as_float(u & 0xffff0000u); }
DI u32 pk2(float a, float b) { f32x2 t = {a, b}; return __builtin_bit_cast(u32, __builtin_convertvector(t, bf16x2)); }
DI f32x4 mfma16(u32x4 a, u32x4 b, f32x4 c) {
  return __builtin_amdgcn_mfma_f32_16x16x32_bf16(__builtin_bit_cast(bf16x8, a), __builtin_bit_cast(bf16x8, b), c, 0, 0, 0);
}
DI u32x2 tr_read(u32 lds_byte) {
  return __builtin_bit_cast(u32x2, __builtin_amdgcn_ds_read_tr16_b64_v4i16((LDSP s16x4*)(size_t)lds_byte));
}
DI u32x4 tr_frag(u32 a0, u32 a1) { u32x2 x = tr_read(a0), y = tr_read(a1); return u32x4{x[0], x[1], y[0], y[1]}; }
DI u32x4 cat(u32x2 x, u32x2 y) { return __builtin_shufflevector(x, y, 0, 1, 2, 3); }
template <int CTRL> DI float dpp_add(float v) {
  return v + __builtin_bit_cast(float, __builtin_amdgcn_update_dpp(0, __builtin_bit_cast(int, v), CTRL, 0xf, 0xf, true));
}
DI float sum_rows(float v) {
  u32 u = __float_as_uint(v);
  auto a = __builtin_amdgcn_permlane16_swap(u, u, false, false);
  v = __uint_as_float(a[0]) + __uint_as_float(a[1]);
  u = __float_as_uint(v);
  auto b = __builtin_amdgcn_permlane32_swap(u, u, false, false);
  return __uint_as_float(b[0]) + __uint_as_float(b[1]);
}
DI float wave_sum(float v) {
  v = dpp_add<0xb1>(v);
  v = dpp_add<0x4e>(v);
  v = dpp_add<0x124>(v);
  v = dpp_add<0x128>(v);
  return sum_rows(v);
}
DI __amdgpu_buffer_rsrc_t ws_rsrc(char* ws) { return __builtin_amdgcn_make_buffer_rsrc(ws, 0, 0x04000000, 0x00020000); }
DI void st16_wt(__amdgpu_buffer_rsrc_t r, u32 off, u32x4 v) { __builtin_amdgcn_raw_buffer_store_b128(v, r, off, 0, 16); }
DI int slice_u0(int j) { return j < 12 ? 11 * j : 132 + 10 * (j - 12); }
DI int slice_nu(int j) { return j < 12 ? 11 : 10; }

template <bool FIRST = false> DI void mfma_vv(f32x4& acc, const u32x4& a, const u32x4& b) {
  if constexpr (FIRST) asm volatile("v_mfma_f32_16x16x32_bf16 %0, %1, %2, 0" : "=&v"(acc) : "v"(a), "v"(b));
  else asm volatile("v_mfma_f32_16x16x32_bf16 %0, %1, %2, %0" : "+v"(acc) : "v"(a), "v"(b));
}
template <bool FIRST = false> DI void mfma_av(f32x4& acc, const u32x4& a, const u32x4& b) {
  if constexpr (FIRST) asm volatile("v_mfma_f32_16x16x32_bf16 %0, %1, %2, 0" : "=&v"(acc) : "a"(a), "v"(b));
  else asm volatile("v_mfma_f32_16x16x32_bf16 %0, %1, %2, %0" : "+v"(acc) : "a"(a), "v"(b));
}
template <int OFF> DI void ds_b64(u32x2& d, u32 addr) { asm volatile("ds_read_b64 %0, %1 offset:%c2" : "=v"(d) : "v"(addr), "i"(OFF) : "memory"); }
template <int OFF> DI void ds_b128(u32x4& d, u32 addr) { asm volatile("ds_read_b128 %0, %1 offset:%c2" : "=v"(d) : "v"(addr), "i"(OFF) : "memory"); }
template <int OFF> DI void ds_tr(u32x2& d, u32 addr) { asm volatile("ds_read_b64_tr_b16 %0, %1 offset:%c2" : "=v"(d) : "v"(addr), "i"(OFF) : "memory"); }
template <int OFF> DI void gld_a(u32x4& d, u32 voff, const void* sbase) {
  asm volatile("global_load_dwordx4 %0, %1, %2 offset:%c3" : "=a"(d) : "v"(voff), "s"(sbase), "i"(OFF) : "memory");
}
template <int OFF> DI void gld_v(u32x4& d, u32 voff, const void* sbase) {
  asm volatile("global_load_dwordx4 %0, %1, %2 offset:%c3" : "=v"(d) : "v"(voff), "s"(sbase), "i"(OFF) : "memory");
}
template <int OFF> DI void gld2_v(u32x2& d, u32 voff, const void* sbase) {
  asm volatile("global_load_dwordx2 %0, %1, %2 offset:%c3" : "=v"(d) : "v"(voff), "s"(sbase), "i"(OFF) : "memory");
}
template <int LOFF> DI void glds16(u32 voff, const void* sbase, u32 ldsbase) {
  asm volatile("s_add_u32 m0, %2, %c3\n\ts_nop 0\n\tglobal_load_lds_dwordx4 %0, %1" ::"v"(voff), "s"(sbase), "s"(ldsbase), "i"(LOFF) : "memory", "scc");
}
template <int N> DI void wait_vm() { asm volatile("s_waitcnt vmcnt(%c0)" ::"i"(N) : "memory"); }
template <int N> DI void wait_lgkm() { asm volatile("s_waitcnt lgkmcnt(%c0)" ::"i"(N) : "memory"); }
template <int OFF> DI void gld_a_sc1(u32x4& d, u32 voff, const void* sbase) {
  asm volatile("global_load_dwordx4 %0, %1, %2 offset:%c3 sc1" : "=a"(d) : "v"(voff), "s"(sbase), "i"(OFF) : "memory");
}
DI void mfma_aa(f32x4& acc, const u32x4& a, const u32x4& b) {
  asm volatile("v_mfma_f32_16x16x32_bf16 %0, %1, %2, %0" : "+v"(acc) : "a"(a), "a"(b));
}
template <bool FIRST = false> DI void mfma_va(f32x4& acc, const u32x4& a, const u32x4& b) {
  if constexpr (FIRST) asm volatile("v_mfma_f32_16x16x32_bf16 %0, %1, %2, 0" : "=&v"(acc) : "v"(a), "a"(b));
  else asm volatile("v_mfma_f32_16x16x32_bf16 %0, %1, %2, %0" : "+v"(acc) : "v"(a), "a"(b));
}
DI void mfma_fence(f32x4& a, f32x4& b, f32x4& c, f32x4& d) { asm volatile("s_nop 7\n\ts_nop 1" : "+v"(a), "+v"(b), "+v"(c), "+v"(d)); }
DI void after_fence(f32x4& a, f32x4& b, f32x4& c, f32x4& d) { asm volatile("" : "+v"(a), "+v"(b), "+v"(c), "+v"(d)); }
DI void after_fence(f32x4& a, f32x4& b, f32x4& c) { asm volatile("" : "+v"(a), "+v"(b), "+v"(c)); }
template <int STAGE> DI void dpp_stage(float& v) {
  if constexpr (STAGE == 0) asm volatile("v_add_f32_dpp %0, %0, %0 quad_perm:[1,0,3,2] row_mask:0xf bank_mask:0xf" : "+v"(v));
  if constexpr (STAGE == 1) asm volatile("v_add_f32_dpp %0, %0, %0 quad_perm:[2,3,0,1] row_mask:0xf bank_mask:0xf" : "+v"(v));
  if constexpr (STAGE == 2) asm volatile("v_add_f32_dpp %0, %0, %0 row_ror:4 row_mask:0xf bank_mask:0xf" : "+v"(v));
  if constexpr (STAGE == 3) asm volatile("v_add_f32_dpp %0, %0, %0 row_ror:8 row_mask:0xf bank_mask:0xf" : "+v"(v));
}
DI void barrier() { asm volatile("s_barrier" ::: "memory"); }
DI void pin(u32x4& x) { asm volatile("" : "+v"(x)); }
DI void pin(u32x2& x) { asm volatile("" : "+v"(x)); }
DI void pin4(f32x4& x) { asm volatile("" : "+v"(x)); }
DI void pin_a(u32x4& x) { asm volatile("" : "+a"(x)); }

#define STAMP(kern, idx) do {} while (0)

template <class Gate>
DI void w_job(int b, const u16* __restrict__ wg, const u16* __restrict__ wu, const u16* __restrict__ wd, char* __restrict__ ws, char* smem, Gate&& gate) {
  const int t = threadIdx.x, lane = t & 63, w = t >> 6;
  char* tile = smem;
  char* tile2 = smem + 32 * 368;
  const u32 tb = (u32)(size_t)(LDSP char*)smem;
  const int i16 = lane & 15, qd = i16 >> 2, p = i16 & 3, g = lane >> 4;
  const int ks = b & 15, j = b >> 4;
  const int U0 = slice_u0(j), nu = slice_nu(j);
  u32x4 g4[2], u4[2], d4[2];
#pragma unroll
  for (int rep = 0; rep < 2; ++rep) {
    int idx = t + 256 * rep, r = idx / 11, u = idx - 11 * r;
    bool ok = r < 32 && u < nu;
    size_t off = ok ? (size_t)(32 * ks + r) * F + 8 * (U0 + u) : 0;
    g4[rep] = *(const u32x4*)(wg + off); u4[rep] = *(const u32x4*)(wu + off);
  }
  const int ks2 = b >> 2, dp = b & 3;
  if (b < KS2 * 4) {
#pragma unroll
    for (int rep = 0; rep < 2; ++rep) {
      int idx = t + 256 * rep, r = idx >> 4, c = idx & 15;
      d4[rep] = *(const u32x4*)(wd + (size_t)(32 * ks2 + r) * D + 128 * dp + 8 * c);
    }
  }
  gate();
#pragma unroll
  for (int rep = 0; rep < 2; ++rep) {
    int idx = t + 256 * rep, r = idx / 11, u = idx - 11 * r;
    if (r < 32 && u < nu) {
      *(u32x4*)(tile + r * 368 + u * 32) = u32x4{g4[rep][0], u4[rep][0], g4[rep][1], u4[rep][1]};
      *(u32x4*)(tile + r * 368 + u * 32 + 16) = u32x4{g4[rep][2], u4[rep][2], g4[rep][3], u4[rep][3]};
    }
  }
  if (b < KS2 * 4) {
#pragma unroll
    for (int rep = 0; rep < 2; ++rep) { int idx = t + 256 * rep, r = idx >> 4, c = idx & 15; *(u32x4*)(tile2 + r * 272 + c * 16) = d4[rep]; }
  }
  __syncthreads();
  const u32x4 ones = {0x3f803f80u, 0x3f803f80u, 0x3f803f80u, 0x3f803f80u};
  const __amdgpu_buffer_rsrc_t wr = ws_rsrc(ws);
  for (int u = w; u < nu; u += 4) {
    u32 a = tb + (8 * g + qd) * 368 + u * 32 + 8 * p;
    u32x4 fr = tr_frag(a, a + 4 * 368);
    st16_wt(wr, (u32)WS_W1 + (((U0 + u) * KS1 + ks) * 64 + lane) * 16, fr);
    f32x4 cs = mfma16(fr, ones, f32x4{0.f, 0.f, 0.f, 0.f});
    if (i16 == 0) st16_wt(wr, (u32)WS_CS + ((ks * NU + U0 + u) * 16 + 4 * g) * 4, __builtin_bit_cast(u32x4, cs));
  }
  if (b < KS2 * 4) {
#pragma unroll
    for (int rep = 0; rep < 2; ++rep) {
      int fi = w + 4 * rep, dtl = fi >> 2, df = fi & 3;
      u32 a = tb + 32 * 368 + (8 * g + qd) * 272 + (64 * dtl + 32 * (df >> 1) + 8 * p + 4 * (df & 1)) * 2;
      u32x4 fr = tr_frag(a, a + 4 * 272);
      st16_wt(wr, (u32)WS_WD + ((((2 * dp + dtl) * KS2 + ks2) * 4 + df) * 64 + lane) * 16, fr);
    }
  }
}

constexpr int KA_CS = 4160;
constexpr int KA_KC = 4 * KA_CS;
constexpr int KA_XR = 2 * KA_KC;
constexpr int KA_RED = KA_XR + 2 * 4608;
constexpr int KA_LDS = KA_RED + 4 * 4096;
constexpr int KA_TK = KA_XR, KA_TV = KA_TK + 72 * 128, KA_TQ = KA_TV + 64 * 128;
static_assert(KA_TQ + 64 * 128 == KA_LDS, "tile overlay");
constexpr int SPIN_MAX = 1 << 22;
#define HELP_SLEEP 40
#define HINT_TICKS 800ull

template <int OFF> DI void gld_v_sc1(u32x4& d, u32 voff, const void* sbase) {
  asm volatile("global_load_dwordx4 %0, %1, %2 offset:%c3 sc1" : "=v"(d) : "v"(voff), "s"(sbase), "i"(OFF) : "memory");
}
template <int OFF> DI void gld2_v_sc1(u32x2& d, u32 voff, const void* sbase) {
  asm volatile("global_load_dwordx2 %0, %1, %2 offset:%c3 sc1" : "=v"(d) : "v"(voff), "s"(sbase), "i"(OFF) : "memory");
}
DI bool tags_ok(const u32x4& f, const u32x2& tag) { return f[0] == tag[0] && f[1] == tag[1] && f[2] == tag[0] && f[3] == tag[1]; }

__global__ __launch_bounds__(256, 2) void ka_conv(const u16* __restrict__ q, const u16* __restrict__ k, const u16* __restrict__ v,
                                                  const u16* __restrict__ shk, const u16* __restrict__ ssmk, const u16* __restrict__ dskip,
                                                  const u16* __restrict__ wg, const u16* __restrict__ wu, const u16* __restrict__ wd,
                                                  char* __restrict__ ws) {
  extern __shared__ __attribute__((aligned(16))) char smem[];
  const int t = threadIdx.x, lane = t & 63;
  const int w = __builtin_amdgcn_readfirstlane(t >> 6);
  const int b = blockIdx.x;
  char* ws_stamp = ws; (void)ws_stamp;
  STAMP(0, 0);
  if (b >= 256) {
    __builtin_amdgcn_s_sleep(HELP_SLEEP);
    if (b < 264 && w == 1) {
      const char* pc; u32 len;
      asm volatile("s_getpc_b64 %0\n1:\n\ts_mov_b32 %1, kb_code_end-1b" : "=s"(pc), "=s"(len));
      const u32 lb = (u32)(size_t)(LDSP char*)smem + 32768;
      for (u32 o0 = 0; o0 < len; o0 += 64 * 128) {
        u32 o = o0 + lane * 128; o = o < len ? o : 0;
        asm volatile("s_mov_b32 m0, %2\n\ts_nop 0\n\tglobal_load_lds_dword %0, %1" ::"v"(o), "s"(pc), "s"(lb) : "memory");
      }
    }
    w_job(b - 256, wg, wu, wd, ws, smem, [&]() {
      if (w == 0) {
        const unsigned long long e = __hip_atomic_load((const unsigned long long*)(ws + WS_EPO), __ATOMIC_RELAXED, __HIP_MEMORY_SCOPE_AGENT);
        const unsigned long long* hp = (const unsigned long long*)(ws + WS_HNT) + (b - 256);
        const unsigned long long t0 = __builtin_amdgcn_s_memrealtime();
        while (__hip_atomic_load(hp, __ATOMIC_RELAXED, __HIP_MEMORY_SCOPE_AGENT) != e + 1 && __builtin_amdgcn_s_memrealtime() - t0 < HINT_TICKS)
          __builtin_amdgcn_s_sleep(8);
      }
      __builtin_amdgcn_s_barrier();
      STAMP(1, 5);
    });
    STAMP(1, 4);
    return;
  }
  const int x = b & 7, bi = b >> 3, s0 = 64 * bi;
  const int ch0 = 64 * x + 2 * bi;
  const u32 sb = (u32)(size_t)(LDSP char*)smem;
  const int n = lane & 15, g = lane >> 4, qd = n >> 2, p = n & 3;
  const int d = 64 * x + 16 * w + n;
  const int srow = 8 * b + 2 * w;
  const __amdgpu_buffer_rsrc_t wr = ws_rsrc(ws);

  u32x2 epo, sh; u32x4 qa, qb, kp[2], kc[2];
  gld2_v_sc1<0>(epo, 0, ws + WS_EPO);
  gld_v<0>(qa, lane * 16, (const char*)q + (size_t)srow * 1024);
  gld_v<1024>(qb, lane * 16, (const char*)q + (size_t)srow * 1024);
  gld2_v<0>(sh, d * 8, shk);
  {
    const int r8 = lane >> 3, c8 = lane & 7;
    auto voff = [&](int instr) { const int f = ((r8 >> 1) & 1) | ((instr & 1) << 1); return (u32)((((c8 >> 1) ^ f) * 2 + (c8 & 1)) * 16); };
    const size_t col = (size_t)x * 128;
    int row = s0 - 8 + 8 * w + r8; row = row < 0 ? 0 : row;
    glds16<0>((u32)row * 1024 + voff(w), (const char*)k + col, sb + KA_TK + w * 1024);
    glds16<0>((u32)(s0 - 8 + 8 * (w + 4) + r8) * 1024 + voff(w), (const char*)k + col, sb + KA_TK + (w + 4) * 1024);
#pragma unroll
    for (int i = 0; i < 2; ++i) {
      const int in = w + 4 * i;
      glds16<0>((u32)(s0 + 8 * in + r8) * 1024 + voff(in), (const char*)v + col, sb + KA_TV + in * 1024);
      glds16<0>((u32)(s0 + 8 * in + r8) * 1024 + voff(in), (const char*)q + col, sb + KA_TQ + in * 1024);
    }
    if (w == 0) glds16<0>((u32)(s0 + 56 + r8) * 1024 + voff(8), (const char*)k + col, sb + KA_TK + 8 * 1024);
  }
#pragma unroll
  for (int cc = 0; cc < 2; ++cc) {
    const char* kr = (const char*)ssmk + (size_t)(ch0 + cc) * 4096;
    gld_v<0>(kp[cc], (u32)(t > 0 ? t - 1 : 0) * 16, kr);
    gld_v<0>(kc[cc], (u32)t * 16, kr);
  }
  const float dsk = blo(dskip[ch0 + (w >> 1)]);
  if (b == 0 && t < 16) *(u32*)(ws + WS_CNT + t * 128) = 0u;

  if (w == 0) wait_vm<11>(); else wait_vm<10>();
  asm volatile("" : "+v"(qa), "+v"(qb), "+v"(sh), "+v"(epo));
  u32x2 tag;
  { const unsigned long long e = (((unsigned long long)epo[1] << 32) | epo[0]) + 1ull; tag = u32x2{(u32)e, (u32)(e >> 32)}; }
  {
    float sa = 0.f, sbb = 0.f;
#pragma unroll
    for (int e = 0; e < 4; ++e) { sa += blo(qa[e]) + bhi(qa[e]); sbb += blo(qb[e]) + bhi(qb[e]); }
    const float ma = wave_sum(sa) * (1.f / D), mb = wave_sum(sbb) * (1.f / D);
    float va = 0.f, vb = 0.f;
#pragma unroll
    for (int e = 0; e < 4; ++e) {
      float u0 = blo(qa[e]) - ma, u1 = bhi(qa[e]) - ma, u2 = blo(qb[e]) - mb, u3 = bhi(qb[e]) - mb;
      va += u0 * u0 + u1 * u1; vb += u2 * u2 + u3 * u3;
    }
    va = wave_sum(va) * (1.f / D); vb = wave_sum(vb) * (1.f / D);
    const float ra = __builtin_amdgcn_rsqf(va + EPS), rb = __builtin_amdgcn_rsqf(vb + EPS);
    const u32x2 val = lane == 0 ? u32x2{__float_as_uint(ra), __float_as_uint(rb)} : u32x2{__float_as_uint(-ma * ra), __float_as_uint(-mb * rb)};
    const u32 off = lane == 0 ? (u32)WS_SA + srow * 4 : lane == 1 ? (u32)WS_SB + srow * 4 : 0xfffffff0u;
    __builtin_amdgcn_raw_buffer_store_b64(val, wr, off, 0, 16);
  }
  wait_vm<5>();
  barrier();
  STAMP(0, 1);
  {
    auto taddr = [&](int base, int tr) { const int f = ((tr >> 1) & 1) | ((tr >> 2) & 2); return sb + base + tr * 128 + ((w ^ f) * 32) + 8 * p; };
    const float a0 = blo(sh[0]), a1 = bhi(sh[0]), a2 = blo(sh[1]), a3 = bhi(sh[1]);
#pragma unroll
    for (int e = 0; e < 2; ++e) {
      const int rg = g + 4 * e;
      u32x2 km = tr_read(taddr(KA_TK, 8 * rg + 4 + qd));
      const u32x2 k0 = tr_read(taddr(KA_TK, 8 * rg + 8 + qd)), k1 = tr_read(taddr(KA_TK, 8 * rg + 12 + qd));
      const u32x2 v0 = tr_read(taddr(KA_TV, 8 * rg + qd)), v1 = tr_read(taddr(KA_TV, 8 * rg + 4 + qd));
      const u32x2 q0 = tr_read(taddr(KA_TQ, 8 * rg + qd)), q1 = tr_read(taddr(KA_TQ, 8 * rg + 4 + qd));
      if (s0 + rg == 0) km = u32x2{0, 0};
      const float kf[11] = {bhi(km[0]), blo(km[1]), bhi(km[1]), blo(k0[0]), bhi(k0[0]), blo(k0[1]), bhi(k0[1]), blo(k1[0]), bhi(k1[0]), blo(k1[1]), bhi(k1[1])};
      const float vf[8] = {blo(v0[0]), bhi(v0[0]), blo(v0[1]), bhi(v0[1]), blo(v1[0]), bhi(v1[0]), blo(v1[1]), bhi(v1[1])};
      float xo[8];
#pragma unroll
      for (int j = 0; j < 8; ++j) xo[j] = (kf[j + 3] * a0 + kf[j + 2] * a1 + kf[j + 1] * a2 + kf[j] * a3) * vf[j];
      const int r0 = s0 + 8 * rg;
      st16_wt(wr, (u32)WS_XR + (d * S + (2040 - r0)) * 2, u32x4{pk2(xo[7], xo[6]), pk2(xo[5], xo[4]), pk2(xo[3], xo[2]), pk2(xo[1], xo[0])});
      st16_wt(wr, (u32)WS_QT + (d * S + r0) * 2, cat(q0, q1));
    }
  }
  wait_vm<5>();
  asm volatile("" : "+v"(kp[0]), "+v"(kc[0]), "+v"(kp[1]), "+v"(kc[1]));
#pragma unroll
  for (int cc = 0; cc < 2; ++cc) {
    const u32 m = t > 0 ? 0xffffffffu : 0u;
    u32 wv[6] = {kp[cc][0] & m, kp[cc][1] & m, kp[cc][2] & m, kp[cc][3] & m, kc[cc][0], kc[cc][1]};
    u32 o1[5];
#pragma unroll
    for (int i = 0; i < 5; ++i) o1[i] = __builtin_amdgcn_alignbit(wv[i + 1], wv[i], 16);
    char* base = smem + cc * KA_KC + (t + 1) * 16;
    *(u32x4*)(base + 0 * KA_CS) = u32x4{o1[0], o1[1], o1[2], o1[3]};
    *(u32x4*)(base + 1 * KA_CS) = u32x4{wv[1], wv[2], wv[3], wv[4]};
    *(u32x4*)(base + 2 * KA_CS) = u32x4{o1[1], o1[2], o1[3], o1[4]};
    *(u32x4*)(base + 3 * KA_CS) = u32x4{wv[2], wv[3], wv[4], wv[5]};
    if (t == 255) {
      const u32 z = 0;
      const u32 e0 = __builtin_amdgcn_alignbit(kc[cc][1], kc[cc][0], 16), e1 = __builtin_amdgcn_alignbit(kc[cc][2], kc[cc][1], 16);
      const u32 e2 = __builtin_amdgcn_alignbit(kc[cc][3], kc[cc][2], 16), e3 = __builtin_amdgcn_alignbit(z, kc[cc][3], 16);
      *(u32x4*)(base + 16 + 0 * KA_CS) = u32x4{e0, e1, e2, e3};
      *(u32x4*)(base + 16 + 1 * KA_CS) = u32x4{kc[cc][1], kc[cc][2], kc[cc][3], z};
      *(u32x4*)(base + 16 + 2 * KA_CS) = u32x4{e1, e2, e3, z};
      *(u32x4*)(base + 16 + 3 * KA_CS) = u32x4{kc[cc][2], kc[cc][3], z, z};
    }
  }
  if (t < 8) *(u32x4*)(smem + (t >> 2) * KA_KC + (t & 3) * KA_CS) = u32x4{0, 0, 0, 0};
  asm volatile("s_waitcnt vmcnt(0) lgkmcnt(0)\n\ts_barrier" ::: "memory");
  STAMP(0, 2);
  if (t == 0) __builtin_amdgcn_raw_buffer_store_b64(tag, wr, (u32)WS_FLG + (x * 32 + bi) * 8, 0, 16);
  if (t >= 64 && t < 64 + 60) { int i = t - 64; *(u32x4*)(smem + KA_XR + (i / 30) * 4608 + 4096 + (i % 30) * 16) = u32x4{0, 0, 0, 0}; }
  if (w == 0) {
    const unsigned long long* fp = (const unsigned long long*)(ws + WS_FLG) + x * 32 + (lane & 31);
    const unsigned long long want = ((unsigned long long)tag[1] << 32) | tag[0];
    for (int spins = 0; spins < SPIN_MAX; ++spins) {
      if (__all(__hip_atomic_load(fp, __ATOMIC_RELAXED, __HIP_MEMORY_SCOPE_AGENT) == want)) break;
      __builtin_amdgcn_s_sleep(1);
    }
  }
  asm volatile("s_waitcnt vmcnt(0)\n\ts_barrier" ::: "memory");
  STAMP(0, 3);

  const int c = w >> 1, half = w & 1;
  u32x2 qv[4]; u32x4 sa[4], sbv[4], fl[2];
  const u32 eo = (u32)(1024 * half + 16 * n + 4 * g);
  {
    u32x4 x0, x1;
    gld_v_sc1<0>(x0, t * 16, ws + WS_XR + (size_t)ch0 * 4096);
    gld_v_sc1<0>(x1, t * 16, ws + WS_XR + (size_t)(ch0 + 1) * 4096);
    const char* qTc = ws + WS_QT + (size_t)(ch0 + c) * 4096;
    gld2_v_sc1<0>(qv[0], eo * 2, qTc); gld2_v_sc1<512>(qv[1], eo * 2, qTc); gld2_v_sc1<1024>(qv[2], eo * 2, qTc); gld2_v_sc1<1536>(qv[3], eo * 2, qTc);
    wait_vm<4>();
    asm volatile("" : "+v"(x0), "+v"(x1));
    *(u32x4*)(smem + KA_XR + t * 16) = x0;
    *(u32x4*)(smem + KA_XR + 4608 + t * 16) = x1;
  }
  asm volatile("s_waitcnt lgkmcnt(0)\n\ts_barrier" ::: "memory");
  if (t == 0) __builtin_amdgcn_raw_buffer_store_b64(tag, wr, (u32)WS_HNT + b * 8, 0, 16);
  STAMP(1, 1);

  const u32 aa = sb + c * KA_KC + (n & 3) * KA_CS + 16 * g + 2 * (n & 12) + half * 256;
  const u32 ba = sb + KA_XR + c * 4608 + 480 - 32 * n + 16 * g + half * 256;
  f32x4 acc[8];
  u32x2 Alo[2][8], Ahi[2][8];
  u32x4 B[2][8];
  auto rd = [&](auto C0, auto R) {
    constexpr int c0 = C0, r = R, buf = c0 & 1;
    if constexpr (r >= 2 && r < 10) {
      constexpr int u = r - 2;
      ds_b128<512 * (7 - u) + 64 * c0>(B[buf][u], ba);
    } else {
      constexpr int ar = r < 2 ? r : r - 8, i = ar >> 1;
      if constexpr ((ar & 1) == 0) ds_b64<512 * i + 64 * c0>(Alo[buf][i], aa);
      else ds_b64<512 * i + 64 * c0 + 8>(Ahi[buf][i], aa);
    }
  };
  auto ldS = [&](auto E) {
    constexpr int e = E, i = e >> 1;
    if constexpr ((e & 1) == 0) gld_v_sc1<i * 1024>(sa[i], eo * 4, ws + WS_SA);
    else gld_v_sc1<i * 1024>(sbv[i], eo * 4, ws + WS_SB);
  };
  static_for<24>([&](auto R) { rd(std::integral_constant<int, 0>{}, R); });
  static_for<4>([&](auto C0) {
    constexpr int c0 = C0, buf = c0 & 1;
    if constexpr (c0 == 2) {
      wait_vm<0>();
      asm volatile("" : "+v"(fl[0]), "+v"(fl[1]));
      if (!__all(tags_ok(fl[0], tag) && tags_ok(fl[1], tag))) {
        for (int spins = 0; spins < SPIN_MAX; ++spins) {
          gld_v_sc1<0>(fl[0], lane * 32, ws + WS_FLG); gld_v_sc1<16>(fl[1], lane * 32, ws + WS_FLG);
          wait_vm<0>();
          asm volatile("" : "+v"(fl[0]), "+v"(fl[1]));
          if (__all(tags_ok(fl[0], tag) && tags_ok(fl[1], tag))) break;
          __builtin_amdgcn_s_sleep(1);
        }
      }
      if (b == 0 && t == 0) __builtin_amdgcn_raw_buffer_store_b64(tag, wr, (u32)WS_EPO, 0, 16);
    }
    u32x4 A[8];
    static_for<36>([&](auto M) {
      constexpr int m = M;
      constexpr int c1 = m < 8 ? 0 : m < 15 ? 1 : m < 21 ? 2 : m < 26 ? 3 : m < 30 ? 4 : m < 33 ? 5 : m < 35 ? 6 : 7;
      constexpr int first = c1 * 8 - c1 * (c1 - 1) / 2;
      constexpr int u = m - first;
      if constexpr (m == 0) {
        if constexpr (c0 == 0) wait_lgkm<14>(); else wait_lgkm<0>();
        pin(Alo[buf][0]); pin(Ahi[buf][0]);
#pragma unroll
        for (int i = 0; i < 8; ++i) pin(B[buf][i]);
        A[0] = cat(Alo[buf][0], Ahi[buf][0]);
      }
      if constexpr (m == 8) {
        if constexpr (c0 == 0) wait_lgkm<8>();
#pragma unroll
        for (int i = 1; i < 8; ++i) { pin(Alo[buf][i]); pin(Ahi[buf][i]); A[i] = cat(Alo[buf][i], Ahi[buf][i]); }
      }
      mfma_vv<(c0 == 0 && c1 == 0)>(acc[c1 + u], A[c1], B[buf][u]);
      if constexpr (c0 < 3 && m < 24) rd(std::integral_constant<int, c0 + 1>{}, M);
      if constexpr (c0 == 1 && m == 30) gld_v_sc1<0>(fl[0], lane * 32, ws + WS_FLG);
      if constexpr (c0 == 1 && m == 32) gld_v_sc1<16>(fl[1], lane * 32, ws + WS_FLG);
      if constexpr (c0 == 2 && m >= 2 && m < 34 && (m & 3) == 2) ldS(std::integral_constant<int, (m - 2) / 4>{});
    });
  });
  mfma_fence(acc[0], acc[1], acc[2], acc[3]);
  after_fence(acc[4], acc[5], acc[6], acc[7]);
  STAMP(1, 2);
  f32x4 fin[4];
  {
    f32x4* red = (f32x4*)(smem + KA_RED);
    if (half) {
#pragma unroll
      for (int i = 0; i < 4; ++i) { red[(w * 4 + i) * 64 + lane] = acc[i]; fin[i] = acc[4 + i]; }
      asm volatile("; half 1" ::: "memory");
    } else {
#pragma unroll
      for (int i = 0; i < 4; ++i) { red[(w * 4 + i) * 64 + lane] = acc[4 + i]; fin[i] = acc[i]; }
      asm volatile("; half 0" ::: "memory");
    }
    asm volatile("s_waitcnt lgkmcnt(0)\n\ts_barrier" ::: "memory");
#pragma unroll
    for (int i = 0; i < 4; ++i) fin[i] += red[((w ^ 1) * 4 + i) * 64 + lane];
  }
  wait_vm<0>();
#pragma unroll
  for (int i = 0; i < 4; ++i) asm volatile("" : "+v"(qv[i]), "+v"(sa[i]), "+v"(sbv[i]));
  const f32x2 dd = {dsk, dsk};
  u32x2 hv[4];
#pragma unroll
  for (int i = 0; i < 4; ++i) {
    const int s = 256 * (4 * half + i) + 16 * n + 4 * g;
    const u32x2 xq = *(LDSP const u32x2*)(size_t)(sb + KA_XR + c * 4608 + 2 * (2044 - s));
    const f32x2 x0 = {bhi(xq[1]), blo(xq[1])}, x1 = {bhi(xq[0]), blo(xq[0])};
    const f32x2 q0 = {blo(qv[i][0]), bhi(qv[i][0])}, q1 = {blo(qv[i][1]), bhi(qv[i][1])};
    const f32x2 z0 = x0 * dd + f32x2{fin[i][0], fin[i][1]}, z1 = x1 * dd + f32x2{fin[i][2], fin[i][3]};
    const f32x4 a4 = __builtin_bit_cast(f32x4, sa[i]), b4 = __builtin_bit_cast(f32x4, sbv[i]);
    const f32x2 t0 = q0 * f32x2{a4[0], a4[1]} + f32x2{b4[0], b4[1]};
    const f32x2 t1 = q1 * f32x2{a4[2], a4[3]} + f32x2{b4[2], b4[3]};
    const f32x2 h0 = t0 * z0 + q0, h1 = t1 * z1 + q1;
    hv[i] = u32x2{pk2(h0[0], h0[1]), pk2(h1[0], h1[1])};
  }
#pragma unroll
  for (int i = 0; i < 4; i += 2) {
    auto a = __builtin_amdgcn_permlane16_swap(hv[i][0], hv[i + 1][0], false, false);
    auto bb = __builtin_amdgcn_permlane16_swap(hv[i][1], hv[i + 1][1], false, false);
    const int s = 256 * (4 * half + i + (g & 1)) + 16 * n + 4 * (g & 2);
    st16_wt(wr, (u32)WS_HT + ((ch0 + c) * S + s) * 2, u32x4{a[0], bb[0], a[1], bb[1]});
  }
  STAMP(1, 3);
}

constexpr int KB_NL = 39;
constexpr int KB_ST = KB_NL * 4096;
constexpr int KB_LDS = KB_ST + 1024;
constexpr int KB_RING = 8;

constexpr int p2_upto(int ks) { return ks < 0 ? 2 : ks <= 3 ? 2 * ks + 4 : (ks + 7 > 15 ? 15 : ks + 7); }
constexpr int p2_extra(int ks) { return ks == 9 ? 3 : ks >= 10 ? 4 : 0; }
constexpr int p2_extra_first(int ks) { int c = 0; for (int s = 0; s < ks; ++s) c += p2_extra(s); return c; }
constexpr int P2_NEXTRA = 23;
constexpr int p2_extra_real(int ks) { int f = p2_extra_first(ks), n = p2_extra(ks); return f >= P2_NEXTRA ? 0 : (f + n > P2_NEXTRA ? P2_NEXTRA - f : n); }
constexpr int p2_issue_step(int t) { for (int s = -1; s < KS1; ++s) if (p2_upto(s) >= t) return s; return KS1; }
constexpr int p2_younger(int t, int ks, int ops) {
  int c = (p2_upto(ks - 1) - t) * ops;
  for (int s = p2_issue_step(t); s < ks; ++s) if (s >= 0) c += p2_extra_real(s);
  return c;
}

struct KbCtx {
  u32 sb, gv, lv, lb0, lwd, csv;
  const char *hb0, *hb1, *w0, *w1, *w2, *cs0, *cs1, *cs2, *wd, *wdr;
};

template <int GRAM, class Setup>
DI void kb_phase2(const KbCtx& c, f32x4 (&acc)[3][8], f32x4 (&rsum)[2], f32x4 (&csp)[3], u32x4 (&Wr)[4][4], u32 (&ra)[8], Setup&& setup, char* ws_stamp) {
  constexpr int OPS = GRAM ? 4 : 5;
  u32x4 W[KB_RING][3];
  u32x2 Bl[2][8], Bh[2][8];
  auto issue = [&](auto KS, auto O) {
    constexpr int ks = KS, o = O;
    if constexpr (o == 0) glds16<ks * 8192>(c.gv, c.hb0 + (size_t)ks * 131072, c.lb0);
    if constexpr (o == 1) gld_a<(ks & 3) * 1024>(W[ks % KB_RING][0], c.lv, c.w0 + (ks >> 2) * 4096);
    if constexpr (o == 2) gld_a<(ks & 3) * 1024>(W[ks % KB_RING][1], c.lv, c.w1 + (ks >> 2) * 4096);
    if constexpr (o == 3 && !GRAM) gld_a<(ks & 3) * 1024>(W[ks % KB_RING][2], c.lv, c.w2 + (ks >> 2) * 4096);
    if constexpr (o == 4) glds16<ks * 8192 + 1024>(c.gv, c.hb1 + (size_t)ks * 131072, c.lb0);
  };
  auto extra = [&](auto E) {
    constexpr int e = E;
    if constexpr (e < 7) glds16<(32 + e) * 4096>(c.lv, c.wd + (size_t)(32 + e) * 4096, c.lwd);
    else if constexpr (e < P2_NEXTRA) gld_a<((e - 7) & 3) * 1024>(Wr[(e - 7) >> 2][(e - 7) & 3], c.lv, c.wdr + ((e - 7) >> 2) * 4096);
  };
  auto rdB = [&](auto KS, auto R) {
    constexpr int ks = KS, r = R, i = r >> 1;
    if constexpr ((r & 1) == 0) ds_tr<(ks & 7) * 8192>(Bl[ks & 1][i], ra[i] + (ks >= 8 ? 65536 : 0));
    else ds_tr<(ks & 7) * 8192 + 1024>(Bh[ks & 1][i], ra[i] + (ks >= 8 ? 65536 : 0));
  };
  u32x4 c0, c1, c2;
  static_for<5>([&](auto O) { issue(std::integral_constant<int, 0>{}, O); });
  gld_v<0>(c0, c.csv, c.cs0); gld_v<0>(c1, c.csv, c.cs1); gld_v<0>(c2, c.csv, c.cs2);
  static_for<p2_upto(-1)>([&](auto KS) { static_for<5>([&](auto O) { issue(std::integral_constant<int, KS + 1>{}, O); }); });
  setup();
  u32x4 ones = {0x3f803f80u, 0x3f803f80u, 0x3f803f80u, 0x3f803f80u};
  pin(ones);
  wait_vm<p2_upto(-1) * OPS>();
  barrier();
  static_for<16>([&](auto R) { rdB(std::integral_constant<int, 0>{}, R); });
  asm volatile("" : "+v"(c0), "+v"(c1), "+v"(c2));
  float cv[12];
#pragma unroll
  for (int e = 0; e < 4; ++e) { cv[e] = __uint_as_float(c0[e]); cv[4 + e] = __uint_as_float(c1[e]); cv[8 + e] = __uint_as_float(c2[e]); }
  static_for<KS1>([&](auto KS) {
    constexpr int ks = KS, buf = ks & 1, slot = ks % KB_RING;
    constexpr int nmain = (p2_upto(ks) - p2_upto(ks - 1)) * 5, next = p2_extra_real(ks);
    wait_lgkm<0>();
    u32x4 B[8];
#pragma unroll
    for (int i = 0; i < 8; ++i) { pin(Bl[buf][i]); pin(Bh[buf][i]); }
#pragma unroll
    for (int i = 0; i < 8; ++i) B[i] = cat(Bl[buf][i], Bh[buf][i]);
    pin_a(W[slot][0]); pin_a(W[slot][1]);
    if constexpr (!GRAM) pin_a(W[slot][2]);
    static_for<26>([&](auto M) {
      constexpr int m = M, i = m / 3, u = m % 3;
      if constexpr (m >= 24) mfma_vv<ks == 0>(rsum[m - 24], ones, B[m - 24]);
      else if constexpr (u < 2 || !GRAM) mfma_av<ks == 0>(acc[u][i], W[slot][u], B[i]);
      else mfma_vv<ks == 0>(acc[2][i], B[i], B[i]);
      if constexpr (m == 1 && ks + 1 < KS1) { wait_vm<p2_younger(ks + 1, ks, OPS)>(); barrier(); }
      if constexpr (m >= 2 && m < 18 && ks + 1 < KS1) rdB(std::integral_constant<int, ks + 1>{}, std::integral_constant<int, m - 2>{});
      if constexpr (ks >= 9 && ks < 13 && m >= 2 && m < 14) dpp_stage<ks - 9>(cv[m - 2]);
      constexpr int v = m - (26 - nmain - next);
      if constexpr (v >= 0 && v < nmain) issue(std::integral_constant<int, p2_upto(ks - 1) + 1 + v / 5>{}, std::integral_constant<int, v % 5>{});
      if constexpr (v >= nmain) extra(std::integral_constant<int, p2_extra_first(ks) + v - nmain>{});
    });
  });
  mfma_fence(acc[0][0], acc[1][0], rsum[0], rsum[1]);
#pragma unroll
  for (int i = 0; i < 8; ++i) after_fence(acc[0][i], acc[1][i], acc[2][i], rsum[i & 1]);
#pragma unroll
  for (int u = 0; u < 3; ++u) csp[u] = f32x4{cv[4 * u], cv[4 * u + 1], cv[4 * u + 2], cv[4 * u + 3]};
}

__global__ __launch_bounds__(256) void kb_mlp(char* __restrict__ ws, u16* __restrict__ out) {
  extern __shared__ __attribute__((aligned(16))) char smem[];
  const int t = threadIdx.x, lane = t & 63;
  const int w = __builtin_amdgcn_readfirstlane(t >> 6);
  const int b = blockIdx.x, xcd = b & 7, bi = b >> 3;
#define KB_MAP 1
  const int rt = KB_MAP == 0 ? 2 * xcd + (bi & 1) : (xcd >> 1) * 4 + (bi & 3);
  const int j = KB_MAP == 0 ? bi >> 1 : (xcd & 1) * 8 + (bi >> 2);
  const int half = KB_MAP == 0 ? j & 1 : j >> 3, dt = KB_MAP == 0 ? j >> 1 : j & 7;
  const int U0 = slice_u0(j), nu = slice_nu(j);
  const int rem = nu - 8;
  const int nw = w < rem ? 3 : 2;
  const int uw = U0 + (w < rem ? 3 * w : 3 * rem + 2 * (w - rem));
  const int u2 = uw + (nw == 3 ? 2 : 0);
  const int n = lane & 15, g = lane >> 4;
  const int s0 = 128 * rt;
  char* ws_stamp = ws; (void)ws_stamp;
  STAMP(2, 0);
  auto sf = [&](int i) { return ((w + (i >> 1)) & 3) + 4 * (half ^ (i & 1)); };

  KbCtx c;
  c.sb = (u32)(size_t)(LDSP char*)smem;
  c.lv = lane * 16;
  c.gv = (u32)(g * 4096 + (((n >> 1) ^ (g | ((w & 1) << 2))) * 32 + (n & 1) * 16));
  c.hb0 = ws + WS_HT + (size_t)s0 * 2 + (size_t)(8 * w) * 4096;
  c.hb1 = c.hb0 + 4 * 4096;
  c.lb0 = c.sb + 8 * w * 256;
  c.w0 = ws + WS_W1 + (size_t)uw * KS1 * 1024;
  c.w1 = c.w0 + KS1 * 1024;
  c.w2 = ws + WS_W1 + (size_t)u2 * KS1 * 1024;
  c.cs0 = ws + WS_CS + (size_t)uw * 64;
  c.cs1 = c.cs0 + 64;
  c.cs2 = ws + WS_CS + (size_t)u2 * 64;
  c.csv = n * (NU * 64) + g * 16;
  c.wdr = ws + WS_WD + (size_t)dt * KS2 * 4096 + (size_t)KB_NL * 4096;
  c.wd = ws + WS_WD + (size_t)dt * KS2 * 4096 + (size_t)w * 1024;
  c.lwd = c.sb + 1024 * w;
  u32 ra[8], hoff[6];
  auto setup = [&]() {
    const int qd = n >> 2, p = n & 3;
    const int frow = qd | ((g & 1) << 2);
#pragma unroll
    for (int i = 0; i < 8; ++i) { ra[i] = c.sb + (8 * g + qd) * 256 + ((sf(i) ^ frow) * 32) + 8 * p; asm volatile("" : "+v"(ra[i])); }
#pragma unroll
    for (int tt = 0; tt < 6; ++tt) {
      const int q = 4 * tt + g, i = (q * 11) >> 5, u = q - 3 * i, U = uw + u;
      const u32 o = (u32)(((8 * rt + sf(i)) * KS2 + (U >> 2)) * 1024 + ((U & 3) * 16 + n) * 16);
      hoff[tt] = u < nw ? o : 0xfffffff0u;
      asm volatile("" : "+v"(hoff[tt]));
    }
  };
  f32x4 acc[3][8], cs[3], rsum[2];
  u32x4 Wr[4][4];
  if (w == 3) kb_phase2<1>(c, acc, rsum, cs, Wr, ra, setup, ws_stamp);
  else kb_phase2<0>(c, acc, rsum, cs, Wr, ra, setup, ws_stamp);
  STAMP(2, 2);

  u32x2 res[4];
  {
    const u32 rb = ra[0] + dt * 16384;
    ds_tr<0>(res[0], rb); ds_tr<1024>(res[1], rb); ds_tr<8192>(res[2], rb); ds_tr<9216>(res[3], rb);
  }
  float* stl = (float*)(smem + KB_ST);
  if (g == 0) {
#pragma unroll
    for (int i = 0; i < 2; ++i) stl[2 * (16 * sf(i) + n)] = rsum[i][0];
  }
  if (w == 3 && g == (n >> 2)) {
#pragma unroll
    for (int i = 0; i < 8; ++i) {
      f32x4 a = acc[2][i];
      float dg = (n & 2) ? ((n & 1) ? a[3] : a[2]) : ((n & 1) ? a[1] : a[0]);
      stl[2 * (16 * sf(i) + n) + 1] = dg;
    }
  }
  asm volatile("s_waitcnt lgkmcnt(0)\n\ts_barrier" : "+v"(res[0]), "+v"(res[1]), "+v"(res[2]), "+v"(res[3]) :: "memory");
  STAMP(2, 3);
  const __amdgpu_buffer_rsrc_t hrs = __builtin_amdgcn_make_buffer_rsrc(ws + WS_H, 0, (S / 16) * KS2 * 1024, 0x00020000);
  f32x2 sv[8];
#pragma unroll
  for (int i = 0; i < 8; ++i) sv[i] = *(const f32x2*)(stl + 2 * (16 * sf(i) + n));
  u32 hv[24];
  static_for<8>([&](auto I) {
    constexpr int i = I;
    auto fill = [&](auto E) { constexpr int wk = 4 * i + E; glds16<wk * 4096>(c.lv, c.wd + (size_t)wk * 4096, c.lwd); };
    fill(std::integral_constant<int, 3>{});
    const float mean = sv[i][0] * (1.f / D);
    const float var = fmaxf(sv[i][1] * (1.f / D) - mean * mean, 0.f) + EPS;
    const float rstd = __builtin_amdgcn_rsqf(var);
    const float rinv = var * rstd;
    const float ce = -1.4426950408889634f * rstd;
    const f32x2 nm = {-mean, -mean}, ce2 = {ce, ce}, ri2 = {rinv, rinv};
    static_for<3>([&](auto U_) {
      constexpr int u = U_;
      pin4(acc[u][i]);
      fill(U_);
      const f32x4 a = acc[u][i];
      const f32x2 tg = (f32x2{a[0], a[1]} + nm * f32x2{cs[u][0], cs[u][1]}) * ce2;
      const f32x2 tu = f32x2{a[2], a[3]} + nm * f32x2{cs[u][2], cs[u][3]};
      const f32x2 e = {__builtin_amdgcn_exp2f(tg[0]), __builtin_amdgcn_exp2f(tg[1])};
      const f32x2 den = e * ri2 + ri2;
      const f32x2 o = tu * f32x2{__builtin_amdgcn_rcpf(den[0]), __builtin_amdgcn_rcpf(den[1])};
      hv[3 * i + u] = pk2(o[0], o[1]);
    });
    static_for<6>([&](auto T) {
      constexpr int tt = T;
      if constexpr ((4 * tt + 3) / 3 == i) {
        auto s02 = __builtin_amdgcn_permlane32_swap(hv[4 * tt], hv[4 * tt + 2], false, false);
        auto s13 = __builtin_amdgcn_permlane32_swap(hv[4 * tt + 1], hv[4 * tt + 3], false, false);
        auto t01 = __builtin_amdgcn_permlane16_swap(s02[0], s13[0], false, false);
        auto t23 = __builtin_amdgcn_permlane16_swap(s02[1], s13[1], false, false);
        __builtin_amdgcn_raw_buffer_store_b128(u32x4{t01[0], t01[1], t23[0], t23[1]}, hrs, hoff[tt], 0, 16);
      }
    });
  });
  STAMP(2, 4);
  u32* cnt = (u32*)(ws + WS_CNT + rt * 128);
  asm volatile("s_waitcnt vmcnt(0)\n\ts_barrier" ::: "memory");
  STAMP(2, 5);
  if (w == 0) {
    if (lane == 0) __hip_atomic_fetch_add(cnt, 1u, __ATOMIC_RELAXED, __HIP_MEMORY_SCOPE_AGENT);
    for (int spins = 0; spins < (1 << 22); ++spins) {
      const u32 v = __builtin_amdgcn_readfirstlane(__hip_atomic_load(cnt, __ATOMIC_RELAXED, __HIP_MEMORY_SCOPE_AGENT));
      if (v >= 16u) break;
      __builtin_amdgcn_s_sleep(1);
    }
  }
  asm volatile("s_waitcnt vmcnt(0)\n\ts_barrier" ::: "memory");
  STAMP(2, 6);

  const char* hb = ws + WS_H + (size_t)(8 * rt + 4 * half + w) * KS2 * 1024;
  u32x4 Hf[KS2];
  constexpr int P3_AHEAD = 12;
  auto ldH = [&](auto KS) { constexpr int ks = KS; if constexpr (ks < KS2) gld_a_sc1<(ks & 3) * 1024>(Hf[ks], c.lv, hb + (ks >> 2) * 4096); };
  static_for<P3_AHEAD>([&](auto KS) { ldH(KS); });
  STAMP(3, 0);
  f32x4 o4[4];
  u32x4 Af[3][4];
  const u32 la0 = c.sb + lane * 16, la1 = la0 + 65536, la2 = la0 + 131072;
  auto rdA = [&](auto KS, auto DF) {
    constexpr int ks = KS, df = DF;
    if constexpr (ks < KB_NL) ds_b128<(ks & 15) * 4096 + df * 1024>(Af[ks % 3][df], ks < 16 ? la0 : ks < 32 ? la1 : la2);
  };
  static_for<4>([&](auto DF) { rdA(std::integral_constant<int, 0>{}, DF); });
  static_for<4>([&](auto DF) { rdA(std::integral_constant<int, 1>{}, DF); });
  static_for<KS2>([&](auto KS) {
    constexpr int ks = KS;
    wait_vm<(ks + P3_AHEAD <= KS2 ? P3_AHEAD - 1 : KS2 - 1 - ks)>();
    if constexpr (ks + 1 < KB_NL) wait_lgkm<4>(); else wait_lgkm<0>();
    pin_a(Hf[ks]);
    static_for<4>([&](auto DF) {
      constexpr int df = DF;
      if constexpr (ks < KB_NL) { pin(Af[ks % 3][df]); mfma_va<ks == 0>(o4[df], Af[ks % 3][df], Hf[ks]); }
      else { pin_a(Wr[ks - KB_NL][df]); mfma_aa(o4[df], Wr[ks - KB_NL][df], Hf[ks]); }
      rdA(std::integral_constant<int, ks + 2>{}, DF);
      if constexpr (df == 1 && ks + P3_AHEAD < KS2) ldH(std::integral_constant<int, ks + P3_AHEAD>{});
    });
  });
  mfma_fence(o4[0], o4[1], o4[2], o4[3]);
  u32x4 o0, o1;
#pragma unroll
  for (int df = 0; df < 2; ++df) {
    o0[2 * df] = pk2(o4[df][0] + blo(res[df][0]), o4[df][1] + bhi(res[df][0]));
    o0[2 * df + 1] = pk2(o4[df][2] + blo(res[df][1]), o4[df][3] + bhi(res[df][1]));
    o1[2 * df] = pk2(o4[2 + df][0] + blo(res[2 + df][0]), o4[2 + df][1] + bhi(res[2 + df][0]));
    o1[2 * df + 1] = pk2(o4[2 + df][2] + blo(res[2 + df][1]), o4[2 + df][3] + bhi(res[2 + df][1]));
  }
  const __amdgpu_buffer_rsrc_t ors = __builtin_amdgcn_make_buffer_rsrc(out, 0, S * D * 2, 0x00020000);
  const u32 ooff = (u32)((s0 + 64 * half + 16 * w + n) * D + 64 * dt + 8 * g) * 2;
  __builtin_amdgcn_raw_buffer_store_b128(o0, ors, ooff, 0, 16);
  __builtin_amdgcn_raw_buffer_store_b128(o1, ors, ooff + 64, 0, 16);
  asm volatile("kb_code_end:");
  STAMP(2, 7);
}

extern "C" void kernel_launch(void* const* d_in, const int* in_sizes, int n_in, void* d_out, int out_size, void* d_ws,
                              size_t ws_size, hipStream_t stream) {
  const u16 *q = (const u16*)d_in[0], *k = (const u16*)d_in[1], *v = (const u16*)d_in[2], *shk = (const u16*)d_in[3];
  const u16 *ssmk = (const u16*)d_in[4], *dskip = (const u16*)d_in[5];
  const u16 *wg = (const u16*)d_in[7], *wu = (const u16*)d_in[8], *wd = (const u16*)d_in[9];
  char* ws = (char*)d_ws;
  (void)hipFuncSetAttribute((const void*)ka_conv, hipFuncAttributeMaxDynamicSharedMemorySize, KA_LDS);
  (void)hipFuncSetAttribute((const void*)kb_mlp, hipFuncAttributeMaxDynamicSharedMemorySize, KB_LDS);
  ka_conv<<<512, 256, KA_LDS, stream>>>(q, k, v, shk, ssmk, dskip, wg, wu, wd, ws);
  kb_mlp<<<256, 256, KB_LDS, stream>>>(ws, (u16*)d_out);
}
```

```cpp
#include <hip/hip_runtime.h>
#include <stdint.h>
#include <utility>

typedef unsigned short u16;
typedef unsigned u32;
typedef __attribute__((ext_vector_type(2))) u32 u32x2;
typedef __attribute__((ext_vector_type(4))) u32 u32x4;
typedef __attribute__((ext_vector_type(2))) float f32x2;
typedef __attribute__((ext_vector_type(4))) float f32x4;
typedef __attribute__((ext_vector_type(2))) __bf16 bf16x2;
typedef __attribute__((ext_vector_type(8))) __bf16 bf16x8;
typedef __attribute__((ext_vector_type(4))) short s16x4;
#define LDSP __attribute__((address_space(3)))
#define DI __device__ __forceinline__

constexpr int S = 2048, D = 512, F = 1376;
constexpr int NU = F / 8;
constexpr int KS1 = D / 32;
constexpr int KS2 = F / 32;
constexpr float EPS = 1e-5f;

constexpr size_t WS_XR = 0;
constexpr size_t WS_QT = WS_XR + (size_t)D * S * 2;
constexpr size_t WS_HT = WS_QT + (size_t)D * S * 2;
constexpr size_t WS_W1 = WS_HT + (size_t)D * S * 2;
constexpr size_t WS_WD = WS_W1 + (size_t)NU * KS1 * 1024;
constexpr size_t WS_H = WS_WD + (size_t)8 * KS2 * 4 * 1024;
constexpr size_t WS_CS = WS_H + (size_t)(S / 16) * KS2 * 1024;
constexpr size_t WS_SA = WS_CS + (size_t)KS1 * NU * 16 * 4;
constexpr size_t WS_SB = WS_SA + (size_t)S * 4;
constexpr size_t WS_CNT = WS_SB + (size_t)S * 4;
constexpr size_t WS_FLG = WS_CNT + 16 * 128;
constexpr size_t WS_HNT = WS_FLG + 256 * 8;
constexpr size_t WS_EPO = WS_HNT + 256 * 8;

template <int... Is, class Fn> DI void static_for_(std::integer_sequence<int, Is...>, Fn&& f) { (f(std::integral_constant<int, Is>{}), ...); }
template <int N, class Fn> DI void static_for(Fn&& f) { static_for_(std::make_integer_sequence<int, N>{}, f); }

DI float blo(u32 u) { return __uint_as_float(u << 16); }
DI float bhi(u32 u) { return __uint_as_float(u & 0xffff0000u); }
DI u32 pk2(float a, float b) { f32x2 t = {a, b}; return __builtin_bit_cast(u32, __builtin_convertvector(t, bf16x2)); }
DI f32x4 mfma16(u32x4 a, u32x4 b, f32x4 c) {
  return __builtin_amdgcn_mfma_f32_16x16x32_bf16(__builtin_bit_cast(bf16x8, a), __builtin_bit_cast(bf16x8, b), c, 0, 0, 0);
}
DI u32x2 tr_read(u32 lds_byte) {
  return __builtin_bit_cast(u32x2, __builtin_amdgcn_ds_read_tr16_b64_v4i16((LDSP s16x4*)(size_t)lds_byte));
}
DI u32x4 tr_frag(u32 a0, u32 a1) { u32x2 x = tr_read(a0), y = tr_read(a1); return u32x4{x[0], x[1], y[0], y[1]}; }
DI u32x4 cat(u32x2 x, u32x2 y) { return __builtin_shufflevector(x, y, 0, 1, 2, 3); }
template <int CTRL> DI float dpp_add(float v) {
  return v + __builtin_bit_cast(float, __builtin_amdgcn_update_dpp(0, __builtin_bit_cast(int, v), CTRL, 0xf, 0xf, true));
}
DI float sum_rows(float v) {
  u32 u = __float_as_uint(v);
  auto a = __builtin_amdgcn_permlane16_swap(u, u, false, false);
  v = __uint_as_float(a[0]) + __uint_as_float(a[1]);
  u = __float_as_uint(v);
  auto b = __builtin_amdgcn_permlane32_swap(u, u, false, false);
  return __uint_as_float(b[0]) + __uint_as_float(b[1]);
}
DI float wave_sum(float v) {
  v = dpp_add<0xb1>(v);
  v = dpp_add<0x4e>(v);
  v = dpp_add<0x124>(v);
  v = dpp_add<0x128>(v);
  return sum_rows(v);
}
DI __amdgpu_buffer_rsrc_t ws_rsrc(char* ws) { return __builtin_amdgcn_make_buffer_rsrc(ws, 0, 0x04000000, 0x00020000); }
DI void st16_wt(__amdgpu_buffer_rsrc_t r, u32 off, u32x4 v) { __builtin_amdgcn_raw_buffer_store_b128(v, r, off, 0, 16); }
DI int slice_u0(int j) { return j < 12 ? 11 * j : 132 + 10 * (j - 12); }
DI int slice_nu(int j) { return j < 12 ? 11 : 10; }

template <bool FIRST = false> DI void mfma_vv(f32x4& acc, const u32x4& a, const u32x4& b) {
  if constexpr (FIRST) asm volatile("v_mfma_f32_16x16x32_bf16 %0, %1, %2, 0" : "=&v"(acc) : "v"(a), "v"(b));
  else asm volatile("v_mfma_f32_16x16x32_bf16 %0, %1, %2, %0" : "+v"(acc) : "v"(a), "v"(b));
}
template <bool FIRST = false> DI void mfma_av(f32x4& acc, const u32x4& a, const u32x4& b) {
  if constexpr (FIRST) asm volatile("v_mfma_f32_16x16x32_bf16 %0, %1, %2, 0" : "=&v"(acc) : "a"(a), "v"(b));
  else asm volatile("v_mfma_f32_16x16x32_bf16 %0, %1, %2, %0" : "+v"(acc) : "a"(a), "v"(b));
}
template <int OFF> DI void ds_b64(u32x2& d, u32 addr) { asm volatile("ds_read_b64 %0, %1 offset:%c2" : "=v"(d) : "v"(addr), "i"(OFF) : "memory"); }
template <int OFF> DI void ds_b128(u32x4& d, u32 addr) { asm volatile("ds_read_b128 %0, %1 offset:%c2" : "=v"(d) : "v"(addr), "i"(OFF) : "memory"); }
template <int OFF> DI void ds_tr(u32x2& d, u32 addr) { asm volatile("ds_read_b64_tr_b16 %0, %1 offset:%c2" : "=v"(d) : "v"(addr), "i"(OFF) : "memory"); }
template <int OFF> DI void gld_a(u32x4& d, u32 voff, const void* sbase) {
  asm volatile("global_load_dwordx4 %0, %1, %2 offset:%c3" : "=a"(d) : "v"(voff), "s"(sbase), "i"(OFF) : "memory");
}
template <int OFF> DI void gld_v(u32x4& d, u32 voff, const void* sbase) {
  asm volatile("global_load_dwordx4 %0, %1, %2 offset:%c3" : "=v"(d) : "v"(voff), "s"(sbase), "i"(OFF) : "memory");
}
#define NT_IN 0
#define NT_W 1
template <int OFF> DI void gld_v_in(u32x4& d, u32 voff, const void* sbase) {
  if constexpr (NT_IN) asm volatile("global_load_dwordx4 %0, %1, %2 offset:%c3 nt" : "=v"(d) : "v"(voff), "s"(sbase), "i"(OFF) : "memory");
  else asm volatile("global_load_dwordx4 %0, %1, %2 offset:%c3" : "=v"(d) : "v"(voff), "s"(sbase), "i"(OFF) : "memory");
}
template <int LOFF> DI void glds16_in(u32 voff, const void* sbase, u32 ldsbase) {
  if constexpr (NT_IN) asm volatile("s_add_u32 m0, %2, %c3\n\ts_nop 0\n\tglobal_load_lds_dwordx4 %0, %1 nt" ::"v"(voff), "s"(sbase), "s"(ldsbase), "i"(LOFF) : "memory", "scc");
  else asm volatile("s_add_u32 m0, %2, %c3\n\ts_nop 0\n\tglobal_load_lds_dwordx4 %0, %1" ::"v"(voff), "s"(sbase), "s"(ldsbase), "i"(LOFF) : "memory", "scc");
}
DI u32x4 ld_w(const u16* p) {
  if constexpr (NT_W) return __builtin_nontemporal_load((const u32x4*)p);
  else return *(const u32x4*)p;
}
template <int OFF> DI void gld2_v(u32x2& d, u32 voff, const void* sbase) {
  asm volatile("global_load_dwordx2 %0, %1, %2 offset:%c3" : "=v"(d) : "v"(voff), "s"(sbase), "i"(OFF) : "memory");
}
template <int LOFF> DI void glds16(u32 voff, const void* sbase, u32 ldsbase) {
  asm volatile("s_add_u32 m0, %2, %c3\n\ts_nop 0\n\tglobal_load_lds_dwordx4 %0, %1" ::"v"(voff), "s"(sbase), "s"(ldsbase), "i"(LOFF) : "memory", "scc");
}
template <int N> DI void wait_vm() { asm volatile("s_waitcnt vmcnt(%c0)" ::"i"(N) : "memory"); }
template <int N> DI void wait_lgkm() { asm volatile("s_waitcnt lgkmcnt(%c0)" ::"i"(N) : "memory"); }
template <int OFF> DI void gld_a_sc1(u32x4& d, u32 voff, const void* sbase) {
  asm volatile("global_load_dwordx4 %0, %1, %2 offset:%c3 sc1" : "=a"(d) : "v"(voff), "s"(sbase), "i"(OFF) : "memory");
}
DI void mfma_aa(f32x4& acc, const u32x4& a, const u32x4& b) {
  asm volatile("v_mfma_f32_16x16x32_bf16 %0, %1, %2, %0" : "+v"(acc) : "a"(a), "a"(b));
}
template <bool FIRST = false> DI void mfma_va(f32x4& acc, const u32x4& a, const u32x4& b) {
  if constexpr (FIRST) asm volatile("v_mfma_f32_16x16x32_bf16 %0, %1, %2, 0" : "=&v"(acc) : "v"(a), "a"(b));
  else asm volatile("v_mfma_f32_16x16x32_bf16 %0, %1, %2, %0" : "+v"(acc) : "v"(a), "a"(b));
}
DI void mfma_fence(f32x4& a, f32x4& b, f32x4& c, f32x4& d) { asm volatile("s_nop 7\n\ts_nop 1" : "+v"(a), "+v"(b), "+v"(c), "+v"(d)); }
DI void after_fence(f32x4& a, f32x4& b, f32x4& c, f32x4& d) { asm volatile("" : "+v"(a), "+v"(b), "+v"(c), "+v"(d)); }
DI void after_fence(f32x4& a, f32x4& b, f32x4& c) { asm volatile("" : "+v"(a), "+v"(b), "+v"(c)); }
template <int STAGE> DI void dpp_stage(float& v) {
  if constexpr (STAGE == 0) asm volatile("v_add_f32_dpp %0, %0, %0 quad_perm:[1,0,3,2] row_mask:0xf bank_mask:0xf" : "+v"(v));
  if constexpr (STAGE == 1) asm volatile("v_add_f32_dpp %0, %0, %0 quad_perm:[2,3,0,1] row_mask:0xf bank_mask:0xf" : "+v"(v));
  if constexpr (STAGE == 2) asm volatile("v_add_f32_dpp %0, %0, %0 row_ror:4 row_mask:0xf bank_mask:0xf" : "+v"(v));
  if constexpr (STAGE == 3) asm volatile("v_add_f32_dpp %0, %0, %0 row_ror:8 row_mask:0xf bank_mask:0xf" : "+v"(v));
}
DI void barrier() { asm volatile("s_barrier" ::: "memory"); }
DI void pin(u32x4& x) { asm volatile("" : "+v"(x)); }
DI void pin(u32x2& x) { asm volatile("" : "+v"(x)); }
DI void pin4(f32x4& x) { asm volatile("" : "+v"(x)); }
DI void pin_a(u32x4& x) { asm volatile("" : "+a"(x)); }

#define STAMP(kern, idx) do {} while (0)

template <class Gate>
DI void w_job(int b, const u16* __restrict__ wg, const u16* __restrict__ wu, const u16* __restrict__ wd, char* __restrict__ ws, char* smem, Gate&& gate) {
  const int t = threadIdx.x, lane = t & 63, w = t >> 6;
  char* tile = smem;
  char* tile2 = smem + 32 * 368;
  const u32 tb = (u32)(size_t)(LDSP char*)smem;
  const int i16 = lane & 15, qd = i16 >> 2, p = i16 & 3, g = lane >> 4;
  const int ks = b & 15, j = b >> 4;
  const int U0 = slice_u0(j), nu = slice_nu(j);
  u32x4 g4[2], u4[2], d4[2];
#pragma unroll
  for (int rep = 0; rep < 2; ++rep) {
    int idx = t + 256 * rep, r = idx / 11, u = idx - 11 * r;
    bool ok = r < 32 && u < nu;
    size_t off = ok ? (size_t)(32 * ks + r) * F + 8 * (U0 + u) : 0;
    g4[rep] = ld_w(wg + off); u4[rep] = ld_w(wu + off);
  }
  const int ks2 = b >> 2, dp = b & 3;
  if (b < KS2 * 4) {
#pragma unroll
    for (int rep = 0; rep < 2; ++rep) {
      int idx = t + 256 * rep, r = idx >> 4, c = idx & 15;
      d4[rep] = ld_w(wd + (size_t)(32 * ks2 + r) * D + 128 * dp + 8 * c);
    }
  }
  gate();
#pragma unroll
  for (int rep = 0; rep < 2; ++rep) {
    int idx = t + 256 * rep, r = idx / 11, u = idx - 11 * r;
    if (r < 32 && u < nu) {
      *(u32x4*)(tile + r * 368 + u * 32) = u32x4{g4[rep][0], u4[rep][0], g4[rep][1], u4[rep][1]};
      *(u32x4*)(tile + r * 368 + u * 32 + 16) = u32x4{g4[rep][2], u4[rep][2], g4[rep][3], u4[rep][3]};
    }
  }
  if (b < KS2 * 4) {
#pragma unroll
    for (int rep = 0; rep < 2; ++rep) { int idx = t + 256 * rep, r = idx >> 4, c = idx & 15; *(u32x4*)(tile2 + r * 272 + c * 16) = d4[rep]; }
  }
  __syncthreads();
  const u32x4 ones = {0x3f803f80u, 0x3f803f80u, 0x3f803f80u, 0x3f803f80u};
  const __amdgpu_buffer_rsrc_t wr = ws_rsrc(ws);
  for (int u = w; u < nu; u += 4) {
    u32 a = tb + (8 * g + qd) * 368 + u * 32 + 8 * p;
    u32x4 fr = tr_frag(a, a + 4 * 368);
    st16_wt(wr, (u32)WS_W1 + (((U0 + u) * KS1 + ks) * 64 + lane) * 16, fr);
    f32x4 cs = mfma16(fr, ones, f32x4{0.f, 0.f, 0.f, 0.f});
    if (i16 == 0) st16_wt(wr, (u32)WS_CS + ((ks * NU + U0 + u) * 16 + 4 * g) * 4, __builtin_bit_cast(u32x4, cs));
  }
  if (b < KS2 * 4) {
#pragma unroll
    for (int rep = 0; rep < 2; ++rep) {
      int fi = w + 4 * rep, dtl = fi >> 2, df = fi & 3;
      u32 a = tb + 32 * 368 + (8 * g + qd) * 272 + (64 * dtl + 32 * (df >> 1) + 8 * p + 4 * (df & 1)) * 2;
      u32x4 fr = tr_frag(a, a + 4 * 272);
      st16_wt(wr, (u32)WS_WD + ((((2 * dp + dtl) * KS2 + ks2) * 4 + df) * 64 + lane) * 16, fr);
    }
  }
}

constexpr int KA_CS = 4160;
constexpr int KA_KC = 4 * KA_CS;
constexpr int KA_XR = 2 * KA_KC;
constexpr int KA_RED = KA_XR + 2 * 4608;
constexpr int KA_LDS = KA_RED + 4 * 4096;
constexpr int KA_TK = KA_XR, KA_TV = KA_TK + 72 * 128, KA_TQ = KA_TV + 64 * 128;
static_assert(KA_TQ + 64 * 128 == KA_LDS, "tile overlay");
constexpr int SPIN_MAX = 1 << 22;
#define HELP_SLEEP 40
#define HINT_TICKS 800ull

template <int OFF> DI void gld_v_sc1(u32x4& d, u32 voff, const void* sbase) {
  asm volatile("global_load_dwordx4 %0, %1, %2 offset:%c3 sc1" : "=v"(d) : "v"(voff), "s"(sbase), "i"(OFF) : "memory");
}
template <int OFF> DI void gld2_v_sc1(u32x2& d, u32 voff, const void* sbase) {
  asm volatile("global_load_dwordx2 %0, %1, %2 offset:%c3 sc1" : "=v"(d) : "v"(voff), "s"(sbase), "i"(OFF) : "memory");
}
DI bool tags_ok(const u32x4& f, const u32x2& tag) { return f[0] == tag[0] && f[1] == tag[1] && f[2] == tag[0] && f[3] == tag[1]; }

__global__ __launch_bounds__(256, 2) void ka_conv(const u16* __restrict__ q, const u16* __restrict__ k, const u16* __restrict__ v,
                                                  const u16* __restrict__ shk, const u16* __restrict__ ssmk, const u16* __restrict__ dskip,
                                                  const u16* __restrict__ wg, const u16* __restrict__ wu, const u16* __restrict__ wd,
                                                  char* __restrict__ ws) {
  extern __shared__ __attribute__((aligned(16))) char smem[];
  const int t = threadIdx.x, lane = t & 63;
  const int w = __builtin_amdgcn_readfirstlane(t >> 6);
  const int b = blockIdx.x;
  char* ws_stamp = ws; (void)ws_stamp;
  STAMP(0, 0);
  if (b >= 256) {
    __builtin_amdgcn_s_sleep(HELP_SLEEP);
    if (b < 264 && w == 1) {
      const char* pc; u32 len;
      asm volatile("s_getpc_b64 %0\n1:\n\ts_mov_b32 %1, kb_code_end-1b" : "=s"(pc), "=s"(len));
      const u32 lb = (u32)(size_t)(LDSP char*)smem + 32768;
      for (u32 o0 = 0; o0 < len; o0 += 64 * 128) {
        u32 o = o0 + lane * 128; o = o < len ? o : 0;
        asm volatile("s_mov_b32 m0, %2\n\ts_nop 0\n\tglobal_load_lds_dword %0, %1" ::"v"(o), "s"(pc), "s"(lb) : "memory");
      }
    }
    w_job(b - 256, wg, wu, wd, ws, smem, [&]() {
      if (w == 0) {
        const unsigned long long e = __hip_atomic_load((const unsigned long long*)(ws + WS_EPO), __ATOMIC_RELAXED, __HIP_MEMORY_SCOPE_AGENT);
        const unsigned long long* hp = (const unsigned long long*)(ws + WS_HNT) + (b - 256);
        const unsigned long long t0 = __builtin_amdgcn_s_memrealtime();
        while (__hip_atomic_load(hp, __ATOMIC_RELAXED, __HIP_MEMORY_SCOPE_AGENT) != e + 1 && __builtin_amdgcn_s_memrealtime() - t0 < HINT_TICKS)
          __builtin_amdgcn_s_sleep(8);
      }
      __builtin_amdgcn_s_barrier();
      STAMP(1, 5);
    });
    STAMP(1, 4);
    return;
  }
  const int x = b & 7, bi = b >> 3, s0 = 64 * bi;
  const int ch0 = 64 * x + 2 * bi;
  const u32 sb = (u32)(size_t)(LDSP char*)smem;
  const int n = lane & 15, g = lane >> 4, qd = n >> 2, p = n & 3;
  const int d = 64 * x + 16 * w + n;
  const int srow = 8 * b + 2 * w;
  const __amdgpu_buffer_rsrc_t wr = ws_rsrc(ws);

  u32x2 epo, sh; u32x4 qa, qb, kp[2], kc[2];
  gld2_v_sc1<0>(epo, 0, ws + WS_EPO);
  gld_v_in<0>(qa, lane * 16, (const char*)q + (size_t)srow * 1024);
  gld_v_in<1024>(qb, lane * 16, (const char*)q + (size_t)srow * 1024);
  gld2_v<0>(sh, d * 8, shk);
  {
    const int r8 = lane >> 3, c8 = lane & 7;
    auto voff = [&](int instr) { const int f = ((r8 >> 1) & 1) | ((instr & 1) << 1); return (u32)((((c8 >> 1) ^ f) * 2 + (c8 & 1)) * 16); };
    const size_t col = (size_t)x * 128;
    int row = s0 - 8 + 8 * w + r8; row = row < 0 ? 0 : row;
    glds16_in<0>((u32)row * 1024 + voff(w), (const char*)k + col, sb + KA_TK + w * 1024);
    glds16_in<0>((u32)(s0 - 8 + 8 * (w + 4) + r8) * 1024 + voff(w), (const char*)k + col, sb + KA_TK + (w + 4) * 1024);
#pragma unroll
    for (int i = 0; i < 2; ++i) {
      const int in = w + 4 * i;
      glds16_in<0>((u32)(s0 + 8 * in + r8) * 1024 + voff(in), (const char*)v + col, sb + KA_TV + in * 1024);
      glds16_in<0>((u32)(s0 + 8 * in + r8) * 1024 + voff(in), (const char*)q + col, sb + KA_TQ + in * 1024);
    }
    if (w == 0) glds16_in<0>((u32)(s0 + 56 + r8) * 1024 + voff(8), (const char*)k + col, sb + KA_TK + 8 * 1024);
  }
#pragma unroll
  for (int cc = 0; cc < 2; ++cc) {
    const char* kr = (const char*)ssmk + (size_t)(ch0 + cc) * 4096;
    gld_v_in<0>(kp[cc], (u32)(t > 0 ? t - 1 : 0) * 16, kr);
    gld_v_in<0>(kc[cc], (u32)t * 16, kr);
  }
  const float dsk = blo(dskip[ch0 + (w >> 1)]);
  if (b == 0 && t < 16) *(u32*)(ws + WS_CNT + t * 128) = 0u;

  if (w == 0) wait_vm<11>(); else wait_vm<10>();
  asm volatile("" : "+v"(qa), "+v"(qb), "+v"(sh), "+v"(epo));
  u32x2 tag;
  { const unsigned long long e = (((unsigned long long)epo[1] << 32) | epo[0]) + 1ull; tag = u32x2{(u32)e, (u32)(e >> 32)}; }
  {
    float sa = 0.f, sbb = 0.f;
#pragma unroll
    for (int e = 0; e < 4; ++e) { sa += blo(qa[e]) + bhi(qa[e]); sbb += blo(qb[e]) + bhi(qb[e]); }
    const float ma = wave_sum(sa) * (1.f / D), mb = wave_sum(sbb) * (1.f / D);
    float va = 0.f, vb = 0.f;
#pragma unroll
    for (int e = 0; e < 4; ++e) {
      float u0 = blo(qa[e]) - ma, u1 = bhi(qa[e]) - ma, u2 = blo(qb[e]) - mb, u3 = bhi(qb[e]) - mb;
      va += u0 * u0 + u1 * u1; vb += u2 * u2 + u3 * u3;
    }
    va = wave_sum(va) * (1.f / D); vb = wave_sum(vb) * (1.f / D);
    const float ra = __builtin_amdgcn_rsqf(va + EPS), rb = __builtin_amdgcn_rsqf(vb + EPS);
    const u32x2 val = lane == 0 ? u32x2{__float_as_uint(ra), __float_as_uint(rb)} : u32x2{__float_as_uint(-ma * ra), __float_as_uint(-mb * rb)};
    const u32 off = lane == 0 ? (u32)WS_SA + srow * 4 : lane == 1 ? (u32)WS_SB + srow * 4 : 0xfffffff0u;
    __builtin_amdgcn_raw_buffer_store_b64(val, wr, off, 0, 16);
  }
  wait_vm<5>();
  barrier();
  STAMP(0, 1);
  {
    auto taddr = [&](int base, int tr) { const int f = ((tr >> 1) & 1) | ((tr >> 2) & 2); return sb + base + tr * 128 + ((w ^ f) * 32) + 8 * p; };
    const float a0 = blo(sh[0]), a1 = bhi(sh[0]), a2 = blo(sh[1]), a3 = bhi(sh[1]);
#pragma unroll
    for (int e = 0; e < 2; ++e) {
      const int rg = g + 4 * e;
      u32x2 km = tr_read(taddr(KA_TK, 8 * rg + 4 + qd));
      const u32x2 k0 = tr_read(taddr(KA_TK, 8 * rg + 8 + qd)), k1 = tr_read(taddr(KA_TK, 8 * rg + 12 + qd));
      const u32x2 v0 = tr_read(taddr(KA_TV, 8 * rg + qd)), v1 = tr_read(taddr(KA_TV, 8 * rg + 4 + qd));
      const u32x2 q0 = tr_read(taddr(KA_TQ, 8 * rg + qd)), q1 = tr_read(taddr(KA_TQ, 8 * rg + 4 + qd));
      if (s0 + rg == 0) km = u32x2{0, 0};
      const float kf[11] = {bhi(km[0]), blo(km[1]), bhi(km[1]), blo(k0[0]), bhi(k0[0]), blo(k0[1]), bhi(k0[1]), blo(k1[0]), bhi(k1[0]), blo(k1[1]), bhi(k1[1])};
      const float vf[8] = {blo(v0[0]), bhi(v0[0]), blo(v0[1]), bhi(v0[1]), blo(v1[0]), bhi(v1[0]), blo(v1[1]), bhi(v1[1])};
      float xo[8];
#pragma unroll
      for (int j = 0; j < 8; ++j) xo[j] = (kf[j + 3] * a0 + kf[j + 2] * a1 + kf[j + 1] * a2 + kf[j] * a3) * vf[j];
      const int r0 = s0 + 8 * rg;
      st16_wt(wr, (u32)WS_XR + (d * S + (2040 - r0)) * 2, u32x4{pk2(xo[7], xo[6]), pk2(xo[5], xo[4]), pk2(xo[3], xo[2]), pk2(xo[1], xo[0])});
      st16_wt(wr, (u32)WS_QT + (d * S + r0) * 2, cat(q0, q1));
    }
  }
  wait_vm<5>();
  asm volatile("" : "+v"(kp[0]), "+v"(kc[0]), "+v"(kp[1]), "+v"(kc[1]));
#pragma unroll
  for (int cc = 0; cc < 2; ++cc) {
    const u32 m = t > 0 ? 0xffffffffu : 0u;
    u32 wv[6] = {kp[cc][0] & m, kp[cc][1] & m, kp[cc][2] & m, kp[cc][3] & m, kc[cc][0], kc[cc][1]};
    u32 o1[5];
#pragma unroll
    for (int i = 0; i < 5; ++i) o1[i] = __builtin_amdgcn_alignbit(wv[i + 1], wv[i], 16);
    char* base = smem + cc * KA_KC + (t + 1) * 16;
    *(u32x4*)(base + 0 * KA_CS) = u32x4{o1[0], o1[1], o1[2], o1[3]};
    *(u32x4*)(base + 1 * KA_CS) = u32x4{wv[1], wv[2], wv[3], wv[4]};
    *(u32x4*)(base + 2 * KA_CS) = u32x4{o1[1], o1[2], o1[3], o1[4]};
    *(u32x4*)(base + 3 * KA_CS) = u32x4{wv[2], wv[3], wv[4], wv[5]};
    if (t == 255) {
      const u32 z = 0;
      const u32 e0 = __builtin_amdgcn_alignbit(kc[cc][1], kc[cc][0], 16), e1 = __builtin_amdgcn_alignbit(kc[cc][2], kc[cc][1], 16);
      const u32 e2 = __builtin_amdgcn_alignbit(kc[cc][3], kc[cc][2], 16), e3 = __builtin_amdgcn_alignbit(z, kc[cc][3], 16);
      *(u32x4*)(base + 16 + 0 * KA_CS) = u32x4{e0, e1, e2, e3};
      *(u32x4*)(base + 16 + 1 * KA_CS) = u32x4{kc[cc][1], kc[cc][2], kc[cc][3], z};
      *(u32x4*)(base + 16 + 2 * KA_CS) = u32x4{e1, e2, e3, z};
      *(u32x4*)(base + 16 + 3 * KA_CS) = u32x4{kc[cc][2], kc[cc][3], z, z};
    }
  }
  if (t < 8) *(u32x4*)(smem + (t >> 2) * KA_KC + (t & 3) * KA_CS) = u32x4{0, 0, 0, 0};
  asm volatile("s_waitcnt vmcnt(0) lgkmcnt(0)\n\ts_barrier" ::: "memory");
  STAMP(0, 2);
  if (t == 0) __builtin_amdgcn_raw_buffer_store_b64(tag, wr, (u32)WS_FLG + (x * 32 + bi) * 8, 0, 16);
  if (t >= 64 && t < 64 + 60) { int i = t - 64; *(u32x4*)(smem + KA_XR + (i / 30) * 4608 + 4096 + (i % 30) * 16) = u32x4{0, 0, 0, 0}; }
  if (w == 0) {
    const unsigned long long* fp = (const unsigned long long*)(ws + WS_FLG) + x * 32 + (lane & 31);
    const unsigned long long want = ((unsigned long long)tag[1] << 32) | tag[0];
    for (int spins = 0; spins < SPIN_MAX; ++spins) {
      if (__all(__hip_atomic_load(fp, __ATOMIC_RELAXED, __HIP_MEMORY_SCOPE_AGENT) == want)) break;
      __builtin_amdgcn_s_sleep(1);
    }
  }
  asm volatile("s_waitcnt vmcnt(0)\n\ts_barrier" ::: "memory");
  STAMP(0, 3);

  const int c = w >> 1, half = w & 1;
  u32x2 qv[4]; u32x4 sa[4], sbv[4], fl[2];
  const u32 eo = (u32)(1024 * half + 16 * n + 4 * g);
  {
    u32x4 x0, x1;
    gld_v_sc1<0>(x0, t * 16, ws + WS_XR + (size_t)ch0 * 4096);
    gld_v_sc1<0>(x1, t * 16, ws + WS_XR + (size_t)(ch0 + 1) * 4096);
    const char* qTc = ws + WS_QT + (size_t)(ch0 + c) * 4096;
    gld2_v_sc1<0>(qv[0], eo * 2, qTc); gld2_v_sc1<512>(qv[1], eo * 2, qTc); gld2_v_sc1<1024>(qv[2], eo * 2, qTc); gld2_v_sc1<1536>(qv[3], eo * 2, qTc);
    wait_vm<4>();
    asm volatile("" : "+v"(x0), "+v"(x1));
    *(u32x4*)(smem + KA_XR + t * 16) = x0;
    *(u32x4*)(smem + KA_XR + 4608 + t * 16) = x1;
  }
  asm volatile("s_waitcnt lgkmcnt(0)\n\ts_barrier" ::: "memory");
  if (t == 0) __builtin_amdgcn_raw_buffer_store_b64(tag, wr, (u32)WS_HNT + b * 8, 0, 16);
  STAMP(1, 1);

  const u32 aa = sb + c * KA_KC + (n & 3) * KA_CS + 16 * g + 2 * (n & 12) + half * 256;
  const u32 ba = sb + KA_XR + c * 4608 + 480 - 32 * n + 16 * g + half * 256;
  f32x4 acc[8];
  u32x2 Alo[2][8], Ahi[2][8];
  u32x4 B[2][8];
  auto rd = [&](auto C0, auto R) {
    constexpr int c0 = C0, r = R, buf = c0 & 1;
    if constexpr (r >= 2 && r < 10) {
      constexpr int u = r - 2;
      ds_b128<512 * (7 - u) + 64 * c0>(B[buf][u], ba);
    } else {
      constexpr int ar = r < 2 ? r : r - 8, i = ar >> 1;
      if constexpr ((ar & 1) == 0) ds_b64<512 * i + 64 * c0>(Alo[buf][i], aa);
      else ds_b64<512 * i + 64 * c0 + 8>(Ahi[buf][i], aa);
    }
  };
  auto ldS = [&](auto E) {
    constexpr int e = E, i = e >> 1;
    if constexpr ((e & 1) == 0) gld_v_sc1<i * 1024>(sa[i], eo * 4, ws + WS_SA);
    else gld_v_sc1<i * 1024>(sbv[i], eo * 4, ws + WS_SB);
  };
  static_for<24>([&](auto R) { rd(std::integral_constant<int, 0>{}, R); });
  static_for<4>([&](auto C0) {
    constexpr int c0 = C0, buf = c0 & 1;
    if constexpr (c0 == 2) {
      wait_vm<0>();
      asm volatile("" : "+v"(fl[0]), "+v"(fl[1]));
      if (!__all(tags_ok(fl[0], tag) && tags_ok(fl[1], tag))) {
        for (int spins = 0; spins < SPIN_MAX; ++spins) {
          gld_v_sc1<0>(fl[0], lane * 32, ws + WS_FLG); gld_v_sc1<16>(fl[1], lane * 32, ws + WS_FLG);
          wait_vm<0>();
          asm volatile("" : "+v"(fl[0]), "+v"(fl[1]));
          if (__all(tags_ok(fl[0], tag) && tags_ok(fl[1], tag))) break;
          __builtin_amdgcn_s_sleep(1);
        }
      }
      if (b == 0 && t == 0) __builtin_amdgcn_raw_buffer_store_b64(tag, wr, (u32)WS_EPO, 0, 16);
    }
    u32x4 A[8];
    static_for<36>([&](auto M) {
      constexpr int m = M;
      constexpr int c1 = m < 8 ? 0 : m < 15 ? 1 : m < 21 ? 2 : m < 26 ? 3 : m < 30 ? 4 : m < 33 ? 5 : m < 35 ? 6 : 7;
      constexpr int first = c1 * 8 - c1 * (c1 - 1) / 2;
      constexpr int u = m - first;
      if constexpr (m == 0) {
        if constexpr (c0 == 0) wait_lgkm<14>(); else wait_lgkm<0>();
        pin(Alo[buf][0]); pin(Ahi[buf][0]);
#pragma unroll
        for (int i = 0; i < 8; ++i) pin(B[buf][i]);
        A[0] = cat(Alo[buf][0], Ahi[buf][0]);
      }
      if constexpr (m == 8) {
        if constexpr (c0 == 0) wait_lgkm<8>();
#pragma unroll
        for (int i = 1; i < 8; ++i) { pin(Alo[buf][i]); pin(Ahi[buf][i]); A[i] = cat(Alo[buf][i], Ahi[buf][i]); }
      }
      mfma_vv<(c0 == 0 && c1 == 0)>(acc[c1 + u], A[c1], B[buf][u]);
      if constexpr (c0 < 3 && m < 24) rd(std::integral_constant<int, c0 + 1>{}, M);
      if constexpr (c0 == 1 && m == 30) gld_v_sc1<0>(fl[0], lane * 32, ws + WS_FLG);
      if constexpr (c0 == 1 && m == 32) gld_v_sc1<16>(fl[1], lane * 32, ws + WS_FLG);
      if constexpr (c0 == 2 && m >= 2 && m < 34 && (m & 3) == 2) ldS(std::integral_constant<int, (m - 2) / 4>{});
    });
  });
  mfma_fence(acc[0], acc[1], acc[2], acc[3]);
  after_fence(acc[4], acc[5], acc[6], acc[7]);
  STAMP(1, 2);
  f32x4 fin[4];
  {
    f32x4* red = (f32x4*)(smem + KA_RED);
    if (half) {
#pragma unroll
      for (int i = 0; i < 4; ++i) { red[(w * 4 + i) * 64 + lane] = acc[i]; fin[i] = acc[4 + i]; }
      asm volatile("; half 1" ::: "memory");
    } else {
#pragma unroll
      for (int i = 0; i < 4; ++i) { red[(w * 4 + i) * 64 + lane] = acc[4 + i]; fin[i] = acc[i]; }
      asm volatile("; half 0" ::: "memory");
    }
    asm volatile("s_waitcnt lgkmcnt(0)\n\ts_barrier" ::: "memory");
#pragma unroll
    for (int i = 0; i < 4; ++i) fin[i] += red[((w ^ 1) * 4 + i) * 64 + lane];
  }
  wait_vm<0>();
#pragma unroll
  for (int i = 0; i < 4; ++i) asm volatile("" : "+v"(qv[i]), "+v"(sa[i]), "+v"(sbv[i]));
  const f32x2 dd = {dsk, dsk};
  u32x2 hv[4];
#pragma unroll
  for (int i = 0; i < 4; ++i) {
    const int s = 256 * (4 * half + i) + 16 * n + 4 * g;
    const u32x2 xq = *(LDSP const u32x2*)(size_t)(sb + KA_XR + c * 4608 + 2 * (2044 - s));
    const f32x2 x0 = {bhi(xq[1]), blo(xq[1])}, x1 = {bhi(xq[0]), blo(xq[0])};
    const f32x2 q0 = {blo(qv[i][0]), bhi(qv[i][0])}, q1 = {blo(qv[i][1]), bhi(qv[i][1])};
    const f32x2 z0 = x0 * dd + f32x2{fin[i][0], fin[i][1]}, z1 = x1 * dd + f32x2{fin[i][2], fin[i][3]};
    const f32x4 a4 = __builtin_bit_cast(f32x4, sa[i]), b4 = __builtin_bit_cast(f32x4, sbv[i]);
    const f32x2 t0 = q0 * f32x2{a4[0], a4[1]} + f32x2{b4[0], b4[1]};
    const f32x2 t1 = q1 * f32x2{a4[2], a4[3]} + f32x2{b4[2], b4[3]};
    const f32x2 h0 = t0 * z0 + q0, h1 = t1 * z1 + q1;
    hv[i] = u32x2{pk2(h0[0], h0[1]), pk2(h1[0], h1[1])};
  }
#pragma unroll
  for (int i = 0; i < 4; i += 2) {
    auto a = __builtin_amdgcn_permlane16_swap(hv[i][0], hv[i + 1][0], false, false);
    auto bb = __builtin_amdgcn_permlane16_swap(hv[i][1], hv[i + 1][1], false, false);
    const int s = 256 * (4 * half + i + (g & 1)) + 16 * n + 4 * (g & 2);
    st16_wt(wr, (u32)WS_HT + ((ch0 + c) * S + s) * 2, u32x4{a[0], bb[0], a[1], bb[1]});
  }
  STAMP(1, 3);
}

constexpr int KB_NL = 39;
constexpr int KB_ST = KB_NL * 4096;
constexpr int KB_LDS = KB_ST + 1024;
constexpr int KB_RING = 8;

constexpr int p2_upto(int ks) { return ks < 0 ? 2 : ks <= 3 ? 2 * ks + 4 : (ks + 7 > 15 ? 15 : ks + 7); }
constexpr int p2_extra(int ks) { return ks == 9 ? 3 : ks >= 10 ? 4 : 0; }
constexpr int p2_extra_first(int ks) { int c = 0; for (int s = 0; s < ks; ++s) c += p2_extra(s); return c; }
constexpr int P2_NEXTRA = 23;
constexpr int p2_extra_real(int ks) { int f = p2_extra_first(ks), n = p2_extra(ks); return f >= P2_NEXTRA ? 0 : (f + n > P2_NEXTRA ? P2_NEXTRA - f : n); }
constexpr int p2_issue_step(int t) { for (int s = -1; s < KS1; ++s) if (p2_upto(s) >= t) return s; return KS1; }
constexpr int p2_younger(int t, int ks, int ops) {
  int c = (p2_upto(ks - 1) - t) * ops;
  for (int s = p2_issue_step(t); s < ks; ++s) if (s >= 0) c += p2_extra_real(s);
  return c;
}

struct KbCtx {
  u32 sb, gv, lv, lb0, lwd, csv;
  const char *hb0, *hb1, *w0, *w1, *w2, *cs0, *cs1, *cs2, *wd, *wdr;
};

template <int GRAM, class Setup>
DI void kb_phase2(const KbCtx& c, f32x4 (&acc)[3][8], f32x4 (&rsum)[2], f32x4 (&csp)[3], u32x4 (&Wr)[4][4], u32 (&ra)[8], Setup&& setup, char* ws_stamp) {
  constexpr int OPS = GRAM ? 4 : 5;
  u32x4 W[KB_RING][3];
  u32x2 Bl[2][8], Bh[2][8];
  auto issue = [&](auto KS, auto O) {
    constexpr int ks = KS, o = O;
    if constexpr (o == 0) glds16<ks * 8192>(c.gv, c.hb0 + (size_t)ks * 131072, c.lb0);
    if constexpr (o == 1) gld_a<(ks & 3) * 1024>(W[ks % KB_RING][0], c.lv, c.w0 + (ks >> 2) * 4096);
    if constexpr (o == 2) gld_a<(ks & 3) * 1024>(W[ks % KB_RING][1], c.lv, c.w1 + (ks >> 2) * 4096);
    if constexpr (o == 3 && !GRAM) gld_a<(ks & 3) * 1024>(W[ks % KB_RING][2], c.lv, c.w2 + (ks >> 2) * 4096);
    if constexpr (o == 4) glds16<ks * 8192 + 1024>(c.gv, c.hb1 + (size_t)ks * 131072, c.lb0);
  };
  auto extra = [&](auto E) {
    constexpr int e = E;
    if constexpr (e < 7) glds16<(32 + e) * 4096>(c.lv, c.wd + (size_t)(32 + e) * 4096, c.lwd);
    else if constexpr (e < P2_NEXTRA) gld_a<((e - 7) & 3) * 1024>(Wr[(e - 7) >> 2][(e - 7) & 3], c.lv, c.wdr + ((e - 7) >> 2) * 4096);
  };
  auto rdB = [&](auto KS, auto R) {
    constexpr int ks = KS, r = R, i = r >> 1;
    if constexpr ((r & 1) == 0) ds_tr<(ks & 7) * 8192>(Bl[ks & 1][i], ra[i] + (ks >= 8 ? 65536 : 0));
    else ds_tr<(ks & 7) * 8192 + 1024>(Bh[ks & 1][i], ra[i] + (ks >= 8 ? 65536 : 0));
  };
  u32x4 c0, c1, c2;
  static_for<5>([&](auto O) { issue(std::integral_constant<int, 0>{}, O); });
  gld_v<0>(c0, c.csv, c.cs0); gld_v<0>(c1, c.csv, c.cs1); gld_v<0>(c2, c.csv, c.cs2);
  static_for<p2_upto(-1)>([&](auto KS) { static_for<5>([&](auto O) { issue(std::integral_constant<int, KS + 1>{}, O); }); });
  setup();
  u32x4 ones = {0x3f803f80u, 0x3f803f80u, 0x3f803f80u, 0x3f803f80u};
  pin(ones);
  wait_vm<p2_upto(-1) * OPS>();
  barrier();
  static_for<16>([&](auto R) { rdB(std::integral_constant<int, 0>{}, R); });
  asm volatile("" : "+v"(c0), "+v"(c1), "+v"(c2));
  float cv[12];
#pragma unroll
  for (int e = 0; e < 4; ++e) { cv[e] = __uint_as_float(c0[e]); cv[4 + e] = __uint_as_float(c1[e]); cv[8 + e] = __uint_as_float(c2[e]); }
  static_for<KS1>([&](auto KS) {
    constexpr int ks = KS, buf = ks & 1, slot = ks % KB_RING;
    constexpr int nmain = (p2_upto(ks) - p2_upto(ks - 1)) * 5, next = p2_extra_real(ks);
    wait_lgkm<0>();
    u32x4 B[8];
#pragma unroll
    for (int i = 0; i < 8; ++i) { pin(Bl[buf][i]); pin(Bh[buf][i]); }
#pragma unroll
    for (int i = 0; i < 8; ++i) B[i] = cat(Bl[buf][i], Bh[buf][i]);
    pin_a(W[slot][0]); pin_a(W[slot][1]);
    if constexpr (!GRAM) pin_a(W[slot][2]);
    static_for<26>([&](auto M) {
      constexpr int m = M, i = m / 3, u = m % 3;
      if constexpr (m >= 24) mfma_vv<ks == 0>(rsum[m - 24], ones, B[m - 24]);
      else if constexpr (u < 2 || !GRAM) mfma_av<ks == 0>(acc[u][i], W[slot][u], B[i]);
      else mfma_vv<ks == 0>(acc[2][i], B[i], B[i]);
      if constexpr (m == 1 && ks + 1 < KS1) { wait_vm<p2_younger(ks + 1, ks, OPS)>(); barrier(); }
      if constexpr (m >= 2 && m < 18 && ks + 1 < KS1) rdB(std::integral_constant<int, ks + 1>{}, std::integral_constant<int, m - 2>{});
      if constexpr (ks >= 9 && ks < 13 && m >= 2 && m < 14) dpp_stage<ks - 9>(cv[m - 2]);
      constexpr int v = m - (26 - nmain - next);
      if constexpr (v >= 0 && v < nmain) issue(std::integral_constant<int, p2_upto(ks - 1) + 1 + v / 5>{}, std::integral_constant<int, v % 5>{});
      if constexpr (v >= nmain) extra(std::integral_constant<int, p2_extra_first(ks) + v - nmain>{});
    });
  });
  mfma_fence(acc[0][0], acc[1][0], rsum[0], rsum[1]);
#pragma unroll
  for (int i = 0; i < 8; ++i) after_fence(acc[0][i], acc[1][i], acc[2][i], rsum[i & 1]);
#pragma unroll
  for (int u = 0; u < 3; ++u) csp[u] = f32x4{cv[4 * u], cv[4 * u + 1], cv[4 * u + 2], cv[4 * u + 3]};
}

__global__ __launch_bounds__(256) void kb_mlp(char* __restrict__ ws, u16* __restrict__ out) {
  extern __shared__ __attribute__((aligned(16))) char smem[];
  const int t = threadIdx.x, lane = t & 63;
  const int w = __builtin_amdgcn_readfirstlane(t >> 6);
  const int b = blockIdx.x, xcd = b & 7, bi = b >> 3;
#define KB_MAP 1
  const int rt = KB_MAP == 0 ? 2 * xcd + (bi & 1) : (xcd >> 1) * 4 + (bi & 3);
  const int j = KB_MAP == 0 ? bi >> 1 : (xcd & 1) * 8 + (bi >> 2);
  const int half = KB_MAP == 0 ? j & 1 : j >> 3, dt = KB_MAP == 0 ? j >> 1 : j & 7;
  const int U0 = slice_u0(j), nu = slice_nu(j);
  const int rem = nu - 8;
  const int nw = w < rem ? 3 : 2;
  const int uw = U0 + (w < rem ? 3 * w : 3 * rem + 2 * (w - rem));
  const int u2 = uw + (nw == 3 ? 2 : 0);
  const int n = lane & 15, g = lane >> 4;
  const int s0 = 128 * rt;
  char* ws_stamp = ws; (void)ws_stamp;
  STAMP(2, 0);
  auto sf = [&](int i) { return ((w + (i >> 1)) & 3) + 4 * (half ^ (i & 1)); };

  KbCtx c;
  c.sb = (u32)(size_t)(LDSP char*)smem;
  c.lv = lane * 16;
  c.gv = (u32)(g * 4096 + (((n >> 1) ^ (g | ((w & 1) << 2))) * 32 + (n & 1) * 16));
  c.hb0 = ws + WS_HT + (size_t)s0 * 2 + (size_t)(8 * w) * 4096;
  c.hb1 = c.hb0 + 4 * 4096;
  c.lb0 = c.sb + 8 * w * 256;
  c.w0 = ws + WS_W1 + (size_t)uw * KS1 * 1024;
  c.w1 = c.w0 + KS1 * 1024;
  c.w2 = ws + WS_W1 + (size_t)u2 * KS1 * 1024;
  c.cs0 = ws + WS_CS + (size_t)uw * 64;
  c.cs1 = c.cs0 + 64;
  c.cs2 = ws + WS_CS + (size_t)u2 * 64;
  c.csv = n * (NU * 64) + g * 16;
  c.wdr = ws + WS_WD + (size_t)dt * KS2 * 4096 + (size_t)KB_NL * 4096;
  c.wd = ws + WS_WD + (size_t)dt * KS2 * 4096 + (size_t)w * 1024;
  c.lwd = c.sb + 1024 * w;
  u32 ra[8], hoff[6];
  auto setup = [&]() {
    const int qd = n >> 2, p = n & 3;
    const int frow = qd | ((g & 1) << 2);
#pragma unroll
    for (int i = 0; i < 8; ++i) { ra[i] = c.sb + (8 * g + qd) * 256 + ((sf(i) ^ frow) * 32) + 8 * p; asm volatile("" : "+v"(ra[i])); }
#pragma unroll
    for (int tt = 0; tt < 6; ++tt) {
      const int q = 4 * tt + g, i = (q * 11) >> 5, u = q - 3 * i, U = uw + u;
      const u32 o = (u32)(((8 * rt + sf(i)) * KS2 + (U >> 2)) * 1024 + ((U & 3) * 16 + n) * 16);
      hoff[tt] = u < nw ? o : 0xfffffff0u;
      asm volatile("" : "+v"(hoff[tt]));
    }
  };
  f32x4 acc[3][8], cs[3], rsum[2];
  u32x4 Wr[4][4];
  if (w == 3) kb_phase2<1>(c, acc, rsum, cs, Wr, ra, setup, ws_stamp);
  else kb_phase2<0>(c, acc, rsum, cs, Wr, ra, setup, ws_stamp);
  STAMP(2, 2);

  u32x2 res[4];
  {
    const u32 rb = ra[0] + dt * 16384;
    ds_tr<0>(res[0], rb); ds_tr<1024>(res[1], rb); ds_tr<8192>(res[2], rb); ds_tr<9216>(res[3], rb);
  }
  float* stl = (float*)(smem + KB_ST);
  if (g == 0) {
#pragma unroll
    for (int i = 0; i < 2; ++i) stl[2 * (16 * sf(i) + n)] = rsum[i][0];
  }
  if (w == 3 && g == (n >> 2)) {
#pragma unroll
    for (int i = 0; i < 8; ++i) {
      f32x4 a = acc[2][i];
      float dg = (n & 2) ? ((n & 1) ? a[3] : a[2]) : ((n & 1) ? a[1] : a[0]);
      stl[2 * (16 * sf(i) + n) + 1] = dg;
    }
  }
  asm volatile("s_waitcnt lgkmcnt(0)\n\ts_barrier" : "+v"(res[0]), "+v"(res[1]), "+v"(res[2]), "+v"(res[3]) :: "memory");
  STAMP(2, 3);
  const __amdgpu_buffer_rsrc_t hrs = __builtin_amdgcn_make_buffer_rsrc(ws + WS_H, 0, (S / 16) * KS2 * 1024, 0x00020000);
  f32x2 sv[8];
#pragma unroll
  for (int i = 0; i < 8; ++i) sv[i] = *(const f32x2*)(stl + 2 * (16 * sf(i) + n));
  u32 hv[24];
  static_for<8>([&](auto I) {
    constexpr int i = I;
    auto fill = [&](auto E) { constexpr int wk = 4 * i + E; glds16<wk * 4096>(c.lv, c.wd + (size_t)wk * 4096, c.lwd); };
    fill(std::integral_constant<int, 3>{});
    const float mean = sv[i][0] * (1.f / D);
    const float var = fmaxf(sv[i][1] * (1.f / D) - mean * mean, 0.f) + EPS;
    const float rstd = __builtin_amdgcn_rsqf(var);
    const float rinv = var * rstd;
    const float ce = -1.4426950408889634f * rstd;
    const f32x2 nm = {-mean, -mean}, ce2 = {ce, ce}, ri2 = {rinv, rinv};
    static_for<3>([&](auto U_) {
      constexpr int u = U_;
      pin4(acc[u][i]);
      fill(U_);
      const f32x4 a = acc[u][i];
      const f32x2 tg = (f32x2{a[0], a[1]} + nm * f32x2{cs[u][0], cs[u][1]}) * ce2;
      const f32x2 tu = f32x2{a[2], a[3]} + nm * f32x2{cs[u][2], cs[u][3]};
      const f32x2 e = {__builtin_amdgcn_exp2f(tg[0]), __builtin_amdgcn_exp2f(tg[1])};
      const f32x2 den = e * ri2 + ri2;
      const f32x2 o = tu * f32x2{__builtin_amdgcn_rcpf(den[0]), __builtin_amdgcn_rcpf(den[1])};
      hv[3 * i + u] = pk2(o[0], o[1]);
    });
    static_for<6>([&](auto T) {
      constexpr int tt = T;
      if constexpr ((4 * tt + 3) / 3 == i) {
        auto s02 = __builtin_amdgcn_permlane32_swap(hv[4 * tt], hv[4 * tt + 2], false, false);
        auto s13 = __builtin_amdgcn_permlane32_swap(hv[4 * tt + 1], hv[4 * tt + 3], false, false);
        auto t01 = __builtin_amdgcn_permlane16_swap(s02[0], s13[0], false, false);
        auto t23 = __builtin_amdgcn_permlane16_swap(s02[1], s13[1], false, false);
        __builtin_amdgcn_raw_buffer_store_b128(u32x4{t01[0], t01[1], t23[0], t23[1]}, hrs, hoff[tt], 0, 16);
      }
    });
  });
  STAMP(2, 4);
  u32* cnt = (u32*)(ws + WS_CNT + rt * 128);
  asm volatile("s_waitcnt vmcnt(0)\n\ts_barrier" ::: "memory");
  STAMP(2, 5);
  if (w == 0) {
    if (lane == 0) __hip_atomic_fetch_add(cnt, 1u, __ATOMIC_RELAXED, __HIP_MEMORY_SCOPE_AGENT);
    for (int spins = 0; spins < (1 << 22); ++spins) {
      const u32 v = __builtin_amdgcn_readfirstlane(__hip_atomic_load(cnt, __ATOMIC_RELAXED, __HIP_MEMORY_SCOPE_AGENT));
      if (v >= 16u) break;
      __builtin_amdgcn_s_sleep(1);
    }
  }
  asm volatile("s_waitcnt vmcnt(0)\n\ts_barrier" ::: "memory");
  STAMP(2, 6);

  const char* hb = ws + WS_H + (size_t)(8 * rt + 4 * half + w) * KS2 * 1024;
  u32x4 Hf[KS2];
  constexpr int P3_AHEAD = 12;
  auto ldH = [&](auto KS) { constexpr int ks = KS; if constexpr (ks < KS2) gld_a_sc1<(ks & 3) * 1024>(Hf[ks], c.lv, hb + (ks >> 2) * 4096); };
  static_for<P3_AHEAD>([&](auto KS) { ldH(KS); });
  STAMP(3, 0);
  f32x4 o4[4];
  u32x4 Af[3][4];
  const u32 la0 = c.sb + lane * 16, la1 = la0 + 65536, la2 = la0 + 131072;
  auto rdA = [&](auto KS, auto DF) {
    constexpr int ks = KS, df = DF;
    if constexpr (ks < KB_NL) ds_b128<(ks & 15) * 4096 + df * 1024>(Af[ks % 3][df], ks < 16 ? la0 : ks < 32 ? la1 : la2);
  };
  static_for<4>([&](auto DF) { rdA(std::integral_constant<int, 0>{}, DF); });
  static_for<4>([&](auto DF) { rdA(std::integral_constant<int, 1>{}, DF); });
  static_for<KS2>([&](auto KS) {
    constexpr int ks = KS;
    wait_vm<(ks + P3_AHEAD <= KS2 ? P3_AHEAD - 1 : KS2 - 1 - ks)>();
    if constexpr (ks + 1 < KB_NL) wait_lgkm<4>(); else wait_lgkm<0>();
    pin_a(Hf[ks]);
    static_for<4>([&](auto DF) {
      constexpr int df = DF;
      if constexpr (ks < KB_NL) { pin(Af[ks % 3][df]); mfma_va<ks == 0>(o4[df], Af[ks % 3][df], Hf[ks]); }
      else { pin_a(Wr[ks - KB_NL][df]); mfma_aa(o4[df], Wr[ks - KB_NL][df], Hf[ks]); }
      rdA(std::integral_constant<int, ks + 2>{}, DF);
      if constexpr (df == 1 && ks + P3_AHEAD < KS2) ldH(std::integral_constant<int, ks + P3_AHEAD>{});
    });
  });
  mfma_fence(o4[0], o4[1], o4[2], o4[3]);
  u32x4 o0, o1;
#pragma unroll
  for (int df = 0; df < 2; ++df) {
    o0[2 * df] = pk2(o4[df][0] + blo(res[df][0]), o4[df][1] + bhi(res[df][0]));
    o0[2 * df + 1] = pk2(o4[df][2] + blo(res[df][1]), o4[df][3] + bhi(res[df][1]));
    o1[2 * df] = pk2(o4[2 + df][0] + blo(res[2 + df][0]), o4[2 + df][1] + bhi(res[2 + df][0]));
    o1[2 * df + 1] = pk2(o4[2 + df][2] + blo(res[2 + df][1]), o4[2 + df][3] + bhi(res[2 + df][1]));
  }
  const __amdgpu_buffer_rsrc_t ors = __builtin_amdgcn_make_buffer_rsrc(out, 0, S * D * 2, 0x00020000);
  const u32 ooff = (u32)((s0 + 64 * half + 16 * w + n) * D + 64 * dt + 8 * g) * 2;
  __builtin_amdgcn_raw_buffer_store_b128(o0, ors, ooff, 0, 16);
  __builtin_amdgcn_raw_buffer_store_b128(o1, ors, ooff + 64, 0, 16);
  asm volatile("kb_code_end:");
  STAMP(2, 7);
}

extern "C" void kernel_launch(void* const* d_in, const int* in_sizes, int n_in, void* d_out, int out_size, void* d_ws,
                              size_t ws_size, hipStream_t stream) {
  const u16 *q = (const u16*)d_in[0], *k = (const u16*)d_in[1], *v = (const u16*)d_in[2], *shk = (const u16*)d_in[3];
  const u16 *ssmk = (const u16*)d_in[4], *dskip = (const u16*)d_in[5];
  const u16 *wg = (const u16*)d_in[7], *wu = (const u16*)d_in[8], *wd = (const u16*)d_in[9];
  char* ws = (char*)d_ws;
  (void)hipFuncSetAttribute((const void*)ka_conv, hipFuncAttributeMaxDynamicSharedMemorySize, KA_LDS);
  (void)hipFuncSetAttribute((const void*)kb_mlp, hipFuncAttributeMaxDynamicSharedMemorySize, KB_LDS);
  ka_conv<<<512, 256, KA_LDS, stream>>>(q, k, v, shk, ssmk, dskip, wg, wu, wd, ws);
  kb_mlp<<<256, 256, KB_LDS, stream>>>(ws, (u16*)d_out);
}
```

```cpp
#include <hip/hip_runtime.h>
#include <stdint.h>
#include <utility>

typedef unsigned short u16;
typedef unsigned u32;
typedef __attribute__((ext_vector_type(2))) u32 u32x2;
typedef __attribute__((ext_vector_type(4))) u32 u32x4;
typedef __attribute__((ext_vector_type(2))) float f32x2;
typedef __attribute__((ext_vector_type(4))) float f32x4;
typedef __attribute__((ext_vector_type(2))) __bf16 bf16x2;
typedef __attribute__((ext_vector_type(8))) __bf16 bf16x8;
typedef __attribute__((ext_vector_type(4))) short s16x4;
#define LDSP __attribute__((address_space(3)))
#define DI __device__ __forceinline__

constexpr int S = 2048, D = 512, F = 1376;
constexpr int NU = F / 8;
constexpr int KS1 = D / 32;
constexpr int KS2 = F / 32;
constexpr float EPS = 1e-5f;

constexpr size_t WS_XR = 0;
constexpr size_t WS_QT = WS_XR + (size_t)D * S * 2;
constexpr size_t WS_HT = WS_QT + (size_t)D * S * 2;
constexpr size_t WS_W1 = WS_HT + (size_t)D * S * 2;
constexpr size_t WS_WD = WS_W1 + (size_t)NU * KS1 * 1024;
constexpr size_t WS_H = WS_WD + (size_t)8 * KS2 * 4 * 1024;
constexpr size_t WS_CS = WS_H + (size_t)(S / 16) * KS2 * 1024;
constexpr size_t WS_SA = WS_CS + (size_t)KS1 * NU * 16 * 4;
constexpr size_t WS_SB = WS_SA + (size_t)S * 4;
constexpr size_t WS_CNT = WS_SB + (size_t)S * 4;
constexpr size_t WS_FLG = WS_CNT + 16 * 128;
constexpr size_t WS_HNT = WS_FLG + 256 * 8;
constexpr size_t WS_T0 = WS_HNT + 256 * 8 + 256;
constexpr size_t WS_EPO = WS_HNT + 256 * 8;

template <int... Is, class Fn> DI void static_for_(std::integer_sequence<int, Is...>, Fn&& f) { (f(std::integral_constant<int, Is>{}), ...); }
template <int N, class Fn> DI void static_for(Fn&& f) { static_for_(std::make_integer_sequence<int, N>{}, f); }

DI float blo(u32 u) { return __uint_as_float(u << 16); }
DI float bhi(u32 u) { return __uint_as_float(u & 0xffff0000u); }
DI u32 pk2(float a, float b) { f32x2 t = {a, b}; return __builtin_bit_cast(u32, __builtin_convertvector(t, bf16x2)); }
DI f32x4 mfma16(u32x4 a, u32x4 b, f32x4 c) {
  return __builtin_amdgcn_mfma_f32_16x16x32_bf16(__builtin_bit_cast(bf16x8, a), __builtin_bit_cast(bf16x8, b), c, 0, 0, 0);
}
DI u32x2 tr_read(u32 lds_byte) {
  return __builtin_bit_cast(u32x2, __builtin_amdgcn_ds_read_tr16_b64_v4i16((LDSP s16x4*)(size_t)lds_byte));
}
DI u32x4 tr_frag(u32 a0, u32 a1) { u32x2 x = tr_read(a0), y = tr_read(a1); return u32x4{x[0], x[1], y[0], y[1]}; }
DI u32x4 cat(u32x2 x, u32x2 y) { return __builtin_shufflevector(x, y, 0, 1, 2, 3); }
template <int CTRL> DI float dpp_add(float v) {
  return v + __builtin_bit_cast(float, __builtin_amdgcn_update_dpp(0, __builtin_bit_cast(int, v), CTRL, 0xf, 0xf, true));
}
DI float sum_rows(float v) {
  u32 u = __float_as_uint(v);
  auto a = __builtin_amdgcn_permlane16_swap(u, u, false, false);
  v = __uint_as_float(a[0]) + __uint_as_float(a[1]);
  u = __float_as_uint(v);
  auto b = __builtin_amdgcn_permlane32_swap(u, u, false, false);
  return __uint_as_float(b[0]) + __uint_as_float(b[1]);
}
DI float wave_sum(float v) {
  v = dpp_add<0xb1>(v);
  v = dpp_add<0x4e>(v);
  v = dpp_add<0x124>(v);
  v = dpp_add<0x128>(v);
  return sum_rows(v);
}
DI __amdgpu_buffer_rsrc_t ws_rsrc(char* ws) { return __builtin_amdgcn_make_buffer_rsrc(ws, 0, 0x04000000, 0x00020000); }
DI void st16_wt(__amdgpu_buffer_rsrc_t r, u32 off, u32x4 v) { __builtin_amdgcn_raw_buffer_store_b128(v, r, off, 0, 16); }
DI int slice_u0(int j) { return j < 12 ? 11 * j : 132 + 10 * (j - 12); }
DI int slice_nu(int j) { return j < 12 ? 11 : 10; }

template <bool FIRST = false> DI void mfma_vv(f32x4& acc, const u32x4& a, const u32x4& b) {
  if constexpr (FIRST) asm volatile("v_mfma_f32_16x16x32_bf16 %0, %1, %2, 0" : "=&v"(acc) : "v"(a), "v"(b));
  else asm volatile("v_mfma_f32_16x16x32_bf16 %0, %1, %2, %0" : "+v"(acc) : "v"(a), "v"(b));
}
template <bool FIRST = false> DI void mfma_av(f32x4& acc, const u32x4& a, const u32x4& b) {
  if constexpr (FIRST) asm volatile("v_mfma_f32_16x16x32_bf16 %0, %1, %2, 0" : "=&v"(acc) : "a"(a), "v"(b));
  else asm volatile("v_mfma_f32_16x16x32_bf16 %0, %1, %2, %0" : "+v"(acc) : "a"(a), "v"(b));
}
template <int OFF> DI void ds_b64(u32x2& d, u32 addr) { asm volatile("ds_read_b64 %0, %1 offset:%c2" : "=v"(d) : "v"(addr), "i"(OFF) : "memory"); }
template <int OFF> DI void ds_b128(u32x4& d, u32 addr) { asm volatile("ds_read_b128 %0, %1 offset:%c2" : "=v"(d) : "v"(addr), "i"(OFF) : "memory"); }
template <int OFF> DI void ds_tr(u32x2& d, u32 addr) { asm volatile("ds_read_b64_tr_b16 %0, %1 offset:%c2" : "=v"(d) : "v"(addr), "i"(OFF) : "memory"); }
template <int OFF> DI void gld_a(u32x4& d, u32 voff, const void* sbase) {
  asm volatile("global_load_dwordx4 %0, %1, %2 offset:%c3" : "=a"(d) : "v"(voff), "s"(sbase), "i"(OFF) : "memory");
}
template <int OFF> DI void gld_v(u32x4& d, u32 voff, const void* sbase) {
  asm volatile("global_load_dwordx4 %0, %1, %2 offset:%c3" : "=v"(d) : "v"(voff), "s"(sbase), "i"(OFF) : "memory");
}
template <int OFF> DI void gld2_v(u32x2& d, u32 voff, const void* sbase) {
  asm volatile("global_load_dwordx2 %0, %1, %2 offset:%c3" : "=v"(d) : "v"(voff), "s"(sbase), "i"(OFF) : "memory");
}
template <int LOFF> DI void glds16(u32 voff, const void* sbase, u32 ldsbase) {
  asm volatile("s_add_u32 m0, %2, %c3\n\ts_nop 0\n\tglobal_load_lds_dwordx4 %0, %1" ::"v"(voff), "s"(sbase), "s"(ldsbase), "i"(LOFF) : "memory", "scc");
}
template <int N> DI void wait_vm() { asm volatile("s_waitcnt vmcnt(%c0)" ::"i"(N) : "memory"); }
template <int N> DI void wait_lgkm() { asm volatile("s_waitcnt lgkmcnt(%c0)" ::"i"(N) : "memory"); }
template <int OFF> DI void gld_a_sc1(u32x4& d, u32 voff, const void* sbase) {
  asm volatile("global_load_dwordx4 %0, %1, %2 offset:%c3 sc1" : "=a"(d) : "v"(voff), "s"(sbase), "i"(OFF) : "memory");
}
DI void mfma_aa(f32x4& acc, const u32x4& a, const u32x4& b) {
  asm volatile("v_mfma_f32_16x16x32_bf16 %0, %1, %2, %0" : "+v"(acc) : "a"(a), "a"(b));
}
template <bool FIRST = false> DI void mfma_va(f32x4& acc, const u32x4& a, const u32x4& b) {
  if constexpr (FIRST) asm volatile("v_mfma_f32_16x16x32_bf16 %0, %1, %2, 0" : "=&v"(acc) : "v"(a), "a"(b));
  else asm volatile("v_mfma_f32_16x16x32_bf16 %0, %1, %2, %0" : "+v"(acc) : "v"(a), "a"(b));
}
DI void mfma_fence(f32x4& a, f32x4& b, f32x4& c, f32x4& d) { asm volatile("s_nop 7\n\ts_nop 1" : "+v"(a), "+v"(b), "+v"(c), "+v"(d)); }
DI void after_fence(f32x4& a, f32x4& b, f32x4& c, f32x4& d) { asm volatile("" : "+v"(a), "+v"(b), "+v"(c), "+v"(d)); }
DI void after_fence(f32x4& a, f32x4& b, f32x4& c) { asm volatile("" : "+v"(a), "+v"(b), "+v"(c)); }
template <int STAGE> DI void dpp_stage(float& v) {
  if constexpr (STAGE == 0) asm volatile("v_add_f32_dpp %0, %0, %0 quad_perm:[1,0,3,2] row_mask:0xf bank_mask:0xf" : "+v"(v));
  if constexpr (STAGE == 1) asm volatile("v_add_f32_dpp %0, %0, %0 quad_perm:[2,3,0,1] row_mask:0xf bank_mask:0xf" : "+v"(v));
  if constexpr (STAGE == 2) asm volatile("v_add_f32_dpp %0, %0, %0 row_ror:4 row_mask:0xf bank_mask:0xf" : "+v"(v));
  if constexpr (STAGE == 3) asm volatile("v_add_f32_dpp %0, %0, %0 row_ror:8 row_mask:0xf bank_mask:0xf" : "+v"(v));
}
DI void barrier() { asm volatile("s_barrier" ::: "memory"); }
DI void pin(u32x4& x) { asm volatile("" : "+v"(x)); }
DI void pin(u32x2& x) { asm volatile("" : "+v"(x)); }
DI void pin4(f32x4& x) { asm volatile("" : "+v"(x)); }
DI void pin_a(u32x4& x) { asm volatile("" : "+a"(x)); }

#define STAMP(kern, idx) do {} while (0)

template <class Gate>
DI void w_job(int b, const u16* __restrict__ wg, const u16* __restrict__ wu, const u16* __restrict__ wd, char* __restrict__ ws, char* smem, Gate&& gate) {
  const int t = threadIdx.x, lane = t & 63, w = t >> 6;
  char* tile = smem;
  char* tile2 = smem + 32 * 368;
  const u32 tb = (u32)(size_t)(LDSP char*)smem;
  const int i16 = lane & 15, qd = i16 >> 2, p = i16 & 3, g = lane >> 4;
  const int ks = b & 15, j = b >> 4;
  const int U0 = slice_u0(j), nu = slice_nu(j);
  u32x4 g4[2], u4[2], d4[2];
#pragma unroll
  for (int rep = 0; rep < 2; ++rep) {
    int idx = t + 256 * rep, r = idx / 11, u = idx - 11 * r;
    bool ok = r < 32 && u < nu;
    size_t off = ok ? (size_t)(32 * ks + r) * F + 8 * (U0 + u) : 0;
    g4[rep] = *(const u32x4*)(wg + off); u4[rep] = *(const u32x4*)(wu + off);
  }
  const int ks2 = b >> 2, dp = b & 3;
  if (b < KS2 * 4) {
#pragma unroll
    for (int rep = 0; rep < 2; ++rep) {
      int idx = t + 256 * rep, r = idx >> 4, c = idx & 15;
      d4[rep] = *(const u32x4*)(wd + (size_t)(32 * ks2 + r) * D + 128 * dp + 8 * c);
    }
  }
  gate();
#pragma unroll
  for (int rep = 0; rep < 2; ++rep) {
    int idx = t + 256 * rep, r = idx / 11, u = idx - 11 * r;
    if (r < 32 && u < nu) {
      *(u32x4*)(tile + r * 368 + u * 32) = u32x4{g4[rep][0], u4[rep][0], g4[rep][1], u4[rep][1]};
      *(u32x4*)(tile + r * 368 + u * 32 + 16) = u32x4{g4[rep][2], u4[rep][2], g4[rep][3], u4[rep][3]};
    }
  }
  if (b < KS2 * 4) {
#pragma unroll
    for (int rep = 0; rep < 2; ++rep) { int idx = t + 256 * rep, r = idx >> 4, c = idx & 15; *(u32x4*)(tile2 + r * 272 + c * 16) = d4[rep]; }
  }
  __syncthreads();
  const u32x4 ones = {0x3f803f80u, 0x3f803f80u, 0x3f803f80u, 0x3f803f80u};
  const __amdgpu_buffer_rsrc_t wr = ws_rsrc(ws);
  for (int u = w; u < nu; u += 4) {
    u32 a = tb + (8 * g + qd) * 368 + u * 32 + 8 * p;
    u32x4 fr = tr_frag(a, a + 4 * 368);
    st16_wt(wr, (u32)WS_W1 + (((U0 + u) * KS1 + ks) * 64 + lane) * 16, fr);
    f32x4 cs = mfma16(fr, ones, f32x4{0.f, 0.f, 0.f, 0.f});
    if (i16 == 0) st16_wt(wr, (u32)WS_CS + ((ks * NU + U0 + u) * 16 + 4 * g) * 4, __builtin_bit_cast(u32x4, cs));
  }
  if (b < KS2 * 4) {
#pragma unroll
    for (int rep = 0; rep < 2; ++rep) {
      int fi = w + 4 * rep, dtl = fi >> 2, df = fi & 3;
      u32 a = tb + 32 * 368 + (8 * g + qd) * 272 + (64 * dtl + 32 * (df >> 1) + 8 * p + 4 * (df & 1)) * 2;
      u32x4 fr = tr_frag(a, a + 4 * 272);
      st16_wt(wr, (u32)WS_WD + ((((2 * dp + dtl) * KS2 + ks2) * 4 + df) * 64 + lane) * 16, fr);
    }
  }
}

constexpr int KA_CS = 4160;
constexpr int KA_KC = 4 * KA_CS;
constexpr int KA_XR = 2 * KA_KC;
constexpr int KA_RED = KA_XR + 2 * 4608;
constexpr int KA_LDS = KA_RED + 4 * 4096;
constexpr int KA_TK = KA_XR, KA_TV = KA_TK + 72 * 128, KA_TQ = KA_TV + 64 * 128;
static_assert(KA_TQ + 64 * 128 == KA_LDS, "tile overlay");
constexpr int SPIN_MAX = 1 << 22;
#define HELP_SLEEP 40
#define HINT_TICKS 800ull

template <int OFF> DI void gld_v_sc1(u32x4& d, u32 voff, const void* sbase) {
  asm volatile("global_load_dwordx4 %0, %1, %2 offset:%c3 sc1" : "=v"(d) : "v"(voff), "s"(sbase), "i"(OFF) : "memory");
}
template <int OFF> DI void gld2_v_sc1(u32x2& d, u32 voff, const void* sbase) {
  asm volatile("global_load_dwordx2 %0, %1, %2 offset:%c3 sc1" : "=v"(d) : "v"(voff), "s"(sbase), "i"(OFF) : "memory");
}
DI bool tags_ok(const u32x4& f, const u32x2& tag) { return f[0] == tag[0] && f[1] == tag[1] && f[2] == tag[0] && f[3] == tag[1]; }

__global__ __launch_bounds__(256, 2) void ka_conv(const u16* __restrict__ q, const u16* __restrict__ k, const u16* __restrict__ v,
                                                  const u16* __restrict__ shk, const u16* __restrict__ ssmk, const u16* __restrict__ dskip,
                                                  const u16* __restrict__ wg, const u16* __restrict__ wu, const u16* __restrict__ wd,
                                                  char* __restrict__ ws) {
  extern __shared__ __attribute__((aligned(16))) char smem[];
  const int t = threadIdx.x, lane = t & 63;
  const int w = __builtin_amdgcn_readfirstlane(t >> 6);
  const int b = blockIdx.x;
  char* ws_stamp = ws; (void)ws_stamp;
  if (b == 0 && t == 0) __hip_atomic_store((unsigned long long*)(ws + WS_T0), __builtin_amdgcn_s_memrealtime(), __ATOMIC_RELAXED, __HIP_MEMORY_SCOPE_AGENT);
  STAMP(0, 0);
  if (b >= 256) {
    __builtin_amdgcn_s_sleep(HELP_SLEEP);
    if (b < 264 && w == 1) {
      const char* pc; u32 len;
      asm volatile("s_getpc_b64 %0\n1:\n\ts_mov_b32 %1, kb_code_end-1b" : "=s"(pc), "=s"(len));
      const u32 lb = (u32)(size_t)(LDSP char*)smem + 32768;
      for (u32 o0 = 0; o0 < len; o0 += 64 * 128) {
        u32 o = o0 + lane * 128; o = o < len ? o : 0;
        asm volatile("s_mov_b32 m0, %2\n\ts_nop 0\n\tglobal_load_lds_dword %0, %1" ::"v"(o), "s"(pc), "s"(lb) : "memory");
      }
    }
    w_job(b - 256, wg, wu, wd, ws, smem, [&]() {
      if (w == 0) {
        const unsigned long long e = __hip_atomic_load((const unsigned long long*)(ws + WS_EPO), __ATOMIC_RELAXED, __HIP_MEMORY_SCOPE_AGENT);
        const unsigned long long* hp = (const unsigned long long*)(ws + WS_HNT) + (b - 256);
        const unsigned long long t0 = __builtin_amdgcn_s_memrealtime();
        while (__hip_atomic_load(hp, __ATOMIC_RELAXED, __HIP_MEMORY_SCOPE_AGENT) != e + 1 && __builtin_amdgcn_s_memrealtime() - t0 < HINT_TICKS)
          __builtin_amdgcn_s_sleep(8);
      }
      __builtin_amdgcn_s_barrier();
      STAMP(1, 5);
    });
    STAMP(1, 4);
    return;
  }
  const int x = b & 7, bi = b >> 3, s0 = 64 * bi;
  const int ch0 = 64 * x + 2 * bi;
  const u32 sb = (u32)(size_t)(LDSP char*)smem;
  const int n = lane & 15, g = lane >> 4, qd = n >> 2, p = n & 3;
  const int d = 64 * x + 16 * w + n;
  const int srow = 8 * b + 2 * w;
  const __amdgpu_buffer_rsrc_t wr = ws_rsrc(ws);

  u32x2 epo, sh; u32x4 qa, qb, kp[2], kc[2];
  gld2_v_sc1<0>(epo, 0, ws + WS_EPO);
  gld_v<0>(qa, lane * 16, (const char*)q + (size_t)srow * 1024);
  gld_v<1024>(qb, lane * 16, (const char*)q + (size_t)srow * 1024);
  gld2_v<0>(sh, d * 8, shk);
  {
    const int r8 = lane >> 3, c8 = lane & 7;
    auto voff = [&](int instr) { const int f = ((r8 >> 1) & 1) | ((instr & 1) << 1); return (u32)((((c8 >> 1) ^ f) * 2 + (c8 & 1)) * 16); };
    const size_t col = (size_t)x * 128;
    int row = s0 - 8 + 8 * w + r8; row = row < 0 ? 0 : row;
    glds16<0>((u32)row * 1024 + voff(w), (const char*)k + col, sb + KA_TK + w * 1024);
    glds16<0>((u32)(s0 - 8 + 8 * (w + 4) + r8) * 1024 + voff(w), (const char*)k + col, sb + KA_TK + (w + 4) * 1024);
#pragma unroll
    for (int i = 0; i < 2; ++i) {
      const int in = w + 4 * i;
      glds16<0>((u32)(s0 + 8 * in + r8) * 1024 + voff(in), (const char*)v + col, sb + KA_TV + in * 1024);
      glds16<0>((u32)(s0 + 8 * in + r8) * 1024 + voff(in), (const char*)q + col, sb + KA_TQ + in * 1024);
    }
    if (w == 0) glds16<0>((u32)(s0 + 56 + r8) * 1024 + voff(8), (const char*)k + col, sb + KA_TK + 8 * 1024);
  }
#pragma unroll
  for (int cc = 0; cc < 2; ++cc) {
    const char* kr = (const char*)ssmk + (size_t)(ch0 + cc) * 4096;
    gld_v<0>(kp[cc], (u32)(t > 0 ? t - 1 : 0) * 16, kr);
    gld_v<0>(kc[cc], (u32)t * 16, kr);
  }
  const float dsk = blo(dskip[ch0 + (w >> 1)]);
  if (b == 0 && t < 16) *(u32*)(ws + WS_CNT + t * 128) = 0u;

  if (w == 0) wait_vm<11>(); else wait_vm<10>();
  asm volatile("" : "+v"(qa), "+v"(qb), "+v"(sh), "+v"(epo));
  u32x2 tag;
  { const unsigned long long e = (((unsigned long long)epo[1] << 32) | epo[0]) + 1ull; tag = u32x2{(u32)e, (u32)(e >> 32)}; }
  {
    float sa = 0.f, sbb = 0.f;
#pragma unroll
    for (int e = 0; e < 4; ++e) { sa += blo(qa[e]) + bhi(qa[e]); sbb += blo(qb[e]) + bhi(qb[e]); }
    const float ma = wave_sum(sa) * (1.f / D), mb = wave_sum(sbb) * (1.f / D);
    float va = 0.f, vb = 0.f;
#pragma unroll
    for (int e = 0; e < 4; ++e) {
      float u0 = blo(qa[e]) - ma, u1 = bhi(qa[e]) - ma, u2 = blo(qb[e]) - mb, u3 = bhi(qb[e]) - mb;
      va += u0 * u0 + u1 * u1; vb += u2 * u2 + u3 * u3;
    }
    va = wave_sum(va) * (1.f / D); vb = wave_sum(vb) * (1.f / D);
    const float ra = __builtin_amdgcn_rsqf(va + EPS), rb = __builtin_amdgcn_rsqf(vb + EPS);
    const u32x2 val = lane == 0 ? u32x2{__float_as_uint(ra), __float_as_uint(rb)} : u32x2{__float_as_uint(-ma * ra), __float_as_uint(-mb * rb)};
    const u32 off = lane == 0 ? (u32)WS_SA + srow * 4 : lane == 1 ? (u32)WS_SB + srow * 4 : 0xfffffff0u;
    __builtin_amdgcn_raw_buffer_store_b64(val, wr, off, 0, 16);
  }
  wait_vm<5>();
  barrier();
  STAMP(0, 1);
  {
    auto taddr = [&](int base, int tr) { const int f = ((tr >> 1) & 1) | ((tr >> 2) & 2); return sb + base + tr * 128 + ((w ^ f) * 32) + 8 * p; };
    const float a0 = blo(sh[0]), a1 = bhi(sh[0]), a2 = blo(sh[1]), a3 = bhi(sh[1]);
#pragma unroll
    for (int e = 0; e < 2; ++e) {
      const int rg = g + 4 * e;
      u32x2 km = tr_read(taddr(KA_TK, 8 * rg + 4 + qd));
      const u32x2 k0 = tr_read(taddr(KA_TK, 8 * rg + 8 + qd)), k1 = tr_read(taddr(KA_TK, 8 * rg + 12 + qd));
      const u32x2 v0 = tr_read(taddr(KA_TV, 8 * rg + qd)), v1 = tr_read(taddr(KA_TV, 8 * rg + 4 + qd));
      const u32x2 q0 = tr_read(taddr(KA_TQ, 8 * rg + qd)), q1 = tr_read(taddr(KA_TQ, 8 * rg + 4 + qd));
      if (s0 + rg == 0) km = u32x2{0, 0};
      const float kf[11] = {bhi(km[0]), blo(km[1]), bhi(km[1]), blo(k0[0]), bhi(k0[0]), blo(k0[1]), bhi(k0[1]), blo(k1[0]), bhi(k1[0]), blo(k1[1]), bhi(k1[1])};
      const float vf[8] = {blo(v0[0]), bhi(v0[0]), blo(v0[1]), bhi(v0[1]), blo(v1[0]), bhi(v1[0]), blo(v1[1]), bhi(v1[1])};
      float xo[8];
#pragma unroll
      for (int j = 0; j < 8; ++j) xo[j] = (kf[j + 3] * a0 + kf[j + 2] * a1 + kf[j + 1] * a2 + kf[j] * a3) * vf[j];
      const int r0 = s0 + 8 * rg;
      st16_wt(wr, (u32)WS_XR + (d * S + (2040 - r0)) * 2, u32x4{pk2(xo[7], xo[6]), pk2(xo[5], xo[4]), pk2(xo[3], xo[2]), pk2(xo[1], xo[0])});
      st16_wt(wr, (u32)WS_QT + (d * S + r0) * 2, cat(q0, q1));
    }
  }
  wait_vm<5>();
  asm volatile("" : "+v"(kp[0]), "+v"(kc[0]), "+v"(kp[1]), "+v"(kc[1]));
#pragma unroll
  for (int cc = 0; cc < 2; ++cc) {
    const u32 m = t > 0 ? 0xffffffffu : 0u;
    u32 wv[6] = {kp[cc][0] & m, kp[cc][1] & m, kp[cc][2] & m, kp[cc][3] & m, kc[cc][0], kc[cc][1]};
    u32 o1[5];
#pragma unroll
    for (int i = 0; i < 5; ++i) o1[i] = __builtin_amdgcn_alignbit(wv[i + 1], wv[i], 16);
    char* base = smem + cc * KA_KC + (t + 1) * 16;
    *(u32x4*)(base + 0 * KA_CS) = u32x4{o1[0], o1[1], o1[2], o1[3]};
    *(u32x4*)(base + 1 * KA_CS) = u32x4{wv[1], wv[2], wv[3], wv[4]};
    *(u32x4*)(base + 2 * KA_CS) = u32x4{o1[1], o1[2], o1[3], o1[4]};
    *(u32x4*)(base + 3 * KA_CS) = u32x4{wv[2], wv[3], wv[4], wv[5]};
    if (t == 255) {
      const u32 z = 0;
      const u32 e0 = __builtin_amdgcn_alignbit(kc[cc][1], kc[cc][0], 16), e1 = __builtin_amdgcn_alignbit(kc[cc][2], kc[cc][1], 16);
      const u32 e2 = __builtin_amdgcn_alignbit(kc[cc][3], kc[cc][2], 16), e3 = __builtin_amdgcn_alignbit(z, kc[cc][3], 16);
      *(u32x4*)(base + 16 + 0 * KA_CS) = u32x4{e0, e1, e2, e3};
      *(u32x4*)(base + 16 + 1 * KA_CS) = u32x4{kc[cc][1], kc[cc][2], kc[cc][3], z};
      *(u32x4*)(base + 16 + 2 * KA_CS) = u32x4{e1, e2, e3, z};
      *(u32x4*)(base + 16 + 3 * KA_CS) = u32x4{kc[cc][2], kc[cc][3], z, z};
    }
  }
  if (t < 8) *(u32x4*)(smem + (t >> 2) * KA_KC + (t & 3) * KA_CS) = u32x4{0, 0, 0, 0};
  asm volatile("s_waitcnt vmcnt(0) lgkmcnt(0)\n\ts_barrier" ::: "memory");
  STAMP(0, 2);
  if (t == 0) __builtin_amdgcn_raw_buffer_store_b64(tag, wr, (u32)WS_FLG + (x * 32 + bi) * 8, 0, 16);
  if (t >= 64 && t < 64 + 60) { int i = t - 64; *(u32x4*)(smem + KA_XR + (i / 30) * 4608 + 4096 + (i % 30) * 16) = u32x4{0, 0, 0, 0}; }
  if (w == 0) {
    const unsigned long long* fp = (const unsigned long long*)(ws + WS_FLG) + x * 32 + (lane & 31);
    const unsigned long long want = ((unsigned long long)tag[1] << 32) | tag[0];
    for (int spins = 0; spins < SPIN_MAX; ++spins) {
      if (__all(__hip_atomic_load(fp, __ATOMIC_RELAXED, __HIP_MEMORY_SCOPE_AGENT) == want)) break;
      __builtin_amdgcn_s_sleep(1);
    }
  }
  asm volatile("s_waitcnt vmcnt(0)\n\ts_barrier" ::: "memory");
  STAMP(0, 3);

  const int c = w >> 1, half = w & 1;
  u32x2 qv[4]; u32x4 sa[4], sbv[4], fl[2];
  const u32 eo = (u32)(1024 * half + 16 * n + 4 * g);
  {
    u32x4 x0, x1;
    gld_v_sc1<0>(x0, t * 16, ws + WS_XR + (size_t)ch0 * 4096);
    gld_v_sc1<0>(x1, t * 16, ws + WS_XR + (size_t)(ch0 + 1) * 4096);
    const char* qTc = ws + WS_QT + (size_t)(ch0 + c) * 4096;
    gld2_v_sc1<0>(qv[0], eo * 2, qTc); gld2_v_sc1<512>(qv[1], eo * 2, qTc); gld2_v_sc1<1024>(qv[2], eo * 2, qTc); gld2_v_sc1<1536>(qv[3], eo * 2, qTc);
    wait_vm<4>();
    asm volatile("" : "+v"(x0), "+v"(x1));
    *(u32x4*)(smem + KA_XR + t * 16) = x0;
    *(u32x4*)(smem + KA_XR + 4608 + t * 16) = x1;
  }
  asm volatile("s_waitcnt lgkmcnt(0)\n\ts_barrier" ::: "memory");
  if (t == 0) __builtin_amdgcn_raw_buffer_store_b64(tag, wr, (u32)WS_HNT + b * 8, 0, 16);
  STAMP(1, 1);

  const u32 aa = sb + c * KA_KC + (n & 3) * KA_CS + 16 * g + 2 * (n & 12) + half * 256;
  const u32 ba = sb + KA_XR + c * 4608 + 480 - 32 * n + 16 * g + half * 256;
  f32x4 acc[8];
  u32x2 Alo[2][8], Ahi[2][8];
  u32x4 B[2][8];
  auto rd = [&](auto C0, auto R) {
    constexpr int c0 = C0, r = R, buf = c0 & 1;
    if constexpr (r >= 2 && r < 10) {
      constexpr int u = r - 2;
      ds_b128<512 * (7 - u) + 64 * c0>(B[buf][u], ba);
    } else {
      constexpr int ar = r < 2 ? r : r - 8, i = ar >> 1;
      if constexpr ((ar & 1) == 0) ds_b64<512 * i + 64 * c0>(Alo[buf][i], aa);
      else ds_b64<512 * i + 64 * c0 + 8>(Ahi[buf][i], aa);
    }
  };
  auto ldS = [&](auto E) {
    constexpr int e = E, i = e >> 1;
    if constexpr ((e & 1) == 0) gld_v_sc1<i * 1024>(sa[i], eo * 4, ws + WS_SA);
    else gld_v_sc1<i * 1024>(sbv[i], eo * 4, ws + WS_SB);
  };
  static_for<24>([&](auto R) { rd(std::integral_constant<int, 0>{}, R); });
  static_for<4>([&](auto C0) {
    constexpr int c0 = C0, buf = c0 & 1;
    if constexpr (c0 == 2) {
      wait_vm<0>();
      asm volatile("" : "+v"(fl[0]), "+v"(fl[1]));
      if (!__all(tags_ok(fl[0], tag) && tags_ok(fl[1], tag))) {
        for (int spins = 0; spins < SPIN_MAX; ++spins) {
          gld_v_sc1<0>(fl[0], lane * 32, ws + WS_FLG); gld_v_sc1<16>(fl[1], lane * 32, ws + WS_FLG);
          wait_vm<0>();
          asm volatile("" : "+v"(fl[0]), "+v"(fl[1]));
          if (__all(tags_ok(fl[0], tag) && tags_ok(fl[1], tag))) break;
          __builtin_amdgcn_s_sleep(1);
        }
      }
      if (b == 0 && t == 0) __builtin_amdgcn_raw_buffer_store_b64(tag, wr, (u32)WS_EPO, 0, 16);
    }
    u32x4 A[8];
    static_for<36>([&](auto M) {
      constexpr int m = M;
      constexpr int c1 = m < 8 ? 0 : m < 15 ? 1 : m < 21 ? 2 : m < 26 ? 3 : m < 30 ? 4 : m < 33 ? 5 : m < 35 ? 6 : 7;
      constexpr int first = c1 * 8 - c1 * (c1 - 1) / 2;
      constexpr int u = m - first;
      if constexpr (m == 0) {
        if constexpr (c0 == 0) wait_lgkm<14>(); else wait_lgkm<0>();
        pin(Alo[buf][0]); pin(Ahi[buf][0]);
#pragma unroll
        for (int i = 0; i < 8; ++i) pin(B[buf][i]);
        A[0] = cat(Alo[buf][0], Ahi[buf][0]);
      }
      if constexpr (m == 8) {
        if constexpr (c0 == 0) wait_lgkm<8>();
#pragma unroll
        for (int i = 1; i < 8; ++i) { pin(Alo[buf][i]); pin(Ahi[buf][i]); A[i] = cat(Alo[buf][i], Ahi[buf][i]); }
      }
      mfma_vv<(c0 == 0 && c1 == 0)>(acc[c1 + u], A[c1], B[buf][u]);
      if constexpr (c0 < 3 && m < 24) rd(std::integral_constant<int, c0 + 1>{}, M);
      if constexpr (c0 == 1 && m == 30) gld_v_sc1<0>(fl[0], lane * 32, ws + WS_FLG);
      if constexpr (c0 == 1 && m == 32) gld_v_sc1<16>(fl[1], lane * 32, ws + WS_FLG);
      if constexpr (c0 == 2 && m >= 2 && m < 34 && (m & 3) == 2) ldS(std::integral_constant<int, (m - 2) / 4>{});
    });
  });
  mfma_fence(acc[0], acc[1], acc[2], acc[3]);
  after_fence(acc[4], acc[5], acc[6], acc[7]);
  STAMP(1, 2);
  f32x4 fin[4];
  {
    f32x4* red = (f32x4*)(smem + KA_RED);
    if (half) {
#pragma unroll
      for (int i = 0; i < 4; ++i) { red[(w * 4 + i) * 64 + lane] = acc[i]; fin[i] = acc[4 + i]; }
      asm volatile("; half 1" ::: "memory");
    } else {
#pragma unroll
      for (int i = 0; i < 4; ++i) { red[(w * 4 + i) * 64 + lane] = acc[4 + i]; fin[i] = acc[i]; }
      asm volatile("; half 0" ::: "memory");
    }
    asm volatile("s_waitcnt lgkmcnt(0)\n\ts_barrier" ::: "memory");
#pragma unroll
    for (int i = 0; i < 4; ++i) fin[i] += red[((w ^ 1) * 4 + i) * 64 + lane];
  }
  wait_vm<0>();
#pragma unroll
  for (int i = 0; i < 4; ++i) asm volatile("" : "+v"(qv[i]), "+v"(sa[i]), "+v"(sbv[i]));
  const f32x2 dd = {dsk, dsk};
  u32x2 hv[4];
#pragma unroll
  for (int i = 0; i < 4; ++i) {
    const int s = 256 * (4 * half + i) + 16 * n + 4 * g;
    const u32x2 xq = *(LDSP const u32x2*)(size_t)(sb + KA_XR + c * 4608 + 2 * (2044 - s));
    const f32x2 x0 = {bhi(xq[1]), blo(xq[1])}, x1 = {bhi(xq[0]), blo(xq[0])};
    const f32x2 q0 = {blo(qv[i][0]), bhi(qv[i][0])}, q1 = {blo(qv[i][1]), bhi(qv[i][1])};
    const f32x2 z0 = x0 * dd + f32x2{fin[i][0], fin[i][1]}, z1 = x1 * dd + f32x2{fin[i][2], fin[i][3]};
    const f32x4 a4 = __builtin_bit_cast(f32x4, sa[i]), b4 = __builtin_bit_cast(f32x4, sbv[i]);
    const f32x2 t0 = q0 * f32x2{a4[0], a4[1]} + f32x2{b4[0], b4[1]};
    const f32x2 t1 = q1 * f32x2{a4[2], a4[3]} + f32x2{b4[2], b4[3]};
    const f32x2 h0 = t0 * z0 + q0, h1 = t1 * z1 + q1;
    hv[i] = u32x2{pk2(h0[0], h0[1]), pk2(h1[0], h1[1])};
  }
#pragma unroll
  for (int i = 0; i < 4; i += 2) {
    auto a = __builtin_amdgcn_permlane16_swap(hv[i][0], hv[i + 1][0], false, false);
    auto bb = __builtin_amdgcn_permlane16_swap(hv[i][1], hv[i + 1][1], false, false);
    const int s = 256 * (4 * half + i + (g & 1)) + 16 * n + 4 * (g & 2);
    st16_wt(wr, (u32)WS_HT + ((ch0 + c) * S + s) * 2, u32x4{a[0], bb[0], a[1], bb[1]});
  }
  STAMP(1, 3);
}

constexpr int KB_NL = 39;
constexpr int KB_ST = KB_NL * 4096;
constexpr int KB_LDS = KB_ST + 1024;
constexpr int KB_RING = 8;

constexpr int p2_upto(int ks) { return ks < 0 ? 2 : ks <= 3 ? 2 * ks + 4 : (ks + 7 > 15 ? 15 : ks + 7); }
constexpr int p2_extra(int ks) { return ks == 9 ? 3 : ks >= 10 ? 4 : 0; }
constexpr int p2_extra_first(int ks) { int c = 0; for (int s = 0; s < ks; ++s) c += p2_extra(s); return c; }
constexpr int P2_NEXTRA = 23;
constexpr int p2_extra_real(int ks) { int f = p2_extra_first(ks), n = p2_extra(ks); return f >= P2_NEXTRA ? 0 : (f + n > P2_NEXTRA ? P2_NEXTRA - f : n); }
constexpr int p2_issue_step(int t) { for (int s = -1; s < KS1; ++s) if (p2_upto(s) >= t) return s; return KS1; }
constexpr int p2_younger(int t, int ks, int ops) {
  int c = (p2_upto(ks - 1) - t) * ops;
  for (int s = p2_issue_step(t); s < ks; ++s) if (s >= 0) c += p2_extra_real(s);
  return c;
}

struct KbCtx {
  u32 sb, gv, lv, lb0, lwd, csv;
  const char *hb0, *hb1, *w0, *w1, *w2, *cs0, *cs1, *cs2, *wd, *wdr;
};

template <int GRAM, class Setup>
DI void kb_phase2(const KbCtx& c, f32x4 (&acc)[3][8], f32x4 (&rsum)[2], f32x4 (&csp)[3], u32x4 (&Wr)[4][4], u32 (&ra)[8], Setup&& setup, char* ws_stamp) {
  constexpr int OPS = GRAM ? 4 : 5;
  u32x4 W[KB_RING][3];
  u32x2 Bl[2][8], Bh[2][8];
  auto issue = [&](auto KS, auto O) {
    constexpr int ks = KS, o = O;
    if constexpr (o == 0) glds16<ks * 8192>(c.gv, c.hb0 + (size_t)ks * 131072, c.lb0);
    if constexpr (o == 1) gld_a<(ks & 3) * 1024>(W[ks % KB_RING][0], c.lv, c.w0 + (ks >> 2) * 4096);
    if constexpr (o == 2) gld_a<(ks & 3) * 1024>(W[ks % KB_RING][1], c.lv, c.w1 + (ks >> 2) * 4096);
    if constexpr (o == 3 && !GRAM) gld_a<(ks & 3) * 1024>(W[ks % KB_RING][2], c.lv, c.w2 + (ks >> 2) * 4096);
    if constexpr (o == 4) glds16<ks * 8192 + 1024>(c.gv, c.hb1 + (size_t)ks * 131072, c.lb0);
  };
  auto extra = [&](auto E) {
    constexpr int e = E;
    if constexpr (e < 7) glds16<(32 + e) * 4096>(c.lv, c.wd + (size_t)(32 + e) * 4096, c.lwd);
    else if constexpr (e < P2_NEXTRA) gld_a<((e - 7) & 3) * 1024>(Wr[(e - 7) >> 2][(e - 7) & 3], c.lv, c.wdr + ((e - 7) >> 2) * 4096);
  };
  auto rdB = [&](auto KS, auto R) {
    constexpr int ks = KS, r = R, i = r >> 1;
    if constexpr ((r & 1) == 0) ds_tr<(ks & 7) * 8192>(Bl[ks & 1][i], ra[i] + (ks >= 8 ? 65536 : 0));
    else ds_tr<(ks & 7) * 8192 + 1024>(Bh[ks & 1][i], ra[i] + (ks >= 8 ? 65536 : 0));
  };
  u32x4 c0, c1, c2;
  static_for<5>([&](auto O) { issue(std::integral_constant<int, 0>{}, O); });
  gld_v<0>(c0, c.csv, c.cs0); gld_v<0>(c1, c.csv, c.cs1); gld_v<0>(c2, c.csv, c.cs2);
  static_for<p2_upto(-1)>([&](auto KS) { static_for<5>([&](auto O) { issue(std::integral_constant<int, KS + 1>{}, O); }); });
  setup();
  u32x4 ones = {0x3f803f80u, 0x3f803f80u, 0x3f803f80u, 0x3f803f80u};
  pin(ones);
  wait_vm<p2_upto(-1) * OPS>();
  barrier();
  static_for<16>([&](auto R) { rdB(std::integral_constant<int, 0>{}, R); });
  asm volatile("" : "+v"(c0), "+v"(c1), "+v"(c2));
  float cv[12];
#pragma unroll
  for (int e = 0; e < 4; ++e) { cv[e] = __uint_as_float(c0[e]); cv[4 + e] = __uint_as_float(c1[e]); cv[8 + e] = __uint_as_float(c2[e]); }
  static_for<KS1>([&](auto KS) {
    constexpr int ks = KS, buf = ks & 1, slot = ks % KB_RING;
    constexpr int nmain = (p2_upto(ks) - p2_upto(ks - 1)) * 5, next = p2_extra_real(ks);
    wait_lgkm<0>();
    u32x4 B[8];
#pragma unroll
    for (int i = 0; i < 8; ++i) { pin(Bl[buf][i]); pin(Bh[buf][i]); }
#pragma unroll
    for (int i = 0; i < 8; ++i) B[i] = cat(Bl[buf][i], Bh[buf][i]);
    pin_a(W[slot][0]); pin_a(W[slot][1]);
    if constexpr (!GRAM) pin_a(W[slot][2]);
    static_for<26>([&](auto M) {
      constexpr int m = M, i = m / 3, u = m % 3;
      if constexpr (m >= 24) mfma_vv<ks == 0>(rsum[m - 24], ones, B[m - 24]);
      else if constexpr (u < 2 || !GRAM) mfma_av<ks == 0>(acc[u][i], W[slot][u], B[i]);
      else mfma_vv<ks == 0>(acc[2][i], B[i], B[i]);
      if constexpr (m == 1 && ks + 1 < KS1) { wait_vm<p2_younger(ks + 1, ks, OPS)>(); barrier(); }
      if constexpr (m >= 2 && m < 18 && ks + 1 < KS1) rdB(std::integral_constant<int, ks + 1>{}, std::integral_constant<int, m - 2>{});
      if constexpr (ks >= 9 && ks < 13 && m >= 2 && m < 14) dpp_stage<ks - 9>(cv[m - 2]);
      constexpr int v = m - (26 - nmain - next);
      if constexpr (v >= 0 && v < nmain) issue(std::integral_constant<int, p2_upto(ks - 1) + 1 + v / 5>{}, std::integral_constant<int, v % 5>{});
      if constexpr (v >= nmain) extra(std::integral_constant<int, p2_extra_first(ks) + v - nmain>{});
    });
  });
  mfma_fence(acc[0][0], acc[1][0], rsum[0], rsum[1]);
#pragma unroll
  for (int i = 0; i < 8; ++i) after_fence(acc[0][i], acc[1][i], acc[2][i], rsum[i & 1]);
#pragma unroll
  for (int u = 0; u < 3; ++u) csp[u] = f32x4{cv[4 * u], cv[4 * u + 1], cv[4 * u + 2], cv[4 * u + 3]};
}

__global__ __launch_bounds__(256) void kb_mlp(char* __restrict__ ws, u16* __restrict__ out) {
  extern __shared__ __attribute__((aligned(16))) char smem[];
  const int t = threadIdx.x, lane = t & 63;
  const int w = __builtin_amdgcn_readfirstlane(t >> 6);
  const int b = blockIdx.x, xcd = b & 7, bi = b >> 3;
#define KB_MAP 1
  const int rt = KB_MAP == 0 ? 2 * xcd + (bi & 1) : (xcd >> 1) * 4 + (bi & 3);
  const int j = KB_MAP == 0 ? bi >> 1 : (xcd & 1) * 8 + (bi >> 2);
  const int half = KB_MAP == 0 ? j & 1 : j >> 3, dt = KB_MAP == 0 ? j >> 1 : j & 7;
  const int U0 = slice_u0(j), nu = slice_nu(j);
  const int rem = nu - 8;
  const int nw = w < rem ? 3 : 2;
  const int uw = U0 + (w < rem ? 3 * w : 3 * rem + 2 * (w - rem));
  const int u2 = uw + (nw == 3 ? 2 : 0);
  const int n = lane & 15, g = lane >> 4;
  const int s0 = 128 * rt;
  char* ws_stamp = ws; (void)ws_stamp;
  STAMP(2, 0);
  auto sf = [&](int i) { return ((w + (i >> 1)) & 3) + 4 * (half ^ (i & 1)); };

  KbCtx c;
  c.sb = (u32)(size_t)(LDSP char*)smem;
  c.lv = lane * 16;
  c.gv = (u32)(g * 4096 + (((n >> 1) ^ (g | ((w & 1) << 2))) * 32 + (n & 1) * 16));
  c.hb0 = ws + WS_HT + (size_t)s0 * 2 + (size_t)(8 * w) * 4096;
  c.hb1 = c.hb0 + 4 * 4096;
  c.lb0 = c.sb + 8 * w * 256;
  c.w0 = ws + WS_W1 + (size_t)uw * KS1 * 1024;
  c.w1 = c.w0 + KS1 * 1024;
  c.w2 = ws + WS_W1 + (size_t)u2 * KS1 * 1024;
  c.cs0 = ws + WS_CS + (size_t)uw * 64;
  c.cs1 = c.cs0 + 64;
  c.cs2 = ws + WS_CS + (size_t)u2 * 64;
  c.csv = n * (NU * 64) + g * 16;
  c.wdr = ws + WS_WD + (size_t)dt * KS2 * 4096 + (size_t)KB_NL * 4096;
  c.wd = ws + WS_WD + (size_t)dt * KS2 * 4096 + (size_t)w * 1024;
  c.lwd = c.sb + 1024 * w;
  u32 ra[8], hoff[6];
  auto setup = [&]() {
    const int qd = n >> 2, p = n & 3;
    const int frow = qd | ((g & 1) << 2);
#pragma unroll
    for (int i = 0; i < 8; ++i) { ra[i] = c.sb + (8 * g + qd) * 256 + ((sf(i) ^ frow) * 32) + 8 * p; asm volatile("" : "+v"(ra[i])); }
#pragma unroll
    for (int tt = 0; tt < 6; ++tt) {
      const int q = 4 * tt + g, i = (q * 11) >> 5, u = q - 3 * i, U = uw + u;
      const u32 o = (u32)(((8 * rt + sf(i)) * KS2 + (U >> 2)) * 1024 + ((U & 3) * 16 + n) * 16);
      hoff[tt] = u < nw ? o : 0xfffffff0u;
      asm volatile("" : "+v"(hoff[tt]));
    }
  };
  f32x4 acc[3][8], cs[3], rsum[2];
  u32x4 Wr[4][4];
  if (w == 3) kb_phase2<1>(c, acc, rsum, cs, Wr, ra, setup, ws_stamp);
  else kb_phase2<0>(c, acc, rsum, cs, Wr, ra, setup, ws_stamp);
  STAMP(2, 2);

  u32x2 res[4];
  {
    const u32 rb = ra[0] + dt * 16384;
    ds_tr<0>(res[0], rb); ds_tr<1024>(res[1], rb); ds_tr<8192>(res[2], rb); ds_tr<9216>(res[3], rb);
  }
  float* stl = (float*)(smem + KB_ST);
  if (g == 0) {
#pragma unroll
    for (int i = 0; i < 2; ++i) stl[2 * (16 * sf(i) + n)] = rsum[i][0];
  }
  if (w == 3 && g == (n >> 2)) {
#pragma unroll
    for (int i = 0; i < 8; ++i) {
      f32x4 a = acc[2][i];
      float dg = (n & 2) ? ((n & 1) ? a[3] : a[2]) : ((n & 1) ? a[1] : a[0]);
      stl[2 * (16 * sf(i) + n) + 1] = dg;
    }
  }
  asm volatile("s_waitcnt lgkmcnt(0)\n\ts_barrier" : "+v"(res[0]), "+v"(res[1]), "+v"(res[2]), "+v"(res[3]) :: "memory");
  STAMP(2, 3);
  const __amdgpu_buffer_rsrc_t hrs = __builtin_amdgcn_make_buffer_rsrc(ws + WS_H, 0, (S / 16) * KS2 * 1024, 0x00020000);
  f32x2 sv[8];
#pragma unroll
  for (int i = 0; i < 8; ++i) sv[i] = *(const f32x2*)(stl + 2 * (16 * sf(i) + n));
  u32 hv[24];
  static_for<8>([&](auto I) {
    constexpr int i = I;
    auto fill = [&](auto E) { constexpr int wk = 4 * i + E; glds16<wk * 4096>(c.lv, c.wd + (size_t)wk * 4096, c.lwd); };
    fill(std::integral_constant<int, 3>{});
    const float mean = sv[i][0] * (1.f / D);
    const float var = fmaxf(sv[i][1] * (1.f / D) - mean * mean, 0.f) + EPS;
    const float rstd = __builtin_amdgcn_rsqf(var);
    const float rinv = var * rstd;
    const float ce = -1.4426950408889634f * rstd;
    const f32x2 nm = {-mean, -mean}, ce2 = {ce, ce}, ri2 = {rinv, rinv};
    static_for<3>([&](auto U_) {
      constexpr int u = U_;
      pin4(acc[u][i]);
      fill(U_);
      const f32x4 a = acc[u][i];
      const f32x2 tg = (f32x2{a[0], a[1]} + nm * f32x2{cs[u][0], cs[u][1]}) * ce2;
      const f32x2 tu = f32x2{a[2], a[3]} + nm * f32x2{cs[u][2], cs[u][3]};
      const f32x2 e = {__builtin_amdgcn_exp2f(tg[0]), __builtin_amdgcn_exp2f(tg[1])};
      const f32x2 den = e * ri2 + ri2;
      const f32x2 o = tu * f32x2{__builtin_amdgcn_rcpf(den[0]), __builtin_amdgcn_rcpf(den[1])};
      hv[3 * i + u] = pk2(o[0], o[1]);
    });
    static_for<6>([&](auto T) {
      constexpr int tt = T;
      if constexpr ((4 * tt + 3) / 3 == i) {
        auto s02 = __builtin_amdgcn_permlane32_swap(hv[4 * tt], hv[4 * tt + 2], false, false);
        auto s13 = __builtin_amdgcn_permlane32_swap(hv[4 * tt + 1], hv[4 * tt + 3], false, false);
        auto t01 = __builtin_amdgcn_permlane16_swap(s02[0], s13[0], false, false);
        auto t23 = __builtin_amdgcn_permlane16_swap(s02[1], s13[1], false, false);
        __builtin_amdgcn_raw_buffer_store_b128(u32x4{t01[0], t01[1], t23[0], t23[1]}, hrs, hoff[tt], 0, 16);
      }
    });
  });
  STAMP(2, 4);
  u32* cnt = (u32*)(ws + WS_CNT + rt * 128);
  asm volatile("s_waitcnt vmcnt(0)\n\ts_barrier" ::: "memory");
  STAMP(2, 5);
  if (w == 0) {
    if (lane == 0) __hip_atomic_fetch_add(cnt, 1u, __ATOMIC_RELAXED, __HIP_MEMORY_SCOPE_AGENT);
    for (int spins = 0; spins < (1 << 22); ++spins) {
      const u32 v = __builtin_amdgcn_readfirstlane(__hip_atomic_load(cnt, __ATOMIC_RELAXED, __HIP_MEMORY_SCOPE_AGENT));
      if (v >= 16u) break;
      __builtin_amdgcn_s_sleep(1);
    }
  }
  asm volatile("s_waitcnt vmcnt(0)\n\ts_barrier" ::: "memory");
  STAMP(2, 6);

  const char* hb = ws + WS_H + (size_t)(8 * rt + 4 * half + w) * KS2 * 1024;
  u32x4 Hf[KS2];
  constexpr int P3_AHEAD = 12;
  auto ldH = [&](auto KS) { constexpr int ks = KS; if constexpr (ks < KS2) gld_a_sc1<(ks & 3) * 1024>(Hf[ks], c.lv, hb + (ks >> 2) * 4096); };
  static_for<P3_AHEAD>([&](auto KS) { ldH(KS); });
  STAMP(3, 0);
  f32x4 o4[4];
  u32x4 Af[3][4];
  const u32 la0 = c.sb + lane * 16, la1 = la0 + 65536, la2 = la0 + 131072;
  auto rdA = [&](auto KS, auto DF) {
    constexpr int ks = KS, df = DF;
    if constexpr (ks < KB_NL) ds_b128<(ks & 15) * 4096 + df * 1024>(Af[ks % 3][df], ks < 16 ? la0 : ks < 32 ? la1 : la2);
  };
  static_for<4>([&](auto DF) { rdA(std::integral_constant<int, 0>{}, DF); });
  static_for<4>([&](auto DF) { rdA(std::integral_constant<int, 1>{}, DF); });
  static_for<KS2>([&](auto KS) {
    constexpr int ks = KS;
    wait_vm<(ks + P3_AHEAD <= KS2 ? P3_AHEAD - 1 : KS2 - 1 - ks)>();
    if constexpr (ks + 1 < KB_NL) wait_lgkm<4>(); else wait_lgkm<0>();
    pin_a(Hf[ks]);
    static_for<4>([&](auto DF) {
      constexpr int df = DF;
      if constexpr (ks < KB_NL) { pin(Af[ks % 3][df]); mfma_va<ks == 0>(o4[df], Af[ks % 3][df], Hf[ks]); }
      else { pin_a(Wr[ks - KB_NL][df]); mfma_aa(o4[df], Wr[ks - KB_NL][df], Hf[ks]); }
      rdA(std::integral_constant<int, ks + 2>{}, DF);
      if constexpr (df == 1 && ks + P3_AHEAD < KS2) ldH(std::integral_constant<int, ks + P3_AHEAD>{});
    });
  });
  mfma_fence(o4[0], o4[1], o4[2], o4[3]);
  u32x4 o0, o1;
#pragma unroll
  for (int df = 0; df < 2; ++df) {
    o0[2 * df] = pk2(o4[df][0] + blo(res[df][0]), o4[df][1] + bhi(res[df][0]));
    o0[2 * df + 1] = pk2(o4[df][2] + blo(res[df][1]), o4[df][3] + bhi(res[df][1]));
    o1[2 * df] = pk2(o4[2 + df][0] + blo(res[2 + df][0]), o4[2 + df][1] + bhi(res[2 + df][0]));
    o1[2 * df + 1] = pk2(o4[2 + df][2] + blo(res[2 + df][1]), o4[2 + df][3] + bhi(res[2 + df][1]));
  }
  const __amdgpu_buffer_rsrc_t ors = __builtin_amdgcn_make_buffer_rsrc(out, 0, S * D * 2, 0x00020000);
  const u32 ooff = (u32)((s0 + 64 * half + 16 * w + n) * D + 64 * dt + 8 * g) * 2;
  __builtin_amdgcn_raw_buffer_store_b128(o0, ors, ooff, 0, 16);
  __builtin_amdgcn_raw_buffer_store_b128(o1, ors, ooff + 64, 0, 16);
  asm volatile("kb_code_end:");
  STAMP(2, 7);
}

extern "C" void kernel_launch(void* const* d_in, const int* in_sizes, int n_in, void* d_out, int out_size, void* d_ws,
                              size_t ws_size, hipStream_t stream) {
  const u16 *q = (const u16*)d_in[0], *k = (const u16*)d_in[1], *v = (const u16*)d_in[2], *shk = (const u16*)d_in[3];
  const u16 *ssmk = (const u16*)d_in[4], *dskip = (const u16*)d_in[5];
  const u16 *wg = (const u16*)d_in[7], *wu = (const u16*)d_in[8], *wd = (const u16*)d_in[9];
  char* ws = (char*)d_ws;
  (void)hipFuncSetAttribute((const void*)ka_conv, hipFuncAttributeMaxDynamicSharedMemorySize, KA_LDS);
  (void)hipFuncSetAttribute((const void*)kb_mlp, hipFuncAttributeMaxDynamicSharedMemorySize, KB_LDS);
  ka_conv<<<512, 256, KA_LDS, stream>>>(q, k, v, shk, ssmk, dskip, wg, wu, wd, ws);
  kb_mlp<<<256, 256, KB_LDS, stream>>>(ws, (u16*)d_out);
}
```

```cpp
#include <hip/hip_runtime.h>
#include <stdint.h>
#include <utility>

typedef unsigned short u16;
typedef unsigned u32;
typedef __attribute__((ext_vector_type(2))) u32 u32x2;
typedef __attribute__((ext_vector_type(4))) u32 u32x4;
typedef __attribute__((ext_vector_type(2))) float f32x2;
typedef __attribute__((ext_vector_type(4))) float f32x4;
typedef __attribute__((ext_vector_type(2))) __bf16 bf16x2;
typedef __attribute__((ext_vector_type(8))) __bf16 bf16x8;
typedef __attribute__((ext_vector_type(4))) short s16x4;
#define LDSP __attribute__((address_space(3)))
#define DI __device__ __forceinline__

constexpr int S = 2048, D = 512, F = 1376;
constexpr int NU = F / 8;
constexpr int KS1 = D / 32;
constexpr int KS2 = F / 32;
constexpr float EPS = 1e-5f;

constexpr size_t WS_XR = 0;
constexpr size_t WS_QT = WS_XR + (size_t)D * S * 2;
constexpr size_t WS_HT = WS_QT + (size_t)D * S * 2;
constexpr size_t WS_W1 = WS_HT + (size_t)D * S * 2;
constexpr size_t WS_WD = WS_W1 + (size_t)NU * KS1 * 1024;
constexpr size_t WS_H = WS_WD + (size_t)8 * KS2 * 4 * 1024;
constexpr size_t WS_CS = WS_H + (size_t)(S / 16) * KS2 * 1024;
constexpr size_t WS_SA = WS_CS + (size_t)KS1 * NU * 16 * 4;
constexpr size_t WS_SB = WS_SA + (size_t)S * 4;
constexpr size_t WS_CNT = WS_SB + (size_t)S * 4;
constexpr size_t WS_FLG = WS_CNT + 16 * 128;
constexpr size_t WS_HNT = WS_FLG + 256 * 8;
constexpr size_t WS_T0 = WS_HNT + 256 * 8 + 256;
constexpr size_t WS_EPO = WS_HNT + 256 * 8;

template <int... Is, class Fn> DI void static_for_(std::integer_sequence<int, Is...>, Fn&& f) { (f(std::integral_constant<int, Is>{}), ...); }
template <int N, class Fn> DI void static_for(Fn&& f) { static_for_(std::make_integer_sequence<int, N>{}, f); }

DI float blo(u32 u) { return __uint_as_float(u << 16); }
DI float bhi(u32 u) { return __uint_as_float(u & 0xffff0000u); }
DI u32 pk2(float a, float b) { f32x2 t = {a, b}; return __builtin_bit_cast(u32, __builtin_convertvector(t, bf16x2)); }
DI f32x4 mfma16(u32x4 a, u32x4 b, f32x4 c) {
  return __builtin_amdgcn_mfma_f32_16x16x32_bf16(__builtin_bit_cast(bf16x8, a), __builtin_bit_cast(bf16x8, b), c, 0, 0, 0);
}
DI u32x2 tr_read(u32 lds_byte) {
  return __builtin_bit_cast(u32x2, __builtin_amdgcn_ds_read_tr16_b64_v4i16((LDSP s16x4*)(size_t)lds_byte));
}
DI u32x4 tr_frag(u32 a0, u32 a1) { u32x2 x = tr_read(a0), y = tr_read(a1); return u32x4{x[0], x[1], y[0], y[1]}; }
DI u32x4 cat(u32x2 x, u32x2 y) { return __builtin_shufflevector(x, y, 0, 1, 2, 3); }
template <int CTRL> DI float dpp_add(float v) {
  return v + __builtin_bit_cast(float, __builtin_amdgcn_update_dpp(0, __builtin_bit_cast(int, v), CTRL, 0xf, 0xf, true));
}
DI float sum_rows(float v) {
  u32 u = __float_as_uint(v);
  auto a = __builtin_amdgcn_permlane16_swap(u, u, false, false);
  v = __uint_as_float(a[0]) + __uint_as_float(a[1]);
  u = __float_as_uint(v);
  auto b = __builtin_amdgcn_permlane32_swap(u, u, false, false);
  return __uint_as_float(b[0]) + __uint_as_float(b[1]);
}
DI float wave_sum(float v) {
  v = dpp_add<0xb1>(v);
  v = dpp_add<0x4e>(v);
  v = dpp_add<0x124>(v);
  v = dpp_add<0x128>(v);
  return sum_rows(v);
}
DI __amdgpu_buffer_rsrc_t ws_rsrc(char* ws) { return __builtin_amdgcn_make_buffer_rsrc(ws, 0, 0x04000000, 0x00020000); }
DI void st16_wt(__amdgpu_buffer_rsrc_t r, u32 off, u32x4 v) { __builtin_amdgcn_raw_buffer_store_b128(v, r, off, 0, 16); }
DI int slice_u0(int j) { return j < 12 ? 11 * j : 132 + 10 * (j - 12); }
DI int slice_nu(int j) { return j < 12 ? 11 : 10; }

template <bool FIRST = false> DI void mfma_vv(f32x4& acc, const u32x4& a, const u32x4& b) {
  if constexpr (FIRST) asm volatile("v_mfma_f32_16x16x32_bf16 %0, %1, %2, 0" : "=&v"(acc) : "v"(a), "v"(b));
  else asm volatile("v_mfma_f32_16x16x32_bf16 %0, %1, %2, %0" : "+v"(acc) : "v"(a), "v"(b));
}
template <bool FIRST = false> DI void mfma_av(f32x4& acc, const u32x4& a, const u32x4& b) {
  if constexpr (FIRST) asm volatile("v_mfma_f32_16x16x32_bf16 %0, %1, %2, 0" : "=&v"(acc) : "a"(a), "v"(b));
  else asm volatile("v_mfma_f32_16x16x32_bf16 %0, %1, %2, %0" : "+v"(acc) : "a"(a), "v"(b));
}
template <int OFF> DI void ds_b64(u32x2& d, u32 addr) { asm volatile("ds_read_b64 %0, %1 offset:%c2" : "=v"(d) : "v"(addr), "i"(OFF) : "memory"); }
template <int OFF> DI void ds_b128(u32x4& d, u32 addr) { asm volatile("ds_read_b128 %0, %1 offset:%c2" : "=v"(d) : "v"(addr), "i"(OFF) : "memory"); }
template <int OFF> DI void ds_tr(u32x2& d, u32 addr) { asm volatile("ds_read_b64_tr_b16 %0, %1 offset:%c2" : "=v"(d) : "v"(addr), "i"(OFF) : "memory"); }
template <int OFF> DI void gld_a(u32x4& d, u32 voff, const void* sbase) {
  asm volatile("global_load_dwordx4 %0, %1, %2 offset:%c3" : "=a"(d) : "v"(voff), "s"(sbase), "i"(OFF) : "memory");
}
template <int OFF> DI void gld_v(u32x4& d, u32 voff, const void* sbase) {
  asm volatile("global_load_dwordx4 %0, %1, %2 offset:%c3" : "=v"(d) : "v"(voff), "s"(sbase), "i"(OFF) : "memory");
}
template <int OFF> DI void gld2_v(u32x2& d, u32 voff, const void* sbase) {
  asm volatile("global_load_dwordx2 %0, %1, %2 offset:%c3" : "=v"(d) : "v"(voff), "s"(sbase), "i"(OFF) : "memory");
}
template <int LOFF> DI void glds16(u32 voff, const void* sbase, u32 ldsbase) {
  asm volatile("s_add_u32 m0, %2, %c3\n\ts_nop 0\n\tglobal_load_lds_dwordx4 %0, %1" ::"v"(voff), "s"(sbase), "s"(ldsbase), "i"(LOFF) : "memory", "scc");
}
template <int N> DI void wait_vm() { asm volatile("s_waitcnt vmcnt(%c0)" ::"i"(N) : "memory"); }
template <int N> DI void wait_lgkm() { asm volatile("s_waitcnt lgkmcnt(%c0)" ::"i"(N) : "memory"); }
template <int OFF> DI void gld_a_sc1(u32x4& d, u32 voff, const void* sbase) {
  asm volatile("global_load_dwordx4 %0, %1, %2 offset:%c3 sc1" : "=a"(d) : "v"(voff), "s"(sbase), "i"(OFF) : "memory");
}
DI void mfma_aa(f32x4& acc, const u32x4& a, const u32x4& b) {
  asm volatile("v_mfma_f32_16x16x32_bf16 %0, %1, %2, %0" : "+v"(acc) : "a"(a), "a"(b));
}
template <bool FIRST = false> DI void mfma_va(f32x4& acc, const u32x4& a, const u32x4& b) {
  if constexpr (FIRST) asm volatile("v_mfma_f32_16x16x32_bf16 %0, %1, %2, 0" : "=&v"(acc) : "v"(a), "a"(b));
  else asm volatile("v_mfma_f32_16x16x32_bf16 %0, %1, %2, %0" : "+v"(acc) : "v"(a), "a"(b));
}
DI void mfma_fence(f32x4& a, f32x4& b, f32x4& c, f32x4& d) { asm volatile("s_nop 7\n\ts_nop 1" : "+v"(a), "+v"(b), "+v"(c), "+v"(d)); }
DI void after_fence(f32x4& a, f32x4& b, f32x4& c, f32x4& d) { asm volatile("" : "+v"(a), "+v"(b), "+v"(c), "+v"(d)); }
DI void after_fence(f32x4& a, f32x4& b, f32x4& c) { asm volatile("" : "+v"(a), "+v"(b), "+v"(c)); }
template <int STAGE> DI void dpp_stage(float& v) {
  if constexpr (STAGE == 0) asm volatile("v_add_f32_dpp %0, %0, %0 quad_perm:[1,0,3,2] row_mask:0xf bank_mask:0xf" : "+v"(v));
  if constexpr (STAGE == 1) asm volatile("v_add_f32_dpp %0, %0, %0 quad_perm:[2,3,0,1] row_mask:0xf bank_mask:0xf" : "+v"(v));
  if constexpr (STAGE == 2) asm volatile("v_add_f32_dpp %0, %0, %0 row_ror:4 row_mask:0xf bank_mask:0xf" : "+v"(v));
  if constexpr (STAGE == 3) asm volatile("v_add_f32_dpp %0, %0, %0 row_ror:8 row_mask:0xf bank_mask:0xf" : "+v"(v));
}
DI void barrier() { asm volatile("s_barrier" ::: "memory"); }
DI void pin(u32x4& x) { asm volatile("" : "+v"(x)); }
DI void pin(u32x2& x) { asm volatile("" : "+v"(x)); }
DI void pin4(f32x4& x) { asm volatile("" : "+v"(x)); }
DI void pin_a(u32x4& x) { asm volatile("" : "+a"(x)); }

#define STAMP(kern, idx) do {} while (0)

template <class Gate>
DI void w_job(int b, const u16* __restrict__ wg, const u16* __restrict__ wu, const u16* __restrict__ wd, char* __restrict__ ws, char* smem, Gate&& gate) {
  const int t = threadIdx.x, lane = t & 63, w = t >> 6;
  char* tile = smem;
  char* tile2 = smem + 32 * 368;
  const u32 tb = (u32)(size_t)(LDSP char*)smem;
  const int i16 = lane & 15, qd = i16 >> 2, p = i16 & 3, g = lane >> 4;
  const int ks = b & 15, j = b >> 4;
  const int U0 = slice_u0(j), nu = slice_nu(j);
  u32x4 g4[2], u4[2], d4[2];
#pragma unroll
  for (int rep = 0; rep < 2; ++rep) {
    int idx = t + 256 * rep, r = idx / 11, u = idx - 11 * r;
    bool ok = r < 32 && u < nu;
    size_t off = ok ? (size_t)(32 * ks + r) * F + 8 * (U0 + u) : 0;
    g4[rep] = *(const u32x4*)(wg + off); u4[rep] = *(const u32x4*)(wu + off);
  }
  const int ks2 = b >> 2, dp = b & 3;
  if (b < KS2 * 4) {
#pragma unroll
    for (int rep = 0; rep < 2; ++rep) {
      int idx = t + 256 * rep, r = idx >> 4, c = idx & 15;
      d4[rep] = *(const u32x4*)(wd + (size_t)(32 * ks2 + r) * D + 128 * dp + 8 * c);
    }
  }
  gate();
#pragma unroll
  for (int rep = 0; rep < 2; ++rep) {
    int idx = t + 256 * rep, r = idx / 11, u = idx - 11 * r;
    if (r < 32 && u < nu) {
      *(u32x4*)(tile + r * 368 + u * 32) = u32x4{g4[rep][0], u4[rep][0], g4[rep][1], u4[rep][1]};
      *(u32x4*)(tile + r * 368 + u * 32 + 16) = u32x4{g4[rep][2], u4[rep][2], g4[rep][3], u4[rep][3]};
    }
  }
  if (b < KS2 * 4) {
#pragma unroll
    for (int rep = 0; rep < 2; ++rep) { int idx = t + 256 * rep, r = idx >> 4, c = idx & 15; *(u32x4*)(tile2 + r * 272 + c * 16) = d4[rep]; }
  }
  __syncthreads();
  const u32x4 ones = {0x3f803f80u, 0x3f803f80u, 0x3f803f80u, 0x3f803f80u};
  const __amdgpu_buffer_rsrc_t wr = ws_rsrc(ws);
  for (int u = w; u < nu; u += 4) {
    u32 a = tb + (8 * g + qd) * 368 + u * 32 + 8 * p;
    u32x4 fr = tr_frag(a, a + 4 * 368);
    st16_wt(wr, (u32)WS_W1 + (((U0 + u) * KS1 + ks) * 64 + lane) * 16, fr);
    f32x4 cs = mfma16(fr, ones, f32x4{0.f, 0.f, 0.f, 0.f});
    if (i16 == 0) st16_wt(wr, (u32)WS_CS + ((ks * NU + U0 + u) * 16 + 4 * g) * 4, __builtin_bit_cast(u32x4, cs));
  }
  if (b < KS2 * 4) {
#pragma unroll
    for (int rep = 0; rep < 2; ++rep) {
      int fi = w + 4 * rep, dtl = fi >> 2, df = fi & 3;
      u32 a = tb + 32 * 368 + (8 * g + qd) * 272 + (64 * dtl + 32 * (df >> 1) + 8 * p + 4 * (df & 1)) * 2;
      u32x4 fr = tr_frag(a, a + 4 * 272);
      st16_wt(wr, (u32)WS_WD + ((((2 * dp + dtl) * KS2 + ks2) * 4 + df) * 64 + lane) * 16, fr);
    }
  }
}

constexpr int KA_CS = 4160;
constexpr int KA_KC = 4 * KA_CS;
constexpr int KA_XR = 2 * KA_KC;
constexpr int KA_RED = KA_XR + 2 * 4608;
constexpr int KA_LDS = KA_RED + 4 * 4096;
constexpr int KA_TK = KA_XR, KA_TV = KA_TK + 72 * 128, KA_TQ = KA_TV + 64 * 128;
static_assert(KA_TQ + 64 * 128 == KA_LDS, "tile overlay");
constexpr int SPIN_MAX = 1 << 22;
#define HELP_SLEEP 40
#define HINT_TICKS 800ull

template <int OFF> DI void gld_v_sc1(u32x4& d, u32 voff, const void* sbase) {
  asm volatile("global_load_dwordx4 %0, %1, %2 offset:%c3 sc1" : "=v"(d) : "v"(voff), "s"(sbase), "i"(OFF) : "memory");
}
template <int OFF> DI void gld2_v_sc1(u32x2& d, u32 voff, const void* sbase) {
  asm volatile("global_load_dwordx2 %0, %1, %2 offset:%c3 sc1" : "=v"(d) : "v"(voff), "s"(sbase), "i"(OFF) : "memory");
}
DI bool tags_ok(const u32x4& f, const u32x2& tag) { return f[0] == tag[0] && f[1] == tag[1] && f[2] == tag[0] && f[3] == tag[1]; }

__global__ __launch_bounds__(256, 2) void ka_conv(const u16* __restrict__ q, const u16* __restrict__ k, const u16* __restrict__ v,
                                                  const u16* __restrict__ shk, const u16* __restrict__ ssmk, const u16* __restrict__ dskip,
                                                  const u16* __restrict__ wg, const u16* __restrict__ wu, const u16* __restrict__ wd,
                                                  char* __restrict__ ws) {
  unsigned long long t_entry;
  asm volatile("s_memrealtime %0\n\ts_waitcnt lgkmcnt(0)" : "=s"(t_entry) :: "memory");
  extern __shared__ __attribute__((aligned(16))) char smem[];
  const int t = threadIdx.x, lane = t & 63;
  const int w = __builtin_amdgcn_readfirstlane(t >> 6);
  const int b = blockIdx.x;
  char* ws_stamp = ws; (void)ws_stamp;
  if (b == 0 && t == 0) __hip_atomic_store((unsigned long long*)(ws + WS_T0), t_entry, __ATOMIC_RELAXED, __HIP_MEMORY_SCOPE_AGENT);
  STAMP(0, 0);
  if (b >= 256) {
    __builtin_amdgcn_s_sleep(HELP_SLEEP);
    if (b < 264 && w == 1) {
      const char* pc; u32 len;
      asm volatile("s_getpc_b64 %0\n1:\n\ts_mov_b32 %1, kb_code_end-1b" : "=s"(pc), "=s"(len));
      const u32 lb = (u32)(size_t)(LDSP char*)smem + 32768;
      for (u32 o0 = 0; o0 < len; o0 += 64 * 128) {
        u32 o = o0 + lane * 128; o = o < len ? o : 0;
        asm volatile("s_mov_b32 m0, %2\n\ts_nop 0\n\tglobal_load_lds_dword %0, %1" ::"v"(o), "s"(pc), "s"(lb) : "memory");
      }
    }
    w_job(b - 256, wg, wu, wd, ws, smem, [&]() {
      if (w == 0) {
        const unsigned long long e = __hip_atomic_load((const unsigned long long*)(ws + WS_EPO), __ATOMIC_RELAXED, __HIP_MEMORY_SCOPE_AGENT);
        const unsigned long long* hp = (const unsigned long long*)(ws + WS_HNT) + (b - 256);
        const unsigned long long t0 = __builtin_amdgcn_s_memrealtime();
        while (__hip_atomic_load(hp, __ATOMIC_RELAXED, __HIP_MEMORY_SCOPE_AGENT) != e + 1 && __builtin_amdgcn_s_memrealtime() - t0 < HINT_TICKS)
          __builtin_amdgcn_s_sleep(8);
      }
      __builtin_amdgcn_s_barrier();
      STAMP(1, 5);
    });
    STAMP(1, 4);
    return;
  }
  const int x = b & 7, bi = b >> 3, s0 = 64 * bi;
  const int ch0 = 64 * x + 2 * bi;
  const u32 sb = (u32)(size_t)(LDSP char*)smem;
  const int n = lane & 15, g = lane >> 4, qd = n >> 2, p = n & 3;
  const int d = 64 * x + 16 * w + n;
  const int srow = 8 * b + 2 * w;
  const __amdgpu_buffer_rsrc_t wr = ws_rsrc(ws);

  u32x2 epo, sh; u32x4 qa, qb, kp[2], kc[2];
  gld2_v_sc1<0>(epo, 0, ws + WS_EPO);
  gld_v<0>(qa, lane * 16, (const char*)q + (size_t)srow * 1024);
  gld_v<1024>(qb, lane * 16, (const char*)q + (size_t)srow * 1024);
  gld2_v<0>(sh, d * 8, shk);
  {
    const int r8 = lane >> 3, c8 = lane & 7;
    auto voff = [&](int instr) { const int f = ((r8 >> 1) & 1) | ((instr & 1) << 1); return (u32)((((c8 >> 1) ^ f) * 2 + (c8 & 1)) * 16); };
    const size_t col = (size_t)x * 128;
    int row = s0 - 8 + 8 * w + r8; row = row < 0 ? 0 : row;
    glds16<0>((u32)row * 1024 + voff(w), (const char*)k + col, sb + KA_TK + w * 1024);
    glds16<0>((u32)(s0 - 8 + 8 * (w + 4) + r8) * 1024 + voff(w), (const char*)k + col, sb + KA_TK + (w + 4) * 1024);
#pragma unroll
    for (int i = 0; i < 2; ++i) {
      const int in = w + 4 * i;
      glds16<0>((u32)(s0 + 8 * in + r8) * 1024 + voff(in), (const char*)v + col, sb + KA_TV + in * 1024);
      glds16<0>((u32)(s0 + 8 * in + r8) * 1024 + voff(in), (const char*)q + col, sb + KA_TQ + in * 1024);
    }
    if (w == 0) glds16<0>((u32)(s0 + 56 + r8) * 1024 + voff(8), (const char*)k + col, sb + KA_TK + 8 * 1024);
  }
#pragma unroll
  for (int cc = 0; cc < 2; ++cc) {
    const char* kr = (const char*)ssmk + (size_t)(ch0 + cc) * 4096;
    gld_v<0>(kp[cc], (u32)(t > 0 ? t - 1 : 0) * 16, kr);
    gld_v<0>(kc[cc], (u32)t * 16, kr);
  }
  const float dsk = blo(dskip[ch0 + (w >> 1)]);
  if (b == 0 && t < 16) *(u32*)(ws + WS_CNT + t * 128) = 0u;

  if (w == 0) wait_vm<11>(); else wait_vm<10>();
  asm volatile("" : "+v"(qa), "+v"(qb), "+v"(sh), "+v"(epo));
  u32x2 tag;
  { const unsigned long long e = (((unsigned long long)epo[1] << 32) | epo[0]) + 1ull; tag = u32x2{(u32)e, (u32)(e >> 32)}; }
  {
    float sa = 0.f, sbb = 0.f;
#pragma unroll
    for (int e = 0; e < 4; ++e) { sa += blo(qa[e]) + bhi(qa[e]); sbb += blo(qb[e]) + bhi(qb[e]); }
    const float ma = wave_sum(sa) * (1.f / D), mb = wave_sum(sbb) * (1.f / D);
    float va = 0.f, vb = 0.f;
#pragma unroll
    for (int e = 0; e < 4; ++e) {
      float u0 = blo(qa[e]) - ma, u1 = bhi(qa[e]) - ma, u2 = blo(qb[e]) - mb, u3 = bhi(qb[e]) - mb;
      va += u0 * u0 + u1 * u1; vb += u2 * u2 + u3 * u3;
    }
    va = wave_sum(va) * (1.f / D); vb = wave_sum(vb) * (1.f / D);
    const float ra = __builtin_amdgcn_rsqf(va + EPS), rb = __builtin_amdgcn_rsqf(vb + EPS);
    const u32x2 val = lane == 0 ? u32x2{__float_as_uint(ra), __float_as_uint(rb)} : u32x2{__float_as_uint(-ma * ra), __float_as_uint(-mb * rb)};
    const u32 off = lane == 0 ? (u32)WS_SA + srow * 4 : lane == 1 ? (u32)WS_SB + srow * 4 : 0xfffffff0u;
    __builtin_amdgcn_raw_buffer_store_b64(val, wr, off, 0, 16);
  }
  wait_vm<5>();
  barrier();
  STAMP(0, 1);
  {
    auto taddr = [&](int base, int tr) { const int f = ((tr >> 1) & 1) | ((tr >> 2) & 2); return sb + base + tr * 128 + ((w ^ f) * 32) + 8 * p; };
    const float a0 = blo(sh[0]), a1 = bhi(sh[0]), a2 = blo(sh[1]), a3 = bhi(sh[1]);
#pragma unroll
    for (int e = 0; e < 2; ++e) {
      const int rg = g + 4 * e;
      u32x2 km = tr_read(taddr(KA_TK, 8 * rg + 4 + qd));
      const u32x2 k0 = tr_read(taddr(KA_TK, 8 * rg + 8 + qd)), k1 = tr_read(taddr(KA_TK, 8 * rg + 12 + qd));
      const u32x2 v0 = tr_read(taddr(KA_TV, 8 * rg + qd)), v1 = tr_read(taddr(KA_TV, 8 * rg + 4 + qd));
      const u32x2 q0 = tr_read(taddr(KA_TQ, 8 * rg + qd)), q1 = tr_read(taddr(KA_TQ, 8 * rg + 4 + qd));
      if (s0 + rg == 0) km = u32x2{0, 0};
      const float kf[11] = {bhi(km[0]), blo(km[1]), bhi(km[1]), blo(k0[0]), bhi(k0[0]), blo(k0[1]), bhi(k0[1]), blo(k1[0]), bhi(k1[0]), blo(k1[1]), bhi(k1[1])};
      const float vf[8] = {blo(v0[0]), bhi(v0[0]), blo(v0[1]), bhi(v0[1]), blo(v1[0]), bhi(v1[0]), blo(v1[1]), bhi(v1[1])};
      float xo[8];
#pragma unroll
      for (int j = 0; j < 8; ++j) xo[j] = (kf[j + 3] * a0 + kf[j + 2] * a1 + kf[j + 1] * a2 + kf[j] * a3) * vf[j];
      const int r0 = s0 + 8 * rg;
      st16_wt(wr, (u32)WS_XR + (d * S + (2040 - r0)) * 2, u32x4{pk2(xo[7], xo[6]), pk2(xo[5], xo[4]), pk2(xo[3], xo[2]), pk2(xo[1], xo[0])});
      st16_wt(wr, (u32)WS_QT + (d * S + r0) * 2, cat(q0, q1));
    }
  }
  wait_vm<5>();
  asm volatile("" : "+v"(kp[0]), "+v"(kc[0]), "+v"(kp[1]), "+v"(kc[1]));
#pragma unroll
  for (int cc = 0; cc < 2; ++cc) {
    const u32 m = t > 0 ? 0xffffffffu : 0u;
    u32 wv[6] = {kp[cc][0] & m, kp[cc][1] & m, kp[cc][2] & m, kp[cc][3] & m, kc[cc][0], kc[cc][1]};
    u32 o1[5];
#pragma unroll
    for (int i = 0; i < 5; ++i) o1[i] = __builtin_amdgcn_alignbit(wv[i + 1], wv[i], 16);
    char* base = smem + cc * KA_KC + (t + 1) * 16;
    *(u32x4*)(base + 0 * KA_CS) = u32x4{o1[0], o1[1], o1[2], o1[3]};
    *(u32x4*)(base + 1 * KA_CS) = u32x4{wv[1], wv[2], wv[3], wv[4]};
    *(u32x4*)(base + 2 * KA_CS) = u32x4{o1[1], o1[2], o1[3], o1[4]};
    *(u32x4*)(base + 3 * KA_CS) = u32x4{wv[2], wv[3], wv[4], wv[5]};
    if (t == 255) {
      const u32 z = 0;
      const u32 e0 = __builtin_amdgcn_alignbit(kc[cc][1], kc[cc][0], 16), e1 = __builtin_amdgcn_alignbit(kc[cc][2], kc[cc][1], 16);
      const u32 e2 = __builtin_amdgcn_alignbit(kc[cc][3], kc[cc][2], 16), e3 = __builtin_amdgcn_alignbit(z, kc[cc][3], 16);
      *(u32x4*)(base + 16 + 0 * KA_CS) = u32x4{e0, e1, e2, e3};
      *(u32x4*)(base + 16 + 1 * KA_CS) = u32x4{kc[cc][1], kc[cc][2], kc[cc][3], z};
      *(u32x4*)(base + 16 + 2 * KA_CS) = u32x4{e1, e2, e3, z};
      *(u32x4*)(base + 16 + 3 * KA_CS) = u32x4{kc[cc][2], kc[cc][3], z, z};
    }
  }
  if (t < 8) *(u32x4*)(smem + (t >> 2) * KA_KC + (t & 3) * KA_CS) = u32x4{0, 0, 0, 0};
  asm volatile("s_waitcnt vmcnt(0) lgkmcnt(0)\n\ts_barrier" ::: "memory");
  STAMP(0, 2);
  if (t == 0) __builtin_amdgcn_raw_buffer_store_b64(tag, wr, (u32)WS_FLG + (x * 32 + bi) * 8, 0, 16);
  if (t >= 64 && t < 64 + 60) { int i = t - 64; *(u32x4*)(smem + KA_XR + (i / 30) * 4608 + 4096 + (i % 30) * 16) = u32x4{0, 0, 0, 0}; }
  if (w == 0) {
    const unsigned long long* fp = (const unsigned long long*)(ws + WS_FLG) + x * 32 + (lane & 31);
    const unsigned long long want = ((unsigned long long)tag[1] << 32) | tag[0];
    for (int spins = 0; spins < SPIN_MAX; ++spins) {
      if (__all(__hip_atomic_load(fp, __ATOMIC_RELAXED, __HIP_MEMORY_SCOPE_AGENT) == want)) break;
      __builtin_amdgcn_s_sleep(1);
    }
  }
  asm volatile("s_waitcnt vmcnt(0)\n\ts_barrier" ::: "memory");
  STAMP(0, 3);

  const int c = w >> 1, half = w & 1;
  u32x2 qv[4]; u32x4 sa[4], sbv[4], fl[2];
  const u32 eo = (u32)(1024 * half + 16 * n + 4 * g);
  {
    u32x4 x0, x1;
    gld_v_sc1<0>(x0, t * 16, ws + WS_XR + (size_t)ch0 * 4096);
    gld_v_sc1<0>(x1, t * 16, ws + WS_XR + (size_t)(ch0 + 1) * 4096);
    const char* qTc = ws + WS_QT + (size_t)(ch0 + c) * 4096;
    gld2_v_sc1<0>(qv[0], eo * 2, qTc); gld2_v_sc1<512>(qv[1], eo * 2, qTc); gld2_v_sc1<1024>(qv[2], eo * 2, qTc); gld2_v_sc1<1536>(qv[3], eo * 2, qTc);
    wait_vm<4>();
    asm volatile("" : "+v"(x0), "+v"(x1));
    *(u32x4*)(smem + KA_XR + t * 16) = x0;
    *(u32x4*)(smem + KA_XR + 4608 + t * 16) = x1;
  }
  asm volatile("s_waitcnt lgkmcnt(0)\n\ts_barrier" ::: "memory");
  if (t == 0) __builtin_amdgcn_raw_buffer_store_b64(tag, wr, (u32)WS_HNT + b * 8, 0, 16);
  STAMP(1, 1);

  const u32 aa = sb + c * KA_KC + (n & 3) * KA_CS + 16 * g + 2 * (n & 12) + half * 256;
  const u32 ba = sb + KA_XR + c * 4608 + 480 - 32 * n + 16 * g + half * 256;
  f32x4 acc[8];
  u32x2 Alo[2][8], Ahi[2][8];
  u32x4 B[2][8];
  auto rd = [&](auto C0, auto R) {
    constexpr int c0 = C0, r = R, buf = c0 & 1;
    if constexpr (r >= 2 && r < 10) {
      constexpr int u = r - 2;
      ds_b128<512 * (7 - u) + 64 * c0>(B[buf][u], ba);
    } else {
      constexpr int ar = r < 2 ? r : r - 8, i = ar >> 1;
      if constexpr ((ar & 1) == 0) ds_b64<512 * i + 64 * c0>(Alo[buf][i], aa);
      else ds_b64<512 * i + 64 * c0 + 8>(Ahi[buf][i], aa);
    }
  };
  auto ldS = [&](auto E) {
    constexpr int e = E, i = e >> 1;
    if constexpr ((e & 1) == 0) gld_v_sc1<i * 1024>(sa[i], eo * 4, ws + WS_SA);
    else gld_v_sc1<i * 1024>(sbv[i], eo * 4, ws + WS_SB);
  };
  static_for<24>([&](auto R) { rd(std::integral_constant<int, 0>{}, R); });
  static_for<4>([&](auto C0) {
    constexpr int c0 = C0, buf = c0 & 1;
    if constexpr (c0 == 2) {
      wait_vm<0>();
      asm volatile("" : "+v"(fl[0]), "+v"(fl[1]));
      if (!__all(tags_ok(fl[0], tag) && tags_ok(fl[1], tag))) {
        for (int spins = 0; spins < SPIN_MAX; ++spins) {
          gld_v_sc1<0>(fl[0], lane * 32, ws + WS_FLG); gld_v_sc1<16>(fl[1], lane * 32, ws + WS_FLG);
          wait_vm<0>();
          asm volatile("" : "+v"(fl[0]), "+v"(fl[1]));
          if (__all(tags_ok(fl[0], tag) && tags_ok(fl[1], tag))) break;
          __builtin_amdgcn_s_sleep(1);
        }
      }
      if (b == 0 && t == 0) __builtin_amdgcn_raw_buffer_store_b64(tag, wr, (u32)WS_EPO, 0, 16);
    }
    u32x4 A[8];
    static_for<36>([&](auto M) {
      constexpr int m = M;
      constexpr int c1 = m < 8 ? 0 : m < 15 ? 1 : m < 21 ? 2 : m < 26 ? 3 : m < 30 ? 4 : m < 33 ? 5 : m < 35 ? 6 : 7;
      constexpr int first = c1 * 8 - c1 * (c1 - 1) / 2;
      constexpr int u = m - first;
      if constexpr (m == 0) {
        if constexpr (c0 == 0) wait_lgkm<14>(); else wait_lgkm<0>();
        pin(Alo[buf][0]); pin(Ahi[buf][0]);
#pragma unroll
        for (int i = 0; i < 8; ++i) pin(B[buf][i]);
        A[0] = cat(Alo[buf][0], Ahi[buf][0]);
      }
      if constexpr (m == 8) {
        if constexpr (c0 == 0) wait_lgkm<8>();
#pragma unroll
        for (int i = 1; i < 8; ++i) { pin(Alo[buf][i]); pin(Ahi[buf][i]); A[i] = cat(Alo[buf][i], Ahi[buf][i]); }
      }
      mfma_vv<(c0 == 0 && c1 == 0)>(acc[c1 + u], A[c1], B[buf][u]);
      if constexpr (c0 < 3 && m < 24) rd(std::integral_constant<int, c0 + 1>{}, M);
      if constexpr (c0 == 1 && m == 30) gld_v_sc1<0>(fl[0], lane * 32, ws + WS_FLG);
      if constexpr (c0 == 1 && m == 32) gld_v_sc1<16>(fl[1], lane * 32, ws + WS_FLG);
      if constexpr (c0 == 2 && m >= 2 && m < 34 && (m & 3) == 2) ldS(std::integral_constant<int, (m - 2) / 4>{});
    });
  });
  mfma_fence(acc[0], acc[1], acc[2], acc[3]);
  after_fence(acc[4], acc[5], acc[6], acc[7]);
  STAMP(1, 2);
  f32x4 fin[4];
  {
    f32x4* red = (f32x4*)(smem + KA_RED);
    if (half) {
#pragma unroll
      for (int i = 0; i < 4; ++i) { red[(w * 4 + i) * 64 + lane] = acc[i]; fin[i] = acc[4 + i]; }
      asm volatile("; half 1" ::: "memory");
    } else {
#pragma unroll
      for (int i = 0; i < 4; ++i) { red[(w * 4 + i) * 64 + lane] = acc[4 + i]; fin[i] = acc[i]; }
      asm volatile("; half 0" ::: "memory");
    }
    asm volatile("s_waitcnt lgkmcnt(0)\n\ts_barrier" ::: "memory");
#pragma unroll
    for (int i = 0; i < 4; ++i) fin[i] += red[((w ^ 1) * 4 + i) * 64 + lane];
  }
  wait_vm<0>();
#pragma unroll
  for (int i = 0; i < 4; ++i) asm volatile("" : "+v"(qv[i]), "+v"(sa[i]), "+v"(sbv[i]));
  const f32x2 dd = {dsk, dsk};
  u32x2 hv[4];
#pragma unroll
  for (int i = 0; i < 4; ++i) {
    const int s = 256 * (4 * half + i) + 16 * n + 4 * g;
    const u32x2 xq = *(LDSP const u32x2*)(size_t)(sb + KA_XR + c * 4608 + 2 * (2044 - s));
    const f32x2 x0 = {bhi(xq[1]), blo(xq[1])}, x1 = {bhi(xq[0]), blo(xq[0])};
    const f32x2 q0 = {blo(qv[i][0]), bhi(qv[i][0])}, q1 = {blo(qv[i][1]), bhi(qv[i][1])};
    const f32x2 z0 = x0 * dd + f32x2{fin[i][0], fin[i][1]}, z1 = x1 * dd + f32x2{fin[i][2], fin[i][3]};
    const f32x4 a4 = __builtin_bit_cast(f32x4, sa[i]), b4 = __builtin_bit_cast(f32x4, sbv[i]);
    const f32x2 t0 = q0 * f32x2{a4[0], a4[1]} + f32x2{b4[0], b4[1]};
    const f32x2 t1 = q1 * f32x2{a4[2], a4[3]} + f32x2{b4[2], b4[3]};
    const f32x2 h0 = t0 * z0 + q0, h1 = t1 * z1 + q1;
    hv[i] = u32x2{pk2(h0[0], h0[1]), pk2(h1[0], h1[1])};
  }
#pragma unroll
  for (int i = 0; i < 4; i += 2) {
    auto a = __builtin_amdgcn_permlane16_swap(hv[i][0], hv[i + 1][0], false, false);
    auto bb = __builtin_amdgcn_permlane16_swap(hv[i][1], hv[i + 1][1], false, false);
    const int s = 256 * (4 * half + i + (g & 1)) + 16 * n + 4 * (g & 2);
    st16_wt(wr, (u32)WS_HT + ((ch0 + c) * S + s) * 2, u32x4{a[0], bb[0], a[1], bb[1]});
  }
  STAMP(1, 3);
}

constexpr int KB_NL = 39;
constexpr int KB_ST = KB_NL * 4096;
constexpr int KB_LDS = KB_ST + 1024;
constexpr int KB_RING = 8;

constexpr int p2_upto(int ks) { return ks < 0 ? 2 : ks <= 3 ? 2 * ks + 4 : (ks + 7 > 15 ? 15 : ks + 7); }
constexpr int p2_extra(int ks) { return ks == 9 ? 3 : ks >= 10 ? 4 : 0; }
constexpr int p2_extra_first(int ks) { int c = 0; for (int s = 0; s < ks; ++s) c += p2_extra(s); return c; }
constexpr int P2_NEXTRA = 23;
constexpr int p2_extra_real(int ks) { int f = p2_extra_first(ks), n = p2_extra(ks); return f >= P2_NEXTRA ? 0 : (f + n > P2_NEXTRA ? P2_NEXTRA - f : n); }
constexpr int p2_issue_step(int t) { for (int s = -1; s < KS1; ++s) if (p2_upto(s) >= t) return s; return KS1; }
constexpr int p2_younger(int t, int ks, int ops) {
  int c = (p2_upto(ks - 1) - t) * ops;
  for (int s = p2_issue_step(t); s < ks; ++s) if (s >= 0) c += p2_extra_real(s);
  return c;
}

struct KbCtx {
  u32 sb, gv, lv, lb0, lwd, csv;
  const char *hb0, *hb1, *w0, *w1, *w2, *cs0, *cs1, *cs2, *wd, *wdr;
};

template <int GRAM, class Setup>
DI void kb_phase2(const KbCtx& c, f32x4 (&acc)[3][8], f32x4 (&rsum)[2], f32x4 (&csp)[3], u32x4 (&Wr)[4][4], u32 (&ra)[8], Setup&& setup, char* ws_stamp) {
  constexpr int OPS = GRAM ? 4 : 5;
  u32x4 W[KB_RING][3];
  u32x2 Bl[2][8], Bh[2][8];
  auto issue = [&](auto KS, auto O) {
    constexpr int ks = KS, o = O;
    if constexpr (o == 0) glds16<ks * 8192>(c.gv, c.hb0 + (size_t)ks * 131072, c.lb0);
    if constexpr (o == 1) gld_a<(ks & 3) * 1024>(W[ks % KB_RING][0], c.lv, c.w0 + (ks >> 2) * 4096);
    if constexpr (o == 2) gld_a<(ks & 3) * 1024>(W[ks % KB_RING][1], c.lv, c.w1 + (ks >> 2) * 4096);
    if constexpr (o == 3 && !GRAM) gld_a<(ks & 3) * 1024>(W[ks % KB_RING][2], c.lv, c.w2 + (ks >> 2) * 4096);
    if constexpr (o == 4) glds16<ks * 8192 + 1024>(c.gv, c.hb1 + (size_t)ks * 131072, c.lb0);
  };
  auto extra = [&](auto E) {
    constexpr int e = E;
    if constexpr (e < 7) glds16<(32 + e) * 4096>(c.lv, c.wd + (size_t)(32 + e) * 4096, c.lwd);
    else if constexpr (e < P2_NEXTRA) gld_a<((e - 7) & 3) * 1024>(Wr[(e - 7) >> 2][(e - 7) & 3], c.lv, c.wdr + ((e - 7) >> 2) * 4096);
  };
  auto rdB = [&](auto KS, auto R) {
    constexpr int ks = KS, r = R, i = r >> 1;
    if constexpr ((r & 1) == 0) ds_tr<(ks & 7) * 8192>(Bl[ks & 1][i], ra[i] + (ks >= 8 ? 65536 : 0));
    else ds_tr<(ks & 7) * 8192 + 1024>(Bh[ks & 1][i], ra[i] + (ks >= 8 ? 65536 : 0));
  };
  u32x4 c0, c1, c2;
  static_for<5>([&](auto O) { issue(std::integral_constant<int, 0>{}, O); });
  gld_v<0>(c0, c.csv, c.cs0); gld_v<0>(c1, c.csv, c.cs1); gld_v<0>(c2, c.csv, c.cs2);
  static_for<p2_upto(-1)>([&](auto KS) { static_for<5>([&](auto O) { issue(std::integral_constant<int, KS + 1>{}, O); }); });
  setup();
  u32x4 ones = {0x3f803f80u, 0x3f803f80u, 0x3f803f80u, 0x3f803f80u};
  pin(ones);
  wait_vm<p2_upto(-1) * OPS>();
  barrier();
  static_for<16>([&](auto R) { rdB(std::integral_constant<int, 0>{}, R); });
  asm volatile("" : "+v"(c0), "+v"(c1), "+v"(c2));
  float cv[12];
#pragma unroll
  for (int e = 0; e < 4; ++e) { cv[e] = __uint_as_float(c0[e]); cv[4 + e] = __uint_as_float(c1[e]); cv[8 + e] = __uint_as_float(c2[e]); }
  static_for<KS1>([&](auto KS) {
    constexpr int ks = KS, buf = ks & 1, slot = ks % KB_RING;
    constexpr int nmain = (p2_upto(ks) - p2_upto(ks - 1)) * 5, next = p2_extra_real(ks);
    wait_lgkm<0>();
    u32x4 B[8];
#pragma unroll
    for (int i = 0; i < 8; ++i) { pin(Bl[buf][i]); pin(Bh[buf][i]); }
#pragma unroll
    for (int i = 0; i < 8; ++i) B[i] = cat(Bl[buf][i], Bh[buf][i]);
    pin_a(W[slot][0]); pin_a(W[slot][1]);
    if constexpr (!GRAM) pin_a(W[slot][2]);
    static_for<26>([&](auto M) {
      constexpr int m = M, i = m / 3, u = m % 3;
      if constexpr (m >= 24) mfma_vv<ks == 0>(rsum[m - 24], ones, B[m - 24]);
      else if constexpr (u < 2 || !GRAM) mfma_av<ks == 0>(acc[u][i], W[slot][u], B[i]);
      else mfma_vv<ks == 0>(acc[2][i], B[i], B[i]);
      if constexpr (m == 1 && ks + 1 < KS1) { wait_vm<p2_younger(ks + 1, ks, OPS)>(); barrier(); }
      if constexpr (m >= 2 && m < 18 && ks + 1 < KS1) rdB(std::integral_constant<int, ks + 1>{}, std::integral_constant<int, m - 2>{});
      if constexpr (ks >= 9 && ks < 13 && m >= 2 && m < 14) dpp_stage<ks - 9>(cv[m - 2]);
      constexpr int v = m - (26 - nmain - next);
      if constexpr (v >= 0 && v < nmain) issue(std::integral_constant<int, p2_upto(ks - 1) + 1 + v / 5>{}, std::integral_constant<int, v % 5>{});
      if constexpr (v >= nmain) extra(std::integral_constant<int, p2_extra_first(ks) + v - nmain>{});
    });
  });
  mfma_fence(acc[0][0], acc[1][0], rsum[0], rsum[1]);
#pragma unroll
  for (int i = 0; i < 8; ++i) after_fence(acc[0][i], acc[1][i], acc[2][i], rsum[i & 1]);
#pragma unroll
  for (int u = 0; u < 3; ++u) csp[u] = f32x4{cv[4 * u], cv[4 * u + 1], cv[4 * u + 2], cv[4 * u + 3]};
}

__global__ __launch_bounds__(256) void kb_mlp(char* __restrict__ ws, u16* __restrict__ out) {
  extern __shared__ __attribute__((aligned(16))) char smem[];
  const int t = threadIdx.x, lane = t & 63;
  const int w = __builtin_amdgcn_readfirstlane(t >> 6);
  const int b = blockIdx.x, xcd = b & 7, bi = b >> 3;
#define KB_MAP 1
  const int rt = KB_MAP == 0 ? 2 * xcd + (bi & 1) : (xcd >> 1) * 4 + (bi & 3);
  const int j = KB_MAP == 0 ? bi >> 1 : (xcd & 1) * 8 + (bi >> 2);
  const int half = KB_MAP == 0 ? j & 1 : j >> 3, dt = KB_MAP == 0 ? j >> 1 : j & 7;
  const int U0 = slice_u0(j), nu = slice_nu(j);
  const int rem = nu - 8;
  const int nw = w < rem ? 3 : 2;
  const int uw = U0 + (w < rem ? 3 * w : 3 * rem + 2 * (w - rem));
  const int u2 = uw + (nw == 3 ? 2 : 0);
  const int n = lane & 15, g = lane >> 4;
  const int s0 = 128 * rt;
  char* ws_stamp = ws; (void)ws_stamp;
  STAMP(2, 0);
  auto sf = [&](int i) { return ((w + (i >> 1)) & 3) + 4 * (half ^ (i & 1)); };

  KbCtx c;
  c.sb = (u32)(size_t)(LDSP char*)smem;
  c.lv = lane * 16;
  c.gv = (u32)(g * 4096 + (((n >> 1) ^ (g | ((w & 1) << 2))) * 32 + (n & 1) * 16));
  c.hb0 = ws + WS_HT + (size_t)s0 * 2 + (size_t)(8 * w) * 4096;
  c.hb1 = c.hb0 + 4 * 4096;
  c.lb0 = c.sb + 8 * w * 256;
  c.w0 = ws + WS_W1 + (size_t)uw * KS1 * 1024;
  c.w1 = c.w0 + KS1 * 1024;
  c.w2 = ws + WS_W1 + (size_t)u2 * KS1 * 1024;
  c.cs0 = ws + WS_CS + (size_t)uw * 64;
  c.cs1 = c.cs0 + 64;
  c.cs2 = ws + WS_CS + (size_t)u2 * 64;
  c.csv = n * (NU * 64) + g * 16;
  c.wdr = ws + WS_WD + (size_t)dt * KS2 * 4096 + (size_t)KB_NL * 4096;
  c.wd = ws + WS_WD + (size_t)dt * KS2 * 4096 + (size_t)w * 1024;
  c.lwd = c.sb + 1024 * w;
  u32 ra[8], hoff[6];
  auto setup = [&]() {
    const int qd = n >> 2, p = n & 3;
    const int frow = qd | ((g & 1) << 2);
#pragma unroll
    for (int i = 0; i < 8; ++i) { ra[i] = c.sb + (8 * g + qd) * 256 + ((sf(i) ^ frow) * 32) + 8 * p; asm volatile("" : "+v"(ra[i])); }
#pragma unroll
    for (int tt = 0; tt < 6; ++tt) {
      const int q = 4 * tt + g, i = (q * 11) >> 5, u = q - 3 * i, U = uw + u;
      const u32 o = (u32)(((8 * rt + sf(i)) * KS2 + (U >> 2)) * 1024 + ((U & 3) * 16 + n) * 16);
      hoff[tt] = u < nw ? o : 0xfffffff0u;
      asm volatile("" : "+v"(hoff[tt]));
    }
  };
  f32x4 acc[3][8], cs[3], rsum[2];
  u32x4 Wr[4][4];
  if (w == 3) kb_phase2<1>(c, acc, rsum, cs, Wr, ra, setup, ws_stamp);
  else kb_phase2<0>(c, acc, rsum, cs, Wr, ra, setup, ws_stamp);
  STAMP(2, 2);

  u32x2 res[4];
  {
    const u32 rb = ra[0] + dt * 16384;
    ds_tr<0>(res[0], rb); ds_tr<1024>(res[1], rb); ds_tr<8192>(res[2], rb); ds_tr<9216>(res[3], rb);
  }
  float* stl = (float*)(smem + KB_ST);
  if (g == 0) {
#pragma unroll
    for (int i = 0; i < 2; ++i) stl[2 * (16 * sf(i) + n)] = rsum[i][0];
  }
  if (w == 3 && g == (n >> 2)) {
#pragma unroll
    for (int i = 0; i < 8; ++i) {
      f32x4 a = acc[2][i];
      float dg = (n & 2) ? ((n & 1) ? a[3] : a[2]) : ((n & 1) ? a[1] : a[0]);
      stl[2 * (16 * sf(i) + n) + 1] = dg;
    }
  }
  asm volatile("s_waitcnt lgkmcnt(0)\n\ts_barrier" : "+v"(res[0]), "+v"(res[1]), "+v"(res[2]), "+v"(res[3]) :: "memory");
  STAMP(2, 3);
  const __amdgpu_buffer_rsrc_t hrs = __builtin_amdgcn_make_buffer_rsrc(ws + WS_H, 0, (S / 16) * KS2 * 1024, 0x00020000);
  f32x2 sv[8];
#pragma unroll
  for (int i = 0; i < 8; ++i) sv[i] = *(const f32x2*)(stl + 2 * (16 * sf(i) + n));
  u32 hv[24];
  static_for<8>([&](auto I) {
    constexpr int i = I;
    auto fill = [&](auto E) { constexpr int wk = 4 * i + E; glds16<wk * 4096>(c.lv, c.wd + (size_t)wk * 4096, c.lwd); };
    fill(std::integral_constant<int, 3>{});
    const float mean = sv[i][0] * (1.f / D);
    const float var = fmaxf(sv[i][1] * (1.f / D) - mean * mean, 0.f) + EPS;
    const float rstd = __builtin_amdgcn_rsqf(var);
    const float rinv = var * rstd;
    const float ce = -1.4426950408889634f * rstd;
    const f32x2 nm = {-mean, -mean}, ce2 = {ce, ce}, ri2 = {rinv, rinv};
    static_for<3>([&](auto U_) {
      constexpr int u = U_;
      pin4(acc[u][i]);
      fill(U_);
      const f32x4 a = acc[u][i];
      const f32x2 tg = (f32x2{a[0], a[1]} + nm * f32x2{cs[u][0], cs[u][1]}) * ce2;
      const f32x2 tu = f32x2{a[2], a[3]} + nm * f32x2{cs[u][2], cs[u][3]};
      const f32x2 e = {__builtin_amdgcn_exp2f(tg[0]), __builtin_amdgcn_exp2f(tg[1])};
      const f32x2 den = e * ri2 + ri2;
      const f32x2 o = tu * f32x2{__builtin_amdgcn_rcpf(den[0]), __builtin_amdgcn_rcpf(den[1])};
      hv[3 * i + u] = pk2(o[0], o[1]);
    });
    static_for<6>([&](auto T) {
      constexpr int tt = T;
      if constexpr ((4 * tt + 3) / 3 == i) {
        auto s02 = __builtin_amdgcn_permlane32_swap(hv[4 * tt], hv[4 * tt + 2], false, false);
        auto s13 = __builtin_amdgcn_permlane32_swap(hv[4 * tt + 1], hv[4 * tt + 3], false, false);
        auto t01 = __builtin_amdgcn_permlane16_swap(s02[0], s13[0], false, false);
        auto t23 = __builtin_amdgcn_permlane16_swap(s02[1], s13[1], false, false);
        __builtin_amdgcn_raw_buffer_store_b128(u32x4{t01[0], t01[1], t23[0], t23[1]}, hrs, hoff[tt], 0, 16);
      }
    });
  });
  STAMP(2, 4);
  u32* cnt = (u32*)(ws + WS_CNT + rt * 128);
  asm volatile("s_waitcnt vmcnt(0)\n\ts_barrier" ::: "memory");
  STAMP(2, 5);
  if (w == 0) {
    if (lane == 0) __hip_atomic_fetch_add(cnt, 1u, __ATOMIC_RELAXED, __HIP_MEMORY_SCOPE_AGENT);
    for (int spins = 0; spins < (1 << 22); ++spins) {
      const u32 v = __builtin_amdgcn_readfirstlane(__hip_atomic_load(cnt, __ATOMIC_RELAXED, __HIP_MEMORY_SCOPE_AGENT));
      if (v >= 16u) break;
      __builtin_amdgcn_s_sleep(1);
    }
  }
  asm volatile("s_waitcnt vmcnt(0)\n\ts_barrier" ::: "memory");
  STAMP(2, 6);

  const char* hb = ws + WS_H + (size_t)(8 * rt + 4 * half + w) * KS2 * 1024;
  u32x4 Hf[KS2];
  constexpr int P3_AHEAD = 12;
  auto ldH = [&](auto KS) { constexpr int ks = KS; if constexpr (ks < KS2) gld_a_sc1<(ks & 3) * 1024>(Hf[ks], c.lv, hb + (ks >> 2) * 4096); };
  static_for<P3_AHEAD>([&](auto KS) { ldH(KS); });
  STAMP(3, 0);
  f32x4 o4[4];
  u32x4 Af[3][4];
  const u32 la0 = c.sb + lane * 16, la1 = la0 + 65536, la2 = la0 + 131072;
  auto rdA = [&](auto KS, auto DF) {
    constexpr int ks = KS, df = DF;
    if constexpr (ks < KB_NL) ds_b128<(ks & 15) * 4096 + df * 1024>(Af[ks % 3][df], ks < 16 ? la0 : ks < 32 ? la1 : la2);
  };
  static_for<4>([&](auto DF) { rdA(std::integral_constant<int, 0>{}, DF); });
  static_for<4>([&](auto DF) { rdA(std::integral_constant<int, 1>{}, DF); });
  static_for<KS2>([&](auto KS) {
    constexpr int ks = KS;
    wait_vm<(ks + P3_AHEAD <= KS2 ? P3_AHEAD - 1 : KS2 - 1 - ks)>();
    if constexpr (ks + 1 < KB_NL) wait_lgkm<4>(); else wait_lgkm<0>();
    pin_a(Hf[ks]);
    static_for<4>([&](auto DF) {
      constexpr int df = DF;
      if constexpr (ks < KB_NL) { pin(Af[ks % 3][df]); mfma_va<ks == 0>(o4[df], Af[ks % 3][df], Hf[ks]); }
      else { pin_a(Wr[ks - KB_NL][df]); mfma_aa(o4[df], Wr[ks - KB_NL][df], Hf[ks]); }
      rdA(std::integral_constant<int, ks + 2>{}, DF);
      if constexpr (df == 1 && ks + P3_AHEAD < KS2) ldH(std::integral_constant<int, ks + P3_AHEAD>{});
    });
  });
  mfma_fence(o4[0], o4[1], o4[2], o4[3]);
  u32x4 o0, o1;
#pragma unroll
  for (int df = 0; df < 2; ++df) {
    o0[2 * df] = pk2(o4[df][0] + blo(res[df][0]), o4[df][1] + bhi(res[df][0]));
    o0[2 * df + 1] = pk2(o4[df][2] + blo(res[df][1]), o4[df][3] + bhi(res[df][1]));
    o1[2 * df] = pk2(o4[2 + df][0] + blo(res[2 + df][0]), o4[2 + df][1] + bhi(res[2 + df][0]));
    o1[2 * df + 1] = pk2(o4[2 + df][2] + blo(res[2 + df][1]), o4[2 + df][3] + bhi(res[2 + df][1]));
  }
  const __amdgpu_buffer_rsrc_t ors = __builtin_amdgcn_make_buffer_rsrc(out, 0, S * D * 2, 0x00020000);
  const u32 ooff = (u32)((s0 + 64 * half + 16 * w + n) * D + 64 * dt + 8 * g) * 2;
  __builtin_amdgcn_raw_buffer_store_b128(o0, ors, ooff, 0, 16);
  __builtin_amdgcn_raw_buffer_store_b128(o1, ors, ooff + 64, 0, 16);
  {
    asm volatile("s_waitcnt vmcnt(0)" ::: "memory");
    const unsigned long long t0_ = __hip_atomic_load((const unsigned long long*)(ws + WS_T0), __ATOMIC_RELAXED, __HIP_MEMORY_SCOPE_AGENT);
    const unsigned long long own_ = __builtin_amdgcn_s_memrealtime();
    while (__builtin_amdgcn_s_memrealtime() - t0_ < 3500ull && __builtin_amdgcn_s_memrealtime() - own_ < 3500ull) __builtin_amdgcn_s_sleep(1);
  }
  asm volatile("kb_code_end:");
  STAMP(2, 7);
}

extern "C" void kernel_launch(void* const* d_in, const int* in_sizes, int n_in, void* d_out, int out_size, void* d_ws,
                              size_t ws_size, hipStream_t stream) {
  const u16 *q = (const u16*)d_in[0], *k = (const u16*)d_in[1], *v = (const u16*)d_in[2], *shk = (const u16*)d_in[3];
  const u16 *ssmk = (const u16*)d_in[4], *dskip = (const u16*)d_in[5];
  const u16 *wg = (const u16*)d_in[7], *wu = (const u16*)d_in[8], *wd = (const u16*)d_in[9];
  char* ws = (char*)d_ws;
  (void)hipFuncSetAttribute((const void*)ka_conv, hipFuncAttributeMaxDynamicSharedMemorySize, KA_LDS);
  (void)hipFuncSetAttribute((const void*)kb_mlp, hipFuncAttributeMaxDynamicSharedMemorySize, KB_LDS);
  ka_conv<<<512, 256, KA_LDS, stream>>>(q, k, v, shk, ssmk, dskip, wg, wu, wd, ws);
  kb_mlp<<<256, 256, KB_LDS, stream>>>(ws, (u16*)d_out);
}
```

```cpp
#include <hip/hip_runtime.h>
#include <stdint.h>
#include <utility>

typedef unsigned short u16;
typedef unsigned u32;
typedef __attribute__((ext_vector_type(2))) u32 u32x2;
typedef __attribute__((ext_vector_type(4))) u32 u32x4;
typedef __attribute__((ext_vector_type(2))) float f32x2;
typedef __attribute__((ext_vector_type(4))) float f32x4;
typedef __attribute__((ext_vector_type(2))) __bf16 bf16x2;
typedef __attribute__((ext_vector_type(8))) __bf16 bf16x8;
typedef __attribute__((ext_vector_type(4))) short s16x4;
#define LDSP __attribute__((address_space(3)))
#define DI __device__ __forceinline__

constexpr int S = 2048, D = 512, F = 1376;
constexpr int NU = F / 8;
constexpr int KS1 = D / 32;
constexpr int KS2 = F / 32;
constexpr float EPS = 1e-5f;

constexpr size_t WS_XR = 0;
constexpr size_t WS_QT = WS_XR + (size_t)D * S * 2;
constexpr size_t WS_HT = WS_QT + (size_t)D * S * 2;
constexpr size_t WS_W1 = WS_HT + (size_t)D * S * 2;
constexpr size_t WS_WD = WS_W1 + (size_t)NU * KS1 * 1024;
constexpr size_t WS_H = WS_WD + (size_t)8 * KS2 * 4 * 1024;
constexpr size_t WS_CS = WS_H + (size_t)(S / 16) * KS2 * 1024;
constexpr size_t WS_SA = WS_CS + (size_t)KS1 * NU * 16 * 4;
constexpr size_t WS_SB = WS_SA + (size_t)S * 4;
constexpr size_t WS_CNT = WS_SB + (size_t)S * 4;
constexpr size_t WS_FLA = WS_CNT + 16 * 128;
constexpr size_t WS_FLG = WS_FLA + 256 * 8;
constexpr size_t WS_HNT = WS_FLG + 256 * 8;
constexpr size_t WS_T0 = WS_HNT + 256 * 8 + 256;
constexpr size_t WS_EPO = WS_HNT + 256 * 8;

template <int... Is, class Fn> DI void static_for_(std::integer_sequence<int, Is...>, Fn&& f) { (f(std::integral_constant<int, Is>{}), ...); }
template <int N, class Fn> DI void static_for(Fn&& f) { static_for_(std::make_integer_sequence<int, N>{}, f); }

DI float blo(u32 u) { return __uint_as_float(u << 16); }
DI float bhi(u32 u) { return __uint_as_float(u & 0xffff0000u); }
DI u32 pk2(float a, float b) { f32x2 t = {a, b}; return __builtin_bit_cast(u32, __builtin_convertvector(t, bf16x2)); }
DI f32x4 mfma16(u32x4 a, u32x4 b, f32x4 c) {
  return __builtin_amdgcn_mfma_f32_16x16x32_bf16(__builtin_bit_cast(bf16x8, a), __builtin_bit_cast(bf16x8, b), c, 0, 0, 0);
}
DI u32x2 tr_read(u32 lds_byte) {
  return __builtin_bit_cast(u32x2, __builtin_amdgcn_ds_read_tr16_b64_v4i16((LDSP s16x4*)(size_t)lds_byte));
}
DI u32x4 tr_frag(u32 a0, u32 a1) { u32x2 x = tr_read(a0), y = tr_read(a1); return u32x4{x[0], x[1], y[0], y[1]}; }
DI u32x4 cat(u32x2 x, u32x2 y) { return __builtin_shufflevector(x, y, 0, 1, 2, 3); }
template <int CTRL> DI float dpp_add(float v) {
  return v + __builtin_bit_cast(float, __builtin_amdgcn_update_dpp(0, __builtin_bit_cast(int, v), CTRL, 0xf, 0xf, true));
}
DI float sum_rows(float v) {
  u32 u = __float_as_uint(v);
  auto a = __builtin_amdgcn_permlane16_swap(u, u, false, false);
  v = __uint_as_float(a[0]) + __uint_as_float(a[1]);
  u = __float_as_uint(v);
  auto b = __builtin_amdgcn_permlane32_swap(u, u, false, false);
  return __uint_as_float(b[0]) + __uint_as_float(b[1]);
}
DI float wave_sum(float v) {
  v = dpp_add<0xb1>(v);
  v = dpp_add<0x4e>(v);
  v = dpp_add<0x124>(v);
  v = dpp_add<0x128>(v);
  return sum_rows(v);
}
DI __amdgpu_buffer_rsrc_t ws_rsrc(char* ws) { return __builtin_amdgcn_make_buffer_rsrc(ws, 0, 0x04000000, 0x00020000); }
DI void st16_wt(__amdgpu_buffer_rsrc_t r, u32 off, u32x4 v) { __builtin_amdgcn_raw_buffer_store_b128(v, r, off, 0, 16); }
DI int slice_u0(int j) { return j < 12 ? 11 * j : 132 + 10 * (j - 12); }
DI int slice_nu(int j) { return j < 12 ? 11 : 10; }

template <bool FIRST = false> DI void mfma_vv(f32x4& acc, const u32x4& a, const u32x4& b) {
  if constexpr (FIRST) asm volatile("v_mfma_f32_16x16x32_bf16 %0, %1, %2, 0" : "=&v"(acc) : "v"(a), "v"(b));
  else asm volatile("v_mfma_f32_16x16x32_bf16 %0, %1, %2, %0" : "+v"(acc) : "v"(a), "v"(b));
}
template <bool FIRST = false> DI void mfma_av(f32x4& acc, const u32x4& a, const u32x4& b) {
  if constexpr (FIRST) asm volatile("v_mfma_f32_16x16x32_bf16 %0, %1, %2, 0" : "=&v"(acc) : "a"(a), "v"(b));
  else asm volatile("v_mfma_f32_16x16x32_bf16 %0, %1, %2, %0" : "+v"(acc) : "a"(a), "v"(b));
}
template <int OFF> DI void ds_b64(u32x2& d, u32 addr) { asm volatile("ds_read_b64 %0, %1 offset:%c2" : "=v"(d) : "v"(addr), "i"(OFF) : "memory"); }
template <int OFF> DI void ds_b128(u32x4& d, u32 addr) { asm volatile("ds_read_b128 %0, %1 offset:%c2" : "=v"(d) : "v"(addr), "i"(OFF) : "memory"); }
template <int OFF> DI void ds_tr(u32x2& d, u32 addr) { asm volatile("ds_read_b64_tr_b16 %0, %1 offset:%c2" : "=v"(d) : "v"(addr), "i"(OFF) : "memory"); }
template <int OFF> DI void gld_a(u32x4& d, u32 voff, const void* sbase) {
  asm volatile("global_load_dwordx4 %0, %1, %2 offset:%c3" : "=a"(d) : "v"(voff), "s"(sbase), "i"(OFF) : "memory");
}
template <int OFF> DI void gld_v(u32x4& d, u32 voff, const void* sbase) {
  asm volatile("global_load_dwordx4 %0, %1, %2 offset:%c3" : "=v"(d) : "v"(voff), "s"(sbase), "i"(OFF) : "memory");
}
template <int OFF> DI void gld2_v(u32x2& d, u32 voff, const void* sbase) {
  asm volatile("global_load_dwordx2 %0, %1, %2 offset:%c3" : "=v"(d) : "v"(voff), "s"(sbase), "i"(OFF) : "memory");
}
template <int LOFF> DI void glds16(u32 voff, const void* sbase, u32 ldsbase) {
  asm volatile("s_add_u32 m0, %2, %c3\n\ts_nop 0\n\tglobal_load_lds_dwordx4 %0, %1" ::"v"(voff), "s"(sbase), "s"(ldsbase), "i"(LOFF) : "memory", "scc");
}
template <int N> DI void wait_vm() { asm volatile("s_waitcnt vmcnt(%c0)" ::"i"(N) : "memory"); }
template <int N> DI void wait_lgkm() { asm volatile("s_waitcnt lgkmcnt(%c0)" ::"i"(N) : "memory"); }
template <int OFF> DI void gld_a_sc1(u32x4& d, u32 voff, const void* sbase) {
  asm volatile("global_load_dwordx4 %0, %1, %2 offset:%c3 sc1" : "=a"(d) : "v"(voff), "s"(sbase), "i"(OFF) : "memory");
}
DI void mfma_aa(f32x4& acc, const u32x4& a, const u32x4& b) {
  asm volatile("v_mfma_f32_16x16x32_bf16 %0, %1, %2, %0" : "+v"(acc) : "a"(a), "a"(b));
}
template <bool FIRST = false> DI void mfma_va(f32x4& acc, const u32x4& a, const u32x4& b) {
  if constexpr (FIRST) asm volatile("v_mfma_f32_16x16x32_bf16 %0, %1, %2, 0" : "=&v"(acc) : "v"(a), "a"(b));
  else asm volatile("v_mfma_f32_16x16x32_bf16 %0, %1, %2, %0" : "+v"(acc) : "v"(a), "a"(b));
}
DI void mfma_fence(f32x4& a, f32x4& b, f32x4& c, f32x4& d) { asm volatile("s_nop 7\n\ts_nop 1" : "+v"(a), "+v"(b), "+v"(c), "+v"(d)); }
DI void after_fence(f32x4& a, f32x4& b, f32x4& c, f32x4& d) { asm volatile("" : "+v"(a), "+v"(b), "+v"(c), "+v"(d)); }
DI void after_fence(f32x4& a, f32x4& b, f32x4& c) { asm volatile("" : "+v"(a), "+v"(b), "+v"(c)); }
template <int STAGE> DI void dpp_stage(float& v) {
  if constexpr (STAGE == 0) asm volatile("v_add_f32_dpp %0, %0, %0 quad_perm:[1,0,3,2] row_mask:0xf bank_mask:0xf" : "+v"(v));
  if constexpr (STAGE == 1) asm volatile("v_add_f32_dpp %0, %0, %0 quad_perm:[2,3,0,1] row_mask:0xf bank_mask:0xf" : "+v"(v));
  if constexpr (STAGE == 2) asm volatile("v_add_f32_dpp %0, %0, %0 row_ror:4 row_mask:0xf bank_mask:0xf" : "+v"(v));
  if constexpr (STAGE == 3) asm volatile("v_add_f32_dpp %0, %0, %0 row_ror:8 row_mask:0xf bank_mask:0xf" : "+v"(v));
}
DI void barrier() { asm volatile("s_barrier" ::: "memory"); }
DI void pin(u32x4& x) { asm volatile("" : "+v"(x)); }
DI void pin(u32x2& x) { asm volatile("" : "+v"(x)); }
DI void pin4(f32x4& x) { asm volatile("" : "+v"(x)); }
DI void pin_a(u32x4& x) { asm volatile("" : "+a"(x)); }

#define STAMP(kern, idx) do {} while (0)

template <class Gate>
DI void w_job(int b, const u16* __restrict__ wg, const u16* __restrict__ wu, const u16* __restrict__ wd, char* __restrict__ ws, char* smem, Gate&& gate) {
  const int t = threadIdx.x, lane = t & 63, w = t >> 6;
  char* tile = smem;
  char* tile2 = smem + 32 * 368;
  const u32 tb = (u32)(size_t)(LDSP char*)smem;
  const int i16 = lane & 15, qd = i16 >> 2, p = i16 & 3, g = lane >> 4;
  const int ks = b & 15, j = b >> 4;
  const int U0 = slice_u0(j), nu = slice_nu(j);
  u32x4 g4[2], u4[2], d4[2];
#pragma unroll
  for (int rep = 0; rep < 2; ++rep) {
    int idx = t + 256 * rep, r = idx / 11, u = idx - 11 * r;
    bool ok = r < 32 && u < nu;
    size_t off = ok ? (size_t)(32 * ks + r) * F + 8 * (U0 + u) : 0;
    g4[rep] = *(const u32x4*)(wg + off); u4[rep] = *(const u32x4*)(wu + off);
  }
  const int ks2 = b >> 2, dp = b & 3;
  if (b < KS2 * 4) {
#pragma unroll
    for (int rep = 0; rep < 2; ++rep) {
      int idx = t + 256 * rep, r = idx >> 4, c = idx & 15;
      d4[rep] = *(const u32x4*)(wd + (size_t)(32 * ks2 + r) * D + 128 * dp + 8 * c);
    }
  }
  gate();
#pragma unroll
  for (int rep = 0; rep < 2; ++rep) {
    int idx = t + 256 * rep, r = idx / 11, u = idx - 11 * r;
    if (r < 32 && u < nu) {
      *(u32x4*)(tile + r * 368 + u * 32) = u32x4{g4[rep][0], u4[rep][0], g4[rep][1], u4[rep][1]};
      *(u32x4*)(tile + r * 368 + u * 32 + 16) = u32x4{g4[rep][2], u4[rep][2], g4[rep][3], u4[rep][3]};
    }
  }
  if (b < KS2 * 4) {
#pragma unroll
    for (int rep = 0; rep < 2; ++rep) { int idx = t + 256 * rep, r = idx >> 4, c = idx & 15; *(u32x4*)(tile2 + r * 272 + c * 16) = d4[rep]; }
  }
  __syncthreads();
  const u32x4 ones = {0x3f803f80u, 0x3f803f80u, 0x3f803f80u, 0x3f803f80u};
  const __amdgpu_buffer_rsrc_t wr = ws_rsrc(ws);
  for (int u = w; u < nu; u += 4) {
    u32 a = tb + (8 * g + qd) * 368 + u * 32 + 8 * p;
    u32x4 fr = tr_frag(a, a + 4 * 368);
    st16_wt(wr, (u32)WS_W1 + (((U0 + u) * KS1 + ks) * 64 + lane) * 16, fr);
    f32x4 cs = mfma16(fr, ones, f32x4{0.f, 0.f, 0.f, 0.f});
    if (i16 == 0) st16_wt(wr, (u32)WS_CS + ((ks * NU + U0 + u) * 16 + 4 * g) * 4, __builtin_bit_cast(u32x4, cs));
  }
  if (b < KS2 * 4) {
#pragma unroll
    for (int rep = 0; rep < 2; ++rep) {
      int fi = w + 4 * rep, dtl = fi >> 2, df = fi & 3;
      u32 a = tb + 32 * 368 + (8 * g + qd) * 272 + (64 * dtl + 32 * (df >> 1) + 8 * p + 4 * (df & 1)) * 2;
      u32x4 fr = tr_frag(a, a + 4 * 272);
      st16_wt(wr, (u32)WS_WD + ((((2 * dp + dtl) * KS2 + ks2) * 4 + df) * 64 + lane) * 16, fr);
    }
  }
}

constexpr int KA_CS = 4160;
constexpr int KA_KC = 4 * KA_CS;
constexpr int KA_XR = 2 * KA_KC;
constexpr int KA_RED = KA_XR + 2 * 4608;
constexpr int KA_LDS = KA_RED + 4 * 4096;
constexpr int KA_TK = KA_XR, KA_TV = KA_TK + 72 * 128, KA_TQ = KA_TV + 64 * 128;
static_assert(KA_TQ + 64 * 128 == KA_LDS, "tile overlay");
constexpr int SPIN_MAX = 1 << 22;
#define HINT_AT 1
#define HELP_SLEEP 40
#define HINT_TICKS 800ull

template <int OFF> DI void gld_v_sc1(u32x4& d, u32 voff, const void* sbase) {
  asm volatile("global_load_dwordx4 %0, %1, %2 offset:%c3 sc1" : "=v"(d) : "v"(voff), "s"(sbase), "i"(OFF) : "memory");
}
template <int OFF> DI void gld2_v_sc1(u32x2& d, u32 voff, const void* sbase) {
  asm volatile("global_load_dwordx2 %0, %1, %2 offset:%c3 sc1" : "=v"(d) : "v"(voff), "s"(sbase), "i"(OFF) : "memory");
}
DI bool tags_ok(const u32x4& f, const u32x2& tag) { return f[0] == tag[0] && f[1] == tag[1] && f[2] == tag[0] && f[3] == tag[1]; }

__global__ __launch_bounds__(256, 2) void ka_conv(const u16* __restrict__ q, const u16* __restrict__ k, const u16* __restrict__ v,
                                                  const u16* __restrict__ shk, const u16* __restrict__ ssmk, const u16* __restrict__ dskip,
                                                  const u16* __restrict__ wg, const u16* __restrict__ wu, const u16* __restrict__ wd,
                                                  char* __restrict__ ws) {
  extern __shared__ __attribute__((aligned(16))) char smem[];
  const int t = threadIdx.x, lane = t & 63;
  const int w = __builtin_amdgcn_readfirstlane(t >> 6);
  const int b = blockIdx.x;
  char* ws_stamp = ws; (void)ws_stamp;
  if (b == 0 && t == 0) __hip_atomic_store((unsigned long long*)(ws + WS_T0), __builtin_amdgcn_s_memrealtime(), __ATOMIC_RELAXED, __HIP_MEMORY_SCOPE_AGENT);
  STAMP(0, 0);
  if (b >= 256) {
    __builtin_amdgcn_s_sleep(HELP_SLEEP);
    if (b < 264 && w == 1) {
      const char* pc; u32 len;
      asm volatile("s_getpc_b64 %0\n1:\n\ts_mov_b32 %1, kb_code_end-1b" : "=s"(pc), "=s"(len));
      const u32 lb = (u32)(size_t)(LDSP char*)smem + 32768;
      for (u32 o0 = 0; o0 < len; o0 += 64 * 128) {
        u32 o = o0 + lane * 128; o = o < len ? o : 0;
        asm volatile("s_mov_b32 m0, %2\n\ts_nop 0\n\tglobal_load_lds_dword %0, %1" ::"v"(o), "s"(pc), "s"(lb) : "memory");
      }
    }
    w_job(b - 256, wg, wu, wd, ws, smem, [&]() {
      if (w == 0) {
        const unsigned long long e = __hip_atomic_load((const unsigned long long*)(ws + WS_EPO), __ATOMIC_RELAXED, __HIP_MEMORY_SCOPE_AGENT);
        const unsigned long long* hp = (const unsigned long long*)(ws + WS_HNT) + (b - 256);
        const unsigned long long t0 = __builtin_amdgcn_s_memrealtime();
        while (__hip_atomic_load(hp, __ATOMIC_RELAXED, __HIP_MEMORY_SCOPE_AGENT) != e + 1 && __builtin_amdgcn_s_memrealtime() - t0 < HINT_TICKS)
          __builtin_amdgcn_s_sleep(8);
      }
      __builtin_amdgcn_s_barrier();
      STAMP(1, 5);
    });
    STAMP(1, 4);
    return;
  }
  const int x = b & 7, bi = b >> 3, s0 = 64 * bi;
  const int ch0 = 64 * x + 2 * bi;
  const u32 sb = (u32)(size_t)(LDSP char*)smem;
  const int n = lane & 15, g = lane >> 4, qd = n >> 2, p = n & 3;
  const int d = 64 * x + 16 * w + n;
  const int srow = 8 * b + 2 * w;
  const __amdgpu_buffer_rsrc_t wr = ws_rsrc(ws);

  u32x2 epo, sh; u32x4 qa, qb, kp[2], kc[2];
  gld2_v_sc1<0>(epo, 0, ws + WS_EPO);
  gld2_v<0>(sh, d * 8, shk);
  const int r8 = lane >> 3, c8 = lane & 7;
  auto voff = [&](int instr) { const int f = ((r8 >> 1) & 1) | ((instr & 1) << 1); return (u32)((((c8 >> 1) ^ f) * 2 + (c8 & 1)) * 16); };
  const size_t col = (size_t)x * 128;
  {
    int row = s0 - 8 + 8 * w + r8; row = row < 0 ? 0 : row;
    glds16<0>((u32)row * 1024 + voff(w), (const char*)k + col, sb + KA_TK + w * 1024);
    glds16<0>((u32)(s0 + 8 * w + r8) * 1024 + voff(w), (const char*)v + col, sb + KA_TV + w * 1024);
    glds16<0>((u32)(s0 - 8 + 8 * (w + 4) + r8) * 1024 + voff(w), (const char*)k + col, sb + KA_TK + (w + 4) * 1024);
    glds16<0>((u32)(s0 + 8 * (w + 4) + r8) * 1024 + voff(w), (const char*)v + col, sb + KA_TV + (w + 4) * 1024);
    if (w == 0) glds16<0>((u32)(s0 + 56 + r8) * 1024 + voff(8), (const char*)k + col, sb + KA_TK + 8 * 1024);
  }
#pragma unroll
  for (int cc = 0; cc < 2; ++cc) {
    const char* kr = (const char*)ssmk + (size_t)(ch0 + cc) * 4096;
    gld_v<0>(kp[cc], (u32)(t > 0 ? t - 1 : 0) * 16, kr);
    gld_v<0>(kc[cc], (u32)t * 16, kr);
  }
  glds16<0>((u32)(s0 + 8 * w + r8) * 1024 + voff(w), (const char*)q + col, sb + KA_TQ + w * 1024);
  glds16<0>((u32)(s0 + 8 * (w + 4) + r8) * 1024 + voff(w), (const char*)q + col, sb + KA_TQ + (w + 4) * 1024);
  gld_v<0>(qa, lane * 16, (const char*)q + (size_t)srow * 1024);
  gld_v<1024>(qb, lane * 16, (const char*)q + (size_t)srow * 1024);
  const float dsk = blo(dskip[ch0 + (w >> 1)]);
  if (b == 0 && t < 16) *(u32*)(ws + WS_CNT + t * 128) = 0u;

  wait_vm<8>();
  barrier();
  asm volatile("" : "+v"(sh), "+v"(epo));
  STAMP(0, 1);
  u32x2 tag;
  { const unsigned long long e = (((unsigned long long)epo[1] << 32) | epo[0]) + 1ull; tag = u32x2{(u32)e, (u32)(e >> 32)}; }
  auto taddr = [&](int base, int tr) { const int f = ((tr >> 1) & 1) | ((tr >> 2) & 2); return sb + base + tr * 128 + ((w ^ f) * 32) + 8 * p; };
  {
    const float a0 = blo(sh[0]), a1 = bhi(sh[0]), a2 = blo(sh[1]), a3 = bhi(sh[1]);
#pragma unroll
    for (int e = 0; e < 2; ++e) {
      const int rg = g + 4 * e;
      u32x2 km = tr_read(taddr(KA_TK, 8 * rg + 4 + qd));
      const u32x2 k0 = tr_read(taddr(KA_TK, 8 * rg + 8 + qd)), k1 = tr_read(taddr(KA_TK, 8 * rg + 12 + qd));
      const u32x2 v0 = tr_read(taddr(KA_TV, 8 * rg + qd)), v1 = tr_read(taddr(KA_TV, 8 * rg + 4 + qd));
      if (s0 + rg == 0) km = u32x2{0, 0};
      const float kf[11] = {bhi(km[0]), blo(km[1]), bhi(km[1]), blo(k0[0]), bhi(k0[0]), blo(k0[1]), bhi(k0[1]), blo(k1[0]), bhi(k1[0]), blo(k1[1]), bhi(k1[1])};
      const float vf[8] = {blo(v0[0]), bhi(v0[0]), blo(v0[1]), bhi(v0[1]), blo(v1[0]), bhi(v1[0]), blo(v1[1]), bhi(v1[1])};
      float xo[8];
#pragma unroll
      for (int j = 0; j < 8; ++j) xo[j] = (kf[j + 3] * a0 + kf[j + 2] * a1 + kf[j + 1] * a2 + kf[j] * a3) * vf[j];
      st16_wt(wr, (u32)WS_XR + (d * S + (2040 - (s0 + 8 * rg))) * 2, u32x4{pk2(xo[7], xo[6]), pk2(xo[5], xo[4]), pk2(xo[3], xo[2]), pk2(xo[1], xo[0])});
    }
  }
  asm volatile("s_waitcnt vmcnt(0)\n\ts_barrier" : "+v"(kp[0]), "+v"(kc[0]), "+v"(kp[1]), "+v"(kc[1]), "+v"(qa), "+v"(qb) :: "memory");
  if (t == 0) __builtin_amdgcn_raw_buffer_store_b64(tag, wr, (u32)WS_FLA + (x * 32 + bi) * 8, 0, 16);
  STAMP(0, 2);
#pragma unroll
  for (int e = 0; e < 2; ++e) {
    const int rg = g + 4 * e;
    const u32x2 q0 = tr_read(taddr(KA_TQ, 8 * rg + qd)), q1 = tr_read(taddr(KA_TQ, 8 * rg + 4 + qd));
    st16_wt(wr, (u32)WS_QT + (d * S + s0 + 8 * rg) * 2, cat(q0, q1));
  }
  {
    float sa = 0.f, sbb = 0.f;
#pragma unroll
    for (int e = 0; e < 4; ++e) { sa += blo(qa[e]) + bhi(qa[e]); sbb += blo(qb[e]) + bhi(qb[e]); }
    const float ma = wave_sum(sa) * (1.f / D), mb = wave_sum(sbb) * (1.f / D);
    float va = 0.f, vb = 0.f;
#pragma unroll
    for (int e = 0; e < 4; ++e) {
      float u0 = blo(qa[e]) - ma, u1 = bhi(qa[e]) - ma, u2 = blo(qb[e]) - mb, u3 = bhi(qb[e]) - mb;
      va += u0 * u0 + u1 * u1; vb += u2 * u2 + u3 * u3;
    }
    va = wave_sum(va) * (1.f / D); vb = wave_sum(vb) * (1.f / D);
    const float ra = __builtin_amdgcn_rsqf(va + EPS), rb = __builtin_amdgcn_rsqf(vb + EPS);
    const u32x2 val = lane == 0 ? u32x2{__float_as_uint(ra), __float_as_uint(rb)} : u32x2{__float_as_uint(-ma * ra), __float_as_uint(-mb * rb)};
    const u32 off = lane == 0 ? (u32)WS_SA + srow * 4 : lane == 1 ? (u32)WS_SB + srow * 4 : 0xfffffff0u;
    __builtin_amdgcn_raw_buffer_store_b64(val, wr, off, 0, 16);
  }
#pragma unroll
  for (int cc = 0; cc < 2; ++cc) {
    const u32 m = t > 0 ? 0xffffffffu : 0u;
    u32 wv[6] = {kp[cc][0] & m, kp[cc][1] & m, kp[cc][2] & m, kp[cc][3] & m, kc[cc][0], kc[cc][1]};
    u32 o1[5];
#pragma unroll
    for (int i = 0; i < 5; ++i) o1[i] = __builtin_amdgcn_alignbit(wv[i + 1], wv[i], 16);
    char* base = smem + cc * KA_KC + (t + 1) * 16;
    *(u32x4*)(base + 0 * KA_CS) = u32x4{o1[0], o1[1], o1[2], o1[3]};
    *(u32x4*)(base + 1 * KA_CS) = u32x4{wv[1], wv[2], wv[3], wv[4]};
    *(u32x4*)(base + 2 * KA_CS) = u32x4{o1[1], o1[2], o1[3], o1[4]};
    *(u32x4*)(base + 3 * KA_CS) = u32x4{wv[2], wv[3], wv[4], wv[5]};
    if (t == 255) {
      const u32 z = 0;
      const u32 e0 = __builtin_amdgcn_alignbit(kc[cc][1], kc[cc][0], 16), e1 = __builtin_amdgcn_alignbit(kc[cc][2], kc[cc][1], 16);
      const u32 e2 = __builtin_amdgcn_alignbit(kc[cc][3], kc[cc][2], 16), e3 = __builtin_amdgcn_alignbit(z, kc[cc][3], 16);
      *(u32x4*)(base + 16 + 0 * KA_CS) = u32x4{e0, e1, e2, e3};
      *(u32x4*)(base + 16 + 1 * KA_CS) = u32x4{kc[cc][1], kc[cc][2], kc[cc][3], z};
      *(u32x4*)(base + 16 + 2 * KA_CS) = u32x4{e1, e2, e3, z};
      *(u32x4*)(base + 16 + 3 * KA_CS) = u32x4{kc[cc][2], kc[cc][3], z, z};
    }
  }
  if (t < 8) *(u32x4*)(smem + (t >> 2) * KA_KC + (t & 3) * KA_CS) = u32x4{0, 0, 0, 0};
  asm volatile("s_waitcnt vmcnt(0) lgkmcnt(0)\n\ts_barrier" ::: "memory");
  if (t == 0) {
    __builtin_amdgcn_raw_buffer_store_b64(tag, wr, (u32)WS_FLG + (x * 32 + bi) * 8, 0, 16);
    if (HINT_AT == 0) __builtin_amdgcn_raw_buffer_store_b64(tag, wr, (u32)WS_HNT + b * 8, 0, 16);
  }
  if (t >= 64 && t < 64 + 60) { int i = t - 64; *(u32x4*)(smem + KA_XR + (i / 30) * 4608 + 4096 + (i % 30) * 16) = u32x4{0, 0, 0, 0}; }
  if (w == 0) {
    const unsigned long long* fp = (const unsigned long long*)(ws + WS_FLA) + x * 32 + (lane & 31);
    const unsigned long long want = ((unsigned long long)tag[1] << 32) | tag[0];
    for (int spins = 0; spins < SPIN_MAX; ++spins) {
      if (__all(__hip_atomic_load(fp, __ATOMIC_RELAXED, __HIP_MEMORY_SCOPE_AGENT) == want)) break;
      __builtin_amdgcn_s_sleep(1);
    }
  }
  asm volatile("s_waitcnt vmcnt(0)\n\ts_barrier" ::: "memory");
  STAMP(0, 3);

  const int c = w >> 1, half = w & 1;
  u32x2 qv[4]; u32x4 sa[4], sbv[4], fl[2];
  const u32 eo = (u32)(1024 * half + 16 * n + 4 * g);
  const char* qTc = ws + WS_QT + (size_t)(ch0 + c) * 4096;
  {
    u32x4 x0, x1;
    gld_v_sc1<0>(x0, t * 16, ws + WS_XR + (size_t)ch0 * 4096);
    gld_v_sc1<0>(x1, t * 16, ws + WS_XR + (size_t)(ch0 + 1) * 4096);
    wait_vm<0>();
    asm volatile("" : "+v"(x0), "+v"(x1));
    *(u32x4*)(smem + KA_XR + t * 16) = x0;
    *(u32x4*)(smem + KA_XR + 4608 + t * 16) = x1;
  }
  asm volatile("s_waitcnt lgkmcnt(0)\n\ts_barrier" ::: "memory");
  if (HINT_AT == 1 && t == 0) __builtin_amdgcn_raw_buffer_store_b64(tag, wr, (u32)WS_HNT + b * 8, 0, 16);
  STAMP(1, 1);

  const u32 aa = sb + c * KA_KC + (n & 3) * KA_CS + 16 * g + 2 * (n & 12) + half * 256;
  const u32 ba = sb + KA_XR + c * 4608 + 480 - 32 * n + 16 * g + half * 256;
  f32x4 acc[8];
  u32x2 Alo[2][8], Ahi[2][8];
  u32x4 B[2][8];
  auto rd = [&](auto C0, auto R) {
    constexpr int c0 = C0, r = R, buf = c0 & 1;
    if constexpr (r >= 2 && r < 10) {
      constexpr int u = r - 2;
      ds_b128<512 * (7 - u) + 64 * c0>(B[buf][u], ba);
    } else {
      constexpr int ar = r < 2 ? r : r - 8, i = ar >> 1;
      if constexpr ((ar & 1) == 0) ds_b64<512 * i + 64 * c0>(Alo[buf][i], aa);
      else ds_b64<512 * i + 64 * c0 + 8>(Ahi[buf][i], aa);
    }
  };
  auto ldS = [&](auto E) {
    constexpr int e = E, i = e / 3, kind = e % 3;
    if constexpr (kind == 0) gld_v_sc1<i * 1024>(sa[i], eo * 4, ws + WS_SA);
    if constexpr (kind == 1) gld_v_sc1<i * 1024>(sbv[i], eo * 4, ws + WS_SB);
    if constexpr (kind == 2) gld2_v_sc1<i * 512>(qv[i], eo * 2, qTc);
  };
  static_for<24>([&](auto R) { rd(std::integral_constant<int, 0>{}, R); });
  static_for<4>([&](auto C0) {
    constexpr int c0 = C0, buf = c0 & 1;
    if constexpr (c0 == 2) {
      wait_vm<0>();
      asm volatile("" : "+v"(fl[0]), "+v"(fl[1]));
      if (!__all(tags_ok(fl[0], tag) && tags_ok(fl[1], tag))) {
        for (int spins = 0; spins < SPIN_MAX; ++spins) {
          gld_v_sc1<0>(fl[0], lane * 32, ws + WS_FLG); gld_v_sc1<16>(fl[1], lane * 32, ws + WS_FLG);
          wait_vm<0>();
          asm volatile("" : "+v"(fl[0]), "+v"(fl[1]));
          if (__all(tags_ok(fl[0], tag) && tags_ok(fl[1], tag))) break;
          __builtin_amdgcn_s_sleep(1);
        }
      }
      if (b == 0 && t == 0) __builtin_amdgcn_raw_buffer_store_b64(tag, wr, (u32)WS_EPO, 0, 16);
    }
    u32x4 A[8];
    static_for<36>([&](auto M) {
      constexpr int m = M;
      constexpr int c1 = m < 8 ? 0 : m < 15 ? 1 : m < 21 ? 2 : m < 26 ? 3 : m < 30 ? 4 : m < 33 ? 5 : m < 35 ? 6 : 7;
      constexpr int first = c1 * 8 - c1 * (c1 - 1) / 2;
      constexpr int u = m - first;
      if constexpr (m == 0) {
        if constexpr (c0 == 0) wait_lgkm<14>(); else wait_lgkm<0>();
        pin(Alo[buf][0]); pin(Ahi[buf][0]);
#pragma unroll
        for (int i = 0; i < 8; ++i) pin(B[buf][i]);
        A[0] = cat(Alo[buf][0], Ahi[buf][0]);
      }
      if constexpr (m == 8) {
        if constexpr (c0 == 0) wait_lgkm<8>();
#pragma unroll
        for (int i = 1; i < 8; ++i) { pin(Alo[buf][i]); pin(Ahi[buf][i]); A[i] = cat(Alo[buf][i], Ahi[buf][i]); }
      }
      mfma_vv<(c0 == 0 && c1 == 0)>(acc[c1 + u], A[c1], B[buf][u]);
      if constexpr (c0 < 3 && m < 24) rd(std::integral_constant<int, c0 + 1>{}, M);
      if constexpr (c0 == 1 && m == 2) gld_v_sc1<0>(fl[0], lane * 32, ws + WS_FLG);
      if constexpr (c0 == 1 && m == 4) gld_v_sc1<16>(fl[1], lane * 32, ws + WS_FLG);
      if constexpr (c0 == 2 && m >= 1 && m < 25 && (m & 1) == 1) ldS(std::integral_constant<int, (m - 1) / 2>{});
    });
  });
  mfma_fence(acc[0], acc[1], acc[2], acc[3]);
  after_fence(acc[4], acc[5], acc[6], acc[7]);
  STAMP(1, 2);
  f32x4 fin[4];
  {
    f32x4* red = (f32x4*)(smem + KA_RED);
    if (half) {
#pragma unroll
      for (int i = 0; i < 4; ++i) { red[(w * 4 + i) * 64 + lane] = acc[i]; fin[i] = acc[4 + i]; }
      asm volatile("; half 1" ::: "memory");
    } else {
#pragma unroll
      for (int i = 0; i < 4; ++i) { red[(w * 4 + i) * 64 + lane] = acc[4 + i]; fin[i] = acc[i]; }
      asm volatile("; half 0" ::: "memory");
    }
    asm volatile("s_waitcnt lgkmcnt(0)\n\ts_barrier" ::: "memory");
#pragma unroll
    for (int i = 0; i < 4; ++i) fin[i] += red[((w ^ 1) * 4 + i) * 64 + lane];
  }
  wait_vm<0>();
#pragma unroll
  for (int i = 0; i < 4; ++i) asm volatile("" : "+v"(qv[i]), "+v"(sa[i]), "+v"(sbv[i]));
  const f32x2 dd = {dsk, dsk};
  u32x2 hv[4];
#pragma unroll
  for (int i = 0; i < 4; ++i) {
    const int s = 256 * (4 * half + i) + 16 * n + 4 * g;
    const u32x2 xq = *(LDSP const u32x2*)(size_t)(sb + KA_XR + c * 4608 + 2 * (2044 - s));
    const f32x2 x0 = {bhi(xq[1]), blo(xq[1])}, x1 = {bhi(xq[0]), blo(xq[0])};
    const f32x2 q0 = {blo(qv[i][0]), bhi(qv[i][0])}, q1 = {blo(qv[i][1]), bhi(qv[i][1])};
    const f32x2 z0 = x0 * dd + f32x2{fin[i][0], fin[i][1]}, z1 = x1 * dd + f32x2{fin[i][2], fin[i][3]};
    const f32x4 a4 = __builtin_bit_cast(f32x4, sa[i]), b4 = __builtin_bit_cast(f32x4, sbv[i]);
    const f32x2 t0 = q0 * f32x2{a4[0], a4[1]} + f32x2{b4[0], b4[1]};
    const f32x2 t1 = q1 * f32x2{a4[2], a4[3]} + f32x2{b4[2], b4[3]};
    const f32x2 h0 = t0 * z0 + q0, h1 = t1 * z1 + q1;
    hv[i] = u32x2{pk2(h0[0], h0[1]), pk2(h1[0], h1[1])};
  }
#pragma unroll
  for (int i = 0; i < 4; i += 2) {
    auto a = __builtin_amdgcn_permlane16_swap(hv[i][0], hv[i + 1][0], false, false);
    auto bb = __builtin_amdgcn_permlane16_swap(hv[i][1], hv[i + 1][1], false, false);
    const int s = 256 * (4 * half + i + (g & 1)) + 16 * n + 4 * (g & 2);
    st16_wt(wr, (u32)WS_HT + ((ch0 + c) * S + s) * 2, u32x4{a[0], bb[0], a[1], bb[1]});
  }
  STAMP(1, 3);
}

constexpr int KB_NL = 39;
constexpr int KB_ST = KB_NL * 4096;
constexpr int KB_LDS = KB_ST + 1024;
#define EPI_FILL 8
constexpr int KB_RING = 8;

#define P2_PRO 3
constexpr int p2_upto(int ks) {
  const int ramp = P2_PRO - 1 + 2 * (ks + 1), steady = ks + 7;
  const int u = ks < 0 ? P2_PRO - 1 : (ramp < steady ? ramp : steady);
  return u > 15 ? 15 : u;
}
constexpr int p2_extra(int ks) { return ks == 9 ? 3 : ks >= 10 ? 4 : 0; }
constexpr int p2_extra_first(int ks) { int c = 0; for (int s = 0; s < ks; ++s) c += p2_extra(s); return c; }
constexpr int P2_NEXTRA = 23;
constexpr int p2_extra_real(int ks) { int f = p2_extra_first(ks), n = p2_extra(ks); return f >= P2_NEXTRA ? 0 : (f + n > P2_NEXTRA ? P2_NEXTRA - f : n); }
constexpr int p2_issue_step(int t) { for (int s = -1; s < KS1; ++s) if (p2_upto(s) >= t) return s; return KS1; }
constexpr int p2_younger(int t, int ks, int ops) {
  int c = (p2_upto(ks - 1) - t) * ops;
  for (int s = p2_issue_step(t); s < ks; ++s) if (s >= 0) c += p2_extra_real(s);
  return c;
}

struct KbCtx {
  u32 sb, gv, lv, lb0, lwd, csv;
  const char *hb0, *hb1, *w0, *w1, *w2, *cs0, *cs1, *cs2, *wd, *wdr;
};

template <int GRAM, class Setup>
DI void kb_phase2(const KbCtx& c, f32x4 (&acc)[3][8], f32x4 (&rsum)[2], f32x4 (&csp)[3], u32x4 (&Wr)[4][4], u32 (&ra)[8], Setup&& setup, char* ws_stamp) {
  constexpr int OPS = GRAM ? 4 : 5;
  u32x4 W[KB_RING][3];
  u32x2 Bl[2][8], Bh[2][8];
  auto issue = [&](auto KS, auto O) {
    constexpr int ks = KS, o = O;
    if constexpr (o == 0) glds16<ks * 8192>(c.gv, c.hb0 + (size_t)ks * 131072, c.lb0);
    if constexpr (o == 1) gld_a<(ks & 3) * 1024>(W[ks % KB_RING][0], c.lv, c.w0 + (ks >> 2) * 4096);
    if constexpr (o == 2) gld_a<(ks & 3) * 1024>(W[ks % KB_RING][1], c.lv, c.w1 + (ks >> 2) * 4096);
    if constexpr (o == 3 && !GRAM) gld_a<(ks & 3) * 1024>(W[ks % KB_RING][2], c.lv, c.w2 + (ks >> 2) * 4096);
    if constexpr (o == 4) glds16<ks * 8192 + 1024>(c.gv, c.hb1 + (size_t)ks * 131072, c.lb0);
  };
  auto extra = [&](auto E) {
    constexpr int e = E;
    if constexpr (e < 7) glds16<(32 + e) * 4096>(c.lv, c.wd + (size_t)(32 + e) * 4096, c.lwd);
    else if constexpr (e < P2_NEXTRA) gld_a<((e - 7) & 3) * 1024>(Wr[(e - 7) >> 2][(e - 7) & 3], c.lv, c.wdr + ((e - 7) >> 2) * 4096);
  };
  auto rdB = [&](auto KS, auto R) {
    constexpr int ks = KS, r = R, i = r >> 1;
    if constexpr ((r & 1) == 0) ds_tr<(ks & 7) * 8192>(Bl[ks & 1][i], ra[i] + (ks >= 8 ? 65536 : 0));
    else ds_tr<(ks & 7) * 8192 + 1024>(Bh[ks & 1][i], ra[i] + (ks >= 8 ? 65536 : 0));
  };
  u32x4 c0, c1, c2;
  static_for<5>([&](auto O) { issue(std::integral_constant<int, 0>{}, O); });
  gld_v<0>(c0, c.csv, c.cs0); gld_v<0>(c1, c.csv, c.cs1); gld_v<0>(c2, c.csv, c.cs2);
  static_for<p2_upto(-1)>([&](auto KS) { static_for<5>([&](auto O) { issue(std::integral_constant<int, KS + 1>{}, O); }); });
  setup();
  u32x4 ones = {0x3f803f80u, 0x3f803f80u, 0x3f803f80u, 0x3f803f80u};
  pin(ones);
  wait_vm<p2_upto(-1) * OPS>();
  barrier();
  static_for<16>([&](auto R) { rdB(std::integral_constant<int, 0>{}, R); });
  asm volatile("" : "+v"(c0), "+v"(c1), "+v"(c2));
  float cv[12];
#pragma unroll
  for (int e = 0; e < 4; ++e) { cv[e] = __uint_as_float(c0[e]); cv[4 + e] = __uint_as_float(c1[e]); cv[8 + e] = __uint_as_float(c2[e]); }
  static_for<KS1>([&](auto KS) {
    constexpr int ks = KS, buf = ks & 1, slot = ks % KB_RING;
    constexpr int nmain = (p2_upto(ks) - p2_upto(ks - 1)) * 5, next = p2_extra_real(ks);
    wait_lgkm<0>();
    u32x4 B[8];
#pragma unroll
    for (int i = 0; i < 8; ++i) { pin(Bl[buf][i]); pin(Bh[buf][i]); }
#pragma unroll
    for (int i = 0; i < 8; ++i) B[i] = cat(Bl[buf][i], Bh[buf][i]);
    pin_a(W[slot][0]); pin_a(W[slot][1]);
    if constexpr (!GRAM) pin_a(W[slot][2]);
    static_for<26>([&](auto M) {
      constexpr int m = M, i = m / 3, u = m % 3;
      if constexpr (m >= 24) mfma_vv<ks == 0>(rsum[m - 24], ones, B[m - 24]);
      else if constexpr (u < 2 || !GRAM) mfma_av<ks == 0>(acc[u][i], W[slot][u], B[i]);
      else mfma_vv<ks == 0>(acc[2][i], B[i], B[i]);
      if constexpr (m == 1 && ks + 1 < KS1) { wait_vm<p2_younger(ks + 1, ks, OPS)>(); barrier(); }
      if constexpr (m >= 2 && m < 18 && ks + 1 < KS1) rdB(std::integral_constant<int, ks + 1>{}, std::integral_constant<int, m - 2>{});
      if constexpr (ks >= 9 && ks < 13 && m >= 2 && m < 14) dpp_stage<ks - 9>(cv[m - 2]);
      constexpr int v = m - (26 - nmain - next);
      if constexpr (v >= 0 && v < nmain) issue(std::integral_constant<int, p2_upto(ks - 1) + 1 + v / 5>{}, std::integral_constant<int, v % 5>{});
      if constexpr (v >= nmain) extra(std::integral_constant<int, p2_extra_first(ks) + v - nmain>{});
    });
  });
  mfma_fence(acc[0][0], acc[1][0], rsum[0], rsum[1]);
#pragma unroll
  for (int i = 0; i < 8; ++i) after_fence(acc[0][i], acc[1][i], acc[2][i], rsum[i & 1]);
#pragma unroll
  for (int u = 0; u < 3; ++u) csp[u] = f32x4{cv[4 * u], cv[4 * u + 1], cv[4 * u + 2], cv[4 * u + 3]};
}

__global__ __launch_bounds__(256) void kb_mlp(char* __restrict__ ws, u16* __restrict__ out) {
  extern __shared__ __attribute__((aligned(16))) char smem[];
  const int t = threadIdx.x, lane = t & 63;
  const int w = __builtin_amdgcn_readfirstlane(t >> 6);
  const int b = blockIdx.x, xcd = b & 7, bi = b >> 3;
#define KB_MAP 1
  const int rt = KB_MAP == 0 ? 2 * xcd + (bi & 1) : (xcd >> 1) * 4 + (bi & 3);
  const int j = KB_MAP == 0 ? bi >> 1 : (xcd & 1) * 8 + (bi >> 2);
  const int half = KB_MAP == 0 ? j & 1 : j >> 3, dt = KB_MAP == 0 ? j >> 1 : j & 7;
  const int U0 = slice_u0(j), nu = slice_nu(j);
  const int rem = nu - 8;
  const int nw = w < rem ? 3 : 2;
  const int uw = U0 + (w < rem ? 3 * w : 3 * rem + 2 * (w - rem));
  const int u2 = uw + (nw == 3 ? 2 : 0);
  const int n = lane & 15, g = lane >> 4;
  const int s0 = 128 * rt;
  char* ws_stamp = ws; (void)ws_stamp;
  STAMP(2, 0);
  auto sf = [&](int i) { return ((w + (i >> 1)) & 3) + 4 * (half ^ (i & 1)); };

  KbCtx c;
  c.sb = (u32)(size_t)(LDSP char*)smem;
  c.lv = lane * 16;
  c.gv = (u32)(g * 4096 + (((n >> 1) ^ (g | ((w & 1) << 2))) * 32 + (n & 1) * 16));
  c.hb0 = ws + WS_HT + (size_t)s0 * 2 + (size_t)(8 * w) * 4096;
  c.hb1 = c.hb0 + 4 * 4096;
  c.lb0 = c.sb + 8 * w * 256;
  c.w0 = ws + WS_W1 + (size_t)uw * KS1 * 1024;
  c.w1 = c.w0 + KS1 * 1024;
  c.w2 = ws + WS_W1 + (size_t)u2 * KS1 * 1024;
  c.cs0 = ws + WS_CS + (size_t)uw * 64;
  c.cs1 = c.cs0 + 64;
  c.cs2 = ws + WS_CS + (size_t)u2 * 64;
  c.csv = n * (NU * 64) + g * 16;
  c.wdr = ws + WS_WD + (size_t)dt * KS2 * 4096 + (size_t)KB_NL * 4096;
  c.wd = ws + WS_WD + (size_t)dt * KS2 * 4096 + (size_t)w * 1024;
  c.lwd = c.sb + 1024 * w;
  u32 ra[8], hoff[6];
  auto setup = [&]() {
    const int qd = n >> 2, p = n & 3;
    const int frow = qd | ((g & 1) << 2);
#pragma unroll
    for (int i = 0; i < 8; ++i) { ra[i] = c.sb + (8 * g + qd) * 256 + ((sf(i) ^ frow) * 32) + 8 * p; asm volatile("" : "+v"(ra[i])); }
#pragma unroll
    for (int tt = 0; tt < 6; ++tt) {
      const int q = 4 * tt + g, i = (q * 11) >> 5, u = q - 3 * i, U = uw + u;
      const u32 o = (u32)(((8 * rt + sf(i)) * KS2 + (U >> 2)) * 1024 + ((U & 3) * 16 + n) * 16);
      hoff[tt] = u < nw ? o : 0xfffffff0u;
      asm volatile("" : "+v"(hoff[tt]));
    }
  };
  f32x4 acc[3][8], cs[3], rsum[2];
  u32x4 Wr[4][4];
  if (w == 3) kb_phase2<1>(c, acc, rsum, cs, Wr, ra, setup, ws_stamp);
  else kb_phase2<0>(c, acc, rsum, cs, Wr, ra, setup, ws_stamp);
  STAMP(2, 2);

  u32x2 res[4];
  {
    const u32 rb = ra[0] + dt * 16384;
    ds_tr<0>(res[0], rb); ds_tr<1024>(res[1], rb); ds_tr<8192>(res[2], rb); ds_tr<9216>(res[3], rb);
  }
  float* stl = (float*)(smem + KB_ST);
  if (g == 0) {
#pragma unroll
    for (int i = 0; i < 2; ++i) stl[2 * (16 * sf(i) + n)] = rsum[i][0];
  }
  if (w == 3 && g == (n >> 2)) {
#pragma unroll
    for (int i = 0; i < 8; ++i) {
      f32x4 a = acc[2][i];
      float dg = (n & 2) ? ((n & 1) ? a[3] : a[2]) : ((n & 1) ? a[1] : a[0]);
      stl[2 * (16 * sf(i) + n) + 1] = dg;
    }
  }
  asm volatile("s_waitcnt lgkmcnt(0)\n\ts_barrier" : "+v"(res[0]), "+v"(res[1]), "+v"(res[2]), "+v"(res[3]) :: "memory");
  STAMP(2, 3);
  const __amdgpu_buffer_rsrc_t hrs = __builtin_amdgcn_make_buffer_rsrc(ws + WS_H, 0, (S / 16) * KS2 * 1024, 0x00020000);
  f32x2 sv[8];
#pragma unroll
  for (int i = 0; i < 8; ++i) sv[i] = *(const f32x2*)(stl + 2 * (16 * sf(i) + n));
  u32 hv[24];
  static_for<8>([&](auto I) {
    constexpr int i = I;
    const float mean = sv[i][0] * (1.f / D);
    const float var = fmaxf(sv[i][1] * (1.f / D) - mean * mean, 0.f) + EPS;
    const float rstd = __builtin_amdgcn_rsqf(var);
    const float rinv = var * rstd;
    const float ce = -1.4426950408889634f * rstd;
    const f32x2 nm = {-mean, -mean}, ce2 = {ce, ce}, ri2 = {rinv, rinv};
    static_for<3>([&](auto U_) {
      constexpr int u = U_;
      pin4(acc[u][i]);
      constexpr int slot = 3 * i + u;
      constexpr int wk = slot * EPI_FILL / 24;
      if constexpr (EPI_FILL > 0 && (slot + 1) * EPI_FILL / 24 > wk) glds16<wk * 4096>(c.lv, c.wd + (size_t)wk * 4096, c.lwd);
      const f32x4 a = acc[u][i];
      const f32x2 tg = (f32x2{a[0], a[1]} + nm * f32x2{cs[u][0], cs[u][1]}) * ce2;
      const f32x2 tu = f32x2{a[2], a[3]} + nm * f32x2{cs[u][2], cs[u][3]};
      const f32x2 e = {__builtin_amdgcn_exp2f(tg[0]), __builtin_amdgcn_exp2f(tg[1])};
      const f32x2 den = e * ri2 + ri2;
      const f32x2 o = tu * f32x2{__builtin_amdgcn_rcpf(den[0]), __builtin_amdgcn_rcpf(den[1])};
      hv[3 * i + u] = pk2(o[0], o[1]);
    });
    static_for<6>([&](auto T) {
      constexpr int tt = T;
      if constexpr ((4 * tt + 3) / 3 == i) {
        auto s02 = __builtin_amdgcn_permlane32_swap(hv[4 * tt], hv[4 * tt + 2], false, false);
        auto s13 = __builtin_amdgcn_permlane32_swap(hv[4 * tt + 1], hv[4 * tt + 3], false, false);
        auto t01 = __builtin_amdgcn_permlane16_swap(s02[0], s13[0], false, false);
        auto t23 = __builtin_amdgcn_permlane16_swap(s02[1], s13[1], false, false);
        __builtin_amdgcn_raw_buffer_store_b128(u32x4{t01[0], t01[1], t23[0], t23[1]}, hrs, hoff[tt], 0, 16);
      }
    });
  });
  STAMP(2, 4);
  u32* cnt = (u32*)(ws + WS_CNT + rt * 128);
  asm volatile("s_waitcnt vmcnt(0)\n\ts_barrier" ::: "memory");
  STAMP(2, 5);
  if (w == 0) {
    if (lane == 0) __hip_atomic_fetch_add(cnt, 1u, __ATOMIC_RELAXED, __HIP_MEMORY_SCOPE_AGENT);
    for (int spins = 0; spins < SPIN_MAX; ++spins) {
      const u32 v = __builtin_amdgcn_readfirstlane(__hip_atomic_load(cnt, __ATOMIC_RELAXED, __HIP_MEMORY_SCOPE_AGENT));
      if (v >= 16u) break;
      __builtin_amdgcn_s_sleep(1);
    }
    wait_vm<0>();
  } else {
    const char* wsrc = c.wd - (size_t)w * 1024;
    for (int e = EPI_FILL + w - 1; e < 32; e += 3) {
      const char* src = wsrc + (size_t)e * 4096;
      const u32 dst = c.sb + e * 4096;
      glds16<0>(c.lv, src, dst); glds16<1024>(c.lv, src + 1024, dst); glds16<2048>(c.lv, src + 2048, dst); glds16<3072>(c.lv, src + 3072, dst);
    }
  }
  barrier();
  STAMP(2, 6);

  const char* hb = ws + WS_H + (size_t)(8 * rt + 4 * half + w) * KS2 * 1024;
  u32x4 Hf[KS2];
  constexpr int P3_AHEAD = 12;
  auto ldH = [&](auto KS) { constexpr int ks = KS; if constexpr (ks < KS2) gld_a_sc1<(ks & 3) * 1024>(Hf[ks], c.lv, hb + (ks >> 2) * 4096); };
  static_for<P3_AHEAD>([&](auto KS) { ldH(KS); });
  STAMP(3, 0);
  f32x4 o4[4];
  u32x4 Af[3][4];
  const u32 la0 = c.sb + lane * 16, la1 = la0 + 65536, la2 = la0 + 131072;
  auto rdA = [&](auto KS, auto DF) {
    constexpr int ks = KS, df = DF;
    if constexpr (ks < KB_NL) ds_b128<(ks & 15) * 4096 + df * 1024>(Af[ks % 3][df], ks < 16 ? la0 : ks < 32 ? la1 : la2);
  };
  wait_vm<P3_AHEAD>();
  barrier();
  static_for<4>([&](auto DF) { rdA(std::integral_constant<int, 0>{}, DF); });
  static_for<4>([&](auto DF) { rdA(std::integral_constant<int, 1>{}, DF); });
  static_for<KS2>([&](auto KS) {
    constexpr int ks = KS;
    wait_vm<(ks + P3_AHEAD <= KS2 ? P3_AHEAD - 1 : KS2 - 1 - ks)>();
    if constexpr (ks + 1 < KB_NL) wait_lgkm<4>(); else wait_lgkm<0>();
    pin_a(Hf[ks]);
    static_for<4>([&](auto DF) {
      constexpr int df = DF;
      if constexpr (ks < KB_NL) { pin(Af[ks % 3][df]); mfma_va<ks == 0>(o4[df], Af[ks % 3][df], Hf[ks]); }
      else { pin_a(Wr[ks - KB_NL][df]); mfma_aa(o4[df], Wr[ks - KB_NL][df], Hf[ks]); }
      rdA(std::integral_constant<int, ks + 2>{}, DF);
      if constexpr (df == 1 && ks + P3_AHEAD < KS2) ldH(std::integral_constant<int, ks + P3_AHEAD>{});
    });
  });
  mfma_fence(o4[0], o4[1], o4[2], o4[3]);
  u32x4 o0, o1;
#pragma unroll
  for (int df = 0; df < 2; ++df) {
    o0[2 * df] = pk2(o4[df][0] + blo(res[df][0]), o4[df][1] + bhi(res[df][0]));
    o0[2 * df + 1] = pk2(o4[df][2] + blo(res[df][1]), o4[df][3] + bhi(res[df][1]));
    o1[2 * df] = pk2(o4[2 + df][0] + blo(res[2 + df][0]), o4[2 + df][1] + bhi(res[2 + df][0]));
    o1[2 * df + 1] = pk2(o4[2 + df][2] + blo(res[2 + df][1]), o4[2 + df][3] + bhi(res[2 + df][1]));
  }
  const __amdgpu_buffer_rsrc_t ors = __builtin_amdgcn_make_buffer_rsrc(out, 0, S * D * 2, 0x00020000);
  const u32 ooff = (u32)((s0 + 64 * half + 16 * w + n) * D + 64 * dt + 8 * g) * 2;
  __builtin_amdgcn_raw_buffer_store_b128(o0, ors, ooff, 0, 16);
  __builtin_amdgcn_raw_buffer_store_b128(o1, ors, ooff + 64, 0, 16);
  asm volatile("kb_code_end:");
  STAMP(2, 7);
}

extern "C" void kernel_launch(void* const* d_in, const int* in_sizes, int n_in, void* d_out, int out_size, void* d_ws,
                              size_t ws_size, hipStream_t stream) {
  const u16 *q = (const u16*)d_in[0], *k = (const u16*)d_in[1], *v = (const u16*)d_in[2], *shk = (const u16*)d_in[3];
  const u16 *ssmk = (const u16*)d_in[4], *dskip = (const u16*)d_in[5];
  const u16 *wg = (const u16*)d_in[7], *wu = (const u16*)d_in[8], *wd = (const u16*)d_in[9];
  char* ws = (char*)d_ws;
  (void)hipFuncSetAttribute((const void*)ka_conv, hipFuncAttributeMaxDynamicSharedMemorySize, KA_LDS);
  (void)hipFuncSetAttribute((const void*)kb_mlp, hipFuncAttributeMaxDynamicSharedMemorySize, KB_LDS);
  ka_conv<<<512, 256, KA_LDS, stream>>>(q, k, v, shk, ssmk, dskip, wg, wu, wd, ws);
  kb_mlp<<<256, 256, KB_LDS, stream>>>(ws, (u16*)d_out);
}
```

```cpp
#include <hip/hip_runtime.h>
#include <stdint.h>
#include <utility>

typedef unsigned short u16;
typedef unsigned u32;
typedef __attribute__((ext_vector_type(2))) u32 u32x2;
typedef __attribute__((ext_vector_type(4))) u32 u32x4;
typedef __attribute__((ext_vector_type(2))) float f32x2;
typedef __attribute__((ext_vector_type(4))) float f32x4;
typedef __attribute__((ext_vector_type(2))) __bf16 bf16x2;
typedef __attribute__((ext_vector_type(8))) __bf16 bf16x8;
typedef __attribute__((ext_vector_type(4))) short s16x4;
#define LDSP __attribute__((address_space(3)))
#define DI __device__ __forceinline__

constexpr int S = 2048, D = 512, F = 1376;
constexpr int NU = F / 8;
constexpr int KS1 = D / 32;
constexpr int KS2 = F / 32;
constexpr float EPS = 1e-5f;

constexpr size_t WS_XR = 0;
constexpr size_t WS_QT = WS_XR + (size_t)D * S * 2;
constexpr size_t WS_HT = WS_QT + (size_t)D * S * 2;
constexpr size_t WS_W1 = WS_HT + (size_t)D * S * 2;
constexpr size_t WS_WD = WS_W1 + (size_t)NU * KS1 * 1024;
constexpr size_t WS_H = WS_WD + (size_t)8 * KS2 * 4 * 1024;
constexpr size_t WS_CS = WS_H + (size_t)(S / 16) * KS2 * 1024;
constexpr size_t WS_SA = WS_CS + (size_t)KS1 * NU * 16 * 4;
constexpr size_t WS_SB = WS_SA + (size_t)S * 4;
constexpr size_t WS_CNT = WS_SB + (size_t)S * 4;
constexpr size_t WS_FLA = WS_CNT + 16 * 128;
constexpr size_t WS_FLG = WS_FLA + 256 * 8;
constexpr size_t WS_HNT = WS_FLG + 256 * 8;
constexpr size_t WS_EPO = WS_HNT + 256 * 8;

template <int... Is, class Fn> DI void static_for_(std::integer_sequence<int, Is...>, Fn&& f) { (f(std::integral_constant<int, Is>{}), ...); }
template <int N, class Fn> DI void static_for(Fn&& f) { static_for_(std::make_integer_sequence<int, N>{}, f); }

DI float blo(u32 u) { return __uint_as_float(u << 16); }
DI float bhi(u32 u) { return __uint_as_float(u & 0xffff0000u); }
DI u32 pk2(float a, float b) { f32x2 t = {a, b}; return __builtin_bit_cast(u32, __builtin_convertvector(t, bf16x2)); }
DI f32x4 mfma16(u32x4 a, u32x4 b, f32x4 c) {
  return __builtin_amdgcn_mfma_f32_16x16x32_bf16(__builtin_bit_cast(bf16x8, a), __builtin_bit_cast(bf16x8, b), c, 0, 0, 0);
}
DI u32x2 tr_read(u32 lds_byte) {
  return __builtin_bit_cast(u32x2, __builtin_amdgcn_ds_read_tr16_b64_v4i16((LDSP s16x4*)(size_t)lds_byte));
}
DI u32x4 tr_frag(u32 a0, u32 a1) { u32x2 x = tr_read(a0), y = tr_read(a1); return u32x4{x[0], x[1], y[0], y[1]}; }
DI u32x4 cat(u32x2 x, u32x2 y) { return __builtin_shufflevector(x, y, 0, 1, 2, 3); }
template <int CTRL> DI float dpp_add(float v) {
  return v + __builtin_bit_cast(float, __builtin_amdgcn_update_dpp(0, __builtin_bit_cast(int, v), CTRL, 0xf, 0xf, true));
}
DI float sum_rows(float v) {
  u32 u = __float_as_uint(v);
  auto a = __builtin_amdgcn_permlane16_swap(u, u, false, false);
  v = __uint_as_float(a[0]) + __uint_as_float(a[1]);
  u = __float_as_uint(v);
  auto b = __builtin_amdgcn_permlane32_swap(u, u, false, false);
  return __uint_as_float(b[0]) + __uint_as_float(b[1]);
}
DI float wave_sum(float v) {
  v = dpp_add<0xb1>(v);
  v = dpp_add<0x4e>(v);
  v = dpp_add<0x124>(v);
  v = dpp_add<0x128>(v);
  return sum_rows(v);
}
DI __amdgpu_buffer_rsrc_t ws_rsrc(char* ws) { return __builtin_amdgcn_make_buffer_rsrc(ws, 0, 0x04000000, 0x00020000); }
DI void st16_wt(__amdgpu_buffer_rsrc_t r, u32 off, u32x4 v) { __builtin_amdgcn_raw_buffer_store_b128(v, r, off, 0, 16); }
DI int slice_u0(int j) { return j < 12 ? 11 * j : 132 + 10 * (j - 12); }
DI int slice_nu(int j) { return j < 12 ? 11 : 10; }

template <bool FIRST = false> DI void mfma_vv(f32x4& acc, const u32x4& a, const u32x4& b) {
  if constexpr (FIRST) asm volatile("v_mfma_f32_16x16x32_bf16 %0, %1, %2, 0" : "=&v"(acc) : "v"(a), "v"(b));
  else asm volatile("v_mfma_f32_16x16x32_bf16 %0, %1, %2, %0" : "+v"(acc) : "v"(a), "v"(b));
}
template <bool FIRST = false> DI void mfma_av(f32x4& acc, const u32x4& a, const u32x4& b) {
  if constexpr (FIRST) asm volatile("v_mfma_f32_16x16x32_bf16 %0, %1, %2, 0" : "=&v"(acc) : "a"(a), "v"(b));
  else asm volatile("v_mfma_f32_16x16x32_bf16 %0, %1, %2, %0" : "+v"(acc) : "a"(a), "v"(b));
}
template <int OFF> DI void ds_b64(u32x2& d, u32 addr) { asm volatile("ds_read_b64 %0, %1 offset:%c2" : "=v"(d) : "v"(addr), "i"(OFF) : "memory"); }
template <int OFF> DI void ds_b128(u32x4& d, u32 addr) { asm volatile("ds_read_b128 %0, %1 offset:%c2" : "=v"(d) : "v"(addr), "i"(OFF) : "memory"); }
template <int OFF> DI void ds_tr(u32x2& d, u32 addr) { asm volatile("ds_read_b64_tr_b16 %0, %1 offset:%c2" : "=v"(d) : "v"(addr), "i"(OFF) : "memory"); }
template <int OFF> DI void gld_a(u32x4& d, u32 voff, const void* sbase) {
  asm volatile("global_load_dwordx4 %0, %1, %2 offset:%c3" : "=a"(d) : "v"(voff), "s"(sbase), "i"(OFF) : "memory");
}
template <int OFF> DI void gld_v(u32x4& d, u32 voff, const void* sbase) {
  asm volatile("global_load_dwordx4 %0, %1, %2 offset:%c3" : "=v"(d) : "v"(voff), "s"(sbase), "i"(OFF) : "memory");
}
template <int OFF> DI void gld2_v(u32x2& d, u32 voff, const void* sbase) {
  asm volatile("global_load_dwordx2 %0, %1, %2 offset:%c3" : "=v"(d) : "v"(voff), "s"(sbase), "i"(OFF) : "memory");
}
template <int LOFF> DI void glds16(u32 voff, const void* sbase, u32 ldsbase) {
  asm volatile("s_add_u32 m0, %2, %c3\n\ts_nop 0\n\tglobal_load_lds_dwordx4 %0, %1" ::"v"(voff), "s"(sbase), "s"(ldsbase), "i"(LOFF) : "memory", "scc");
}
template <int N> DI void wait_vm() { asm volatile("s_waitcnt vmcnt(%c0)" ::"i"(N) : "memory"); }
template <int N> DI void wait_lgkm() { asm volatile("s_waitcnt lgkmcnt(%c0)" ::"i"(N) : "memory"); }
template <int OFF> DI void gld_a_sc1(u32x4& d, u32 voff, const void* sbase) {
  asm volatile("global_load_dwordx4 %0, %1, %2 offset:%c3 sc1" : "=a"(d) : "v"(voff), "s"(sbase), "i"(OFF) : "memory");
}
DI void mfma_aa(f32x4& acc, const u32x4& a, const u32x4& b) {
  asm volatile("v_mfma_f32_16x16x32_bf16 %0, %1, %2, %0" : "+v"(acc) : "a"(a), "a"(b));
}
template <bool FIRST = false> DI void mfma_va(f32x4& acc, const u32x4& a, const u32x4& b) {
  if constexpr (FIRST) asm volatile("v_mfma_f32_16x16x32_bf16 %0, %1, %2, 0" : "=&v"(acc) : "v"(a), "a"(b));
  else asm volatile("v_mfma_f32_16x16x32_bf16 %0, %1, %2, %0" : "+v"(acc) : "v"(a), "a"(b));
}
DI void mfma_fence(f32x4& a, f32x4& b, f32x4& c, f32x4& d) { asm volatile("s_nop 7\n\ts_nop 1" : "+v"(a), "+v"(b), "+v"(c), "+v"(d)); }
DI void after_fence(f32x4& a, f32x4& b, f32x4& c, f32x4& d) { asm volatile("" : "+v"(a), "+v"(b), "+v"(c), "+v"(d)); }
DI void after_fence(f32x4& a, f32x4& b, f32x4& c) { asm volatile("" : "+v"(a), "+v"(b), "+v"(c)); }
template <int STAGE> DI void dpp_stage(float& v) {
  if constexpr (STAGE == 0) asm volatile("v_add_f32_dpp %0, %0, %0 quad_perm:[1,0,3,2] row_mask:0xf bank_mask:0xf" : "+v"(v));
  if constexpr (STAGE == 1) asm volatile("v_add_f32_dpp %0, %0, %0 quad_perm:[2,3,0,1] row_mask:0xf bank_mask:0xf" : "+v"(v));
  if constexpr (STAGE == 2) asm volatile("v_add_f32_dpp %0, %0, %0 row_ror:4 row_mask:0xf bank_mask:0xf" : "+v"(v));
  if constexpr (STAGE == 3) asm volatile("v_add_f32_dpp %0, %0, %0 row_ror:8 row_mask:0xf bank_mask:0xf" : "+v"(v));
}
DI void barrier() { asm volatile("s_barrier" ::: "memory"); }
DI void pin(u32x4& x) { asm volatile("" : "+v"(x)); }
DI void pin(u32x2& x) { asm volatile("" : "+v"(x)); }
DI void pin4(f32x4& x) { asm volatile("" : "+v"(x)); }
DI void pin_a(u32x4& x) { asm volatile("" : "+a"(x)); }

#define STAMP(kern, idx) do {} while (0)

template <class Gate>
DI void w_job(int b, const u16* __restrict__ wg, const u16* __restrict__ wu, const u16* __restrict__ wd, char* __restrict__ ws, char* smem, Gate&& gate) {
  const int t = threadIdx.x, lane = t & 63, w = t >> 6;
  char* tile = smem;
  char* tile2 = smem + 32 * 368;
  const u32 tb = (u32)(size_t)(LDSP char*)smem;
  const int i16 = lane & 15, qd = i16 >> 2, p = i16 & 3, g = lane >> 4;
  const int ks = b & 15, j = b >> 4;
  const int U0 = slice_u0(j), nu = slice_nu(j);
  u32x4 g4[2], u4[2], d4[2];
#pragma unroll
  for (int rep = 0; rep < 2; ++rep) {
    int idx = t + 256 * rep, r = idx / 11, u = idx - 11 * r;
    bool ok = r < 32 && u < nu;
    size_t off = ok ? (size_t)(32 * ks + r) * F + 8 * (U0 + u) : 0;
    g4[rep] = *(const u32x4*)(wg + off); u4[rep] = *(const u32x4*)(wu + off);
  }
  const int ks2 = b >> 2, dp = b & 3;
  if (b < KS2 * 4) {
#pragma unroll
    for (int rep = 0; rep < 2; ++rep) {
      int idx = t + 256 * rep, r = idx >> 4, c = idx & 15;
      d4[rep] = *(const u32x4*)(wd + (size_t)(32 * ks2 + r) * D + 128 * dp + 8 * c);
    }
  }
  gate();
#pragma unroll
  for (int rep = 0; rep < 2; ++rep) {
    int idx = t + 256 * rep, r = idx / 11, u = idx - 11 * r;
    if (r < 32 && u < nu) {
      *(u32x4*)(tile + r * 368 + u * 32) = u32x4{g4[rep][0], u4[rep][0], g4[rep][1], u4[rep][1]};
      *(u32x4*)(tile + r * 368 + u * 32 + 16) = u32x4{g4[rep][2], u4[rep][2], g4[rep][3], u4[rep][3]};
    }
  }
  if (b < KS2 * 4) {
#pragma unroll
    for (int rep = 0; rep < 2; ++rep) { int idx = t + 256 * rep, r = idx >> 4, c = idx & 15; *(u32x4*)(tile2 + r * 272 + c * 16) = d4[rep]; }
  }
  __syncthreads();
  const u32x4 ones = {0x3f803f80u, 0x3f803f80u, 0x3f803f80u, 0x3f803f80u};
  const __amdgpu_buffer_rsrc_t wr = ws_rsrc(ws);
  for (int u = w; u < nu; u += 4) {
    u32 a = tb + (8 * g + qd) * 368 + u * 32 + 8 * p;
    u32x4 fr = tr_frag(a, a + 4 * 368);
    st16_wt(wr, (u32)WS_W1 + (((U0 + u) * KS1 + ks) * 64 + lane) * 16, fr);
    f32x4 cs = mfma16(fr, ones, f32x4{0.f, 0.f, 0.f, 0.f});
    if (i16 == 0) st16_wt(wr, (u32)WS_CS + ((ks * NU + U0 + u) * 16 + 4 * g) * 4, __builtin_bit_cast(u32x4, cs));
  }
  if (b < KS2 * 4) {
#pragma unroll
    for (int rep = 0; rep < 2; ++rep) {
      int fi = w + 4 * rep, dtl = fi >> 2, df = fi & 3;
      u32 a = tb + 32 * 368 + (8 * g + qd) * 272 + (64 * dtl + 32 * (df >> 1) + 8 * p + 4 * (df & 1)) * 2;
      u32x4 fr = tr_frag(a, a + 4 * 272);
      st16_wt(wr, (u32)WS_WD + ((((2 * dp + dtl) * KS2 + ks2) * 4 + df) * 64 + lane) * 16, fr);
    }
  }
}

constexpr int KA_CS = 4160;
constexpr int KA_KC = 4 * KA_CS;
constexpr int KA_XR = 2 * KA_KC;
constexpr int KA_RED = KA_XR + 2 * 4608;
constexpr int KA_LDS = KA_RED + 4 * 4096;
constexpr int KA_TK = KA_XR, KA_TV = KA_TK + 72 * 128, KA_TQ = KA_TV + 64 * 128;
static_assert(KA_TQ + 64 * 128 == KA_LDS, "tile overlay");
constexpr int SPIN_MAX = 1 << 22;
#define HINT_AT 1
#define HELP_SLEEP 40
#define HINT_TICKS 800ull

template <int OFF> DI void gld_v_sc1(u32x4& d, u32 voff, const void* sbase) {
  asm volatile("global_load_dwordx4 %0, %1, %2 offset:%c3 sc1" : "=v"(d) : "v"(voff), "s"(sbase), "i"(OFF) : "memory");
}
template <int OFF> DI void gld2_v_sc1(u32x2& d, u32 voff, const void* sbase) {
  asm volatile("global_load_dwordx2 %0, %1, %2 offset:%c3 sc1" : "=v"(d) : "v"(voff), "s"(sbase), "i"(OFF) : "memory");
}
DI bool tags_ok(const u32x4& f, const u32x2& tag) { return f[0] == tag[0] && f[1] == tag[1] && f[2] == tag[0] && f[3] == tag[1]; }

__global__ __launch_bounds__(256, 2) void ka_conv(const u16* __restrict__ q, const u16* __restrict__ k, const u16* __restrict__ v,
                                                  const u16* __restrict__ shk, const u16* __restrict__ ssmk, const u16* __restrict__ dskip,
                                                  const u16* __restrict__ wg, const u16* __restrict__ wu, const u16* __restrict__ wd,
                                                  char* __restrict__ ws) {
  extern __shared__ __attribute__((aligned(16))) char smem[];
  const int t = threadIdx.x, lane = t & 63;
  const int w = __builtin_amdgcn_readfirstlane(t >> 6);
  const int b = blockIdx.x;
  char* ws_stamp = ws; (void)ws_stamp;
  STAMP(0, 0);
  if (b >= 256) {
    __builtin_amdgcn_s_sleep(HELP_SLEEP);
    if (b < 264 && w == 1) {
      const char* pc; u32 len;
      asm volatile("s_getpc_b64 %0\n1:\n\ts_mov_b32 %1, kb_code_end-1b" : "=s"(pc), "=s"(len));
      const u32 lb = (u32)(size_t)(LDSP char*)smem + 32768;
      for (u32 o0 = 0; o0 < len; o0 += 64 * 128) {
        u32 o = o0 + lane * 128; o = o < len ? o : 0;
        asm volatile("s_mov_b32 m0, %2\n\ts_nop 0\n\tglobal_load_lds_dword %0, %1" ::"v"(o), "s"(pc), "s"(lb) : "memory");
      }
    }
    w_job(b - 256, wg, wu, wd, ws, smem, [&]() {
      if (w == 0) {
        const unsigned long long e = __hip_atomic_load((const unsigned long long*)(ws + WS_EPO), __ATOMIC_RELAXED, __HIP_MEMORY_SCOPE_AGENT);
        const unsigned long long* hp = (const unsigned long long*)(ws + WS_HNT) + (b - 256);
        const unsigned long long t0 = __builtin_amdgcn_s_memrealtime();
        while (__hip_atomic_load(hp, __ATOMIC_RELAXED, __HIP_MEMORY_SCOPE_AGENT) != e + 1 && __builtin_amdgcn_s_memrealtime() - t0 < HINT_TICKS)
          __builtin_amdgcn_s_sleep(8);
      }
      __builtin_amdgcn_s_barrier();
      STAMP(1, 5);
    });
    STAMP(1, 4);
    return;
  }
  const int x = b & 7, bi = b >> 3, s0 = 64 * bi;
  const int ch0 = 64 * x + 2 * bi;
  const u32 sb = (u32)(size_t)(LDSP char*)smem;
  const int n = lane & 15, g = lane >> 4, qd = n >> 2, p = n & 3;
  const int d = 64 * x + 16 * w + n;
  const int srow = 8 * b + 2 * w;
  const __amdgpu_buffer_rsrc_t wr = ws_rsrc(ws);

  u32x2 epo, sh; u32x4 qa, qb, kp[2], kc[2];
  gld2_v_sc1<0>(epo, 0, ws + WS_EPO);
  gld2_v<0>(sh, d * 8, shk);
  const int r8 = lane >> 3, c8 = lane & 7;
  auto voff = [&](int instr) { const int f = ((r8 >> 1) & 1) | ((instr & 1) << 1); return (u32)((((c8 >> 1) ^ f) * 2 + (c8 & 1)) * 16); };
  const size_t col = (size_t)x * 128;
  {
    int row = s0 - 8 + 8 * w + r8; row = row < 0 ? 0 : row;
    glds16<0>((u32)row * 1024 + voff(w), (const char*)k + col, sb + KA_TK + w * 1024);
    glds16<0>((u32)(s0 + 8 * w + r8) * 1024 + voff(w), (const char*)v + col, sb + KA_TV + w * 1024);
    glds16<0>((u32)(s0 - 8 + 8 * (w + 4) + r8) * 1024 + voff(w), (const char*)k + col, sb + KA_TK + (w + 4) * 1024);
    glds16<0>((u32)(s0 + 8 * (w + 4) + r8) * 1024 + voff(w), (const char*)v + col, sb + KA_TV + (w + 4) * 1024);
    if (w == 0) glds16<0>((u32)(s0 + 56 + r8) * 1024 + voff(8), (const char*)k + col, sb + KA_TK + 8 * 1024);
  }
#pragma unroll
  for (int cc = 0; cc < 2; ++cc) {
    const char* kr = (const char*)ssmk + (size_t)(ch0 + cc) * 4096;
    gld_v<0>(kp[cc], (u32)(t > 0 ? t - 1 : 0) * 16, kr);
    gld_v<0>(kc[cc], (u32)t * 16, kr);
  }
  glds16<0>((u32)(s0 + 8 * w + r8) * 1024 + voff(w), (const char*)q + col, sb + KA_TQ + w * 1024);
  glds16<0>((u32)(s0 + 8 * (w + 4) + r8) * 1024 + voff(w), (const char*)q + col, sb + KA_TQ + (w + 4) * 1024);
  gld_v<0>(qa, lane * 16, (const char*)q + (size_t)srow * 1024);
  gld_v<1024>(qb, lane * 16, (const char*)q + (size_t)srow * 1024);
  const float dsk = blo(dskip[ch0 + (w >> 1)]);
  if (b == 0 && t < 16) *(u32*)(ws + WS_CNT + t * 128) = 0u;

  wait_vm<8>();
  barrier();
  asm volatile("" : "+v"(sh), "+v"(epo));
  STAMP(0, 1);
  u32x2 tag;
  { const unsigned long long e = (((unsigned long long)epo[1] << 32) | epo[0]) + 1ull; tag = u32x2{(u32)e, (u32)(e >> 32)}; }
  auto taddr = [&](int base, int tr) { const int f = ((tr >> 1) & 1) | ((tr >> 2) & 2); return sb + base + tr * 128 + ((w ^ f) * 32) + 8 * p; };
  {
    const float a0 = blo(sh[0]), a1 = bhi(sh[0]), a2 = blo(sh[1]), a3 = bhi(sh[1]);
#pragma unroll
    for (int e = 0; e < 2; ++e) {
      const int rg = g + 4 * e;
      u32x2 km = tr_read(taddr(KA_TK, 8 * rg + 4 + qd));
      const u32x2 k0 = tr_read(taddr(KA_TK, 8 * rg + 8 + qd)), k1 = tr_read(taddr(KA_TK, 8 * rg + 12 + qd));
      const u32x2 v0 = tr_read(taddr(KA_TV, 8 * rg + qd)), v1 = tr_read(taddr(KA_TV, 8 * rg + 4 + qd));
      if (s0 + rg == 0) km = u32x2{0, 0};
      const float kf[11] = {bhi(km[0]), blo(km[1]), bhi(km[1]), blo(k0[0]), bhi(k0[0]), blo(k0[1]), bhi(k0[1]), blo(k1[0]), bhi(k1[0]), blo(k1[1]), bhi(k1[1])};
      const float vf[8] = {blo(v0[0]), bhi(v0[0]), blo(v0[1]), bhi(v0[1]), blo(v1[0]), bhi(v1[0]), blo(v1[1]), bhi(v1[1])};
      float xo[8];
#pragma unroll
      for (int j = 0; j < 8; ++j) xo[j] = (kf[j + 3] * a0 + kf[j + 2] * a1 + kf[j + 1] * a2 + kf[j] * a3) * vf[j];
      st16_wt(wr, (u32)WS_XR + (d * S + (2040 - (s0 + 8 * rg))) * 2, u32x4{pk2(xo[7], xo[6]), pk2(xo[5], xo[4]), pk2(xo[3], xo[2]), pk2(xo[1], xo[0])});
    }
  }
  asm volatile("s_waitcnt vmcnt(0)\n\ts_barrier" : "+v"(kp[0]), "+v"(kc[0]), "+v"(kp[1]), "+v"(kc[1]), "+v"(qa), "+v"(qb) :: "memory");
  if (t == 0) __builtin_amdgcn_raw_buffer_store_b64(tag, wr, (u32)WS_FLA + (x * 32 + bi) * 8, 0, 16);
  STAMP(0, 2);
#pragma unroll
  for (int e = 0; e < 2; ++e) {
    const int rg = g + 4 * e;
    const u32x2 q0 = tr_read(taddr(KA_TQ, 8 * rg + qd)), q1 = tr_read(taddr(KA_TQ, 8 * rg + 4 + qd));
    st16_wt(wr, (u32)WS_QT + (d * S + s0 + 8 * rg) * 2, cat(q0, q1));
  }
  {
    float sa = 0.f, sbb = 0.f;
#pragma unroll
    for (int e = 0; e < 4; ++e) { sa += blo(qa[e]) + bhi(qa[e]); sbb += blo(qb[e]) + bhi(qb[e]); }
    const float ma = wave_sum(sa) * (1.f / D), mb = wave_sum(sbb) * (1.f / D);
    float va = 0.f, vb = 0.f;
#pragma unroll
    for (int e = 0; e < 4; ++e) {
      float u0 = blo(qa[e]) - ma, u1 = bhi(qa[e]) - ma, u2 = blo(qb[e]) - mb, u3 = bhi(qb[e]) - mb;
      va += u0 * u0 + u1 * u1; vb += u2 * u2 + u3 * u3;
    }
    va = wave_sum(va) * (1.f / D); vb = wave_sum(vb) * (1.f / D);
    const float ra = __builtin_amdgcn_rsqf(va + EPS), rb = __builtin_amdgcn_rsqf(vb + EPS);
    const u32x2 val = lane == 0 ? u32x2{__float_as_uint(ra), __float_as_uint(rb)} : u32x2{__float_as_uint(-ma * ra), __float_as_uint(-mb * rb)};
    const u32 off = lane == 0 ? (u32)WS_SA + srow * 4 : lane == 1 ? (u32)WS_SB + srow * 4 : 0xfffffff0u;
    __builtin_amdgcn_raw_buffer_store_b64(val, wr, off, 0, 16);
  }
#pragma unroll
  for (int cc = 0; cc < 2; ++cc) {
    const u32 m = t > 0 ? 0xffffffffu : 0u;
    u32 wv[6] = {kp[cc][0] & m, kp[cc][1] & m, kp[cc][2] & m, kp[cc][3] & m, kc[cc][0], kc[cc][1]};
    u32 o1[5];
#pragma unroll
    for (int i = 0; i < 5; ++i) o1[i] = __builtin_amdgcn_alignbit(wv[i + 1], wv[i], 16);
    char* base = smem + cc * KA_KC + (t + 1) * 16;
    *(u32x4*)(base + 0 * KA_CS) = u32x4{o1[0], o1[1], o1[2], o1[3]};
    *(u32x4*)(base + 1 * KA_CS) = u32x4{wv[1], wv[2], wv[3], wv[4]};
    *(u32x4*)(base + 2 * KA_CS) = u32x4{o1[1], o1[2], o1[3], o1[4]};
    *(u32x4*)(base + 3 * KA_CS) = u32x4{wv[2], wv[3], wv[4], wv[5]};
    if (t == 255) {
      const u32 z = 0;
      const u32 e0 = __builtin_amdgcn_alignbit(kc[cc][1], kc[cc][0], 16), e1 = __builtin_amdgcn_alignbit(kc[cc][2], kc[cc][1], 16);
      const u32 e2 = __builtin_amdgcn_alignbit(kc[cc][3], kc[cc][2], 16), e3 = __builtin_amdgcn_alignbit(z, kc[cc][3], 16);
      *(u32x4*)(base + 16 + 0 * KA_CS) = u32x4{e0, e1, e2, e3};
      *(u32x4*)(base + 16 + 1 * KA_CS) = u32x4{kc[cc][1], kc[cc][2], kc[cc][3], z};
      *(u32x4*)(base + 16 + 2 * KA_CS) = u32x4{e1, e2, e3, z};
      *(u32x4*)(base + 16 + 3 * KA_CS) = u32x4{kc[cc][2], kc[cc][3], z, z};
    }
  }
  if (t < 8) *(u32x4*)(smem + (t >> 2) * KA_KC + (t & 3) * KA_CS) = u32x4{0, 0, 0, 0};
  asm volatile("s_waitcnt vmcnt(0) lgkmcnt(0)\n\ts_barrier" ::: "memory");
  if (t == 0) {
    __builtin_amdgcn_raw_buffer_store_b64(tag, wr, (u32)WS_FLG + (x * 32 + bi) * 8, 0, 16);
    if (HINT_AT == 0) __builtin_amdgcn_raw_buffer_store_b64(tag, wr, (u32)WS_HNT + b * 8, 0, 16);
  }
  if (t >= 64 && t < 64 + 60) { int i = t - 64; *(u32x4*)(smem + KA_XR + (i / 30) * 4608 + 4096 + (i % 30) * 16) = u32x4{0, 0, 0, 0}; }
  if (w == 0) {
    const unsigned long long* fp = (const unsigned long long*)(ws + WS_FLA) + x * 32 + (lane & 31);
    const unsigned long long want = ((unsigned long long)tag[1] << 32) | tag[0];
    for (int spins = 0; spins < SPIN_MAX; ++spins) {
      if (__all(__hip_atomic_load(fp, __ATOMIC_RELAXED, __HIP_MEMORY_SCOPE_AGENT) == want)) break;
      __builtin_amdgcn_s_sleep(1);
    }
  }
  asm volatile("s_waitcnt vmcnt(0)\n\ts_barrier" ::: "memory");
  STAMP(0, 3);

  const int c = w >> 1, half = w & 1;
  u32x2 qv[4]; u32x4 sa[4], sbv[4], fl[2];
  const u32 eo = (u32)(1024 * half + 16 * n + 4 * g);
  const char* qTc = ws + WS_QT + (size_t)(ch0 + c) * 4096;
  {
    u32x4 x0, x1;
    gld_v_sc1<0>(x0, t * 16, ws + WS_XR + (size_t)ch0 * 4096);
    gld_v_sc1<0>(x1, t * 16, ws + WS_XR + (size_t)(ch0 + 1) * 4096);
    wait_vm<0>();
    asm volatile("" : "+v"(x0), "+v"(x1));
    *(u32x4*)(smem + KA_XR + t * 16) = x0;
    *(u32x4*)(smem + KA_XR + 4608 + t * 16) = x1;
  }
  asm volatile("s_waitcnt lgkmcnt(0)\n\ts_barrier" ::: "memory");
  if (HINT_AT == 1 && t == 0) __builtin_amdgcn_raw_buffer_store_b64(tag, wr, (u32)WS_HNT + b * 8, 0, 16);
  STAMP(1, 1);

  const u32 aa = sb + c * KA_KC + (n & 3) * KA_CS + 16 * g + 2 * (n & 12) + half * 256;
  const u32 ba = sb + KA_XR + c * 4608 + 480 - 32 * n + 16 * g + half * 256;
  f32x4 acc[8];
  u32x2 Alo[2][8], Ahi[2][8];
  u32x4 B[2][8];
  auto rd = [&](auto C0, auto R) {
    constexpr int c0 = C0, r = R, buf = c0 & 1;
    if constexpr (r >= 2 && r < 10) {
      constexpr int u = r - 2;
      ds_b128<512 * (7 - u) + 64 * c0>(B[buf][u], ba);
    } else {
      constexpr int ar = r < 2 ? r : r - 8, i = ar >> 1;
      if constexpr ((ar & 1) == 0) ds_b64<512 * i + 64 * c0>(Alo[buf][i], aa);
      else ds_b64<512 * i + 64 * c0 + 8>(Ahi[buf][i], aa);
    }
  };
  auto ldS = [&](auto E) {
    constexpr int e = E, i = e / 3, kind = e % 3;
    if constexpr (kind == 0) gld_v_sc1<i * 1024>(sa[i], eo * 4, ws + WS_SA);
    if constexpr (kind == 1) gld_v_sc1<i * 1024>(sbv[i], eo * 4, ws + WS_SB);
    if constexpr (kind == 2) gld2_v_sc1<i * 512>(qv[i], eo * 2, qTc);
  };
  static_for<24>([&](auto R) { rd(std::integral_constant<int, 0>{}, R); });
  static_for<4>([&](auto C0) {
    constexpr int c0 = C0, buf = c0 & 1;
    if constexpr (c0 == 2) {
      wait_vm<0>();
      asm volatile("" : "+v"(fl[0]), "+v"(fl[1]));
      if (!__all(tags_ok(fl[0], tag) && tags_ok(fl[1], tag))) {
        for (int spins = 0; spins < SPIN_MAX; ++spins) {
          gld_v_sc1<0>(fl[0], lane * 32, ws + WS_FLG); gld_v_sc1<16>(fl[1], lane * 32, ws + WS_FLG);
          wait_vm<0>();
          asm volatile("" : "+v"(fl[0]), "+v"(fl[1]));
          if (__all(tags_ok(fl[0], tag) && tags_ok(fl[1], tag))) break;
          __builtin_amdgcn_s_sleep(1);
        }
      }
      if (b == 0 && t == 0) __builtin_amdgcn_raw_buffer_store_b64(tag, wr, (u32)WS_EPO, 0, 16);
    }
    u32x4 A[8];
    static_for<36>([&](auto M) {
      constexpr int m = M;
      constexpr int c1 = m < 8 ? 0 : m < 15 ? 1 : m < 21 ? 2 : m < 26 ? 3 : m < 30 ? 4 : m < 33 ? 5 : m < 35 ? 6 : 7;
      constexpr int first = c1 * 8 - c1 * (c1 - 1) / 2;
      constexpr int u = m - first;
      if constexpr (m == 0) {
        if constexpr (c0 == 0) wait_lgkm<14>(); else wait_lgkm<0>();
        pin(Alo[buf][0]); pin(Ahi[buf][0]);
#pragma unroll
        for (int i = 0; i < 8; ++i) pin(B[buf][i]);
        A[0] = cat(Alo[buf][0], Ahi[buf][0]);
      }
      if constexpr (m == 8) {
        if constexpr (c0 == 0) wait_lgkm<8>();
#pragma unroll
        for (int i = 1; i < 8; ++i) { pin(Alo[buf][i]); pin(Ahi[buf][i]); A[i] = cat(Alo[buf][i], Ahi[buf][i]); }
      }
      mfma_vv<(c0 == 0 && c1 == 0)>(acc[c1 + u], A[c1], B[buf][u]);
      if constexpr (c0 < 3 && m < 24) rd(std::integral_constant<int, c0 + 1>{}, M);
      if constexpr (c0 == 1 && m == 2) gld_v_sc1<0>(fl[0], lane * 32, ws + WS_FLG);
      if constexpr (c0 == 1 && m == 4) gld_v_sc1<16>(fl[1], lane * 32, ws + WS_FLG);
      if constexpr (c0 == 2 && m >= 1 && m < 25 && (m & 1) == 1) ldS(std::integral_constant<int, (m - 1) / 2>{});
    });
  });
  mfma_fence(acc[0], acc[1], acc[2], acc[3]);
  after_fence(acc[4], acc[5], acc[6], acc[7]);
  STAMP(1, 2);
  f32x4 fin[4];
  {
    f32x4* red = (f32x4*)(smem + KA_RED);
    if (half) {
#pragma unroll
      for (int i = 0; i < 4; ++i) { red[(w * 4 + i) * 64 + lane] = acc[i]; fin[i] = acc[4 + i]; }
      asm volatile("; half 1" ::: "memory");
    } else {
#pragma unroll
      for (int i = 0; i < 4; ++i) { red[(w * 4 + i) * 64 + lane] = acc[4 + i]; fin[i] = acc[i]; }
      asm volatile("; half 0" ::: "memory");
    }
    asm volatile("s_waitcnt lgkmcnt(0)\n\ts_barrier" ::: "memory");
#pragma unroll
    for (int i = 0; i < 4; ++i) fin[i] += red[((w ^ 1) * 4 + i) * 64 + lane];
  }
  wait_vm<0>();
#pragma unroll
  for (int i = 0; i < 4; ++i) asm volatile("" : "+v"(qv[i]), "+v"(sa[i]), "+v"(sbv[i]));
  const f32x2 dd = {dsk, dsk};
  u32x2 hv[4];
#pragma unroll
  for (int i = 0; i < 4; ++i) {
    const int s = 256 * (4 * half + i) + 16 * n + 4 * g;
    const u32x2 xq = *(LDSP const u32x2*)(size_t)(sb + KA_XR + c * 4608 + 2 * (2044 - s));
    const f32x2 x0 = {bhi(xq[1]), blo(xq[1])}, x1 = {bhi(xq[0]), blo(xq[0])};
    const f32x2 q0 = {blo(qv[i][0]), bhi(qv[i][0])}, q1 = {blo(qv[i][1]), bhi(qv[i][1])};
    const f32x2 z0 = x0 * dd + f32x2{fin[i][0], fin[i][1]}, z1 = x1 * dd + f32x2{fin[i][2], fin[i][3]};
    const f32x4 a4 = __builtin_bit_cast(f32x4, sa[i]), b4 = __builtin_bit_cast(f32x4, sbv[i]);
    const f32x2 t0 = q0 * f32x2{a4[0], a4[1]} + f32x2{b4[0], b4[1]};
    const f32x2 t1 = q1 * f32x2{a4[2], a4[3]} + f32x2{b4[2], b4[3]};
    const f32x2 h0 = t0 * z0 + q0, h1 = t1 * z1 + q1;
    hv[i] = u32x2{pk2(h0[0], h0[1]), pk2(h1[0], h1[1])};
  }
#pragma unroll
  for (int i = 0; i < 4; i += 2) {
    auto a = __builtin_amdgcn_permlane16_swap(hv[i][0], hv[i + 1][0], false, false);
    auto bb = __builtin_amdgcn_permlane16_swap(hv[i][1], hv[i + 1][1], false, false);
    const int s = 256 * (4 * half + i + (g & 1)) + 16 * n + 4 * (g & 2);
    st16_wt(wr, (u32)WS_HT + ((ch0 + c) * S + s) * 2, u32x4{a[0], bb[0], a[1], bb[1]});
  }
  STAMP(1, 3);
}

constexpr int KB_NL = 39;
constexpr int KB_ST = KB_NL * 4096;
constexpr int KB_LDS = KB_ST + 1024;
#define EPI_FILL 8
constexpr int KB_RING = 8;

#define P2_AHEAD 4
#define P2_PRO 3
constexpr int p2_upto(int ks) {
  const int ramp = P2_PRO - 1 + 2 * (ks + 1), steady = ks + P2_AHEAD;
  const int u = ks < 0 ? P2_PRO - 1 : (ramp < steady ? ramp : steady);
  return u > 15 ? 15 : u;
}
#define P2_XMAX 5
constexpr int P2_NEXTRA = 23;
constexpr int p2_extra(int ks) {
  const int tail0 = 16 - P2_AHEAD, rest = P2_NEXTRA - P2_AHEAD * P2_XMAX;
  if (ks >= tail0) return P2_XMAX;
  return (rest > 0 && ks >= tail0 - rest) ? 1 : 0;
}
constexpr int p2_extra_first(int ks) { int c = 0; for (int s = 0; s < ks; ++s) c += p2_extra(s); return c; }
constexpr int p2_extra_real(int ks) { int f = p2_extra_first(ks), n = p2_extra(ks); return f >= P2_NEXTRA ? 0 : (f + n > P2_NEXTRA ? P2_NEXTRA - f : n); }
static_assert(p2_extra_first(16) >= P2_NEXTRA, "all Wd operations are scheduled");
constexpr int p2_issue_step(int t) { for (int s = -1; s < KS1; ++s) if (p2_upto(s) >= t) return s; return KS1; }
constexpr int p2_younger(int t, int ks, int ops) {
  int c = (p2_upto(ks - 1) - t) * ops;
  for (int s = p2_issue_step(t); s < ks; ++s) if (s >= 0) c += p2_extra_real(s);
  return c;
}

struct KbCtx {
  u32 sb, gv, lv, lb0, lwd, csv;
  const char *hb0, *hb1, *w0, *w1, *w2, *cs0, *cs1, *cs2, *wd, *wdr;
};

template <int GRAM, class Setup>
DI void kb_phase2(const KbCtx& c, f32x4 (&acc)[3][8], f32x4 (&rsum)[2], f32x4 (&csp)[3], u32x4 (&Wr)[4][4], u32 (&ra)[8], Setup&& setup, char* ws_stamp) {
  constexpr int OPS = GRAM ? 4 : 5;
  u32x4 W[KB_RING][3];
  u32x2 Bl[2][8], Bh[2][8];
  auto issue = [&](auto KS, auto O) {
    constexpr int ks = KS, o = O;
    if constexpr (o == 0) glds16<ks * 8192>(c.gv, c.hb0 + (size_t)ks * 131072, c.lb0);
    if constexpr (o == 1) gld_a<(ks & 3) * 1024>(W[ks % KB_RING][0], c.lv, c.w0 + (ks >> 2) * 4096);
    if constexpr (o == 2) gld_a<(ks & 3) * 1024>(W[ks % KB_RING][1], c.lv, c.w1 + (ks >> 2) * 4096);
    if constexpr (o == 3 && !GRAM) gld_a<(ks & 3) * 1024>(W[ks % KB_RING][2], c.lv, c.w2 + (ks >> 2) * 4096);
    if constexpr (o == 4) glds16<ks * 8192 + 1024>(c.gv, c.hb1 + (size_t)ks * 131072, c.lb0);
  };
  auto extra = [&](auto E) {
    constexpr int e = E;
    if constexpr (e < 7) glds16<(32 + e) * 4096>(c.lv, c.wd + (size_t)(32 + e) * 4096, c.lwd);
    else if constexpr (e < P2_NEXTRA) gld_a<((e - 7) & 3) * 1024>(Wr[(e - 7) >> 2][(e - 7) & 3], c.lv, c.wdr + ((e - 7) >> 2) * 4096);
  };
  auto rdB = [&](auto KS, auto R) {
    constexpr int ks = KS, r = R, i = r >> 1;
    if constexpr ((r & 1) == 0) ds_tr<(ks & 7) * 8192>(Bl[ks & 1][i], ra[i] + (ks >= 8 ? 65536 : 0));
    else ds_tr<(ks & 7) * 8192 + 1024>(Bh[ks & 1][i], ra[i] + (ks >= 8 ? 65536 : 0));
  };
  u32x4 c0, c1, c2;
  static_for<5>([&](auto O) { issue(std::integral_constant<int, 0>{}, O); });
  gld_v<0>(c0, c.csv, c.cs0); gld_v<0>(c1, c.csv, c.cs1); gld_v<0>(c2, c.csv, c.cs2);
  static_for<p2_upto(-1)>([&](auto KS) { static_for<5>([&](auto O) { issue(std::integral_constant<int, KS + 1>{}, O); }); });
  setup();
  u32x4 ones = {0x3f803f80u, 0x3f803f80u, 0x3f803f80u, 0x3f803f80u};
  pin(ones);
  wait_vm<p2_upto(-1) * OPS>();
  barrier();
  static_for<16>([&](auto R) { rdB(std::integral_constant<int, 0>{}, R); });
  asm volatile("" : "+v"(c0), "+v"(c1), "+v"(c2));
  float cv[12];
#pragma unroll
  for (int e = 0; e < 4; ++e) { cv[e] = __uint_as_float(c0[e]); cv[4 + e] = __uint_as_float(c1[e]); cv[8 + e] = __uint_as_float(c2[e]); }
  static_for<KS1>([&](auto KS) {
    constexpr int ks = KS, buf = ks & 1, slot = ks % KB_RING;
    constexpr int nmain = (p2_upto(ks) - p2_upto(ks - 1)) * 5, next = p2_extra_real(ks);
    wait_lgkm<0>();
    u32x4 B[8];
#pragma unroll
    for (int i = 0; i < 8; ++i) { pin(Bl[buf][i]); pin(Bh[buf][i]); }
#pragma unroll
    for (int i = 0; i < 8; ++i) B[i] = cat(Bl[buf][i], Bh[buf][i]);
    pin_a(W[slot][0]); pin_a(W[slot][1]);
    if constexpr (!GRAM) pin_a(W[slot][2]);
    static_for<26>([&](auto M) {
      constexpr int m = M, i = m / 3, u = m % 3;
      if constexpr (m >= 24) mfma_vv<ks == 0>(rsum[m - 24], ones, B[m - 24]);
      else if constexpr (u < 2 || !GRAM) mfma_av<ks == 0>(acc[u][i], W[slot][u], B[i]);
      else mfma_vv<ks == 0>(acc[2][i], B[i], B[i]);
      if constexpr (m == 1 && ks + 1 < KS1) { wait_vm<p2_younger(ks + 1, ks, OPS)>(); barrier(); }
      if constexpr (m >= 2 && m < 18 && ks + 1 < KS1) rdB(std::integral_constant<int, ks + 1>{}, std::integral_constant<int, m - 2>{});
      if constexpr (ks >= 9 && ks < 13 && m >= 2 && m < 14) dpp_stage<ks - 9>(cv[m - 2]);
      constexpr int v = m - (26 - nmain - next);
      if constexpr (v >= 0 && v < nmain) issue(std::integral_constant<int, p2_upto(ks - 1) + 1 + v / 5>{}, std::integral_constant<int, v % 5>{});
      if constexpr (v >= nmain) extra(std::integral_constant<int, p2_extra_first(ks) + v - nmain>{});
    });
  });
  mfma_fence(acc[0][0], acc[1][0], rsum[0], rsum[1]);
#pragma unroll
  for (int i = 0; i < 8; ++i) after_fence(acc[0][i], acc[1][i], acc[2][i], rsum[i & 1]);
#pragma unroll
  for (int u = 0; u < 3; ++u) csp[u] = f32x4{cv[4 * u], cv[4 * u + 1], cv[4 * u + 2], cv[4 * u + 3]};
}

__global__ __launch_bounds__(256) void kb_mlp(char* __restrict__ ws, u16* __restrict__ out) {
  extern __shared__ __attribute__((aligned(16))) char smem[];
  const int t = threadIdx.x, lane = t & 63;
  const int w = __builtin_amdgcn_readfirstlane(t >> 6);
  const int b = blockIdx.x, xcd = b & 7, bi = b >> 3;
#define KB_MAP 1
  const int rt = KB_MAP == 0 ? 2 * xcd + (bi & 1) : (xcd >> 1) * 4 + (bi & 3);
  const int j = KB_MAP == 0 ? bi >> 1 : (xcd & 1) * 8 + (bi >> 2);
  const int half = KB_MAP == 0 ? j & 1 : j >> 3, dt = KB_MAP == 0 ? j >> 1 : j & 7;
  const int U0 = slice_u0(j), nu = slice_nu(j);
  const int rem = nu - 8;
  const int nw = w < rem ? 3 : 2;
  const int uw = U0 + (w < rem ? 3 * w : 3 * rem + 2 * (w - rem));
  const int u2 = uw + (nw == 3 ? 2 : 0);
  const int n = lane & 15, g = lane >> 4;
  const int s0 = 128 * rt;
  char* ws_stamp = ws; (void)ws_stamp;
  STAMP(2, 0);
  auto sf = [&](int i) { return ((w + (i >> 1)) & 3) + 4 * (half ^ (i & 1)); };

  KbCtx c;
  c.sb = (u32)(size_t)(LDSP char*)smem;
  c.lv = lane * 16;
  c.gv = (u32)(g * 4096 + (((n >> 1) ^ (g | ((w & 1) << 2))) * 32 + (n & 1) * 16));
  c.hb0 = ws + WS_HT + (size_t)s0 * 2 + (size_t)(8 * w) * 4096;
  c.hb1 = c.hb0 + 4 * 4096;
  c.lb0 = c.sb + 8 * w * 256;
  c.w0 = ws + WS_W1 + (size_t)uw * KS1 * 1024;
  c.w1 = c.w0 + KS1 * 1024;
  c.w2 = ws + WS_W1 + (size_t)u2 * KS1 * 1024;
  c.cs0 = ws + WS_CS + (size_t)uw * 64;
  c.cs1 = c.cs0 + 64;
  c.cs2 = ws + WS_CS + (size_t)u2 * 64;
  c.csv = n * (NU * 64) + g * 16;
  c.wdr = ws + WS_WD + (size_t)dt * KS2 * 4096 + (size_t)KB_NL * 4096;
  c.wd = ws + WS_WD + (size_t)dt * KS2 * 4096 + (size_t)w * 1024;
  c.lwd = c.sb + 1024 * w;
  u32 ra[8], hoff[6];
  auto setup = [&]() {
    const int qd = n >> 2, p = n & 3;
    const int frow = qd | ((g & 1) << 2);
#pragma unroll
    for (int i = 0; i < 8; ++i) { ra[i] = c.sb + (8 * g + qd) * 256 + ((sf(i) ^ frow) * 32) + 8 * p; asm volatile("" : "+v"(ra[i])); }
#pragma unroll
    for (int tt = 0; tt < 6; ++tt) {
      const int q = 4 * tt + g, i = (q * 11) >> 5, u = q - 3 * i, U = uw + u;
      const u32 o = (u32)(((8 * rt + sf(i)) * KS2 + (U >> 2)) * 1024 + ((U & 3) * 16 + n) * 16);
      hoff[tt] = u < nw ? o : 0xfffffff0u;
      asm volatile("" : "+v"(hoff[tt]));
    }
  };
  f32x4 acc[3][8], cs[3], rsum[2];
  u32x4 Wr[4][4];
  if (w == 3) kb_phase2<1>(c, acc, rsum, cs, Wr, ra, setup, ws_stamp);
  else kb_phase2<0>(c, acc, rsum, cs, Wr, ra, setup, ws_stamp);
  STAMP(2, 2);

  u32x2 res[4];
  {
    const u32 rb = ra[0] + dt * 16384;
    ds_tr<0>(res[0], rb); ds_tr<1024>(res[1], rb); ds_tr<8192>(res[2], rb); ds_tr<9216>(res[3], rb);
  }
  float* stl = (float*)(smem + KB_ST);
  if (g == 0) {
#pragma unroll
    for (int i = 0; i < 2; ++i) stl[2 * (16 * sf(i) + n)] = rsum[i][0];
  }
  if (w == 3 && g == (n >> 2)) {
#pragma unroll
    for (int i = 0; i < 8; ++i) {
      f32x4 a = acc[2][i];
      float dg = (n & 2) ? ((n & 1) ? a[3] : a[2]) : ((n & 1) ? a[1] : a[0]);
      stl[2 * (16 * sf(i) + n) + 1] = dg;
    }
  }
  asm volatile("s_waitcnt lgkmcnt(0)\n\ts_barrier" : "+v"(res[0]), "+v"(res[1]), "+v"(res[2]), "+v"(res[3]) :: "memory");
  STAMP(2, 3);
  const __amdgpu_buffer_rsrc_t hrs = __builtin_amdgcn_make_buffer_rsrc(ws + WS_H, 0, (S / 16) * KS2 * 1024, 0x00020000);
  f32x2 sv[8];
#pragma unroll
  for (int i = 0; i < 8; ++i) sv[i] = *(const f32x2*)(stl + 2 * (16 * sf(i) + n));
  u32 hv[24];
  static_for<8>([&](auto I) {
    constexpr int i = I;
    const float mean = sv[i][0] * (1.f / D);
    const float var = fmaxf(sv[i][1] * (1.f / D) - mean * mean, 0.f) + EPS;
    const float rstd = __builtin_amdgcn_rsqf(var);
    const float rinv = var * rstd;
    const float ce = -1.4426950408889634f * rstd;
    const f32x2 nm = {-mean, -mean}, ce2 = {ce, ce}, ri2 = {rinv, rinv};
    static_for<3>([&](auto U_) {
      constexpr int u = U_;
      pin4(acc[u][i]);
      constexpr int slot = 3 * i + u;
      constexpr int wk = slot * EPI_FILL / 24;
      if constexpr (EPI_FILL > 0 && (slot + 1) * EPI_FILL / 24 > wk) glds16<wk * 4096>(c.lv, c.wd + (size_t)wk * 4096, c.lwd);
      const f32x4 a = acc[u][i];
      const f32x2 tg = (f32x2{a[0], a[1]} + nm * f32x2{cs[u][0], cs[u][1]}) * ce2;
      const f32x2 tu = f32x2{a[2], a[3]} + nm * f32x2{cs[u][2], cs[u][3]};
      const f32x2 e = {__builtin_amdgcn_exp2f(tg[0]), __builtin_amdgcn_exp2f(tg[1])};
      const f32x2 den = e * ri2 + ri2;
      const f32x2 o = tu * f32x2{__builtin_amdgcn_rcpf(den[0]), __builtin_amdgcn_rcpf(den[1])};
      hv[3 * i + u] = pk2(o[0], o[1]);
    });
    static_for<6>([&](auto T) {
      constexpr int tt = T;
      if constexpr ((4 * tt + 3) / 3 == i) {
        auto s02 = __builtin_amdgcn_permlane32_swap(hv[4 * tt], hv[4 * tt + 2], false, false);
        auto s13 = __builtin_amdgcn_permlane32_swap(hv[4 * tt + 1], hv[4 * tt + 3], false, false);
        auto t01 = __builtin_amdgcn_permlane16_swap(s02[0], s13[0], false, false);
        auto t23 = __builtin_amdgcn_permlane16_swap(s02[1], s13[1], false, false);
        __builtin_amdgcn_raw_buffer_store_b128(u32x4{t01[0], t01[1], t23[0], t23[1]}, hrs, hoff[tt], 0, 16);
      }
    });
  });
  STAMP(2, 4);
  u32* cnt = (u32*)(ws + WS_CNT + rt * 128);
  asm volatile("s_waitcnt vmcnt(0)\n\ts_barrier" ::: "memory");
  STAMP(2, 5);
  if (w == 0) {
    if (lane == 0) __hip_atomic_fetch_add(cnt, 1u, __ATOMIC_RELAXED, __HIP_MEMORY_SCOPE_AGENT);
    for (int spins = 0; spins < SPIN_MAX; ++spins) {
      const u32 v = __builtin_amdgcn_readfirstlane(__hip_atomic_load(cnt, __ATOMIC_RELAXED, __HIP_MEMORY_SCOPE_AGENT));
      if (v >= 16u) break;
      __builtin_amdgcn_s_sleep(1);
    }
    wait_vm<0>();
  } else {
    const char* wsrc = c.wd - (size_t)w * 1024;
    for (int e = EPI_FILL + w - 1; e < 32; e += 3) {
      const char* src = wsrc + (size_t)e * 4096;
      const u32 dst = c.sb + e * 4096;
      glds16<0>(c.lv, src, dst); glds16<1024>(c.lv, src + 1024, dst); glds16<2048>(c.lv, src + 2048, dst); glds16<3072>(c.lv, src + 3072, dst);
    }
  }
  barrier();
  STAMP(2, 6);

  const char* hb = ws + WS_H + (size_t)(8 * rt + 4 * half + w) * KS2 * 1024;
  u32x4 Hf[KS2];
  constexpr int P3_AHEAD = 12;
  auto ldH = [&](auto KS) { constexpr int ks = KS; if constexpr (ks < KS2) gld_a_sc1<(ks & 3) * 1024>(Hf[ks], c.lv, hb + (ks >> 2) * 4096); };
  static_for<P3_AHEAD>([&](auto KS) { ldH(KS); });
  STAMP(3, 0);
  f32x4 o4[4];
  u32x4 Af[3][4];
  const u32 la0 = c.sb + lane * 16, la1 = la0 + 65536, la2 = la0 + 131072;
  auto rdA = [&](auto KS, auto DF) {
    constexpr int ks = KS, df = DF;
    if constexpr (ks < KB_NL) ds_b128<(ks & 15) * 4096 + df * 1024>(Af[ks % 3][df], ks < 16 ? la0 : ks < 32 ? la1 : la2);
  };
  wait_vm<P3_AHEAD>();
  barrier();
  static_for<4>([&](auto DF) { rdA(std::integral_constant<int, 0>{}, DF); });
  static_for<4>([&](auto DF) { rdA(std::integral_constant<int, 1>{}, DF); });
  static_for<KS2>([&](auto KS) {
    constexpr int ks = KS;
    wait_vm<(ks + P3_AHEAD <= KS2 ? P3_AHEAD - 1 : KS2 - 1 - ks)>();
    if constexpr (ks + 1 < KB_NL) wait_lgkm<4>(); else wait_lgkm<0>();
    pin_a(Hf[ks]);
    static_for<4>([&](auto DF) {
      constexpr int df = DF;
      if constexpr (ks < KB_NL) { pin(Af[ks % 3][df]); mfma_va<ks == 0>(o4[df], Af[ks % 3][df], Hf[ks]); }
      else { pin_a(Wr[ks - KB_NL][df]); mfma_aa(o4[df], Wr[ks - KB_NL][df], Hf[ks]); }
      rdA(std::integral_constant<int, ks + 2>{}, DF);
      if constexpr (df == 1 && ks + P3_AHEAD < KS2) ldH(std::integral_constant<int, ks + P3_AHEAD>{});
    });
  });
  mfma_fence(o4[0], o4[1], o4[2], o4[3]);
  u32x4 o0, o1;
#pragma unroll
  for (int df = 0; df < 2; ++df) {
    o0[2 * df] = pk2(o4[df][0] + blo(res[df][0]), o4[df][1] + bhi(res[df][0]));
    o0[2 * df + 1] = pk2(o4[df][2] + blo(res[df][1]), o4[df][3] + bhi(res[df][1]));
    o1[2 * df] = pk2(o4[2 + df][0] + blo(res[2 + df][0]), o4[2 + df][1] + bhi(res[2 + df][0]));
    o1[2 * df + 1] = pk2(o4[2 + df][2] + blo(res[2 + df][1]), o4[2 + df][3] + bhi(res[2 + df][1]));
  }
  const __amdgpu_buffer_rsrc_t ors = __builtin_amdgcn_make_buffer_rsrc(out, 0, S * D * 2, 0x00020000);
  const u32 ooff = (u32)((s0 + 64 * half + 16 * w + n) * D + 64 * dt + 8 * g) * 2;
  __builtin_amdgcn_raw_buffer_store_b128(o0, ors, ooff, 0, 16);
  __builtin_amdgcn_raw_buffer_store_b128(o1, ors, ooff + 64, 0, 16);
  asm volatile("kb_code_end:");
  STAMP(2, 7);
}

extern "C" void kernel_launch(void* const* d_in, const int* in_sizes, int n_in, void* d_out, int out_size, void* d_ws,
                              size_t ws_size, hipStream_t stream) {
  const u16 *q = (const u16*)d_in[0], *k = (const u16*)d_in[1], *v = (const u16*)d_in[2], *shk = (const u16*)d_in[3];
  const u16 *ssmk = (const u16*)d_in[4], *dskip = (const u16*)d_in[5];
  const u16 *wg = (const u16*)d_in[7], *wu = (const u16*)d_in[8], *wd = (const u16*)d_in[9];
  char* ws = (char*)d_ws + ((ws_size - ((size_t)32  __H0__ )) & ~(size_t)0x1fffff);
  (void)hipFuncSetAttribute((const void*)ka_conv, hipFuncAttributeMaxDynamicSharedMemorySize, KA_LDS);
  (void)hipFuncSetAttribute((const void*)kb_mlp, hipFuncAttributeMaxDynamicSharedMemorySize, KB_LDS);
  ka_conv<<<512, 256, KA_LDS, stream>>>(q, k, v, shk, ssmk, dskip, wg, wu, wd, ws);
  kb_mlp<<<256, 256, KB_LDS, stream>>>(ws, (u16*)d_out);
}
```

```cpp
#include <hip/hip_runtime.h>
#include <stdint.h>
#include <utility>

typedef unsigned short u16;
typedef unsigned u32;
typedef __attribute__((ext_vector_type(2))) u32 u32x2;
typedef __attribute__((ext_vector_type(4))) u32 u32x4;
typedef __attribute__((ext_vector_type(2))) float f32x2;
typedef __attribute__((ext_vector_type(4))) float f32x4;
typedef __attribute__((ext_vector_type(2))) __bf16 bf16x2;
typedef __attribute__((ext_vector_type(8))) __bf16 bf16x8;
typedef __attribute__((ext_vector_type(4))) short s16x4;
#define LDSP __attribute__((address_space(3)))
#define DI __device__ __forceinline__

constexpr int S = 2048, D = 512, F = 1376;
constexpr int NU = F / 8;
constexpr int KS1 = D / 32;
constexpr int KS2 = F / 32;
constexpr float EPS = 1e-5f;

constexpr size_t WS_XR = 0;
constexpr size_t WS_QT = WS_XR + (size_t)D * S * 2;
constexpr size_t WS_HT = WS_QT + (size_t)D * S * 2;
constexpr size_t WS_W1 = WS_HT + (size_t)D * S * 2;
constexpr size_t WS_WD = WS_W1 + (size_t)NU * KS1 * 1024;
constexpr size_t WS_H = WS_WD + (size_t)8 * KS2 * 4 * 1024;
constexpr size_t WS_CS = WS_H + (size_t)(S / 16) * KS2 * 1024;
constexpr size_t WS_SA = WS_CS + (size_t)KS1 * NU * 16 * 4;
constexpr size_t WS_SB = WS_SA + (size_t)S * 4;
constexpr size_t WS_CNT = WS_SB + (size_t)S * 4;
constexpr size_t WS_FLA = WS_CNT + 16 * 128;
constexpr size_t WS_FLG = WS_FLA + 256 * 8;
constexpr size_t WS_HNT = WS_FLG + 256 * 8;
constexpr size_t WS_EPO = WS_HNT + 256 * 8;

template <int... Is, class Fn> DI void static_for_(std::integer_sequence<int, Is...>, Fn&& f) { (f(std::integral_constant<int, Is>{}), ...); }
template <int N, class Fn> DI void static_for(Fn&& f) { static_for_(std::make_integer_sequence<int, N>{}, f); }

DI float blo(u32 u) { return __uint_as_float(u << 16); }
DI float bhi(u32 u) { return __uint_as_float(u & 0xffff0000u); }
DI u32 pk2(float a, float b) { f32x2 t = {a, b}; return __builtin_bit_cast(u32, __builtin_convertvector(t, bf16x2)); }
DI f32x4 mfma16(u32x4 a, u32x4 b, f32x4 c) {
  return __builtin_amdgcn_mfma_f32_16x16x32_bf16(__builtin_bit_cast(bf16x8, a), __builtin_bit_cast(bf16x8, b), c, 0, 0, 0);
}
DI u32x2 tr_read(u32 lds_byte) {
  return __builtin_bit_cast(u32x2, __builtin_amdgcn_ds_read_tr16_b64_v4i16((LDSP s16x4*)(size_t)lds_byte));
}
DI u32x4 tr_frag(u32 a0, u32 a1) { u32x2 x = tr_read(a0), y = tr_read(a1); return u32x4{x[0], x[1], y[0], y[1]}; }
DI u32x4 cat(u32x2 x, u32x2 y) { return __builtin_shufflevector(x, y, 0, 1, 2, 3); }
template <int CTRL> DI float dpp_add(float v) {
  return v + __builtin_bit_cast(float, __builtin_amdgcn_update_dpp(0, __builtin_bit_cast(int, v), CTRL, 0xf, 0xf, true));
}
DI float sum_rows(float v) {
  u32 u = __float_as_uint(v);
  auto a = __builtin_amdgcn_permlane16_swap(u, u, false, false);
  v = __uint_as_float(a[0]) + __uint_as_float(a[1]);
  u = __float_as_uint(v);
  auto b = __builtin_amdgcn_permlane32_swap(u, u, false, false);
  return __uint_as_float(b[0]) + __uint_as_float(b[1]);
}
DI float wave_sum(float v) {
  v = dpp_add<0xb1>(v);
  v = dpp_add<0x4e>(v);
  v = dpp_add<0x124>(v);
  v = dpp_add<0x128>(v);
  return sum_rows(v);
}
DI __amdgpu_buffer_rsrc_t ws_rsrc(char* ws) { return __builtin_amdgcn_make_buffer_rsrc(ws, 0, 0x04000000, 0x00020000); }
DI void st16_wt(__amdgpu_buffer_rsrc_t r, u32 off, u32x4 v) { __builtin_amdgcn_raw_buffer_store_b128(v, r, off, 0, 16); }
DI int slice_u0(int j) { return j < 12 ? 11 * j : 132 + 10 * (j - 12); }
DI int slice_nu(int j) { return j < 12 ? 11 : 10; }

template <bool FIRST = false> DI void mfma_vv(f32x4& acc, const u32x4& a, const u32x4& b) {
  if constexpr (FIRST) asm volatile("v_mfma_f32_16x16x32_bf16 %0, %1, %2, 0" : "=&v"(acc) : "v"(a), "v"(b));
  else asm volatile("v_mfma_f32_16x16x32_bf16 %0, %1, %2, %0" : "+v"(acc) : "v"(a), "v"(b));
}
template <bool FIRST = false> DI void mfma_av(f32x4& acc, const u32x4& a, const u32x4& b) {
  if constexpr (FIRST) asm volatile("v_mfma_f32_16x16x32_bf16 %0, %1, %2, 0" : "=&v"(acc) : "a"(a), "v"(b));
  else asm volatile("v_mfma_f32_16x16x32_bf16 %0, %1, %2, %0" : "+v"(acc) : "a"(a), "v"(b));
}
template <int OFF> DI void ds_b64(u32x2& d, u32 addr) { asm volatile("ds_read_b64 %0, %1 offset:%c2" : "=v"(d) : "v"(addr), "i"(OFF) : "memory"); }
template <int OFF> DI void ds_b128(u32x4& d, u32 addr) { asm volatile("ds_read_b128 %0, %1 offset:%c2" : "=v"(d) : "v"(addr), "i"(OFF) : "memory"); }
template <int OFF> DI void ds_tr(u32x2& d, u32 addr) { asm volatile("ds_read_b64_tr_b16 %0, %1 offset:%c2" : "=v"(d) : "v"(addr), "i"(OFF) : "memory"); }
template <int OFF> DI void gld_a(u32x4& d, u32 voff, const void* sbase) {
  asm volatile("global_load_dwordx4 %0, %1, %2 offset:%c3" : "=a"(d) : "v"(voff), "s"(sbase), "i"(OFF) : "memory");
}
template <int OFF> DI void gld_v(u32x4& d, u32 voff, const void* sbase) {
  asm volatile("global_load_dwordx4 %0, %1, %2 offset:%c3" : "=v"(d) : "v"(voff), "s"(sbase), "i"(OFF) : "memory");
}
template <int OFF> DI void gld2_v(u32x2& d, u32 voff, const void* sbase) {
  asm volatile("global_load_dwordx2 %0, %1, %2 offset:%c3" : "=v"(d) : "v"(voff), "s"(sbase), "i"(OFF) : "memory");
}
template <int LOFF> DI void glds16(u32 voff, const void* sbase, u32 ldsbase) {
  asm volatile("s_add_u32 m0, %2, %c3\n\ts_nop 0\n\tglobal_load_lds_dwordx4 %0, %1" ::"v"(voff), "s"(sbase), "s"(ldsbase), "i"(LOFF) : "memory", "scc");
}
template <int N> DI void wait_vm() { asm volatile("s_waitcnt vmcnt(%c0)" ::"i"(N) : "memory"); }
template <int N> DI void wait_lgkm() { asm volatile("s_waitcnt lgkmcnt(%c0)" ::"i"(N) : "memory"); }
template <int OFF> DI void gld_a_sc1(u32x4& d, u32 voff, const void* sbase) {
  asm volatile("global_load_dwordx4 %0, %1, %2 offset:%c3 sc1" : "=a"(d) : "v"(voff), "s"(sbase), "i"(OFF) : "memory");
}
DI void mfma_aa(f32x4& acc, const u32x4& a, const u32x4& b) {
  asm volatile("v_mfma_f32_16x16x32_bf16 %0, %1, %2, %0" : "+v"(acc) : "a"(a), "a"(b));
}
template <bool FIRST = false> DI void mfma_va(f32x4& acc, const u32x4& a, const u32x4& b) {
  if constexpr (FIRST) asm volatile("v_mfma_f32_16x16x32_bf16 %0, %1, %2, 0" : "=&v"(acc) : "v"(a), "a"(b));
  else asm volatile("v_mfma_f32_16x16x32_bf16 %0, %1, %2, %0" : "+v"(acc) : "v"(a), "a"(b));
}
DI void mfma_fence(f32x4& a, f32x4& b, f32x4& c, f32x4& d) { asm volatile("s_nop 7\n\ts_nop 1" : "+v"(a), "+v"(b), "+v"(c), "+v"(d)); }
DI void after_fence(f32x4& a, f32x4& b, f32x4& c, f32x4& d) { asm volatile("" : "+v"(a), "+v"(b), "+v"(c), "+v"(d)); }
DI void after_fence(f32x4& a, f32x4& b, f32x4& c) { asm volatile("" : "+v"(a), "+v"(b), "+v"(c)); }
template <int STAGE> DI void dpp_stage(float& v) {
  if constexpr (STAGE == 0) asm volatile("v_add_f32_dpp %0, %0, %0 quad_perm:[1,0,3,2] row_mask:0xf bank_mask:0xf" : "+v"(v));
  if constexpr (STAGE == 1) asm volatile("v_add_f32_dpp %0, %0, %0 quad_perm:[2,3,0,1] row_mask:0xf bank_mask:0xf" : "+v"(v));
  if constexpr (STAGE == 2) asm volatile("v_add_f32_dpp %0, %0, %0 row_ror:4 row_mask:0xf bank_mask:0xf" : "+v"(v));
  if constexpr (STAGE == 3) asm volatile("v_add_f32_dpp %0, %0, %0 row_ror:8 row_mask:0xf bank_mask:0xf" : "+v"(v));
}
DI void barrier() { asm volatile("s_barrier" ::: "memory"); }
DI void pin(u32x4& x) { asm volatile("" : "+v"(x)); }
DI void pin(u32x2& x) { asm volatile("" : "+v"(x)); }
DI void pin4(f32x4& x) { asm volatile("" : "+v"(x)); }
DI void pin_a(u32x4& x) { asm volatile("" : "+a"(x)); }

#define STAMP(kern, idx) do {} while (0)

template <class Gate>
DI void w_job(int b, const u16* __restrict__ wg, const u16* __restrict__ wu, const u16* __restrict__ wd, char* __restrict__ ws, char* smem, Gate&& gate) {
  const int t = threadIdx.x, lane = t & 63, w = t >> 6;
  char* tile = smem;
  char* tile2 = smem + 32 * 368;
  const u32 tb = (u32)(size_t)(LDSP char*)smem;
  const int i16 = lane & 15, qd = i16 >> 2, p = i16 & 3, g = lane >> 4;
  const int ks = b & 15, j = b >> 4;
  const int U0 = slice_u0(j), nu = slice_nu(j);
  u32x4 g4[2], u4[2], d4[2];
#pragma unroll
  for (int rep = 0; rep < 2; ++rep) {
    int idx = t + 256 * rep, r = idx / 11, u = idx - 11 * r;
    bool ok = r < 32 && u < nu;
    size_t off = ok ? (size_t)(32 * ks + r) * F + 8 * (U0 + u) : 0;
    g4[rep] = *(const u32x4*)(wg + off); u4[rep] = *(const u32x4*)(wu + off);
  }
  const int ks2 = b >> 2, dp = b & 3;
  if (b < KS2 * 4) {
#pragma unroll
    for (int rep = 0; rep < 2; ++rep) {
      int idx = t + 256 * rep, r = idx >> 4, c = idx & 15;
      d4[rep] = *(const u32x4*)(wd + (size_t)(32 * ks2 + r) * D + 128 * dp + 8 * c);
    }
  }
  gate();
#pragma unroll
  for (int rep = 0; rep < 2; ++rep) {
    int idx = t + 256 * rep, r = idx / 11, u = idx - 11 * r;
    if (r < 32 && u < nu) {
      *(u32x4*)(tile + r * 368 + u * 32) = u32x4{g4[rep][0], u4[rep][0], g4[rep][1], u4[rep][1]};
      *(u32x4*)(tile + r * 368 + u * 32 + 16) = u32x4{g4[rep][2], u4[rep][2], g4[rep][3], u4[rep][3]};
    }
  }
  if (b < KS2 * 4) {
#pragma unroll
    for (int rep = 0; rep < 2; ++rep) { int idx = t + 256 * rep, r = idx >> 4, c = idx & 15; *(u32x4*)(tile2 + r * 272 + c * 16) = d4[rep]; }
  }
  __syncthreads();
  const u32x4 ones = {0x3f803f80u, 0x3f803f80u, 0x3f803f80u, 0x3f803f80u};
  const __amdgpu_buffer_rsrc_t wr = ws_rsrc(ws);
  for (int u = w; u < nu; u += 4) {
    u32 a = tb + (8 * g + qd) * 368 + u * 32 + 8 * p;
    u32x4 fr = tr_frag(a, a + 4 * 368);
    st16_wt(wr, (u32)WS_W1 + (((U0 + u) * KS1 + ks) * 64 + lane) * 16, fr);
    f32x4 cs = mfma16(fr, ones, f32x4{0.f, 0.f, 0.f, 0.f});
    if (i16 == 0) st16_wt(wr, (u32)WS_CS + ((ks * NU + U0 + u) * 16 + 4 * g) * 4, __builtin_bit_cast(u32x4, cs));
  }
  if (b < KS2 * 4) {
#pragma unroll
    for (int rep = 0; rep < 2; ++rep) {
      int fi = w + 4 * rep, dtl = fi >> 2, df = fi & 3;
      u32 a = tb + 32 * 368 + (8 * g + qd) * 272 + (64 * dtl + 32 * (df >> 1) + 8 * p + 4 * (df & 1)) * 2;
      u32x4 fr = tr_frag(a, a + 4 * 272);
      st16_wt(wr, (u32)WS_WD + ((((2 * dp + dtl) * KS2 + ks2) * 4 + df) * 64 + lane) * 16, fr);
    }
  }
}

constexpr int KA_CS = 4160;
constexpr int KA_KC = 4 * KA_CS;
constexpr int KA_XR = 2 * KA_KC;
constexpr int KA_RED = KA_XR + 2 * 4608;
constexpr int KA_LDS = KA_RED + 4 * 4096;
constexpr int KA_TK = KA_XR, KA_TV = KA_TK + 72 * 128, KA_TQ = KA_TV + 64 * 128;
static_assert(KA_TQ + 64 * 128 == KA_LDS, "tile overlay");
constexpr int SPIN_MAX = 1 << 22;
#define HINT_AT 1
#define HELP_SLEEP 40
#define HINT_TICKS 800ull

template <int OFF> DI void gld_v_sc1(u32x4& d, u32 voff, const void* sbase) {
  asm volatile("global_load_dwordx4 %0, %1, %2 offset:%c3 sc1" : "=v"(d) : "v"(voff), "s"(sbase), "i"(OFF) : "memory");
}
template <int OFF> DI void gld2_v_sc1(u32x2& d, u32 voff, const void* sbase) {
  asm volatile("global_load_dwordx2 %0, %1, %2 offset:%c3 sc1" : "=v"(d) : "v"(voff), "s"(sbase), "i"(OFF) : "memory");
}
DI bool tags_ok(const u32x4& f, const u32x2& tag) { return f[0] == tag[0] && f[1] == tag[1] && f[2] == tag[0] && f[3] == tag[1]; }

__global__ __launch_bounds__(256, 2) void ka_conv(const u16* __restrict__ q, const u16* __restrict__ k, const u16* __restrict__ v,
                                                  const u16* __restrict__ shk, const u16* __restrict__ ssmk, const u16* __restrict__ dskip,
                                                  const u16* __restrict__ wg, const u16* __restrict__ wu, const u16* __restrict__ wd,
                                                  char* __restrict__ ws) {
  extern __shared__ __attribute__((aligned(16))) char smem[];
  const int t = threadIdx.x, lane = t & 63;
  const int w = __builtin_amdgcn_readfirstlane(t >> 6);
  const int b = blockIdx.x;
  char* ws_stamp = ws; (void)ws_stamp;
  STAMP(0, 0);
  if (b >= 256) {
    __builtin_amdgcn_s_sleep(HELP_SLEEP);
    if (b < 264 && w == 1) {
      const char* pc; u32 len;
      asm volatile("s_getpc_b64 %0\n1:\n\ts_mov_b32 %1, kb_code_end-1b" : "=s"(pc), "=s"(len));
      const u32 lb = (u32)(size_t)(LDSP char*)smem + 32768;
      for (u32 o0 = 0; o0 < len; o0 += 64 * 128) {
        u32 o = o0 + lane * 128; o = o < len ? o : 0;
        asm volatile("s_mov_b32 m0, %2\n\ts_nop 0\n\tglobal_load_lds_dword %0, %1" ::"v"(o), "s"(pc), "s"(lb) : "memory");
      }
    }
    w_job(b - 256, wg, wu, wd, ws, smem, [&]() {
      if (w == 0) {
        const unsigned long long e = __hip_atomic_load((const unsigned long long*)(ws + WS_EPO), __ATOMIC_RELAXED, __HIP_MEMORY_SCOPE_AGENT);
        const unsigned long long* hp = (const unsigned long long*)(ws + WS_HNT) + (b - 256);
        const unsigned long long t0 = __builtin_amdgcn_s_memrealtime();
        while (__hip_atomic_load(hp, __ATOMIC_RELAXED, __HIP_MEMORY_SCOPE_AGENT) != e + 1 && __builtin_amdgcn_s_memrealtime() - t0 < HINT_TICKS)
          __builtin_amdgcn_s_sleep(8);
      }
      __builtin_amdgcn_s_barrier();
      STAMP(1, 5);
    });
    STAMP(1, 4);
    return;
  }
  const int x = b & 7, bi = b >> 3, s0 = 64 * bi;
  const int ch0 = 64 * x + 2 * bi;
  const u32 sb = (u32)(size_t)(LDSP char*)smem;
  const int n = lane & 15, g = lane >> 4, qd = n >> 2, p = n & 3;
  const int d = 64 * x + 16 * w + n;
  const int srow = 8 * b + 2 * w;
  const __amdgpu_buffer_rsrc_t wr = ws_rsrc(ws);

  u32x2 epo, sh; u32x4 qa, qb, kp[2], kc[2];
  gld2_v_sc1<0>(epo, 0, ws + WS_EPO);
  gld2_v<0>(sh, d * 8, shk);
  const int r8 = lane >> 3, c8 = lane & 7;
  auto voff = [&](int instr) { const int f = ((r8 >> 1) & 1) | ((instr & 1) << 1); return (u32)((((c8 >> 1) ^ f) * 2 + (c8 & 1)) * 16); };
  const size_t col = (size_t)x * 128;
  {
    int row = s0 - 8 + 8 * w + r8; row = row < 0 ? 0 : row;
    glds16<0>((u32)row * 1024 + voff(w), (const char*)k + col, sb + KA_TK + w * 1024);
    glds16<0>((u32)(s0 + 8 * w + r8) * 1024 + voff(w), (const char*)v + col, sb + KA_TV + w * 1024);
    glds16<0>((u32)(s0 - 8 + 8 * (w + 4) + r8) * 1024 + voff(w), (const char*)k + col, sb + KA_TK + (w + 4) * 1024);
    glds16<0>((u32)(s0 + 8 * (w + 4) + r8) * 1024 + voff(w), (const char*)v + col, sb + KA_TV + (w + 4) * 1024);
    if (w == 0) glds16<0>((u32)(s0 + 56 + r8) * 1024 + voff(8), (const char*)k + col, sb + KA_TK + 8 * 1024);
  }
#pragma unroll
  for (int cc = 0; cc < 2; ++cc) {
    const char* kr = (const char*)ssmk + (size_t)(ch0 + cc) * 4096;
    gld_v<0>(kp[cc], (u32)(t > 0 ? t - 1 : 0) * 16, kr);
    gld_v<0>(kc[cc], (u32)t * 16, kr);
  }
  glds16<0>((u32)(s0 + 8 * w + r8) * 1024 + voff(w), (const char*)q + col, sb + KA_TQ + w * 1024);
  glds16<0>((u32)(s0 + 8 * (w + 4) + r8) * 1024 + voff(w), (const char*)q + col, sb + KA_TQ + (w + 4) * 1024);
  gld_v<0>(qa, lane * 16, (const char*)q + (size_t)srow * 1024);
  gld_v<1024>(qb, lane * 16, (const char*)q + (size_t)srow * 1024);
  const float dsk = blo(dskip[ch0 + (w >> 1)]);
  if (b == 0 && t < 16) *(u32*)(ws + WS_CNT + t * 128) = 0u;

  wait_vm<8>();
  barrier();
  asm volatile("" : "+v"(sh), "+v"(epo));
  STAMP(0, 1);
  u32x2 tag;
  { const unsigned long long e = (((unsigned long long)epo[1] << 32) | epo[0]) + 1ull; tag = u32x2{(u32)e, (u32)(e >> 32)}; }
  auto taddr = [&](int base, int tr) { const int f = ((tr >> 1) & 1) | ((tr >> 2) & 2); return sb + base + tr * 128 + ((w ^ f) * 32) + 8 * p; };
  {
    const float a0 = blo(sh[0]), a1 = bhi(sh[0]), a2 = blo(sh[1]), a3 = bhi(sh[1]);
#pragma unroll
    for (int e = 0; e < 2; ++e) {
      const int rg = g + 4 * e;
      u32x2 km = tr_read(taddr(KA_TK, 8 * rg + 4 + qd));
      const u32x2 k0 = tr_read(taddr(KA_TK, 8 * rg + 8 + qd)), k1 = tr_read(taddr(KA_TK, 8 * rg + 12 + qd));
      const u32x2 v0 = tr_read(taddr(KA_TV, 8 * rg + qd)), v1 = tr_read(taddr(KA_TV, 8 * rg + 4 + qd));
      if (s0 + rg == 0) km = u32x2{0, 0};
      const float kf[11] = {bhi(km[0]), blo(km[1]), bhi(km[1]), blo(k0[0]), bhi(k0[0]), blo(k0[1]), bhi(k0[1]), blo(k1[0]), bhi(k1[0]), blo(k1[1]), bhi(k1[1])};
      const float vf[8] = {blo(v0[0]), bhi(v0[0]), blo(v0[1]), bhi(v0[1]), blo(v1[0]), bhi(v1[0]), blo(v1[1]), bhi(v1[1])};
      float xo[8];
#pragma unroll
      for (int j = 0; j < 8; ++j) xo[j] = (kf[j + 3] * a0 + kf[j + 2] * a1 + kf[j + 1] * a2 + kf[j] * a3) * vf[j];
      st16_wt(wr, (u32)WS_XR + (d * S + (2040 - (s0 + 8 * rg))) * 2, u32x4{pk2(xo[7], xo[6]), pk2(xo[5], xo[4]), pk2(xo[3], xo[2]), pk2(xo[1], xo[0])});
    }
  }
  asm volatile("s_waitcnt vmcnt(0)\n\ts_barrier" : "+v"(kp[0]), "+v"(kc[0]), "+v"(kp[1]), "+v"(kc[1]), "+v"(qa), "+v"(qb) :: "memory");
  if (t == 0) __builtin_amdgcn_raw_buffer_store_b64(tag, wr, (u32)WS_FLA + (x * 32 + bi) * 8, 0, 16);
  STAMP(0, 2);
#pragma unroll
  for (int e = 0; e < 2; ++e) {
    const int rg = g + 4 * e;
    const u32x2 q0 = tr_read(taddr(KA_TQ, 8 * rg + qd)), q1 = tr_read(taddr(KA_TQ, 8 * rg + 4 + qd));
    st16_wt(wr, (u32)WS_QT + (d * S + s0 + 8 * rg) * 2, cat(q0, q1));
  }
  {
    float sa = 0.f, sbb = 0.f;
#pragma unroll
    for (int e = 0; e < 4; ++e) { sa += blo(qa[e]) + bhi(qa[e]); sbb += blo(qb[e]) + bhi(qb[e]); }
    const float ma = wave_sum(sa) * (1.f / D), mb = wave_sum(sbb) * (1.f / D);
    float va = 0.f, vb = 0.f;
#pragma unroll
    for (int e = 0; e < 4; ++e) {
      float u0 = blo(qa[e]) - ma, u1 = bhi(qa[e]) - ma, u2 = blo(qb[e]) - mb, u3 = bhi(qb[e]) - mb;
      va += u0 * u0 + u1 * u1; vb += u2 * u2 + u3 * u3;
    }
    va = wave_sum(va) * (1.f / D); vb = wave_sum(vb) * (1.f / D);
    const float ra = __builtin_amdgcn_rsqf(va + EPS), rb = __builtin_amdgcn_rsqf(vb + EPS);
    const u32x2 val = lane == 0 ? u32x2{__float_as_uint(ra), __float_as_uint(rb)} : u32x2{__float_as_uint(-ma * ra), __float_as_uint(-mb * rb)};
    const u32 off = lane == 0 ? (u32)WS_SA + srow * 4 : lane == 1 ? (u32)WS_SB + srow * 4 : 0xfffffff0u;
    __builtin_amdgcn_raw_buffer_store_b64(val, wr, off, 0, 16);
  }
#pragma unroll
  for (int cc = 0; cc < 2; ++cc) {
    const u32 m = t > 0 ? 0xffffffffu : 0u;
    u32 wv[6] = {kp[cc][0] & m, kp[cc][1] & m, kp[cc][2] & m, kp[cc][3] & m, kc[cc][0], kc[cc][1]};
    u32 o1[5];
#pragma unroll
    for (int i = 0; i < 5; ++i) o1[i] = __builtin_amdgcn_alignbit(wv[i + 1], wv[i], 16);
    char* base = smem + cc * KA_KC + (t + 1) * 16;
    *(u32x4*)(base + 0 * KA_CS) = u32x4{o1[0], o1[1], o1[2], o1[3]};
    *(u32x4*)(base + 1 * KA_CS) = u32x4{wv[1], wv[2], wv[3], wv[4]};
    *(u32x4*)(base + 2 * KA_CS) = u32x4{o1[1], o1[2], o1[3], o1[4]};
    *(u32x4*)(base + 3 * KA_CS) = u32x4{wv[2], wv[3], wv[4], wv[5]};
    if (t == 255) {
      const u32 z = 0;
      const u32 e0 = __builtin_amdgcn_alignbit(kc[cc][1], kc[cc][0], 16), e1 = __builtin_amdgcn_alignbit(kc[cc][2], kc[cc][1], 16);
      const u32 e2 = __builtin_amdgcn_alignbit(kc[cc][3], kc[cc][2], 16), e3 = __builtin_amdgcn_alignbit(z, kc[cc][3], 16);
      *(u32x4*)(base + 16 + 0 * KA_CS) = u32x4{e0, e1, e2, e3};
      *(u32x4*)(base + 16 + 1 * KA_CS) = u32x4{kc[cc][1], kc[cc][2], kc[cc][3], z};
      *(u32x4*)(base + 16 + 2 * KA_CS) = u32x4{e1, e2, e3, z};
      *(u32x4*)(base + 16 + 3 * KA_CS) = u32x4{kc[cc][2], kc[cc][3], z, z};
    }
  }
  if (t < 8) *(u32x4*)(smem + (t >> 2) * KA_KC + (t & 3) * KA_CS) = u32x4{0, 0, 0, 0};
  asm volatile("s_waitcnt vmcnt(0) lgkmcnt(0)\n\ts_barrier" ::: "memory");
  if (t == 0) {
    __builtin_amdgcn_raw_buffer_store_b64(tag, wr, (u32)WS_FLG + (x * 32 + bi) * 8, 0, 16);
    if (HINT_AT == 0) __builtin_amdgcn_raw_buffer_store_b64(tag, wr, (u32)WS_HNT + b * 8, 0, 16);
  }
  if (t >= 64 && t < 64 + 60) { int i = t - 64; *(u32x4*)(smem + KA_XR + (i / 30) * 4608 + 4096 + (i % 30) * 16) = u32x4{0, 0, 0, 0}; }
  if (w == 0) {
    const unsigned long long* fp = (const unsigned long long*)(ws + WS_FLA) + x * 32 + (lane & 31);
    const unsigned long long want = ((unsigned long long)tag[1] << 32) | tag[0];
    for (int spins = 0; spins < SPIN_MAX; ++spins) {
      if (__all(__hip_atomic_load(fp, __ATOMIC_RELAXED, __HIP_MEMORY_SCOPE_AGENT) == want)) break;
      __builtin_amdgcn_s_sleep(1);
    }
  }
  asm volatile("s_waitcnt vmcnt(0)\n\ts_barrier" ::: "memory");
  STAMP(0, 3);

  const int c = w >> 1, half = w & 1;
  u32x2 qv[4]; u32x4 sa[4], sbv[4], fl[2];
  const u32 eo = (u32)(1024 * half + 16 * n + 4 * g);
  const char* qTc = ws + WS_QT + (size_t)(ch0 + c) * 4096;
  {
    u32x4 x0, x1;
    gld_v_sc1<0>(x0, t * 16, ws + WS_XR + (size_t)ch0 * 4096);
    gld_v_sc1<0>(x1, t * 16, ws + WS_XR + (size_t)(ch0 + 1) * 4096);
    wait_vm<0>();
    asm volatile("" : "+v"(x0), "+v"(x1));
    *(u32x4*)(smem + KA_XR + t * 16) = x0;
    *(u32x4*)(smem + KA_XR + 4608 + t * 16) = x1;
  }
  asm volatile("s_waitcnt lgkmcnt(0)\n\ts_barrier" ::: "memory");
  if (HINT_AT == 1 && t == 0) __builtin_amdgcn_raw_buffer_store_b64(tag, wr, (u32)WS_HNT + b * 8, 0, 16);
  STAMP(1, 1);

  const u32 aa = sb + c * KA_KC + (n & 3) * KA_CS + 16 * g + 2 * (n & 12) + half * 256;
  const u32 ba = sb + KA_XR + c * 4608 + 480 - 32 * n + 16 * g + half * 256;
  f32x4 acc[8];
  u32x2 Alo[2][8], Ahi[2][8];
  u32x4 B[2][8];
  auto rd = [&](auto C0, auto R) {
    constexpr int c0 = C0, r = R, buf = c0 & 1;
    if constexpr (r >= 2 && r < 10) {
      constexpr int u = r - 2;
      ds_b128<512 * (7 - u) + 64 * c0>(B[buf][u], ba);
    } else {
      constexpr int ar = r < 2 ? r : r - 8, i = ar >> 1;
      if constexpr ((ar & 1) == 0) ds_b64<512 * i + 64 * c0>(Alo[buf][i], aa);
      else ds_b64<512 * i + 64 * c0 + 8>(Ahi[buf][i], aa);
    }
  };
  auto ldS = [&](auto E) {
    constexpr int e = E, i = e / 3, kind = e % 3;
    if constexpr (kind == 0) gld_v_sc1<i * 1024>(sa[i], eo * 4, ws + WS_SA);
    if constexpr (kind == 1) gld_v_sc1<i * 1024>(sbv[i], eo * 4, ws + WS_SB);
    if constexpr (kind == 2) gld2_v_sc1<i * 512>(qv[i], eo * 2, qTc);
  };
  static_for<24>([&](auto R) { rd(std::integral_constant<int, 0>{}, R); });
  static_for<4>([&](auto C0) {
    constexpr int c0 = C0, buf = c0 & 1;
    if constexpr (c0 == 2) {
      wait_vm<0>();
      asm volatile("" : "+v"(fl[0]), "+v"(fl[1]));
      if (!__all(tags_ok(fl[0], tag) && tags_ok(fl[1], tag))) {
        for (int spins = 0; spins < SPIN_MAX; ++spins) {
          gld_v_sc1<0>(fl[0], lane * 32, ws + WS_FLG); gld_v_sc1<16>(fl[1], lane * 32, ws + WS_FLG);
          wait_vm<0>();
          asm volatile("" : "+v"(fl[0]), "+v"(fl[1]));
          if (__all(tags_ok(fl[0], tag) && tags_ok(fl[1], tag))) break;
          __builtin_amdgcn_s_sleep(1);
        }
      }
      if (b == 0 && t == 0) __builtin_amdgcn_raw_buffer_store_b64(tag, wr, (u32)WS_EPO, 0, 16);
    }
    u32x4 A[8];
    static_for<36>([&](auto M) {
      constexpr int m = M;
      constexpr int c1 = m < 8 ? 0 : m < 15 ? 1 : m < 21 ? 2 : m < 26 ? 3 : m < 30 ? 4 : m < 33 ? 5 : m < 35 ? 6 : 7;
      constexpr int first = c1 * 8 - c1 * (c1 - 1) / 2;
      constexpr int u = m - first;
      if constexpr (m == 0) {
        if constexpr (c0 == 0) wait_lgkm<14>(); else wait_lgkm<0>();
        pin(Alo[buf][0]); pin(Ahi[buf][0]);
#pragma unroll
        for (int i = 0; i < 8; ++i) pin(B[buf][i]);
        A[0] = cat(Alo[buf][0], Ahi[buf][0]);
      }
      if constexpr (m == 8) {
        if constexpr (c0 == 0) wait_lgkm<8>();
#pragma unroll
        for (int i = 1; i < 8; ++i) { pin(Alo[buf][i]); pin(Ahi[buf][i]); A[i] = cat(Alo[buf][i], Ahi[buf][i]); }
      }
      mfma_vv<(c0 == 0 && c1 == 0)>(acc[c1 + u], A[c1], B[buf][u]);
      if constexpr (c0 < 3 && m < 24) rd(std::integral_constant<int, c0 + 1>{}, M);
      if constexpr (c0 == 1 && m == 2) gld_v_sc1<0>(fl[0], lane * 32, ws + WS_FLG);
      if constexpr (c0 == 1 && m == 4) gld_v_sc1<16>(fl[1], lane * 32, ws + WS_FLG);
      if constexpr (c0 == 2 && m >= 1 && m < 25 && (m & 1) == 1) ldS(std::integral_constant<int, (m - 1) / 2>{});
    });
  });
  mfma_fence(acc[0], acc[1], acc[2], acc[3]);
  after_fence(acc[4], acc[5], acc[6], acc[7]);
  STAMP(1, 2);
  f32x4 fin[4];
  {
    f32x4* red = (f32x4*)(smem + KA_RED);
    if (half) {
#pragma unroll
      for (int i = 0; i < 4; ++i) { red[(w * 4 + i) * 64 + lane] = acc[i]; fin[i] = acc[4 + i]; }
      asm volatile("; half 1" ::: "memory");
    } else {
#pragma unroll
      for (int i = 0; i < 4; ++i) { red[(w * 4 + i) * 64 + lane] = acc[4 + i]; fin[i] = acc[i]; }
      asm volatile("; half 0" ::: "memory");
    }
    asm volatile("s_waitcnt lgkmcnt(0)\n\ts_barrier" ::: "memory");
#pragma unroll
    for (int i = 0; i < 4; ++i) fin[i] += red[((w ^ 1) * 4 + i) * 64 + lane];
  }
  wait_vm<0>();
#pragma unroll
  for (int i = 0; i < 4; ++i) asm volatile("" : "+v"(qv[i]), "+v"(sa[i]), "+v"(sbv[i]));
  const f32x2 dd = {dsk, dsk};
  u32x2 hv[4];
#pragma unroll
  for (int i = 0; i < 4; ++i) {
    const int s = 256 * (4 * half + i) + 16 * n + 4 * g;
    const u32x2 xq = *(LDSP const u32x2*)(size_t)(sb + KA_XR + c * 4608 + 2 * (2044 - s));
    const f32x2 x0 = {bhi(xq[1]), blo(xq[1])}, x1 = {bhi(xq[0]), blo(xq[0])};
    const f32x2 q0 = {blo(qv[i][0]), bhi(qv[i][0])}, q1 = {blo(qv[i][1]), bhi(qv[i][1])};
    const f32x2 z0 = x0 * dd + f32x2{fin[i][0], fin[i][1]}, z1 = x1 * dd + f32x2{fin[i][2], fin[i][3]};
    const f32x4 a4 = __builtin_bit_cast(f32x4, sa[i]), b4 = __builtin_bit_cast(f32x4, sbv[i]);
    const f32x2 t0 = q0 * f32x2{a4[0], a4[1]} + f32x2{b4[0], b4[1]};
    const f32x2 t1 = q1 * f32x2{a4[2], a4[3]} + f32x2{b4[2], b4[3]};
    const f32x2 h0 = t0 * z0 + q0, h1 = t1 * z1 + q1;
    hv[i] = u32x2{pk2(h0[0], h0[1]), pk2(h1[0], h1[1])};
  }
#pragma unroll
  for (int i = 0; i < 4; i += 2) {
    auto a = __builtin_amdgcn_permlane16_swap(hv[i][0], hv[i + 1][0], false, false);
    auto bb = __builtin_amdgcn_permlane16_swap(hv[i][1], hv[i + 1][1], false, false);
    const int s = 256 * (4 * half + i + (g & 1)) + 16 * n + 4 * (g & 2);
    st16_wt(wr, (u32)WS_HT + ((ch0 + c) * S + s) * 2, u32x4{a[0], bb[0], a[1], bb[1]});
  }
  STAMP(1, 3);
}

constexpr int KB_NL = 39;
constexpr int KB_ST = KB_NL * 4096;
constexpr int KB_LDS = KB_ST + 1024;
#define EPI_FILL 8
constexpr int KB_RING = 8;

#define P2_AHEAD 4
#define P2_PRO 3
constexpr int p2_upto(int ks) {
  const int ramp = P2_PRO - 1 + 2 * (ks + 1), steady = ks + P2_AHEAD;
  const int u = ks < 0 ? P2_PRO - 1 : (ramp < steady ? ramp : steady);
  return u > 15 ? 15 : u;
}
#define P2_XMAX 5
#define KB_CODEPF 0
constexpr int P2_NEXTRA = 23 + KB_CODEPF;
constexpr int p2_extra(int ks) {
  const int tail0 = 16 - P2_AHEAD, rest = 23 - P2_AHEAD * P2_XMAX;
  if (ks == 0) return KB_CODEPF;
  if (ks >= tail0) return P2_XMAX;
  return (rest > 0 && ks >= tail0 - rest) ? 1 : 0;
}
constexpr int p2_extra_first(int ks) { int c = 0; for (int s = 0; s < ks; ++s) c += p2_extra(s); return c; }
constexpr int p2_extra_real(int ks) { int f = p2_extra_first(ks), n = p2_extra(ks); return f >= P2_NEXTRA ? 0 : (f + n > P2_NEXTRA ? P2_NEXTRA - f : n); }
static_assert(p2_extra_first(16) >= P2_NEXTRA, "all Wd operations are scheduled");
constexpr int p2_issue_step(int t) { for (int s = -1; s < KS1; ++s) if (p2_upto(s) >= t) return s; return KS1; }
constexpr int p2_younger(int t, int ks, int ops) {
  int c = (p2_upto(ks - 1) - t) * ops;
  for (int s = p2_issue_step(t); s < ks; ++s) if (s >= 0) c += p2_extra_real(s);
  return c;
}

struct KbCtx {
  u32 sb, gv, lv, lb0, lwd, csv, cpv, cpl;
  const char* cpb;
  const char *hb0, *hb1, *w0, *w1, *w2, *cs0, *cs1, *cs2, *wd, *wdr;
};

template <int GRAM, class Setup>
DI void kb_phase2(const KbCtx& c, f32x4 (&acc)[3][8], f32x4 (&rsum)[2], f32x4 (&csp)[3], u32x4 (&Wr)[4][4], u32 (&ra)[8], Setup&& setup, char* ws_stamp) {
  constexpr int OPS = GRAM ? 4 : 5;
  u32x4 W[KB_RING][3];
  u32x2 Bl[2][8], Bh[2][8];
  auto issue = [&](auto KS, auto O) {
    constexpr int ks = KS, o = O;
    if constexpr (o == 0) glds16<ks * 8192>(c.gv, c.hb0 + (size_t)ks * 131072, c.lb0);
    if constexpr (o == 1) gld_a<(ks & 3) * 1024>(W[ks % KB_RING][0], c.lv, c.w0 + (ks >> 2) * 4096);
    if constexpr (o == 2) gld_a<(ks & 3) * 1024>(W[ks % KB_RING][1], c.lv, c.w1 + (ks >> 2) * 4096);
    if constexpr (o == 3 && !GRAM) gld_a<(ks & 3) * 1024>(W[ks % KB_RING][2], c.lv, c.w2 + (ks >> 2) * 4096);
    if constexpr (o == 4) glds16<ks * 8192 + 1024>(c.gv, c.hb1 + (size_t)ks * 131072, c.lb0);
  };
  auto extra = [&](auto E0) {
    constexpr int e = E0 - KB_CODEPF;
    if constexpr (e < 0) asm volatile("s_mov_b32 m0, %2\n\ts_nop 0\n\tglobal_load_lds_dword %0, %1" ::"v"(c.cpv), "s"(c.cpb), "s"(c.cpl) : "memory");
    else if constexpr (e < 7) glds16<(32 + e) * 4096>(c.lv, c.wd + (size_t)(32 + e) * 4096, c.lwd);
    else if constexpr (e < 23) gld_a<((e - 7) & 3) * 1024>(Wr[(e - 7) >> 2][(e - 7) & 3], c.lv, c.wdr + ((e - 7) >> 2) * 4096);
  };
  auto rdB = [&](auto KS, auto R) {
    constexpr int ks = KS, r = R, i = r >> 1;
    if constexpr ((r & 1) == 0) ds_tr<(ks & 7) * 8192>(Bl[ks & 1][i], ra[i] + (ks >= 8 ? 65536 : 0));
    else ds_tr<(ks & 7) * 8192 + 1024>(Bh[ks & 1][i], ra[i] + (ks >= 8 ? 65536 : 0));
  };
  u32x4 c0, c1, c2;
  static_for<5>([&](auto O) { issue(std::integral_constant<int, 0>{}, O); });
  gld_v<0>(c0, c.csv, c.cs0); gld_v<0>(c1, c.csv, c.cs1); gld_v<0>(c2, c.csv, c.cs2);
  static_for<p2_upto(-1)>([&](auto KS) { static_for<5>([&](auto O) { issue(std::integral_constant<int, KS + 1>{}, O); }); });
  setup();
  u32x4 ones = {0x3f803f80u, 0x3f803f80u, 0x3f803f80u, 0x3f803f80u};
  pin(ones);
  wait_vm<p2_upto(-1) * OPS>();
  barrier();
  static_for<16>([&](auto R) { rdB(std::integral_constant<int, 0>{}, R); });
  asm volatile("" : "+v"(c0), "+v"(c1), "+v"(c2));
  float cv[12];
#pragma unroll
  for (int e = 0; e < 4; ++e) { cv[e] = __uint_as_float(c0[e]); cv[4 + e] = __uint_as_float(c1[e]); cv[8 + e] = __uint_as_float(c2[e]); }
  static_for<KS1>([&](auto KS) {
    constexpr int ks = KS, buf = ks & 1, slot = ks % KB_RING;
    constexpr int nmain = (p2_upto(ks) - p2_upto(ks - 1)) * 5, next = p2_extra_real(ks);
    wait_lgkm<0>();
    u32x4 B[8];
#pragma unroll
    for (int i = 0; i < 8; ++i) { pin(Bl[buf][i]); pin(Bh[buf][i]); }
#pragma unroll
    for (int i = 0; i < 8; ++i) B[i] = cat(Bl[buf][i], Bh[buf][i]);
    pin_a(W[slot][0]); pin_a(W[slot][1]);
    if constexpr (!GRAM) pin_a(W[slot][2]);
    static_for<26>([&](auto M) {
      constexpr int m = M, i = m / 3, u = m % 3;
      if constexpr (m >= 24) mfma_vv<ks == 0>(rsum[m - 24], ones, B[m - 24]);
      else if constexpr (u < 2 || !GRAM) mfma_av<ks == 0>(acc[u][i], W[slot][u], B[i]);
      else mfma_vv<ks == 0>(acc[2][i], B[i], B[i]);
      if constexpr (m == 1 && ks + 1 < KS1) { wait_vm<p2_younger(ks + 1, ks, OPS)>(); barrier(); }
      if constexpr (m >= 2 && m < 18 && ks + 1 < KS1) rdB(std::integral_constant<int, ks + 1>{}, std::integral_constant<int, m - 2>{});
      if constexpr (ks >= 9 && ks < 13 && m >= 2 && m < 14) dpp_stage<ks - 9>(cv[m - 2]);
      constexpr int v = m - (26 - nmain - next);
      if constexpr (v >= 0 && v < nmain) issue(std::integral_constant<int, p2_upto(ks - 1) + 1 + v / 5>{}, std::integral_constant<int, v % 5>{});
      if constexpr (v >= nmain) extra(std::integral_constant<int, p2_extra_first(ks) + v - nmain>{});
    });
  });
  mfma_fence(acc[0][0], acc[1][0], rsum[0], rsum[1]);
#pragma unroll
  for (int i = 0; i < 8; ++i) after_fence(acc[0][i], acc[1][i], acc[2][i], rsum[i & 1]);
#pragma unroll
  for (int u = 0; u < 3; ++u) csp[u] = f32x4{cv[4 * u], cv[4 * u + 1], cv[4 * u + 2], cv[4 * u + 3]};
}

__global__ __launch_bounds__(256) void kb_mlp(char* __restrict__ ws, u16* __restrict__ out) {
  extern __shared__ __attribute__((aligned(16))) char smem[];
  const int t = threadIdx.x, lane = t & 63;
  const int w = __builtin_amdgcn_readfirstlane(t >> 6);
  const int b = blockIdx.x, xcd = b & 7, bi = b >> 3;
#define KB_MAP 1
  const int rt = KB_MAP == 0 ? 2 * xcd + (bi & 1) : (xcd >> 1) * 4 + (bi & 3);
  const int j = KB_MAP == 0 ? bi >> 1 : (xcd & 1) * 8 + (bi >> 2);
  const int half = KB_MAP == 0 ? j & 1 : j >> 3, dt = KB_MAP == 0 ? j >> 1 : j & 7;
  const int U0 = slice_u0(j), nu = slice_nu(j);
  const int rem = nu - 8;
  const int nw = w < rem ? 3 : 2;
  const int uw = U0 + (w < rem ? 3 * w : 3 * rem + 2 * (w - rem));
  const int u2 = uw + (nw == 3 ? 2 : 0);
  const int n = lane & 15, g = lane >> 4;
  const int s0 = 128 * rt;
  char* ws_stamp = ws; (void)ws_stamp;
  STAMP(2, 0);
  auto sf = [&](int i) { return ((w + (i >> 1)) & 3) + 4 * (half ^ (i & 1)); };

  KbCtx c;
  c.sb = (u32)(size_t)(LDSP char*)smem;
  c.lv = lane * 16;
  c.gv = (u32)(g * 4096 + (((n >> 1) ^ (g | ((w & 1) << 2))) * 32 + (n & 1) * 16));
  c.hb0 = ws + WS_HT + (size_t)s0 * 2 + (size_t)(8 * w) * 4096;
  c.hb1 = c.hb0 + 4 * 4096;
  c.lb0 = c.sb + 8 * w * 256;
  c.w0 = ws + WS_W1 + (size_t)uw * KS1 * 1024;
  c.w1 = c.w0 + KS1 * 1024;
  c.w2 = ws + WS_W1 + (size_t)u2 * KS1 * 1024;
  c.cs0 = ws + WS_CS + (size_t)uw * 64;
  c.cs1 = c.cs0 + 64;
  c.cs2 = ws + WS_CS + (size_t)u2 * 64;
  c.csv = n * (NU * 64) + g * 16;
  c.wdr = ws + WS_WD + (size_t)dt * KS2 * 4096 + (size_t)KB_NL * 4096;
  c.wd = ws + WS_WD + (size_t)dt * KS2 * 4096 + (size_t)w * 1024;
  {
    u32 len;
    asm volatile("s_getpc_b64 %0\n1:\n\ts_mov_b32 %1, kb_code_end-1b" : "=s"(c.cpb), "=s"(len));
    const u32 o = (u32)(w * 64 + lane) * 128;
    c.cpv = o < len ? o : 0;
    c.cpl = c.sb + KB_ST + w * 256;
  }
  c.lwd = c.sb + 1024 * w;
  u32 ra[8], hoff[6];
  auto setup = [&]() {
    const int qd = n >> 2, p = n & 3;
    const int frow = qd | ((g & 1) << 2);
#pragma unroll
    for (int i = 0; i < 8; ++i) { ra[i] = c.sb + (8 * g + qd) * 256 + ((sf(i) ^ frow) * 32) + 8 * p; asm volatile("" : "+v"(ra[i])); }
#pragma unroll
    for (int tt = 0; tt < 6; ++tt) {
      const int q = 4 * tt + g, i = (q * 11) >> 5, u = q - 3 * i, U = uw + u;
      const u32 o = (u32)(((8 * rt + sf(i)) * KS2 + (U >> 2)) * 1024 + ((U & 3) * 16 + n) * 16);
      hoff[tt] = u < nw ? o : 0xfffffff0u;
      asm volatile("" : "+v"(hoff[tt]));
    }
  };
  f32x4 acc[3][8], cs[3], rsum[2];
  u32x4 Wr[4][4];
  if (w == 3) kb_phase2<1>(c, acc, rsum, cs, Wr, ra, setup, ws_stamp);
  else kb_phase2<0>(c, acc, rsum, cs, Wr, ra, setup, ws_stamp);
  STAMP(2, 2);

  u32x2 res[4];
  {
    const u32 rb = ra[0] + dt * 16384;
    ds_tr<0>(res[0], rb); ds_tr<1024>(res[1], rb); ds_tr<8192>(res[2], rb); ds_tr<9216>(res[3], rb);
  }
  float* stl = (float*)(smem + KB_ST);
  if (g == 0) {
#pragma unroll
    for (int i = 0; i < 2; ++i) stl[2 * (16 * sf(i) + n)] = rsum[i][0];
  }
  if (w == 3 && g == (n >> 2)) {
#pragma unroll
    for (int i = 0; i < 8; ++i) {
      f32x4 a = acc[2][i];
      float dg = (n & 2) ? ((n & 1) ? a[3] : a[2]) : ((n & 1) ? a[1] : a[0]);
      stl[2 * (16 * sf(i) + n) + 1] = dg;
    }
  }
  asm volatile("s_waitcnt lgkmcnt(0)\n\ts_barrier" : "+v"(res[0]), "+v"(res[1]), "+v"(res[2]), "+v"(res[3]) :: "memory");
  STAMP(2, 3);
  const __amdgpu_buffer_rsrc_t hrs = __builtin_amdgcn_make_buffer_rsrc(ws + WS_H, 0, (S / 16) * KS2 * 1024, 0x00020000);
  f32x2 sv[8];
#pragma unroll
  for (int i = 0; i < 8; ++i) sv[i] = *(const f32x2*)(stl + 2 * (16 * sf(i) + n));
  u32 hv[24];
  static_for<8>([&](auto I) {
    constexpr int i = I;
    const float mean = sv[i][0] * (1.f / D);
    const float var = fmaxf(sv[i][1] * (1.f / D) - mean * mean, 0.f) + EPS;
    const float rstd = __builtin_amdgcn_rsqf(var);
    const float rinv = var * rstd;
    const float ce = -1.4426950408889634f * rstd;
    const f32x2 nm = {-mean, -mean}, ce2 = {ce, ce}, ri2 = {rinv, rinv};
    static_for<3>([&](auto U_) {
      constexpr int u = U_;
      pin4(acc[u][i]);
      constexpr int slot = 3 * i + u;
      constexpr int wk = slot * EPI_FILL / 24;
      if constexpr (EPI_FILL > 0 && (slot + 1) * EPI_FILL / 24 > wk) glds16<wk * 4096>(c.lv, c.wd + (size_t)wk * 4096, c.lwd);
      const f32x4 a = acc[u][i];
      const f32x2 tg = (f32x2{a[0], a[1]} + nm * f32x2{cs[u][0], cs[u][1]}) * ce2;
      const f32x2 tu = f32x2{a[2], a[3]} + nm * f32x2{cs[u][2], cs[u][3]};
      const f32x2 e = {__builtin_amdgcn_exp2f(tg[0]), __builtin_amdgcn_exp2f(tg[1])};
      const f32x2 den = e * ri2 + ri2;
      const f32x2 o = tu * f32x2{__builtin_amdgcn_rcpf(den[0]), __builtin_amdgcn_rcpf(den[1])};
      hv[3 * i + u] = pk2(o[0], o[1]);
    });
    static_for<6>([&](auto T) {
      constexpr int tt = T;
      if constexpr ((4 * tt + 3) / 3 == i) {
        auto s02 = __builtin_amdgcn_permlane32_swap(hv[4 * tt], hv[4 * tt + 2], false, false);
        auto s13 = __builtin_amdgcn_permlane32_swap(hv[4 * tt + 1], hv[4 * tt + 3], false, false);
        auto t01 = __builtin_amdgcn_permlane16_swap(s02[0], s13[0], false, false);
        auto t23 = __builtin_amdgcn_permlane16_swap(s02[1], s13[1], false, false);
        __builtin_amdgcn_raw_buffer_store_b128(u32x4{t01[0], t01[1], t23[0], t23[1]}, hrs, hoff[tt], 0, 16);
      }
    });
  });
  STAMP(2, 4);
  u32* cnt = (u32*)(ws + WS_CNT + rt * 128);
  asm volatile("s_waitcnt vmcnt(0)\n\ts_barrier" ::: "memory");
  STAMP(2, 5);
  if (w == 0) {
    if (lane == 0) __hip_atomic_fetch_add(cnt, 1u, __ATOMIC_RELAXED, __HIP_MEMORY_SCOPE_AGENT);
    for (int spins = 0; spins < SPIN_MAX; ++spins) {
      const u32 v = __builtin_amdgcn_readfirstlane(__hip_atomic_load(cnt, __ATOMIC_RELAXED, __HIP_MEMORY_SCOPE_AGENT));
      if (v >= 16u) break;
      __builtin_amdgcn_s_sleep(1);
    }
    wait_vm<0>();
  } else {
    const char* wsrc = c.wd - (size_t)w * 1024;
    for (int e = EPI_FILL + w - 1; e < 32; e += 3) {
      const char* src = wsrc + (size_t)e * 4096;
      const u32 dst = c.sb + e * 4096;
      glds16<0>(c.lv, src, dst); glds16<1024>(c.lv, src + 1024, dst); glds16<2048>(c.lv, src + 2048, dst); glds16<3072>(c.lv, src + 3072, dst);
    }
  }
  barrier();
  STAMP(2, 6);

  const char* hb = ws + WS_H + (size_t)(8 * rt + 4 * half + w) * KS2 * 1024;
  u32x4 Hf[KS2];
  constexpr int P3_AHEAD = 12;
  auto ldH = [&](auto KS) { constexpr int ks = KS; if constexpr (ks < KS2) gld_a_sc1<(ks & 3) * 1024>(Hf[ks], c.lv, hb + (ks >> 2) * 4096); };
  static_for<P3_AHEAD>([&](auto KS) { ldH(KS); });
  STAMP(3, 0);
  f32x4 o4[4];
  u32x4 Af[3][4];
  const u32 la0 = c.sb + lane * 16, la1 = la0 + 65536, la2 = la0 + 131072;
  auto rdA = [&](auto KS, auto DF) {
    constexpr int ks = KS, df = DF;
    if constexpr (ks < KB_NL) ds_b128<(ks & 15) * 4096 + df * 1024>(Af[ks % 3][df], ks < 16 ? la0 : ks < 32 ? la1 : la2);
  };
  wait_vm<P3_AHEAD>();
  barrier();
  static_for<4>([&](auto DF) { rdA(std::integral_constant<int, 0>{}, DF); });
  static_for<4>([&](auto DF) { rdA(std::integral_constant<int, 1>{}, DF); });
  static_for<KS2>([&](auto KS) {
    constexpr int ks = KS;
    wait_vm<(ks + P3_AHEAD <= KS2 ? P3_AHEAD - 1 : KS2 - 1 - ks)>();
    if constexpr (ks + 1 < KB_NL) wait_lgkm<4>(); else wait_lgkm<0>();
    pin_a(Hf[ks]);
    static_for<4>([&](auto DF) {
      constexpr int df = DF;
      if constexpr (ks < KB_NL) { pin(Af[ks % 3][df]); mfma_va<ks == 0>(o4[df], Af[ks % 3][df], Hf[ks]); }
      else { pin_a(Wr[ks - KB_NL][df]); mfma_aa(o4[df], Wr[ks - KB_NL][df], Hf[ks]); }
      rdA(std::integral_constant<int, ks + 2>{}, DF);
      if constexpr (df == 1 && ks + P3_AHEAD < KS2) ldH(std::integral_constant<int, ks + P3_AHEAD>{});
    });
  });
  mfma_fence(o4[0], o4[1], o4[2], o4[3]);
  u32x4 o0, o1;
#pragma unroll
  for (int df = 0; df < 2; ++df) {
    o0[2 * df] = pk2(o4[df][0] + blo(res[df][0]), o4[df][1] + bhi(res[df][0]));
    o0[2 * df + 1] = pk2(o4[df][2] + blo(res[df][1]), o4[df][3] + bhi(res[df][1]));
    o1[2 * df] = pk2(o4[2 + df][0] + blo(res[2 + df][0]), o4[2 + df][1] + bhi(res[2 + df][0]));
    o1[2 * df + 1] = pk2(o4[2 + df][2] + blo(res[2 + df][1]), o4[2 + df][3] + bhi(res[2 + df][1]));
  }
  const __amdgpu_buffer_rsrc_t ors = __builtin_amdgcn_make_buffer_rsrc(out, 0, S * D * 2, 0x00020000);
  const u32 ooff = (u32)((s0 + 64 * half + 16 * w + n) * D + 64 * dt + 8 * g) * 2;
  __builtin_amdgcn_raw_buffer_store_b128(o0, ors, ooff, 0, 16);
  __builtin_amdgcn_raw_buffer_store_b128(o1, ors, ooff + 64, 0, 16);
  asm volatile("kb_code_end:");
  STAMP(2, 7);
}

extern "C" void kernel_launch(void* const* d_in, const int* in_sizes, int n_in, void* d_out, int out_size, void* d_ws,
                              size_t ws_size, hipStream_t stream) {
  const u16 *q = (const u16*)d_in[0], *k = (const u16*)d_in[1], *v = (const u16*)d_in[2], *shk = (const u16*)d_in[3];
  const u16 *ssmk = (const u16*)d_in[4], *dskip = (const u16*)d_in[5];
  const u16 *wg = (const u16*)d_in[7], *wu = (const u16*)d_in[8], *wd = (const u16*)d_in[9];
  char* ws = (char*)d_ws;
  (void)hipFuncSetAttribute((const void*)ka_conv, hipFuncAttributeMaxDynamicSharedMemorySize, KA_LDS);
  (void)hipFuncSetAttribute((const void*)kb_mlp, hipFuncAttributeMaxDynamicSharedMemorySize, KB_LDS);
  ka_conv<<<512, 256, KA_LDS, stream>>>(q, k, v, shk, ssmk, dskip, wg, wu, wd, ws);
  kb_mlp<<<256, 256, KB_LDS, stream>>>(ws, (u16*)d_out);
}
```

```cpp
#include <hip/hip_runtime.h>
#include <stdint.h>
#include <utility>

typedef unsigned short u16;
typedef unsigned u32;
typedef __attribute__((ext_vector_type(2))) u32 u32x2;
typedef __attribute__((ext_vector_type(4))) u32 u32x4;
typedef __attribute__((ext_vector_type(2))) float f32x2;
typedef __attribute__((ext_vector_type(4))) float f32x4;
typedef __attribute__((ext_vector_type(2))) __bf16 bf16x2;
typedef __attribute__((ext_vector_type(8))) __bf16 bf16x8;
typedef __attribute__((ext_vector_type(4))) short s16x4;
#define LDSP __attribute__((address_space(3)))
#define DI __device__ __forceinline__

constexpr int S = 2048, D = 512, F = 1376;
constexpr int NU = F / 8;
constexpr int KS1 = D / 32;
constexpr int KS2 = F / 32;
constexpr float EPS = 1e-5f;

constexpr size_t WS_XR = 0;
constexpr size_t WS_QT = WS_XR + (size_t)D * S * 2;
constexpr size_t WS_HT = WS_QT + (size_t)D * S * 2;
constexpr size_t WS_W1 = WS_HT + (size_t)D * S * 2;
constexpr size_t WS_WD = WS_W1 + (size_t)NU * KS1 * 1024;
constexpr size_t WS_H = WS_WD + (size_t)8 * KS2 * 4 * 1024;
constexpr size_t WS_CS = WS_H + (size_t)(S / 16) * KS2 * 1024;
constexpr size_t WS_SA = WS_CS + (size_t)KS1 * NU * 16 * 4;
constexpr size_t WS_SB = WS_SA + (size_t)S * 4;
constexpr size_t WS_CNT = WS_SB + (size_t)S * 4;
constexpr size_t WS_FLA = WS_CNT + 16 * 128;
constexpr size_t WS_FLG = WS_FLA + 256 * 8;
constexpr size_t WS_FLS = WS_FLG + 256 * 8;
constexpr size_t WS_HNT = WS_FLS + 256 * 8;
constexpr size_t WS_EPO = WS_HNT + 256 * 8;

template <int... Is, class Fn> DI void static_for_(std::integer_sequence<int, Is...>, Fn&& f) { (f(std::integral_constant<int, Is>{}), ...); }
template <int N, class Fn> DI void static_for(Fn&& f) { static_for_(std::make_integer_sequence<int, N>{}, f); }

DI float blo(u32 u) { return __uint_as_float(u << 16); }
DI float bhi(u32 u) { return __uint_as_float(u & 0xffff0000u); }
DI u32 pk2(float a, float b) { f32x2 t = {a, b}; return __builtin_bit_cast(u32, __builtin_convertvector(t, bf16x2)); }
DI f32x4 mfma16(u32x4 a, u32x4 b, f32x4 c) {
  return __builtin_amdgcn_mfma_f32_16x16x32_bf16(__builtin_bit_cast(bf16x8, a), __builtin_bit_cast(bf16x8, b), c, 0, 0, 0);
}
DI u32x2 tr_read(u32 lds_byte) {
  return __builtin_bit_cast(u32x2, __builtin_amdgcn_ds_read_tr16_b64_v4i16((LDSP s16x4*)(size_t)lds_byte));
}
DI u32x4 tr_frag(u32 a0, u32 a1) { u32x2 x = tr_read(a0), y = tr_read(a1); return u32x4{x[0], x[1], y[0], y[1]}; }
DI u32x4 cat(u32x2 x, u32x2 y) { return __builtin_shufflevector(x, y, 0, 1, 2, 3); }
template <int CTRL> DI float dpp_add(float v) {
  return v + __builtin_bit_cast(float, __builtin_amdgcn_update_dpp(0, __builtin_bit_cast(int, v), CTRL, 0xf, 0xf, true));
}
DI float sum_rows(float v) {
  u32 u = __float_as_uint(v);
  auto a = __builtin_amdgcn_permlane16_swap(u, u, false, false);
  v = __uint_as_float(a[0]) + __uint_as_float(a[1]);
  u = __float_as_uint(v);
  auto b = __builtin_amdgcn_permlane32_swap(u, u, false, false);
  return __uint_as_float(b[0]) + __uint_as_float(b[1]);
}
DI float wave_sum(float v) {
  v = dpp_add<0xb1>(v);
  v = dpp_add<0x4e>(v);
  v = dpp_add<0x124>(v);
  v = dpp_add<0x128>(v);
  return sum_rows(v);
}
DI __amdgpu_buffer_rsrc_t ws_rsrc(char* ws) { return __builtin_amdgcn_make_buffer_rsrc(ws, 0, 0x04000000, 0x00020000); }
DI void st16_wt(__amdgpu_buffer_rsrc_t r, u32 off, u32x4 v) { __builtin_amdgcn_raw_buffer_store_b128(v, r, off, 0, 16); }
DI int slice_u0(int j) { return j < 12 ? 11 * j : 132 + 10 * (j - 12); }
DI int slice_nu(int j) { return j < 12 ? 11 : 10; }

template <bool FIRST = false> DI void mfma_vv(f32x4& acc, const u32x4& a, const u32x4& b) {
  if constexpr (FIRST) asm volatile("v_mfma_f32_16x16x32_bf16 %0, %1, %2, 0" : "=&v"(acc) : "v"(a), "v"(b));
  else asm volatile("v_mfma_f32_16x16x32_bf16 %0, %1, %2, %0" : "+v"(acc) : "v"(a), "v"(b));
}
template <bool FIRST = false> DI void mfma_av(f32x4& acc, const u32x4& a, const u32x4& b) {
  if constexpr (FIRST) asm volatile("v_mfma_f32_16x16x32_bf16 %0, %1, %2, 0" : "=&v"(acc) : "a"(a), "v"(b));
  else asm volatile("v_mfma_f32_16x16x32_bf16 %0, %1, %2, %0" : "+v"(acc) : "a"(a), "v"(b));
}
template <int OFF> DI void ds_b64(u32x2& d, u32 addr) { asm volatile("ds_read_b64 %0, %1 offset:%c2" : "=v"(d) : "v"(addr), "i"(OFF) : "memory"); }
template <int OFF> DI void ds_b128(u32x4& d, u32 addr) { asm volatile("ds_read_b128 %0, %1 offset:%c2" : "=v"(d) : "v"(addr), "i"(OFF) : "memory"); }
template <int OFF> DI void ds_tr(u32x2& d, u32 addr) { asm volatile("ds_read_b64_tr_b16 %0, %1 offset:%c2" : "=v"(d) : "v"(addr), "i"(OFF) : "memory"); }
template <int OFF> DI void gld_a(u32x4& d, u32 voff, const void* sbase) {
  asm volatile("global_load_dwordx4 %0, %1, %2 offset:%c3" : "=a"(d) : "v"(voff), "s"(sbase), "i"(OFF) : "memory");
}
template <int OFF> DI void gld_v(u32x4& d, u32 voff, const void* sbase) {
  asm volatile("global_load_dwordx4 %0, %1, %2 offset:%c3" : "=v"(d) : "v"(voff), "s"(sbase), "i"(OFF) : "memory");
}
template <int OFF> DI void gld2_v(u32x2& d, u32 voff, const void* sbase) {
  asm volatile("global_load_dwordx2 %0, %1, %2 offset:%c3" : "=v"(d) : "v"(voff), "s"(sbase), "i"(OFF) : "memory");
}
template <int LOFF> DI void glds16(u32 voff, const void* sbase, u32 ldsbase) {
  asm volatile("s_add_u32 m0, %2, %c3\n\ts_nop 0\n\tglobal_load_lds_dwordx4 %0, %1" ::"v"(voff), "s"(sbase), "s"(ldsbase), "i"(LOFF) : "memory", "scc");
}
template <int N> DI void wait_vm() { asm volatile("s_waitcnt vmcnt(%c0)" ::"i"(N) : "memory"); }
template <int N> DI void wait_lgkm() { asm volatile("s_waitcnt lgkmcnt(%c0)" ::"i"(N) : "memory"); }
template <int OFF> DI void gld_a_sc1(u32x4& d, u32 voff, const void* sbase) {
  asm volatile("global_load_dwordx4 %0, %1, %2 offset:%c3 sc1" : "=a"(d) : "v"(voff), "s"(sbase), "i"(OFF) : "memory");
}
DI void mfma_aa(f32x4& acc, const u32x4& a, const u32x4& b) {
  asm volatile("v_mfma_f32_16x16x32_bf16 %0, %1, %2, %0" : "+v"(acc) : "a"(a), "a"(b));
}
template <bool FIRST = false> DI void mfma_va(f32x4& acc, const u32x4& a, const u32x4& b) {
  if constexpr (FIRST) asm volatile("v_mfma_f32_16x16x32_bf16 %0, %1, %2, 0" : "=&v"(acc) : "v"(a), "a"(b));
  else asm volatile("v_mfma_f32_16x16x32_bf16 %0, %1, %2, %0" : "+v"(acc) : "v"(a), "a"(b));
}
DI void mfma_fence(f32x4& a, f32x4& b, f32x4& c, f32x4& d) { asm volatile("s_nop 7\n\ts_nop 1" : "+v"(a), "+v"(b), "+v"(c), "+v"(d)); }
DI void after_fence(f32x4& a, f32x4& b, f32x4& c, f32x4& d) { asm volatile("" : "+v"(a), "+v"(b), "+v"(c), "+v"(d)); }
DI void after_fence(f32x4& a, f32x4& b, f32x4& c) { asm volatile("" : "+v"(a), "+v"(b), "+v"(c)); }
template <int STAGE> DI void dpp_stage(float& v) {
  if constexpr (STAGE == 0) asm volatile("v_add_f32_dpp %0, %0, %0 quad_perm:[1,0,3,2] row_mask:0xf bank_mask:0xf" : "+v"(v));
  if constexpr (STAGE == 1) asm volatile("v_add_f32_dpp %0, %0, %0 quad_perm:[2,3,0,1] row_mask:0xf bank_mask:0xf" : "+v"(v));
  if constexpr (STAGE == 2) asm volatile("v_add_f32_dpp %0, %0, %0 row_ror:4 row_mask:0xf bank_mask:0xf" : "+v"(v));
  if constexpr (STAGE == 3) asm volatile("v_add_f32_dpp %0, %0, %0 row_ror:8 row_mask:0xf bank_mask:0xf" : "+v"(v));
}
DI void barrier() { asm volatile("s_barrier" ::: "memory"); }
DI void pin(u32x4& x) { asm volatile("" : "+v"(x)); }
DI void pin(u32x2& x) { asm volatile("" : "+v"(x)); }
DI void pin4(f32x4& x) { asm volatile("" : "+v"(x)); }
DI void pin_a(u32x4& x) { asm volatile("" : "+a"(x)); }

#define STAMP(kern, idx) do {} while (0)

template <class Gate>
DI void w_job(int b, const u16* __restrict__ wg, const u16* __restrict__ wu, const u16* __restrict__ wd, char* __restrict__ ws, char* smem, Gate&& gate) {
  const int t = threadIdx.x, lane = t & 63, w = t >> 6;
  char* tile = smem;
  char* tile2 = smem + 32 * 368;
  const u32 tb = (u32)(size_t)(LDSP char*)smem;
  const int i16 = lane & 15, qd = i16 >> 2, p = i16 & 3, g = lane >> 4;
  const int ks = b & 15, j = b >> 4;
  const int U0 = slice_u0(j), nu = slice_nu(j);
  u32x4 g4[2], u4[2], d4[2];
#pragma unroll
  for (int rep = 0; rep < 2; ++rep) {
    int idx = t + 256 * rep, r = idx / 11, u = idx - 11 * r;
    bool ok = r < 32 && u < nu;
    size_t off = ok ? (size_t)(32 * ks + r) * F + 8 * (U0 + u) : 0;
    g4[rep] = *(const u32x4*)(wg + off); u4[rep] = *(const u32x4*)(wu + off);
  }
  const int ks2 = b >> 2, dp = b & 3;
  if (b < KS2 * 4) {
#pragma unroll
    for (int rep = 0; rep < 2; ++rep) {
      int idx = t + 256 * rep, r = idx >> 4, c = idx & 15;
      d4[rep] = *(const u32x4*)(wd + (size_t)(32 * ks2 + r) * D + 128 * dp + 8 * c);
    }
  }
  gate();
#pragma unroll
  for (int rep = 0; rep < 2; ++rep) {
    int idx = t + 256 * rep, r = idx / 11, u = idx - 11 * r;
    if (r < 32 && u < nu) {
      *(u32x4*)(tile + r * 368 + u * 32) = u32x4{g4[rep][0], u4[rep][0], g4[rep][1], u4[rep][1]};
      *(u32x4*)(tile + r * 368 + u * 32 + 16) = u32x4{g4[rep][2], u4[rep][2], g4[rep][3], u4[rep][3]};
    }
  }
  if (b < KS2 * 4) {
#pragma unroll
    for (int rep = 0; rep < 2; ++rep) { int idx = t + 256 * rep, r = idx >> 4, c = idx & 15; *(u32x4*)(tile2 + r * 272 + c * 16) = d4[rep]; }
  }
  __syncthreads();
  const u32x4 ones = {0x3f803f80u, 0x3f803f80u, 0x3f803f80u, 0x3f803f80u};
  const __amdgpu_buffer_rsrc_t wr = ws_rsrc(ws);
  for (int u = w; u < nu; u += 4) {
    u32 a = tb + (8 * g + qd) * 368 + u * 32 + 8 * p;
    u32x4 fr = tr_frag(a, a + 4 * 368);
    st16_wt(wr, (u32)WS_W1 + (((U0 + u) * KS1 + ks) * 64 + lane) * 16, fr);
    f32x4 cs = mfma16(fr, ones, f32x4{0.f, 0.f, 0.f, 0.f});
    if (i16 == 0) st16_wt(wr, (u32)WS_CS + ((ks * NU + U0 + u) * 16 + 4 * g) * 4, __builtin_bit_cast(u32x4, cs));
  }
  if (b < KS2 * 4) {
#pragma unroll
    for (int rep = 0; rep < 2; ++rep) {
      int fi = w + 4 * rep, dtl = fi >> 2, df = fi & 3;
      u32 a = tb + 32 * 368 + (8 * g + qd) * 272 + (64 * dtl + 32 * (df >> 1) + 8 * p + 4 * (df & 1)) * 2;
      u32x4 fr = tr_frag(a, a + 4 * 272);
      st16_wt(wr, (u32)WS_WD + ((((2 * dp + dtl) * KS2 + ks2) * 4 + df) * 64 + lane) * 16, fr);
    }
  }
}

constexpr int KA_CS = 4160;
constexpr int KA_KC = 4 * KA_CS;
constexpr int KA_XR = 2 * KA_KC;
constexpr int KA_RED = KA_XR + 2 * 4608;
constexpr int KA_LDS = KA_RED + 4 * 4096;
constexpr int KA_TK = KA_XR, KA_TV = KA_TK + 72 * 128, KA_TQ = KA_TV + 64 * 128;
static_assert(KA_TQ + 64 * 128 == KA_LDS, "tile overlay");
constexpr int SPIN_MAX = 1 << 22;
#define HINT_AT 1
#define HELP_SLEEP 40
#define CHK 2
#define WEARLY 0
#define HINT_TICKS 800ull

template <int OFF> DI void gld_v_sc1(u32x4& d, u32 voff, const void* sbase) {
  asm volatile("global_load_dwordx4 %0, %1, %2 offset:%c3 sc1" : "=v"(d) : "v"(voff), "s"(sbase), "i"(OFF) : "memory");
}
template <int OFF> DI void gld2_v_sc1(u32x2& d, u32 voff, const void* sbase) {
  asm volatile("global_load_dwordx2 %0, %1, %2 offset:%c3 sc1" : "=v"(d) : "v"(voff), "s"(sbase), "i"(OFF) : "memory");
}
DI bool tags_ok(const u32x4& f, const u32x2& tag) { return f[0] == tag[0] && f[1] == tag[1] && f[2] == tag[0] && f[3] == tag[1]; }

__global__ __launch_bounds__(256, 2) void ka_conv(const u16* __restrict__ q, const u16* __restrict__ k, const u16* __restrict__ v,
                                                  const u16* __restrict__ shk, const u16* __restrict__ ssmk, const u16* __restrict__ dskip,
                                                  const u16* __restrict__ wg, const u16* __restrict__ wu, const u16* __restrict__ wd,
                                                  char* __restrict__ ws) {
  extern __shared__ __attribute__((aligned(16))) char smem[];
  const int t = threadIdx.x, lane = t & 63;
  const int w = __builtin_amdgcn_readfirstlane(t >> 6);
  const int b = blockIdx.x;
  char* ws_stamp = ws; (void)ws_stamp;
  STAMP(0, 0);
  if (b >= 256) {
    const int hb = b - 256, srow = 8 * hb + 2 * w;
    const __amdgpu_buffer_rsrc_t wr = ws_rsrc(ws);
    u32x2 epo; u32x4 qa, qb;
    gld2_v_sc1<0>(epo, 0, ws + WS_EPO);
    gld_v<0>(qa, lane * 16, (const char*)q + (size_t)srow * 1024);
    gld_v<1024>(qb, lane * 16, (const char*)q + (size_t)srow * 1024);
    if (b < 264 && w == 1) {
      const char* pc; u32 len;
      asm volatile("s_getpc_b64 %0\n1:\n\ts_mov_b32 %1, kb_code_end-1b" : "=s"(pc), "=s"(len));
      const u32 lb = (u32)(size_t)(LDSP char*)smem + 32768;
      for (u32 o0 = 0; o0 < len; o0 += 64 * 128) {
        u32 o = o0 + lane * 128; o = o < len ? o : 0;
        asm volatile("s_mov_b32 m0, %2\n\ts_nop 0\n\tglobal_load_lds_dword %0, %1" ::"v"(o), "s"(pc), "s"(lb) : "memory");
      }
    }
    wait_vm<0>();
    asm volatile("" : "+v"(qa), "+v"(qb), "+v"(epo));
    const unsigned long long want = (((unsigned long long)epo[1] << 32) | epo[0]) + 1ull;
    {
      float sa = 0.f, sbb = 0.f;
#pragma unroll
      for (int e = 0; e < 4; ++e) { sa += blo(qa[e]) + bhi(qa[e]); sbb += blo(qb[e]) + bhi(qb[e]); }
      const float ma = wave_sum(sa) * (1.f / D), mb = wave_sum(sbb) * (1.f / D);
      float va = 0.f, vb = 0.f;
#pragma unroll
      for (int e = 0; e < 4; ++e) {
        float u0 = blo(qa[e]) - ma, u1 = bhi(qa[e]) - ma, u2 = blo(qb[e]) - mb, u3 = bhi(qb[e]) - mb;
        va += u0 * u0 + u1 * u1; vb += u2 * u2 + u3 * u3;
      }
      va = wave_sum(va) * (1.f / D); vb = wave_sum(vb) * (1.f / D);
      const float ra = __builtin_amdgcn_rsqf(va + EPS), rb = __builtin_amdgcn_rsqf(vb + EPS);
      const u32x2 val = lane == 0 ? u32x2{__float_as_uint(ra), __float_as_uint(rb)} : u32x2{__float_as_uint(-ma * ra), __float_as_uint(-mb * rb)};
      const u32 off = lane == 0 ? (u32)WS_SA + srow * 4 : lane == 1 ? (u32)WS_SB + srow * 4 : 0xfffffff0u;
      __builtin_amdgcn_raw_buffer_store_b64(val, wr, off, 0, 16);
    }
    asm volatile("s_waitcnt vmcnt(0)\n\ts_barrier" ::: "memory");
    if (t == 0) __builtin_amdgcn_raw_buffer_store_b64(u32x2{(u32)want, (u32)(want >> 32)}, wr, (u32)WS_FLS + hb * 8, 0, 16);
    STAMP(1, 6);
    if (HELP_SLEEP > 0) __builtin_amdgcn_s_sleep(HELP_SLEEP);
    w_job(hb, wg, wu, wd, ws, smem, [&]() {
      if (w == 0) {
        const unsigned long long* hp = (const unsigned long long*)(ws + WS_HNT) + hb;
        const unsigned long long t0 = __builtin_amdgcn_s_memrealtime();
        while (__hip_atomic_load(hp, __ATOMIC_RELAXED, __HIP_MEMORY_SCOPE_AGENT) != want && __builtin_amdgcn_s_memrealtime() - t0 < HINT_TICKS)
          __builtin_amdgcn_s_sleep(8);
      }
      __builtin_amdgcn_s_barrier();
      STAMP(1, 5);
    });
    STAMP(1, 4);
    return;
  }
  const int x = b & 7, bi = b >> 3, s0 = 64 * bi;
  const int ch0 = 64 * x + 2 * bi;
  const u32 sb = (u32)(size_t)(LDSP char*)smem;
  const int n = lane & 15, g = lane >> 4, qd = n >> 2, p = n & 3;
  const int d = 64 * x + 16 * w + n;
  const __amdgpu_buffer_rsrc_t wr = ws_rsrc(ws);

  u32x2 epo, sh; u32x4 kp[2], kc[2];
  gld2_v_sc1<0>(epo, 0, ws + WS_EPO);
  gld2_v<0>(sh, d * 8, shk);
  const int r8 = lane >> 3, c8 = lane & 7;
  auto voff = [&](int instr) { const int f = ((r8 >> 1) & 1) | ((instr & 1) << 1); return (u32)((((c8 >> 1) ^ f) * 2 + (c8 & 1)) * 16); };
  const size_t col = (size_t)x * 128;
  {
    int row = s0 - 8 + 8 * w + r8; row = row < 0 ? 0 : row;
    glds16<0>((u32)row * 1024 + voff(w), (const char*)k + col, sb + KA_TK + w * 1024);
    glds16<0>((u32)(s0 + 8 * w + r8) * 1024 + voff(w), (const char*)v + col, sb + KA_TV + w * 1024);
    glds16<0>((u32)(s0 - 8 + 8 * (w + 4) + r8) * 1024 + voff(w), (const char*)k + col, sb + KA_TK + (w + 4) * 1024);
    glds16<0>((u32)(s0 + 8 * (w + 4) + r8) * 1024 + voff(w), (const char*)v + col, sb + KA_TV + (w + 4) * 1024);
    if (w == 0) glds16<0>((u32)(s0 + 56 + r8) * 1024 + voff(8), (const char*)k + col, sb + KA_TK + 8 * 1024);
  }
#pragma unroll
  for (int cc = 0; cc < 2; ++cc) {
    const char* kr = (const char*)ssmk + (size_t)(ch0 + cc) * 4096;
    gld_v<0>(kp[cc], (u32)(t > 0 ? t - 1 : 0) * 16, kr);
    gld_v<0>(kc[cc], (u32)t * 16, kr);
  }
  glds16<0>((u32)(s0 + 8 * w + r8) * 1024 + voff(w), (const char*)q + col, sb + KA_TQ + w * 1024);
  glds16<0>((u32)(s0 + 8 * (w + 4) + r8) * 1024 + voff(w), (const char*)q + col, sb + KA_TQ + (w + 4) * 1024);
  const float dsk = blo(dskip[ch0 + (w >> 1)]);
  if (b == 0 && t < 16) *(u32*)(ws + WS_CNT + t * 128) = 0u;

  wait_vm<6>();
  barrier();
  asm volatile("" : "+v"(sh), "+v"(epo));
  STAMP(0, 1);
  u32x2 tag;
  { const unsigned long long e = (((unsigned long long)epo[1] << 32) | epo[0]) + 1ull; tag = u32x2{(u32)e, (u32)(e >> 32)}; }
  auto taddr = [&](int base, int tr) { const int f = ((tr >> 1) & 1) | ((tr >> 2) & 2); return sb + base + tr * 128 + ((w ^ f) * 32) + 8 * p; };
  {
    const float a0 = blo(sh[0]), a1 = bhi(sh[0]), a2 = blo(sh[1]), a3 = bhi(sh[1]);
#pragma unroll
    for (int e = 0; e < 2; ++e) {
      const int rg = g + 4 * e;
      u32x2 km = tr_read(taddr(KA_TK, 8 * rg + 4 + qd));
      const u32x2 k0 = tr_read(taddr(KA_TK, 8 * rg + 8 + qd)), k1 = tr_read(taddr(KA_TK, 8 * rg + 12 + qd));
      const u32x2 v0 = tr_read(taddr(KA_TV, 8 * rg + qd)), v1 = tr_read(taddr(KA_TV, 8 * rg + 4 + qd));
      if (s0 + rg == 0) km = u32x2{0, 0};
      const float kf[11] = {bhi(km[0]), blo(km[1]), bhi(km[1]), blo(k0[0]), bhi(k0[0]), blo(k0[1]), bhi(k0[1]), blo(k1[0]), bhi(k1[0]), blo(k1[1]), bhi(k1[1])};
      const float vf[8] = {blo(v0[0]), bhi(v0[0]), blo(v0[1]), bhi(v0[1]), blo(v1[0]), bhi(v1[0]), blo(v1[1]), bhi(v1[1])};
      float xo[8];
#pragma unroll
      for (int j = 0; j < 8; ++j) xo[j] = (kf[j + 3] * a0 + kf[j + 2] * a1 + kf[j + 1] * a2 + kf[j] * a3) * vf[j];
      st16_wt(wr, (u32)WS_XR + (d * S + (2040 - (s0 + 8 * rg))) * 2, u32x4{pk2(xo[7], xo[6]), pk2(xo[5], xo[4]), pk2(xo[3], xo[2]), pk2(xo[1], xo[0])});
    }
  }
  asm volatile("s_waitcnt vmcnt(0)\n\ts_barrier" : "+v"(kp[0]), "+v"(kc[0]), "+v"(kp[1]), "+v"(kc[1]) :: "memory");
  if (t == 0) __builtin_amdgcn_raw_buffer_store_b64(tag, wr, (u32)WS_FLA + (x * 32 + bi) * 8, 0, 16);
  STAMP(0, 2);
#pragma unroll
  for (int e = 0; e < 2; ++e) {
    const int rg = g + 4 * e;
    const u32x2 q0 = tr_read(taddr(KA_TQ, 8 * rg + qd)), q1 = tr_read(taddr(KA_TQ, 8 * rg + 4 + qd));
    st16_wt(wr, (u32)WS_QT + (d * S + s0 + 8 * rg) * 2, cat(q0, q1));
  }
#pragma unroll
  for (int cc = 0; cc < 2; ++cc) {
    const u32 m = t > 0 ? 0xffffffffu : 0u;
    u32 wv[6] = {kp[cc][0] & m, kp[cc][1] & m, kp[cc][2] & m, kp[cc][3] & m, kc[cc][0], kc[cc][1]};
    u32 o1[5];
#pragma unroll
    for (int i = 0; i < 5; ++i) o1[i] = __builtin_amdgcn_alignbit(wv[i + 1], wv[i], 16);
    char* base = smem + cc * KA_KC + (t + 1) * 16;
    *(u32x4*)(base + 0 * KA_CS) = u32x4{o1[0], o1[1], o1[2], o1[3]};
    *(u32x4*)(base + 1 * KA_CS) = u32x4{wv[1], wv[2], wv[3], wv[4]};
    *(u32x4*)(base + 2 * KA_CS) = u32x4{o1[1], o1[2], o1[3], o1[4]};
    *(u32x4*)(base + 3 * KA_CS) = u32x4{wv[2], wv[3], wv[4], wv[5]};
    if (t == 255) {
      const u32 z = 0;
      const u32 e0 = __builtin_amdgcn_alignbit(kc[cc][1], kc[cc][0], 16), e1 = __builtin_amdgcn_alignbit(kc[cc][2], kc[cc][1], 16);
      const u32 e2 = __builtin_amdgcn_alignbit(kc[cc][3], kc[cc][2], 16), e3 = __builtin_amdgcn_alignbit(z, kc[cc][3], 16);
      *(u32x4*)(base + 16 + 0 * KA_CS) = u32x4{e0, e1, e2, e3};
      *(u32x4*)(base + 16 + 1 * KA_CS) = u32x4{kc[cc][1], kc[cc][2], kc[cc][3], z};
      *(u32x4*)(base + 16 + 2 * KA_CS) = u32x4{e1, e2, e3, z};
      *(u32x4*)(base + 16 + 3 * KA_CS) = u32x4{kc[cc][2], kc[cc][3], z, z};
    }
  }
  if (t < 8) *(u32x4*)(smem + (t >> 2) * KA_KC + (t & 3) * KA_CS) = u32x4{0, 0, 0, 0};
  asm volatile("s_waitcnt vmcnt(0) lgkmcnt(0)\n\ts_barrier" ::: "memory");
  if (t == 0) {
    __builtin_amdgcn_raw_buffer_store_b64(tag, wr, (u32)WS_FLG + (x * 32 + bi) * 8, 0, 16);
    if (HINT_AT == 0) __builtin_amdgcn_raw_buffer_store_b64(tag, wr, (u32)WS_HNT + b * 8, 0, 16);
  }
  if (t >= 64 && t < 64 + 60) { int i = t - 64; *(u32x4*)(smem + KA_XR + (i / 30) * 4608 + 4096 + (i % 30) * 16) = u32x4{0, 0, 0, 0}; }
  if (w == 0) {
    const unsigned long long* fp = (const unsigned long long*)(ws + WS_FLA) + x * 32 + (lane & 31);
    const unsigned long long want = ((unsigned long long)tag[1] << 32) | tag[0];
    for (int spins = 0; spins < SPIN_MAX; ++spins) {
      if (__all(__hip_atomic_load(fp, __ATOMIC_RELAXED, __HIP_MEMORY_SCOPE_AGENT) == want)) break;
      __builtin_amdgcn_s_sleep(1);
    }
  }
  asm volatile("s_waitcnt vmcnt(0)\n\ts_barrier" ::: "memory");
  STAMP(0, 3);

  const int c = w >> 1, half = w & 1;
  u32x2 qv[4]; u32x4 sa[4], sbv[4], fl[3];
  const u32 eo = (u32)(1024 * half + 16 * n + 4 * g);
  const char* qTc = ws + WS_QT + (size_t)(ch0 + c) * 4096;
  {
    u32x4 x0, x1;
    gld_v_sc1<0>(x0, t * 16, ws + WS_XR + (size_t)ch0 * 4096);
    gld_v_sc1<0>(x1, t * 16, ws + WS_XR + (size_t)(ch0 + 1) * 4096);
    wait_vm<0>();
    asm volatile("" : "+v"(x0), "+v"(x1));
    *(u32x4*)(smem + KA_XR + t * 16) = x0;
    *(u32x4*)(smem + KA_XR + 4608 + t * 16) = x1;
  }
  asm volatile("s_waitcnt lgkmcnt(0)\n\ts_barrier" ::: "memory");
  if (HINT_AT == 1 && t == 0) __builtin_amdgcn_raw_buffer_store_b64(tag, wr, (u32)WS_HNT + b * 8, 0, 16);
  STAMP(1, 1);

  const u32 aa = sb + c * KA_KC + (n & 3) * KA_CS + 16 * g + 2 * (n & 12) + half * 256;
  const u32 ba = sb + KA_XR + c * 4608 + 480 - 32 * n + 16 * g + half * 256;
  f32x4 acc[8];
  u32x2 Alo[2][8], Ahi[2][8];
  u32x4 B[2][8];
  auto rd = [&](auto C0, auto R) {
    constexpr int c0 = C0, r = R, buf = c0 & 1;
    if constexpr (r >= 2 && r < 10) {
      constexpr int u = r - 2;
      ds_b128<512 * (7 - u) + 64 * c0>(B[buf][u], ba);
    } else {
      constexpr int ar = r < 2 ? r : r - 8, i = ar >> 1;
      if constexpr ((ar & 1) == 0) ds_b64<512 * i + 64 * c0>(Alo[buf][i], aa);
      else ds_b64<512 * i + 64 * c0 + 8>(Ahi[buf][i], aa);
    }
  };
  auto ldS = [&](auto E) {
    constexpr int e = E, i = e / 3, kind = e % 3;
    if constexpr (kind == 0) gld_v_sc1<i * 1024>(sa[i], eo * 4, ws + WS_SA);
    if constexpr (kind == 1) gld_v_sc1<i * 1024>(sbv[i], eo * 4, ws + WS_SB);
    if constexpr (kind == 2) gld2_v_sc1<i * 512>(qv[i], eo * 2, qTc);
  };
  auto ldF = [&](auto E) {
    constexpr int e = E;
    if constexpr (e < 2) gld_v_sc1<16 * e>(fl[e], lane * 32, ws + WS_FLS);
    else gld_v_sc1<0>(fl[2], (x * 32 + 2 * (lane & 15)) * 8, ws + WS_FLG);
  };
  static_for<24>([&](auto R) { rd(std::integral_constant<int, 0>{}, R); });
  static_for<4>([&](auto C0) {
    constexpr int c0 = C0, buf = c0 & 1;
    if constexpr (c0 == CHK) {
      wait_vm<0>();
      asm volatile("" : "+v"(fl[0]), "+v"(fl[1]), "+v"(fl[2]));
      if (!__all(tags_ok(fl[0], tag) && tags_ok(fl[1], tag) && tags_ok(fl[2], tag))) {
        for (int spins = 0; spins < SPIN_MAX; ++spins) {
          ldF(std::integral_constant<int, 0>{}); ldF(std::integral_constant<int, 1>{}); ldF(std::integral_constant<int, 2>{});
          wait_vm<0>();
          asm volatile("" : "+v"(fl[0]), "+v"(fl[1]), "+v"(fl[2]));
          if (__all(tags_ok(fl[0], tag) && tags_ok(fl[1], tag) && tags_ok(fl[2], tag))) break;
          __builtin_amdgcn_s_sleep(1);
        }
      }
    }
    u32x4 A[8];
    static_for<36>([&](auto M) {
      constexpr int m = M;
      constexpr int c1 = m < 8 ? 0 : m < 15 ? 1 : m < 21 ? 2 : m < 26 ? 3 : m < 30 ? 4 : m < 33 ? 5 : m < 35 ? 6 : 7;
      constexpr int first = c1 * 8 - c1 * (c1 - 1) / 2;
      constexpr int u = m - first;
      if constexpr (m == 0) {
        if constexpr (c0 == 0) wait_lgkm<14>(); else wait_lgkm<0>();
        pin(Alo[buf][0]); pin(Ahi[buf][0]);
#pragma unroll
        for (int i = 0; i < 8; ++i) pin(B[buf][i]);
        A[0] = cat(Alo[buf][0], Ahi[buf][0]);
      }
      if constexpr (m == 8) {
        if constexpr (c0 == 0) wait_lgkm<8>();
#pragma unroll
        for (int i = 1; i < 8; ++i) { pin(Alo[buf][i]); pin(Ahi[buf][i]); A[i] = cat(Alo[buf][i], Ahi[buf][i]); }
      }
      mfma_vv<(c0 == 0 && c1 == 0)>(acc[c1 + u], A[c1], B[buf][u]);
      if constexpr (c0 < 3 && m < 24) rd(std::integral_constant<int, c0 + 1>{}, M);
      if constexpr (c0 == CHK - 1 && (m == 2 || m == 4 || m == 6)) ldF(std::integral_constant<int, (m - 2) / 2>{});
      if constexpr (c0 == CHK && m >= 1 && m < 25 && (m & 1) == 1) ldS(std::integral_constant<int, (m - 1) / 2>{});
    });
  });
  mfma_fence(acc[0], acc[1], acc[2], acc[3]);
  after_fence(acc[4], acc[5], acc[6], acc[7]);
  STAMP(1, 2);
  f32x4 fin[4];
  {
    f32x4* red = (f32x4*)(smem + KA_RED);
    if (half) {
#pragma unroll
      for (int i = 0; i < 4; ++i) { red[(w * 4 + i) * 64 + lane] = acc[i]; fin[i] = acc[4 + i]; }
      asm volatile("; half 1" ::: "memory");
    } else {
#pragma unroll
      for (int i = 0; i < 4; ++i) { red[(w * 4 + i) * 64 + lane] = acc[4 + i]; fin[i] = acc[i]; }
      asm volatile("; half 0" ::: "memory");
    }
    asm volatile("s_waitcnt lgkmcnt(0)\n\ts_barrier" ::: "memory");
#pragma unroll
    for (int i = 0; i < 4; ++i) fin[i] += red[((w ^ 1) * 4 + i) * 64 + lane];
  }
  wait_vm<0>();
#pragma unroll
  for (int i = 0; i < 4; ++i) asm volatile("" : "+v"(qv[i]), "+v"(sa[i]), "+v"(sbv[i]));
  const f32x2 dd = {dsk, dsk};
  u32x2 hv[4];
#pragma unroll
  for (int i = 0; i < 4; ++i) {
    const int s = 256 * (4 * half + i) + 16 * n + 4 * g;
    const u32x2 xq = *(LDSP const u32x2*)(size_t)(sb + KA_XR + c * 4608 + 2 * (2044 - s));
    const f32x2 x0 = {bhi(xq[1]), blo(xq[1])}, x1 = {bhi(xq[0]), blo(xq[0])};
    const f32x2 q0 = {blo(qv[i][0]), bhi(qv[i][0])}, q1 = {blo(qv[i][1]), bhi(qv[i][1])};
    const f32x2 z0 = x0 * dd + f32x2{fin[i][0], fin[i][1]}, z1 = x1 * dd + f32x2{fin[i][2], fin[i][3]};
    const f32x4 a4 = __builtin_bit_cast(f32x4, sa[i]), b4 = __builtin_bit_cast(f32x4, sbv[i]);
    const f32x2 t0 = q0 * f32x2{a4[0], a4[1]} + f32x2{b4[0], b4[1]};
    const f32x2 t1 = q1 * f32x2{a4[2], a4[3]} + f32x2{b4[2], b4[3]};
    const f32x2 h0 = t0 * z0 + q0, h1 = t1 * z1 + q1;
    hv[i] = u32x2{pk2(h0[0], h0[1]), pk2(h1[0], h1[1])};
  }
#pragma unroll
  for (int i = 0; i < 4; i += 2) {
    auto a = __builtin_amdgcn_permlane16_swap(hv[i][0], hv[i + 1][0], false, false);
    auto bb = __builtin_amdgcn_permlane16_swap(hv[i][1], hv[i + 1][1], false, false);
    const int s = 256 * (4 * half + i + (g & 1)) + 16 * n + 4 * (g & 2);
    st16_wt(wr, (u32)WS_HT + ((ch0 + c) * S + s) * 2, u32x4{a[0], bb[0], a[1], bb[1]});
  }
  if (b == 0 && w == 0) {
    const unsigned long long want = ((unsigned long long)tag[1] << 32) | tag[0];
    const unsigned long long* fp = (const unsigned long long*)(ws + WS_FLG) + lane;
    for (int spins = 0; spins < SPIN_MAX; ++spins) {
      bool ok = true;
#pragma unroll
      for (int i = 0; i < 4; ++i) ok &= __hip_atomic_load(fp + 64 * i, __ATOMIC_RELAXED, __HIP_MEMORY_SCOPE_AGENT) == want;
      if (__all(ok)) break;
      __builtin_amdgcn_s_sleep(1);
    }
    if (lane == 0) __builtin_amdgcn_raw_buffer_store_b64(tag, wr, (u32)WS_EPO, 0, 16);
  }
  STAMP(1, 3);
}

constexpr int KB_NL = 39;
constexpr int KB_ST = KB_NL * 4096;
constexpr int KB_LDS = KB_ST + 1024;
#define EPI_FILL 8
constexpr int KB_RING = 8;

#define P2_AHEAD 5
#define P2_PRO 3
constexpr int p2_upto(int ks) {
  const int ramp = P2_PRO - 1 + 2 * (ks + 1), steady = ks + P2_AHEAD;
  const int u = ks < 0 ? P2_PRO - 1 : (ramp < steady ? ramp : steady);
  return u > 15 ? 15 : u;
}
#define P2_XMAX 5
constexpr int P2_NEXTRA = 23;
constexpr int p2_extra(int ks) {
  const int tail0 = 16 - P2_AHEAD, rest = P2_NEXTRA - P2_AHEAD * P2_XMAX;
  if (ks >= tail0) return P2_XMAX;
  return (rest > 0 && ks >= tail0 - rest) ? 1 : 0;
}
constexpr int p2_extra_first(int ks) { int c = 0; for (int s = 0; s < ks; ++s) c += p2_extra(s); return c; }
constexpr int p2_extra_real(int ks) { int f = p2_extra_first(ks), n = p2_extra(ks); return f >= P2_NEXTRA ? 0 : (f + n > P2_NEXTRA ? P2_NEXTRA - f : n); }
static_assert(p2_extra_first(16) >= P2_NEXTRA, "all Wd operations are scheduled");
constexpr int p2_issue_step(int t) { for (int s = -1; s < KS1; ++s) if (p2_upto(s) >= t) return s; return KS1; }
constexpr int p2_younger(int t, int ks, int ops) {
  int c = (p2_upto(ks - 1) - t) * ops;
  for (int s = p2_issue_step(t); s < ks; ++s) if (s >= 0) c += p2_extra_real(s);
  return c;
}

struct KbCtx {
  u32 sb, gv, lv, lb0, lwd, csv;
  const char *hb0, *hb1, *w0, *w1, *w2, *cs0, *cs1, *cs2, *wd, *wdr;
};

template <int GRAM, class Setup>
DI void kb_phase2(const KbCtx& c, f32x4 (&acc)[3][8], f32x4 (&rsum)[2], f32x4 (&csp)[3], u32x4 (&Wr)[4][4], u32 (&ra)[8], Setup&& setup, char* ws_stamp) {
  constexpr int OPS = GRAM ? 4 : 5;
  u32x4 W[KB_RING][3];
  u32x2 Bl[2][8], Bh[2][8];
  auto issue = [&](auto KS, auto O) {
    constexpr int ks = KS, o = O;
    if constexpr (o == 0) glds16<ks * 8192>(c.gv, c.hb0 + (size_t)ks * 131072, c.lb0);
    if constexpr (o == 1) gld_a<(ks & 3) * 1024>(W[ks % KB_RING][0], c.lv, c.w0 + (ks >> 2) * 4096);
    if constexpr (o == 2) gld_a<(ks & 3) * 1024>(W[ks % KB_RING][1], c.lv, c.w1 + (ks >> 2) * 4096);
    if constexpr (o == 3 && !GRAM) gld_a<(ks & 3) * 1024>(W[ks % KB_RING][2], c.lv, c.w2 + (ks >> 2) * 4096);
    if constexpr (o == 4) glds16<ks * 8192 + 1024>(c.gv, c.hb1 + (size_t)ks * 131072, c.lb0);
  };
  auto extra = [&](auto E) {
    constexpr int e = E;
    if constexpr (e < 7) glds16<(32 + e) * 4096>(c.lv, c.wd + (size_t)(32 + e) * 4096, c.lwd);
    else if constexpr (e < P2_NEXTRA) gld_a<((e - 7) & 3) * 1024>(Wr[(e - 7) >> 2][(e - 7) & 3], c.lv, c.wdr + ((e - 7) >> 2) * 4096);
  };
  auto rdB = [&](auto KS, auto R) {
    constexpr int ks = KS, r = R, i = r >> 1;
    if constexpr ((r & 1) == 0) ds_tr<(ks & 7) * 8192>(Bl[ks & 1][i], ra[i] + (ks >= 8 ? 65536 : 0));
    else ds_tr<(ks & 7) * 8192 + 1024>(Bh[ks & 1][i], ra[i] + (ks >= 8 ? 65536 : 0));
  };
  u32x4 c0, c1, c2;
  static_for<5>([&](auto O) { issue(std::integral_constant<int, 0>{}, O); });
  gld_v<0>(c0, c.csv, c.cs0); gld_v<0>(c1, c.csv, c.cs1); gld_v<0>(c2, c.csv, c.cs2);
  static_for<p2_upto(-1)>([&](auto KS) { static_for<5>([&](auto O) { issue(std::integral_constant<int, KS + 1>{}, O); }); });
  setup();
  u32x4 ones = {0x3f803f80u, 0x3f803f80u, 0x3f803f80u, 0x3f803f80u};
  pin(ones);
  wait_vm<p2_upto(-1) * OPS>();
  barrier();
  static_for<16>([&](auto R) { rdB(std::integral_constant<int, 0>{}, R); });
  asm volatile("" : "+v"(c0), "+v"(c1), "+v"(c2));
  float cv[12];
#pragma unroll
  for (int e = 0; e < 4; ++e) { cv[e] = __uint_as_float(c0[e]); cv[4 + e] = __uint_as_float(c1[e]); cv[8 + e] = __uint_as_float(c2[e]); }
  static_for<KS1>([&](auto KS) {
    constexpr int ks = KS, buf = ks & 1, slot = ks % KB_RING;
    constexpr int nmain = (p2_upto(ks) - p2_upto(ks - 1)) * 5, next = p2_extra_real(ks);
    wait_lgkm<0>();
    u32x4 B[8];
#pragma unroll
    for (int i = 0; i < 8; ++i) { pin(Bl[buf][i]); pin(Bh[buf][i]); }
#pragma unroll
    for (int i = 0; i < 8; ++i) B[i] = cat(Bl[buf][i], Bh[buf][i]);
    pin_a(W[slot][0]); pin_a(W[slot][1]);
    if constexpr (!GRAM) pin_a(W[slot][2]);
    static_for<26>([&](auto M) {
      constexpr int m = M, i = m / 3, u = m % 3;
      if constexpr (m >= 24) mfma_vv<ks == 0>(rsum[m - 24], ones, B[m - 24]);
      else if constexpr (u < 2 || !GRAM) mfma_av<ks == 0>(acc[u][i], W[slot][u], B[i]);
      else mfma_vv<ks == 0>(acc[2][i], B[i], B[i]);
      if constexpr (m == 1 && ks + 1 < KS1) { wait_vm<p2_younger(ks + 1, ks, OPS)>(); barrier(); }
      if constexpr (m >= 2 && m < 18 && ks + 1 < KS1) rdB(std::integral_constant<int, ks + 1>{}, std::integral_constant<int, m - 2>{});
      if constexpr (ks >= 9 && ks < 13 && m >= 2 && m < 14) dpp_stage<ks - 9>(cv[m - 2]);
      constexpr int v = m - (26 - nmain - next);
      if constexpr (v >= 0 && v < nmain) issue(std::integral_constant<int, p2_upto(ks - 1) + 1 + v / 5>{}, std::integral_constant<int, v % 5>{});
      if constexpr (v >= nmain) extra(std::integral_constant<int, p2_extra_first(ks) + v - nmain>{});
    });
  });
  mfma_fence(acc[0][0], acc[1][0], rsum[0], rsum[1]);
#pragma unroll
  for (int i = 0; i < 8; ++i) after_fence(acc[0][i], acc[1][i], acc[2][i], rsum[i & 1]);
#pragma unroll
  for (int u = 0; u < 3; ++u) csp[u] = f32x4{cv[4 * u], cv[4 * u + 1], cv[4 * u + 2], cv[4 * u + 3]};
}

__global__ __launch_bounds__(256) void kb_mlp(char* __restrict__ ws, u16* __restrict__ out) {
  extern __shared__ __attribute__((aligned(16))) char smem[];
  const int t = threadIdx.x, lane = t & 63;
  const int w = __builtin_amdgcn_readfirstlane(t >> 6);
  const int b = blockIdx.x, xcd = b & 7, bi = b >> 3;
#define KB_MAP 1
  const int rt = KB_MAP == 0 ? 2 * xcd + (bi & 1) : (xcd >> 1) * 4 + (bi & 3);
  const int j = KB_MAP == 0 ? bi >> 1 : (xcd & 1) * 8 + (bi >> 2);
  const int half = KB_MAP == 0 ? j & 1 : j >> 3, dt = KB_MAP == 0 ? j >> 1 : j & 7;
  const int U0 = slice_u0(j), nu = slice_nu(j);
  const int rem = nu - 8;
  const int nw = w < rem ? 3 : 2;
  const int uw = U0 + (w < rem ? 3 * w : 3 * rem + 2 * (w - rem));
  const int u2 = uw + (nw == 3 ? 2 : 0);
  const int n = lane & 15, g = lane >> 4;
  const int s0 = 128 * rt;
  char* ws_stamp = ws; (void)ws_stamp;
  STAMP(2, 0);
  auto sf = [&](int i) { return ((w + (i >> 1)) & 3) + 4 * (half ^ (i & 1)); };

  KbCtx c;
  c.sb = (u32)(size_t)(LDSP char*)smem;
  c.lv = lane * 16;
  c.gv = (u32)(g * 4096 + (((n >> 1) ^ (g | ((w & 1) << 2))) * 32 + (n & 1) * 16));
  c.hb0 = ws + WS_HT + (size_t)s0 * 2 + (size_t)(8 * w) * 4096;
  c.hb1 = c.hb0 + 4 * 4096;
  c.lb0 = c.sb + 8 * w * 256;
  c.w0 = ws + WS_W1 + (size_t)uw * KS1 * 1024;
  c.w1 = c.w0 + KS1 * 1024;
  c.w2 = ws + WS_W1 + (size_t)u2 * KS1 * 1024;
  c.cs0 = ws + WS_CS + (size_t)uw * 64;
  c.cs1 = c.cs0 + 64;
  c.cs2 = ws + WS_CS + (size_t)u2 * 64;
  c.csv = n * (NU * 64) + g * 16;
  c.wdr = ws + WS_WD + (size_t)dt * KS2 * 4096 + (size_t)KB_NL * 4096;
  c.wd = ws + WS_WD + (size_t)dt * KS2 * 4096 + (size_t)w * 1024;
  c.lwd = c.sb + 1024 * w;
  u32 ra[8], hoff[6];
  auto setup = [&]() {
    const int qd = n >> 2, p = n & 3;
    const int frow = qd | ((g & 1) << 2);
#pragma unroll
    for (int i = 0; i < 8; ++i) { ra[i] = c.sb + (8 * g + qd) * 256 + ((sf(i) ^ frow) * 32) + 8 * p; asm volatile("" : "+v"(ra[i])); }
#pragma unroll
    for (int tt = 0; tt < 6; ++tt) {
      const int q = 4 * tt + g, i = (q * 11) >> 5, u = q - 3 * i, U = uw + u;
      const u32 o = (u32)(((8 * rt + sf(i)) * KS2 + (U >> 2)) * 1024 + ((U & 3) * 16 + n) * 16);
      hoff[tt] = u < nw ? o : 0xfffffff0u;
      asm volatile("" : "+v"(hoff[tt]));
    }
  };
  f32x4 acc[3][8], cs[3], rsum[2];
  u32x4 Wr[4][4];
  if (w == 3) kb_phase2<1>(c, acc, rsum, cs, Wr, ra, setup, ws_stamp);
  else kb_phase2<0>(c, acc, rsum, cs, Wr, ra, setup, ws_stamp);
  STAMP(2, 2);

  u32x2 res[4];
  {
    const u32 rb = ra[0] + dt * 16384;
    ds_tr<0>(res[0], rb); ds_tr<1024>(res[1], rb); ds_tr<8192>(res[2], rb); ds_tr<9216>(res[3], rb);
  }
  float* stl = (float*)(smem + KB_ST);
  if (g == 0) {
#pragma unroll
    for (int i = 0; i < 2; ++i) stl[2 * (16 * sf(i) + n)] = rsum[i][0];
  }
  if (w == 3 && g == (n >> 2)) {
#pragma unroll
    for (int i = 0; i < 8; ++i) {
      f32x4 a = acc[2][i];
      float dg = (n & 2) ? ((n & 1) ? a[3] : a[2]) : ((n & 1) ? a[1] : a[0]);
      stl[2 * (16 * sf(i) + n) + 1] = dg;
    }
  }
  asm volatile("s_waitcnt lgkmcnt(0)\n\ts_barrier" : "+v"(res[0]), "+v"(res[1]), "+v"(res[2]), "+v"(res[3]) :: "memory");
  STAMP(2, 3);
  const __amdgpu_buffer_rsrc_t hrs = __builtin_amdgcn_make_buffer_rsrc(ws + WS_H, 0, (S / 16) * KS2 * 1024, 0x00020000);
  f32x2 sv[8];
#pragma unroll
  for (int i = 0; i < 8; ++i) sv[i] = *(const f32x2*)(stl + 2 * (16 * sf(i) + n));
  u32 hv[24];
  static_for<8>([&](auto I) {
    constexpr int i = I;
    const float mean = sv[i][0] * (1.f / D);
    const float var = fmaxf(sv[i][1] * (1.f / D) - mean * mean, 0.f) + EPS;
    const float rstd = __builtin_amdgcn_rsqf(var);
    const float rinv = var * rstd;
    const float ce = -1.4426950408889634f * rstd;
    const f32x2 nm = {-mean, -mean}, ce2 = {ce, ce}, ri2 = {rinv, rinv};
    static_for<3>([&](auto U_) {
      constexpr int u = U_;
      pin4(acc[u][i]);
      constexpr int slot = 3 * i + u;
      constexpr int wk = slot * EPI_FILL / 24;
      if constexpr (EPI_FILL > 0 && (slot + 1) * EPI_FILL / 24 > wk) glds16<wk * 4096>(c.lv, c.wd + (size_t)wk * 4096, c.lwd);
      const f32x4 a = acc[u][i];
      const f32x2 tg = (f32x2{a[0], a[1]} + nm * f32x2{cs[u][0], cs[u][1]}) * ce2;
      const f32x2 tu = f32x2{a[2], a[3]} + nm * f32x2{cs[u][2], cs[u][3]};
      const f32x2 e = {__builtin_amdgcn_exp2f(tg[0]), __builtin_amdgcn_exp2f(tg[1])};
      const f32x2 den = e * ri2 + ri2;
      const f32x2 o = tu * f32x2{__builtin_amdgcn_rcpf(den[0]), __builtin_amdgcn_rcpf(den[1])};
      hv[3 * i + u] = pk2(o[0], o[1]);
    });
    static_for<6>([&](auto T) {
      constexpr int tt = T;
      if constexpr ((4 * tt + 3) / 3 == i) {
        auto s02 = __builtin_amdgcn_permlane32_swap(hv[4 * tt], hv[4 * tt + 2], false, false);
        auto s13 = __builtin_amdgcn_permlane32_swap(hv[4 * tt + 1], hv[4 * tt + 3], false, false);
        auto t01 = __builtin_amdgcn_permlane16_swap(s02[0], s13[0], false, false);
        auto t23 = __builtin_amdgcn_permlane16_swap(s02[1], s13[1], false, false);
        __builtin_amdgcn_raw_buffer_store_b128(u32x4{t01[0], t01[1], t23[0], t23[1]}, hrs, hoff[tt], 0, 16);
      }
    });
  });
  STAMP(2, 4);
  u32* cnt = (u32*)(ws + WS_CNT + rt * 128);
  asm volatile("s_waitcnt vmcnt(0)\n\ts_barrier" ::: "memory");
  STAMP(2, 5);
  if (w == 0) {
    if (lane == 0) __hip_atomic_fetch_add(cnt, 1u, __ATOMIC_RELAXED, __HIP_MEMORY_SCOPE_AGENT);
    for (int spins = 0; spins < SPIN_MAX; ++spins) {
      const u32 v = __builtin_amdgcn_readfirstlane(__hip_atomic_load(cnt, __ATOMIC_RELAXED, __HIP_MEMORY_SCOPE_AGENT));
      if (v >= 16u) break;
      __builtin_amdgcn_s_sleep(1);
    }
    wait_vm<0>();
  } else {
    const char* wsrc = c.wd - (size_t)w * 1024;
    for (int e = EPI_FILL + w - 1; e < 32; e += 3) {
      const char* src = wsrc + (size_t)e * 4096;
      const u32 dst = c.sb + e * 4096;
      glds16<0>(c.lv, src, dst); glds16<1024>(c.lv, src + 1024, dst); glds16<2048>(c.lv, src + 2048, dst); glds16<3072>(c.lv, src + 3072, dst);
    }
  }
  barrier();
  STAMP(2, 6);

  const char* hb = ws + WS_H + (size_t)(8 * rt + 4 * half + w) * KS2 * 1024;
  u32x4 Hf[KS2];
  constexpr int P3_AHEAD = 12;
  auto ldH = [&](auto KS) { constexpr int ks = KS; if constexpr (ks < KS2) gld_a_sc1<(ks & 3) * 1024>(Hf[ks], c.lv, hb + (ks >> 2) * 4096); };
  static_for<P3_AHEAD>([&](auto KS) { ldH(KS); });
  STAMP(3, 0);
  f32x4 o4[4];
  u32x4 Af[3][4];
  const u32 la0 = c.sb + lane * 16, la1 = la0 + 65536, la2 = la0 + 131072;
  auto rdA = [&](auto KS, auto DF) {
    constexpr int ks = KS, df = DF;
    if constexpr (ks < KB_NL) ds_b128<(ks & 15) * 4096 + df * 1024>(Af[ks % 3][df], ks < 16 ? la0 : ks < 32 ? la1 : la2);
  };
  wait_vm<P3_AHEAD>();
  barrier();
  static_for<4>([&](auto DF) { rdA(std::integral_constant<int, 0>{}, DF); });
  static_for<4>([&](auto DF) { rdA(std::integral_constant<int, 1>{}, DF); });
  static_for<KS2>([&](auto KS) {
    constexpr int ks = KS;
    wait_vm<(ks + P3_AHEAD <= KS2 ? P3_AHEAD - 1 : KS2 - 1 - ks)>();
    if constexpr (ks + 1 < KB_NL) wait_lgkm<4>(); else wait_lgkm<0>();
    pin_a(Hf[ks]);
    static_for<4>([&](auto DF) {
      constexpr int df = DF;
      if constexpr (ks < KB_NL) { pin(Af[ks % 3][df]); mfma_va<ks == 0>(o4[df], Af[ks % 3][df], Hf[ks]); }
      else { pin_a(Wr[ks - KB_NL][df]); mfma_aa(o4[df], Wr[ks - KB_NL][df], Hf[ks]); }
      rdA(std::integral_constant<int, ks + 2>{}, DF);
      if constexpr (df == 1 && ks + P3_AHEAD < KS2) ldH(std::integral_constant<int, ks + P3_AHEAD>{});
    });
  });
  mfma_fence(o4[0], o4[1], o4[2], o4[3]);
  u32x4 o0, o1;
#pragma unroll
  for (int df = 0; df < 2; ++df) {
    o0[2 * df] = pk2(o4[df][0] + blo(res[df][0]), o4[df][1] + bhi(res[df][0]));
    o0[2 * df + 1] = pk2(o4[df][2] + blo(res[df][1]), o4[df][3] + bhi(res[df][1]));
    o1[2 * df] = pk2(o4[2 + df][0] + blo(res[2 + df][0]), o4[2 + df][1] + bhi(res[2 + df][0]));
    o1[2 * df + 1] = pk2(o4[2 + df][2] + blo(res[2 + df][1]), o4[2 + df][3] + bhi(res[2 + df][1]));
  }
  const __amdgpu_buffer_rsrc_t ors = __builtin_amdgcn_make_buffer_rsrc(out, 0, S * D * 2, 0x00020000);
  const u32 ooff = (u32)((s0 + 64 * half + 16 * w + n) * D + 64 * dt + 8 * g) * 2;
  __builtin_amdgcn_raw_buffer_store_b128(o0, ors, ooff, 0, 16);
  __builtin_amdgcn_raw_buffer_store_b128(o1, ors, ooff + 64, 0, 16);
  asm volatile("kb_code_end:");
  STAMP(2, 7);
}

extern "C" void kernel_launch(void* const* d_in, const int* in_sizes, int n_in, void* d_out, int out_size, void* d_ws,
                              size_t ws_size, hipStream_t stream) {
  const u16 *q = (const u16*)d_in[0], *k = (const u16*)d_in[1], *v = (const u16*)d_in[2], *shk = (const u16*)d_in[3];
  const u16 *ssmk = (const u16*)d_in[4], *dskip = (const u16*)d_in[5];
  const u16 *wg = (const u16*)d_in[7], *wu = (const u16*)d_in[8], *wd = (const u16*)d_in[9];
  char* ws = (char*)d_ws;
  (void)hipFuncSetAttribute((const void*)ka_conv, hipFuncAttributeMaxDynamicSharedMemorySize, KA_LDS);
  (void)hipFuncSetAttribute((const void*)kb_mlp, hipFuncAttributeMaxDynamicSharedMemorySize, KB_LDS);
  ka_conv<<<512, 256, KA_LDS, stream>>>(q, k, v, shk, ssmk, dskip, wg, wu, wd, ws);
  kb_mlp<<<256, 256, KB_LDS, stream>>>(ws, (u16*)d_out);
}
```

```cpp
#include <hip/hip_runtime.h>
#include <stdint.h>
#include <utility>

typedef unsigned short u16;
typedef unsigned u32;
typedef __attribute__((ext_vector_type(2))) u32 u32x2;
typedef __attribute__((ext_vector_type(4))) u32 u32x4;
typedef __attribute__((ext_vector_type(2))) float f32x2;
typedef __attribute__((ext_vector_type(4))) float f32x4;
typedef __attribute__((ext_vector_type(2))) __bf16 bf16x2;
typedef __attribute__((ext_vector_type(8))) __bf16 bf16x8;
typedef __attribute__((ext_vector_type(4))) short s16x4;
#define LDSP __attribute__((address_space(3)))
#define DI __device__ __forceinline__

constexpr int S = 2048, D = 512, F = 1376;
constexpr int NU = F / 8;
constexpr int KS1 = D / 32;
constexpr int KS2 = F / 32;
constexpr float EPS = 1e-5f;

constexpr size_t WS_XR = 0;
constexpr size_t WS_QT = WS_XR + (size_t)D * S * 2;
constexpr size_t WS_HT = WS_QT + (size_t)D * S * 2;
constexpr size_t WS_W1 = WS_HT + (size_t)D * S * 2;
constexpr size_t WS_WD = WS_W1 + (size_t)NU * KS1 * 1024;
constexpr size_t WS_H = WS_WD + (size_t)8 * KS2 * 4 * 1024;
constexpr size_t WS_CS = WS_H + (size_t)(S / 16) * KS2 * 1024;
constexpr size_t WS_SA = WS_CS + (size_t)KS1 * NU * 16 * 4;
constexpr size_t WS_SB = WS_SA + (size_t)S * 4;
constexpr size_t WS_CNT = WS_SB + (size_t)S * 4;
constexpr size_t WS_FLA = WS_CNT + 16 * 128;
constexpr size_t WS_FLG = WS_FLA + 256 * 8;
constexpr size_t WS_FLS = WS_FLG + 256 * 8;
constexpr size_t WS_FLZ = WS_FLS + 256 * 8;
constexpr size_t WS_HNT = WS_FLZ + 8 * 128;
constexpr size_t WS_EPO = WS_HNT + 256 * 8;

template <int... Is, class Fn> DI void static_for_(std::integer_sequence<int, Is...>, Fn&& f) { (f(std::integral_constant<int, Is>{}), ...); }
template <int N, class Fn> DI void static_for(Fn&& f) { static_for_(std::make_integer_sequence<int, N>{}, f); }

DI float blo(u32 u) { return __uint_as_float(u << 16); }
DI float bhi(u32 u) { return __uint_as_float(u & 0xffff0000u); }
DI u32 pk2(float a, float b) { f32x2 t = {a, b}; return __builtin_bit_cast(u32, __builtin_convertvector(t, bf16x2)); }
DI f32x4 mfma16(u32x4 a, u32x4 b, f32x4 c) {
  return __builtin_amdgcn_mfma_f32_16x16x32_bf16(__builtin_bit_cast(bf16x8, a), __builtin_bit_cast(bf16x8, b), c, 0, 0, 0);
}
DI u32x2 tr_read(u32 lds_byte) {
  return __builtin_bit_cast(u32x2, __builtin_amdgcn_ds_read_tr16_b64_v4i16((LDSP s16x4*)(size_t)lds_byte));
}
DI u32x4 tr_frag(u32 a0, u32 a1) { u32x2 x = tr_read(a0), y = tr_read(a1); return u32x4{x[0], x[1], y[0], y[1]}; }
DI u32x4 cat(u32x2 x, u32x2 y) { return __builtin_shufflevector(x, y, 0, 1, 2, 3); }
template <int CTRL> DI float dpp_add(float v) {
  return v + __builtin_bit_cast(float, __builtin_amdgcn_update_dpp(0, __builtin_bit_cast(int, v), CTRL, 0xf, 0xf, true));
}
DI float sum_rows(float v) {
  u32 u = __float_as_uint(v);
  auto a = __builtin_amdgcn_permlane16_swap(u, u, false, false);
  v = __uint_as_float(a[0]) + __uint_as_float(a[1]);
  u = __float_as_uint(v);
  auto b = __builtin_amdgcn_permlane32_swap(u, u, false, false);
  return __uint_as_float(b[0]) + __uint_as_float(b[1]);
}
DI float wave_sum(float v) {
  v = dpp_add<0xb1>(v);
  v = dpp_add<0x4e>(v);
  v = dpp_add<0x124>(v);
  v = dpp_add<0x128>(v);
  return sum_rows(v);
}
DI __amdgpu_buffer_rsrc_t ws_rsrc(char* ws) { return __builtin_amdgcn_make_buffer_rsrc(ws, 0, 0x04000000, 0x00020000); }
DI void st16_wt(__amdgpu_buffer_rsrc_t r, u32 off, u32x4 v) { __builtin_amdgcn_raw_buffer_store_b128(v, r, off, 0, 16); }
DI int slice_u0(int j) { return j < 12 ? 11 * j : 132 + 10 * (j - 12); }
DI int slice_nu(int j) { return j < 12 ? 11 : 10; }

template <bool FIRST = false> DI void mfma_vv(f32x4& acc, const u32x4& a, const u32x4& b) {
  if constexpr (FIRST) asm volatile("v_mfma_f32_16x16x32_bf16 %0, %1, %2, 0" : "=&v"(acc) : "v"(a), "v"(b));
  else asm volatile("v_mfma_f32_16x16x32_bf16 %0, %1, %2, %0" : "+v"(acc) : "v"(a), "v"(b));
}
template <bool FIRST = false> DI void mfma_av(f32x4& acc, const u32x4& a, const u32x4& b) {
  if constexpr (FIRST) asm volatile("v_mfma_f32_16x16x32_bf16 %0, %1, %2, 0" : "=&v"(acc) : "a"(a), "v"(b));
  else asm volatile("v_mfma_f32_16x16x32_bf16 %0, %1, %2, %0" : "+v"(acc) : "a"(a), "v"(b));
}
template <int OFF> DI void ds_b64(u32x2& d, u32 addr) { asm volatile("ds_read_b64 %0, %1 offset:%c2" : "=v"(d) : "v"(addr), "i"(OFF) : "memory"); }
template <int OFF> DI void ds_b128(u32x4& d, u32 addr) { asm volatile("ds_read_b128 %0, %1 offset:%c2" : "=v"(d) : "v"(addr), "i"(OFF) : "memory"); }
template <int OFF> DI void ds_tr(u32x2& d, u32 addr) { asm volatile("ds_read_b64_tr_b16 %0, %1 offset:%c2" : "=v"(d) : "v"(addr), "i"(OFF) : "memory"); }
template <int OFF> DI void gld_a(u32x4& d, u32 voff, const void* sbase) {
  asm volatile("global_load_dwordx4 %0, %1, %2 offset:%c3" : "=a"(d) : "v"(voff), "s"(sbase), "i"(OFF) : "memory");
}
template <int OFF> DI void gld_v(u32x4& d, u32 voff, const void* sbase) {
  asm volatile("global_load_dwordx4 %0, %1, %2 offset:%c3" : "=v"(d) : "v"(voff), "s"(sbase), "i"(OFF) : "memory");
}
template <int OFF> DI void gld2_v(u32x2& d, u32 voff, const void* sbase) {
  asm volatile("global_load_dwordx2 %0, %1, %2 offset:%c3" : "=v"(d) : "v"(voff), "s"(sbase), "i"(OFF) : "memory");
}
template <int LOFF> DI void glds16(u32 voff, const void* sbase, u32 ldsbase) {
  asm volatile("s_add_u32 m0, %2, %c3\n\ts_nop 0\n\tglobal_load_lds_dwordx4 %0, %1" ::"v"(voff), "s"(sbase), "s"(ldsbase), "i"(LOFF) : "memory", "scc");
}
template <int N> DI void wait_vm() { asm volatile("s_waitcnt vmcnt(%c0)" ::"i"(N) : "memory"); }
template <int N> DI void wait_lgkm() { asm volatile("s_waitcnt lgkmcnt(%c0)" ::"i"(N) : "memory"); }
template <int OFF> DI void gld_a_sc1(u32x4& d, u32 voff, const void* sbase) {
  asm volatile("global_load_dwordx4 %0, %1, %2 offset:%c3 sc1" : "=a"(d) : "v"(voff), "s"(sbase), "i"(OFF) : "memory");
}
DI void mfma_aa(f32x4& acc, const u32x4& a, const u32x4& b) {
  asm volatile("v_mfma_f32_16x16x32_bf16 %0, %1, %2, %0" : "+v"(acc) : "a"(a), "a"(b));
}
template <bool FIRST = false> DI void mfma_va(f32x4& acc, const u32x4& a, const u32x4& b) {
  if constexpr (FIRST) asm volatile("v_mfma_f32_16x16x32_bf16 %0, %1, %2, 0" : "=&v"(acc) : "v"(a), "a"(b));
  else asm volatile("v_mfma_f32_16x16x32_bf16 %0, %1, %2, %0" : "+v"(acc) : "v"(a), "a"(b));
}
DI void mfma_fence(f32x4& a, f32x4& b, f32x4& c, f32x4& d) { asm volatile("s_nop 7\n\ts_nop 1" : "+v"(a), "+v"(b), "+v"(c), "+v"(d)); }
DI void after_fence(f32x4& a, f32x4& b, f32x4& c, f32x4& d) { asm volatile("" : "+v"(a), "+v"(b), "+v"(c), "+v"(d)); }
DI void after_fence(f32x4& a, f32x4& b, f32x4& c) { asm volatile("" : "+v"(a), "+v"(b), "+v"(c)); }
template <int STAGE> DI void dpp_stage(float& v) {
  if constexpr (STAGE == 0) asm volatile("v_add_f32_dpp %0, %0, %0 quad_perm:[1,0,3,2] row_mask:0xf bank_mask:0xf" : "+v"(v));
  if constexpr (STAGE == 1) asm volatile("v_add_f32_dpp %0, %0, %0 quad_perm:[2,3,0,1] row_mask:0xf bank_mask:0xf" : "+v"(v));
  if constexpr (STAGE == 2) asm volatile("v_add_f32_dpp %0, %0, %0 row_ror:4 row_mask:0xf bank_mask:0xf" : "+v"(v));
  if constexpr (STAGE == 3) asm volatile("v_add_f32_dpp %0, %0, %0 row_ror:8 row_mask:0xf bank_mask:0xf" : "+v"(v));
}
DI void barrier() { asm volatile("s_barrier" ::: "memory"); }
DI void pin(u32x4& x) { asm volatile("" : "+v"(x)); }
DI void pin(u32x2& x) { asm volatile("" : "+v"(x)); }
DI void pin4(f32x4& x) { asm volatile("" : "+v"(x)); }
DI void pin_a(u32x4& x) { asm volatile("" : "+a"(x)); }

#define STAMP(kern, idx) do {} while (0)

template <class Gate>
DI void w_job(int b, const u16* __restrict__ wg, const u16* __restrict__ wu, const u16* __restrict__ wd, char* __restrict__ ws, char* smem, Gate&& gate) {
  const int t = threadIdx.x, lane = t & 63, w = t >> 6;
  char* tile = smem;
  char* tile2 = smem + 32 * 368;
  const u32 tb = (u32)(size_t)(LDSP char*)smem;
  const int i16 = lane & 15, qd = i16 >> 2, p = i16 & 3, g = lane >> 4;
  const int ks = b & 15, j = b >> 4;
  const int U0 = slice_u0(j), nu = slice_nu(j);
  u32x4 g4[2], u4[2], d4[2];
#pragma unroll
  for (int rep = 0; rep < 2; ++rep) {
    int idx = t + 256 * rep, r = idx / 11, u = idx - 11 * r;
    bool ok = r < 32 && u < nu;
    size_t off = ok ? (size_t)(32 * ks + r) * F + 8 * (U0 + u) : 0;
    g4[rep] = *(const u32x4*)(wg + off); u4[rep] = *(const u32x4*)(wu + off);
  }
  const int ks2 = b >> 2, dp = b & 3;
  if (b < KS2 * 4) {
#pragma unroll
    for (int rep = 0; rep < 2; ++rep) {
      int idx = t + 256 * rep, r = idx >> 4, c = idx & 15;
      d4[rep] = *(const u32x4*)(wd + (size_t)(32 * ks2 + r) * D + 128 * dp + 8 * c);
    }
  }
#pragma unroll
  for (int rep = 0; rep < 2; ++rep) {
    int idx = t + 256 * rep, r = idx / 11, u = idx - 11 * r;
    if (r < 32 && u < nu) {
      *(u32x4*)(tile + r * 368 + u * 32) = u32x4{g4[rep][0], u4[rep][0], g4[rep][1], u4[rep][1]};
      *(u32x4*)(tile + r * 368 + u * 32 + 16) = u32x4{g4[rep][2], u4[rep][2], g4[rep][3], u4[rep][3]};
    }
  }
  if (b < KS2 * 4) {
#pragma unroll
    for (int rep = 0; rep < 2; ++rep) { int idx = t + 256 * rep, r = idx >> 4, c = idx & 15; *(u32x4*)(tile2 + r * 272 + c * 16) = d4[rep]; }
  }
  __syncthreads();
  gate();
  const u32x4 ones = {0x3f803f80u, 0x3f803f80u, 0x3f803f80u, 0x3f803f80u};
  const __amdgpu_buffer_rsrc_t wr = ws_rsrc(ws);
  for (int u = w; u < nu; u += 4) {
    u32 a = tb + (8 * g + qd) * 368 + u * 32 + 8 * p;
    u32x4 fr = tr_frag(a, a + 4 * 368);
    st16_wt(wr, (u32)WS_W1 + (((U0 + u) * KS1 + ks) * 64 + lane) * 16, fr);
    f32x4 cs = mfma16(fr, ones, f32x4{0.f, 0.f, 0.f, 0.f});
    if (i16 == 0) st16_wt(wr, (u32)WS_CS + ((ks * NU + U0 + u) * 16 + 4 * g) * 4, __builtin_bit_cast(u32x4, cs));
  }
  if (b < KS2 * 4) {
#pragma unroll
    for (int rep = 0; rep < 2; ++rep) {
      int fi = w + 4 * rep, dtl = fi >> 2, df = fi & 3;
      u32 a = tb + 32 * 368 + (8 * g + qd) * 272 + (64 * dtl + 32 * (df >> 1) + 8 * p + 4 * (df & 1)) * 2;
      u32x4 fr = tr_frag(a, a + 4 * 272);
      st16_wt(wr, (u32)WS_WD + ((((2 * dp + dtl) * KS2 + ks2) * 4 + df) * 64 + lane) * 16, fr);
    }
  }
}

constexpr int KA_CS = 4160;
constexpr int KA_KC = 4 * KA_CS;
constexpr int KA_XR = 2 * KA_KC;
constexpr int KA_RED = KA_XR + 2 * 4608;
constexpr int KA_LDS = KA_RED + 4 * 4096;
constexpr int KA_TK = KA_XR, KA_TV = KA_TK + 72 * 128, KA_TQ = KA_TV + 64 * 128;
static_assert(KA_TQ + 64 * 128 == KA_LDS, "tile overlay");
constexpr int SPIN_MAX = 1 << 22;
#define HINT_STEP 0
#define KB_POLL_SLEEP 1
#define KA_POLL_SLEEP 1
#define P3_AHEAD_ 12
#define HELP_PRE 16
#define HELP_SLEEP 20
#define CHK 2
#define CHK_M 2
#define WEARLY 0
#define HINT_TICKS 800ull

template <int OFF> DI void gld_v_sc1(u32x4& d, u32 voff, const void* sbase) {
  asm volatile("global_load_dwordx4 %0, %1, %2 offset:%c3 sc1" : "=v"(d) : "v"(voff), "s"(sbase), "i"(OFF) : "memory");
}
template <int OFF> DI void gld2_v_sc1(u32x2& d, u32 voff, const void* sbase) {
  asm volatile("global_load_dwordx2 %0, %1, %2 offset:%c3 sc1" : "=v"(d) : "v"(voff), "s"(sbase), "i"(OFF) : "memory");
}
DI bool tags_ok(const u32x4& f, const u32x2& tag) { return f[0] == tag[0] && f[1] == tag[1] && f[2] == tag[0] && f[3] == tag[1]; }

__global__ __launch_bounds__(256, 2) void ka_conv(const u16* __restrict__ q, const u16* __restrict__ k, const u16* __restrict__ v,
                                                  const u16* __restrict__ shk, const u16* __restrict__ ssmk, const u16* __restrict__ dskip,
                                                  const u16* __restrict__ wg, const u16* __restrict__ wu, const u16* __restrict__ wd,
                                                  char* __restrict__ ws) {
  extern __shared__ __attribute__((aligned(16))) char smem[];
  const int t = threadIdx.x, lane = t & 63;
  const int w = __builtin_amdgcn_readfirstlane(t >> 6);
  const int b = blockIdx.x;
  char* ws_stamp = ws; (void)ws_stamp;
  STAMP(0, 0);
  if (b >= 256) {
    const int hb = b - 256, srow = 8 * hb + 2 * w;
    const __amdgpu_buffer_rsrc_t wr = ws_rsrc(ws);
    u32x2 epo; u32x4 qa, qb;
    gld2_v_sc1<0>(epo, 0, ws + WS_EPO);
    if (HELP_PRE > 0) __builtin_amdgcn_s_sleep(HELP_PRE);
    gld_v<0>(qa, lane * 16, (const char*)q + (size_t)srow * 1024);
    gld_v<1024>(qb, lane * 16, (const char*)q + (size_t)srow * 1024);
    if (b < 264 && w == 1) {
      const char* pc; u32 len;
      asm volatile("s_getpc_b64 %0\n1:\n\ts_mov_b32 %1, kb_code_end-1b" : "=s"(pc), "=s"(len));
      const u32 lb = (u32)(size_t)(LDSP char*)smem + 32768;
      for (u32 o0 = 0; o0 < len; o0 += 64 * 128) {
        u32 o = o0 + lane * 128; o = o < len ? o : 0;
        asm volatile("s_mov_b32 m0, %2\n\ts_nop 0\n\tglobal_load_lds_dword %0, %1" ::"v"(o), "s"(pc), "s"(lb) : "memory");
      }
    }
    wait_vm<0>();
    asm volatile("" : "+v"(qa), "+v"(qb), "+v"(epo));
    const unsigned long long want = (((unsigned long long)epo[1] << 32) | epo[0]) + 1ull;
    {
      float sa = 0.f, sbb = 0.f;
#pragma unroll
      for (int e = 0; e < 4; ++e) { sa += blo(qa[e]) + bhi(qa[e]); sbb += blo(qb[e]) + bhi(qb[e]); }
      const float ma = wave_sum(sa) * (1.f / D), mb = wave_sum(sbb) * (1.f / D);
      float va = 0.f, vb = 0.f;
#pragma unroll
      for (int e = 0; e < 4; ++e) {
        float u0 = blo(qa[e]) - ma, u1 = bhi(qa[e]) - ma, u2 = blo(qb[e]) - mb, u3 = bhi(qb[e]) - mb;
        va += u0 * u0 + u1 * u1; vb += u2 * u2 + u3 * u3;
      }
      va = wave_sum(va) * (1.f / D); vb = wave_sum(vb) * (1.f / D);
      const float ra = __builtin_amdgcn_rsqf(va + EPS), rb = __builtin_amdgcn_rsqf(vb + EPS);
      const u32x2 val = lane == 0 ? u32x2{__float_as_uint(ra), __float_as_uint(rb)} : u32x2{__float_as_uint(-ma * ra), __float_as_uint(-mb * rb)};
      const u32 off = lane == 0 ? (u32)WS_SA + srow * 4 : lane == 1 ? (u32)WS_SB + srow * 4 : 0xfffffff0u;
      __builtin_amdgcn_raw_buffer_store_b64(val, wr, off, 0, 16);
    }
    asm volatile("s_waitcnt vmcnt(0)\n\ts_barrier" ::: "memory");
    if (t == 0) __builtin_amdgcn_raw_buffer_store_b64(u32x2{(u32)want, (u32)(want >> 32)}, wr, (u32)WS_FLS + hb * 8, 0, 16);
    STAMP(1, 6);
    if (hb == 1 && w == 1) {
      const unsigned long long* fs = (const unsigned long long*)(ws + WS_FLS) + lane;
      for (int spins = 0; spins < SPIN_MAX; ++spins) {
        bool ok = true;
#pragma unroll
        for (int i = 0; i < 4; ++i) ok &= __hip_atomic_load(fs + 64 * i, __ATOMIC_RELAXED, __HIP_MEMORY_SCOPE_AGENT) == want;
        if (__all(ok)) break;
        __builtin_amdgcn_s_sleep(1);
      }
      __builtin_amdgcn_raw_buffer_store_b64(u32x2{(u32)want, (u32)(want >> 32)}, wr, lane < 8 ? (u32)WS_FLZ + lane * 128 : 0xfffffff0u, 0, 16);
    } else if (HELP_SLEEP > 0) __builtin_amdgcn_s_sleep(HELP_SLEEP);
    w_job(hb, wg, wu, wd, ws, smem, [&]() {
      if (hb == 1 && w == 1) {
        const unsigned long long* fa = (const unsigned long long*)(ws + WS_FLA) + lane;
        const u32x2 tg = {(u32)want, (u32)(want >> 32)};
        for (int spins = 0; spins < SPIN_MAX; ++spins) {
          bool ok = true;
#pragma unroll
          for (int i = 0; i < 4; ++i) ok &= __hip_atomic_load(fa + 64 * i, __ATOMIC_RELAXED, __HIP_MEMORY_SCOPE_AGENT) == want;
          if (__all(ok)) break;
          __builtin_amdgcn_s_sleep(16);
        }
        if (lane == 0) __builtin_amdgcn_raw_buffer_store_b64(tg, wr, (u32)WS_EPO, 0, 16);
      }
      if (w == 0) {
        const unsigned long long* hp = (const unsigned long long*)(ws + WS_HNT) + hb;
        const unsigned long long t0 = __builtin_amdgcn_s_memrealtime();
        while (__hip_atomic_load(hp, __ATOMIC_RELAXED, __HIP_MEMORY_SCOPE_AGENT) != want && __builtin_amdgcn_s_memrealtime() - t0 < HINT_TICKS)
          __builtin_amdgcn_s_sleep(8);
      }
      __builtin_amdgcn_s_barrier();
      STAMP(1, 5);
    });
    STAMP(1, 4);
    return;
  }
  const int x = b & 7, bi = b >> 3, s0 = 64 * bi;
  const int ch0 = 64 * x + 2 * bi;
  const u32 sb = (u32)(size_t)(LDSP char*)smem;
  const int n = lane & 15, g = lane >> 4, qd = n >> 2, p = n & 3;
  const int d = 64 * x + 16 * w + n;
  const __amdgpu_buffer_rsrc_t wr = ws_rsrc(ws);

  u32x2 epo, sh; u32x4 kp[2], kc[2];
  gld2_v_sc1<0>(epo, 0, ws + WS_EPO);
  gld2_v<0>(sh, d * 8, shk);
  const int r8 = lane >> 3, c8 = lane & 7;
  auto voff = [&](int instr) { const int f = ((r8 >> 1) & 1) | ((instr & 1) << 1); return (u32)((((c8 >> 1) ^ f) * 2 + (c8 & 1)) * 16); };
  const size_t col = (size_t)x * 128;
  {
    int row = s0 - 8 + 8 * w + r8; row = row < 0 ? 0 : row;
    glds16<0>((u32)row * 1024 + voff(w), (const char*)k + col, sb + KA_TK + w * 1024);
    glds16<0>((u32)(s0 + 8 * w + r8) * 1024 + voff(w), (const char*)v + col, sb + KA_TV + w * 1024);
    glds16<0>((u32)(s0 - 8 + 8 * (w + 4) + r8) * 1024 + voff(w), (const char*)k + col, sb + KA_TK + (w + 4) * 1024);
    glds16<0>((u32)(s0 + 8 * (w + 4) + r8) * 1024 + voff(w), (const char*)v + col, sb + KA_TV + (w + 4) * 1024);
    if (w == 0) glds16<0>((u32)(s0 + 56 + r8) * 1024 + voff(8), (const char*)k + col, sb + KA_TK + 8 * 1024);
  }
#pragma unroll
  for (int cc = 0; cc < 2; ++cc) {
    const char* kr = (const char*)ssmk + (size_t)(ch0 + cc) * 4096;
    gld_v<0>(kp[cc], (u32)(t > 0 ? t - 1 : 0) * 16, kr);
    gld_v<0>(kc[cc], (u32)t * 16, kr);
  }
  glds16<0>((u32)(s0 + 8 * w + r8) * 1024 + voff(w), (const char*)q + col, sb + KA_TQ + w * 1024);
  glds16<0>((u32)(s0 + 8 * (w + 4) + r8) * 1024 + voff(w), (const char*)q + col, sb + KA_TQ + (w + 4) * 1024);
  const float dsk = blo(dskip[ch0 + (w >> 1)]);
  if (b == 0 && t < 16) *(u32*)(ws + WS_CNT + t * 128) = 0u;

  wait_vm<6>();
  barrier();
  asm volatile("" : "+v"(sh), "+v"(epo));
  STAMP(0, 1);
  u32x2 tag;
  { const unsigned long long e = (((unsigned long long)epo[1] << 32) | epo[0]) + 1ull; tag = u32x2{(u32)e, (u32)(e >> 32)}; }
  auto taddr = [&](int base, int tr) { const int f = ((tr >> 1) & 1) | ((tr >> 2) & 2); return sb + base + tr * 128 + ((w ^ f) * 32) + 8 * p; };
  {
    const float a0 = blo(sh[0]), a1 = bhi(sh[0]), a2 = blo(sh[1]), a3 = bhi(sh[1]);
#pragma unroll
    for (int e = 0; e < 2; ++e) {
      const int rg = g + 4 * e;
      u32x2 km = tr_read(taddr(KA_TK, 8 * rg + 4 + qd));
      const u32x2 k0 = tr_read(taddr(KA_TK, 8 * rg + 8 + qd)), k1 = tr_read(taddr(KA_TK, 8 * rg + 12 + qd));
      const u32x2 v0 = tr_read(taddr(KA_TV, 8 * rg + qd)), v1 = tr_read(taddr(KA_TV, 8 * rg + 4 + qd));
      if (s0 + rg == 0) km = u32x2{0, 0};
      const float kf[11] = {bhi(km[0]), blo(km[1]), bhi(km[1]), blo(k0[0]), bhi(k0[0]), blo(k0[1]), bhi(k0[1]), blo(k1[0]), bhi(k1[0]), blo(k1[1]), bhi(k1[1])};
      const float vf[8] = {blo(v0[0]), bhi(v0[0]), blo(v0[1]), bhi(v0[1]), blo(v1[0]), bhi(v1[0]), blo(v1[1]), bhi(v1[1])};
      float xo[8];
#pragma unroll
      for (int j = 0; j < 8; ++j) xo[j] = (kf[j + 3] * a0 + kf[j + 2] * a1 + kf[j + 1] * a2 + kf[j] * a3) * vf[j];
      st16_wt(wr, (u32)WS_XR + (d * S + (2040 - (s0 + 8 * rg))) * 2, u32x4{pk2(xo[7], xo[6]), pk2(xo[5], xo[4]), pk2(xo[3], xo[2]), pk2(xo[1], xo[0])});
    }
  }
  asm volatile("s_waitcnt vmcnt(0)\n\ts_barrier" : "+v"(kp[0]), "+v"(kc[0]), "+v"(kp[1]), "+v"(kc[1]) :: "memory");
  if (t == 0) __builtin_amdgcn_raw_buffer_store_b64(tag, wr, (u32)WS_FLA + (x * 32 + bi) * 8, 0, 16);
  STAMP(0, 2);
#pragma unroll
  for (int e = 0; e < 2; ++e) {
    const int rg = g + 4 * e;
    const u32x2 q0 = tr_read(taddr(KA_TQ, 8 * rg + qd)), q1 = tr_read(taddr(KA_TQ, 8 * rg + 4 + qd));
    st16_wt(wr, (u32)WS_QT + (d * S + s0 + 8 * rg) * 2, cat(q0, q1));
  }
#pragma unroll
  for (int cc = 0; cc < 2; ++cc) {
    const u32 m = t > 0 ? 0xffffffffu : 0u;
    u32 wv[6] = {kp[cc][0] & m, kp[cc][1] & m, kp[cc][2] & m, kp[cc][3] & m, kc[cc][0], kc[cc][1]};
    u32 o1[5];
#pragma unroll
    for (int i = 0; i < 5; ++i) o1[i] = __builtin_amdgcn_alignbit(wv[i + 1], wv[i], 16);
    char* base = smem + cc * KA_KC + (t + 1) * 16;
    *(u32x4*)(base + 0 * KA_CS) = u32x4{o1[0], o1[1], o1[2], o1[3]};
    *(u32x4*)(base + 1 * KA_CS) = u32x4{wv[1], wv[2], wv[3], wv[4]};
    *(u32x4*)(base + 2 * KA_CS) = u32x4{o1[1], o1[2], o1[3], o1[4]};
    *(u32x4*)(base + 3 * KA_CS) = u32x4{wv[2], wv[3], wv[4], wv[5]};
    if (t == 255) {
      const u32 z = 0;
      const u32 e0 = __builtin_amdgcn_alignbit(kc[cc][1], kc[cc][0], 16), e1 = __builtin_amdgcn_alignbit(kc[cc][2], kc[cc][1], 16);
      const u32 e2 = __builtin_amdgcn_alignbit(kc[cc][3], kc[cc][2], 16), e3 = __builtin_amdgcn_alignbit(z, kc[cc][3], 16);
      *(u32x4*)(base + 16 + 0 * KA_CS) = u32x4{e0, e1, e2, e3};
      *(u32x4*)(base + 16 + 1 * KA_CS) = u32x4{kc[cc][1], kc[cc][2], kc[cc][3], z};
      *(u32x4*)(base + 16 + 2 * KA_CS) = u32x4{e1, e2, e3, z};
      *(u32x4*)(base + 16 + 3 * KA_CS) = u32x4{kc[cc][2], kc[cc][3], z, z};
    }
  }
  if (t < 8) *(u32x4*)(smem + (t >> 2) * KA_KC + (t & 3) * KA_CS) = u32x4{0, 0, 0, 0};
  asm volatile("s_waitcnt vmcnt(0) lgkmcnt(0)\n\ts_barrier" ::: "memory");
  if (t == 0) {
    __builtin_amdgcn_raw_buffer_store_b64(tag, wr, (u32)WS_FLG + (x * 32 + bi) * 8, 0, 16);
  }
  if (t >= 64 && t < 64 + 60) { int i = t - 64; *(u32x4*)(smem + KA_XR + (i / 30) * 4608 + 4096 + (i % 30) * 16) = u32x4{0, 0, 0, 0}; }
  if (w == 0) {
    const unsigned long long* fp = (const unsigned long long*)(ws + WS_FLA) + x * 32 + (lane & 31);
    const unsigned long long want = ((unsigned long long)tag[1] << 32) | tag[0];
    for (int spins = 0; spins < SPIN_MAX; ++spins) {
      if (__all(__hip_atomic_load(fp, __ATOMIC_RELAXED, __HIP_MEMORY_SCOPE_AGENT) == want)) break;
      if (KA_POLL_SLEEP > 0) __builtin_amdgcn_s_sleep(KA_POLL_SLEEP);
    }
  }
  asm volatile("s_waitcnt vmcnt(0)\n\ts_barrier" ::: "memory");
  STAMP(0, 3);

  const int c = w >> 1, half = w & 1;
  u32x2 qv[4], fz; u32x4 sa[4], sbv[4], fq;
  const u32 eo = (u32)(1024 * half + 16 * n + 4 * g);
  const char* qTc = ws + WS_QT + (size_t)(ch0 + c) * 4096;
  {
    u32x4 x0, x1;
    gld_v_sc1<0>(x0, t * 16, ws + WS_XR + (size_t)ch0 * 4096);
    gld_v_sc1<0>(x1, t * 16, ws + WS_XR + (size_t)(ch0 + 1) * 4096);
    wait_vm<0>();
    asm volatile("" : "+v"(x0), "+v"(x1));
    *(u32x4*)(smem + KA_XR + t * 16) = x0;
    *(u32x4*)(smem + KA_XR + 4608 + t * 16) = x1;
  }
  asm volatile("s_waitcnt lgkmcnt(0)\n\ts_barrier" ::: "memory");
  if (HINT_STEP == 0 && t == 0) __builtin_amdgcn_raw_buffer_store_b64(tag, wr, (u32)WS_HNT + b * 8, 0, 16);
  STAMP(1, 1);

  const u32 aa = sb + c * KA_KC + (n & 3) * KA_CS + 16 * g + 2 * (n & 12) + half * 256;
  const u32 ba = sb + KA_XR + c * 4608 + 480 - 32 * n + 16 * g + half * 256;
  f32x4 acc[8];
  u32x2 Alo[2][8], Ahi[2][8];
  u32x4 B[2][8];
  auto rd = [&](auto C0, auto R) {
    constexpr int c0 = C0, r = R, buf = c0 & 1;
    if constexpr (r >= 2 && r < 10) {
      constexpr int u = r - 2;
      ds_b128<512 * (7 - u) + 64 * c0>(B[buf][u], ba);
    } else {
      constexpr int ar = r < 2 ? r : r - 8, i = ar >> 1;
      if constexpr ((ar & 1) == 0) ds_b64<512 * i + 64 * c0>(Alo[buf][i], aa);
      else ds_b64<512 * i + 64 * c0 + 8>(Ahi[buf][i], aa);
    }
  };
  auto ldS = [&](auto E) {
    constexpr int e = E, i = e / 3, kind = e % 3;
    if constexpr (kind == 0) gld_v_sc1<i * 1024>(sa[i], eo * 4, ws + WS_SA);
    if constexpr (kind == 1) gld_v_sc1<i * 1024>(sbv[i], eo * 4, ws + WS_SB);
    if constexpr (kind == 2) gld2_v_sc1<i * 512>(qv[i], eo * 2, qTc);
  };
  auto ldF = [&](auto E) {
    constexpr int e = E;
    if constexpr (e == 0) gld2_v_sc1<0>(fz, x * 128, ws + WS_FLZ);
    else gld_v_sc1<0>(fq, (x * 32 + 2 * (lane & 15)) * 8, ws + WS_FLG);
  };
  auto okF = [&]() { return fz[0] == tag[0] && fz[1] == tag[1] && tags_ok(fq, tag); };
  static_for<24>([&](auto R) { rd(std::integral_constant<int, 0>{}, R); });
  static_for<4>([&](auto C0) {
    constexpr int c0 = C0, buf = c0 & 1;
    if constexpr (c0 == CHK) {
      wait_vm<0>();
      asm volatile("" : "+v"(fz), "+v"(fq));
      if (!__all(okF())) {
        for (int spins = 0; spins < SPIN_MAX; ++spins) {
          ldF(std::integral_constant<int, 0>{}); ldF(std::integral_constant<int, 1>{});
          wait_vm<0>();
          asm volatile("" : "+v"(fz), "+v"(fq));
          if (__all(okF())) break;
          __builtin_amdgcn_s_sleep(1);
        }
      }
    }
    if constexpr (c0 > 0) { if (HINT_STEP == c0 && t == 0) __builtin_amdgcn_raw_buffer_store_b64(tag, wr, (u32)WS_HNT + b * 8, 0, 16); }
    u32x4 A[8];
    static_for<36>([&](auto M) {
      constexpr int m = M;
      constexpr int c1 = m < 8 ? 0 : m < 15 ? 1 : m < 21 ? 2 : m < 26 ? 3 : m < 30 ? 4 : m < 33 ? 5 : m < 35 ? 6 : 7;
      constexpr int first = c1 * 8 - c1 * (c1 - 1) / 2;
      constexpr int u = m - first;
      if constexpr (m == 0) {
        if constexpr (c0 == 0) wait_lgkm<14>(); else wait_lgkm<0>();
        pin(Alo[buf][0]); pin(Ahi[buf][0]);
#pragma unroll
        for (int i = 0; i < 8; ++i) pin(B[buf][i]);
        A[0] = cat(Alo[buf][0], Ahi[buf][0]);
      }
      if constexpr (m == 8) {
        if constexpr (c0 == 0) wait_lgkm<8>();
#pragma unroll
        for (int i = 1; i < 8; ++i) { pin(Alo[buf][i]); pin(Ahi[buf][i]); A[i] = cat(Alo[buf][i], Ahi[buf][i]); }
      }
      mfma_vv<(c0 == 0 && c1 == 0)>(acc[c1 + u], A[c1], B[buf][u]);
      if constexpr (c0 < 3 && m < 24) rd(std::integral_constant<int, c0 + 1>{}, M);
      if constexpr (c0 == CHK - 1 && (m == CHK_M || m == CHK_M + 2)) ldF(std::integral_constant<int, (m - CHK_M) / 2>{});
      if constexpr (c0 == CHK && m >= 1 && m < 25 && (m & 1) == 1) ldS(std::integral_constant<int, (m - 1) / 2>{});
    });
  });
  mfma_fence(acc[0], acc[1], acc[2], acc[3]);
  after_fence(acc[4], acc[5], acc[6], acc[7]);
  STAMP(1, 2);
  f32x4 fin[4];
  {
    f32x4* red = (f32x4*)(smem + KA_RED);
    if (half) {
#pragma unroll
      for (int i = 0; i < 4; ++i) { red[(w * 4 + i) * 64 + lane] = acc[i]; fin[i] = acc[4 + i]; }
      asm volatile("; half 1" ::: "memory");
    } else {
#pragma unroll
      for (int i = 0; i < 4; ++i) { red[(w * 4 + i) * 64 + lane] = acc[4 + i]; fin[i] = acc[i]; }
      asm volatile("; half 0" ::: "memory");
    }
    asm volatile("s_waitcnt lgkmcnt(0)\n\ts_barrier" ::: "memory");
#pragma unroll
    for (int i = 0; i < 4; ++i) fin[i] += red[((w ^ 1) * 4 + i) * 64 + lane];
  }
  wait_vm<0>();
#pragma unroll
  for (int i = 0; i < 4; ++i) asm volatile("" : "+v"(qv[i]), "+v"(sa[i]), "+v"(sbv[i]));
  const f32x2 dd = {dsk, dsk};
  u32x2 hv[4];
#pragma unroll
  for (int i = 0; i < 4; ++i) {
    const int s = 256 * (4 * half + i) + 16 * n + 4 * g;
    const u32x2 xq = *(LDSP const u32x2*)(size_t)(sb + KA_XR + c * 4608 + 2 * (2044 - s));
    const f32x2 x0 = {bhi(xq[1]), blo(xq[1])}, x1 = {bhi(xq[0]), blo(xq[0])};
    const f32x2 q0 = {blo(qv[i][0]), bhi(qv[i][0])}, q1 = {blo(qv[i][1]), bhi(qv[i][1])};
    const f32x2 z0 = x0 * dd + f32x2{fin[i][0], fin[i][1]}, z1 = x1 * dd + f32x2{fin[i][2], fin[i][3]};
    const f32x4 a4 = __builtin_bit_cast(f32x4, sa[i]), b4 = __builtin_bit_cast(f32x4, sbv[i]);
    const f32x2 t0 = q0 * f32x2{a4[0], a4[1]} + f32x2{b4[0], b4[1]};
    const f32x2 t1 = q1 * f32x2{a4[2], a4[3]} + f32x2{b4[2], b4[3]};
    const f32x2 h0 = t0 * z0 + q0, h1 = t1 * z1 + q1;
    hv[i] = u32x2{pk2(h0[0], h0[1]), pk2(h1[0], h1[1])};
  }
#pragma unroll
  for (int i = 0; i < 4; i += 2) {
    auto a = __builtin_amdgcn_permlane16_swap(hv[i][0], hv[i + 1][0], false, false);
    auto bb = __builtin_amdgcn_permlane16_swap(hv[i][1], hv[i + 1][1], false, false);
    const int s = 256 * (4 * half + i + (g & 1)) + 16 * n + 4 * (g & 2);
    st16_wt(wr, (u32)WS_HT + ((ch0 + c) * S + s) * 2, u32x4{a[0], bb[0], a[1], bb[1]});
  }
  STAMP(1, 3);
}

constexpr int KB_NL = 39;
constexpr int KB_ST = KB_NL * 4096;
constexpr int KB_LDS = KB_ST + 1024;
#define EPI_FILL 8
constexpr int KB_RING = 8;

#define P2_AHEAD 5
#define P2_PRO 3
constexpr int p2_upto(int ks) {
  const int ramp = P2_PRO - 1 + 2 * (ks + 1), steady = ks + P2_AHEAD;
  const int u = ks < 0 ? P2_PRO - 1 : (ramp < steady ? ramp : steady);
  return u > 15 ? 15 : u;
}
#define P2_XMAX 5
constexpr int P2_NEXTRA = 23;
constexpr int p2_extra(int ks) {
  const int tail0 = 16 - P2_AHEAD, rest = P2_NEXTRA - P2_AHEAD * P2_XMAX;
  if (ks >= tail0) return P2_XMAX;
  return (rest > 0 && ks >= tail0 - rest) ? 1 : 0;
}
constexpr int p2_extra_first(int ks) { int c = 0; for (int s = 0; s < ks; ++s) c += p2_extra(s); return c; }
constexpr int p2_extra_real(int ks) { int f = p2_extra_first(ks), n = p2_extra(ks); return f >= P2_NEXTRA ? 0 : (f + n > P2_NEXTRA ? P2_NEXTRA - f : n); }
static_assert(p2_extra_first(16) >= P2_NEXTRA, "all Wd operations are scheduled");
constexpr int p2_issue_step(int t) { for (int s = -1; s < KS1; ++s) if (p2_upto(s) >= t) return s; return KS1; }
constexpr int p2_younger(int t, int ks, int ops) {
  int c = (p2_upto(ks - 1) - t) * ops;
  for (int s = p2_issue_step(t); s < ks; ++s) if (s >= 0) c += p2_extra_real(s);
  return c;
}

struct KbCtx {
  u32 sb, gv, lv, lb0, lwd, csv;
  const char *hb0, *hb1, *w0, *w1, *w2, *cs0, *cs1, *cs2, *wd, *wdr;
};

template <int GRAM, class Setup>
DI void kb_phase2(const KbCtx& c, f32x4 (&acc)[3][8], f32x4 (&rsum)[2], f32x4 (&csp)[3], u32x4 (&Wr)[4][4], u32 (&ra)[8], Setup&& setup, char* ws_stamp) {
  constexpr int OPS = GRAM ? 4 : 5;
  u32x4 W[KB_RING][3];
  u32x2 Bl[2][8], Bh[2][8];
  auto issue = [&](auto KS, auto O) {
    constexpr int ks = KS, o = O;
    if constexpr (o == 0) glds16<ks * 8192>(c.gv, c.hb0 + (size_t)ks * 131072, c.lb0);
    if constexpr (o == 1) gld_a<(ks & 3) * 1024>(W[ks % KB_RING][0], c.lv, c.w0 + (ks >> 2) * 4096);
    if constexpr (o == 2) gld_a<(ks & 3) * 1024>(W[ks % KB_RING][1], c.lv, c.w1 + (ks >> 2) * 4096);
    if constexpr (o == 3 && !GRAM) gld_a<(ks & 3) * 1024>(W[ks % KB_RING][2], c.lv, c.w2 + (ks >> 2) * 4096);
    if constexpr (o == 4) glds16<ks * 8192 + 1024>(c.gv, c.hb1 + (size_t)ks * 131072, c.lb0);
  };
  auto extra = [&](auto E) {
    constexpr int e = E;
    if constexpr (e < 7) glds16<(32 + e) * 4096>(c.lv, c.wd + (size_t)(32 + e) * 4096, c.lwd);
    else if constexpr (e < P2_NEXTRA) gld_a<((e - 7) & 3) * 1024>(Wr[(e - 7) >> 2][(e - 7) & 3], c.lv, c.wdr + ((e - 7) >> 2) * 4096);
  };
  auto rdB = [&](auto KS, auto R) {
    constexpr int ks = KS, r = R, i = r >> 1;
    if constexpr ((r & 1) == 0) ds_tr<(ks & 7) * 8192>(Bl[ks & 1][i], ra[i] + (ks >= 8 ? 65536 : 0));
    else ds_tr<(ks & 7) * 8192 + 1024>(Bh[ks & 1][i], ra[i] + (ks >= 8 ? 65536 : 0));
  };
  u32x4 c0, c1, c2;
  static_for<5>([&](auto O) { issue(std::integral_constant<int, 0>{}, O); });
  gld_v<0>(c0, c.csv, c.cs0); gld_v<0>(c1, c.csv, c.cs1); gld_v<0>(c2, c.csv, c.cs2);
  static_for<p2_upto(-1)>([&](auto KS) { static_for<5>([&](auto O) { issue(std::integral_constant<int, KS + 1>{}, O); }); });
  setup();
  u32x4 ones = {0x3f803f80u, 0x3f803f80u, 0x3f803f80u, 0x3f803f80u};
  pin(ones);
  wait_vm<p2_upto(-1) * OPS>();
  barrier();
  static_for<16>([&](auto R) { rdB(std::integral_constant<int, 0>{}, R); });
  asm volatile("" : "+v"(c0), "+v"(c1), "+v"(c2));
  float cv[12];
#pragma unroll
  for (int e = 0; e < 4; ++e) { cv[e] = __uint_as_float(c0[e]); cv[4 + e] = __uint_as_float(c1[e]); cv[8 + e] = __uint_as_float(c2[e]); }
  static_for<KS1>([&](auto KS) {
    constexpr int ks = KS, buf = ks & 1, slot = ks % KB_RING;
    constexpr int nmain = (p2_upto(ks) - p2_upto(ks - 1)) * 5, next = p2_extra_real(ks);
    wait_lgkm<0>();
    u32x4 B[8];
#pragma unroll
    for (int i = 0; i < 8; ++i) { pin(Bl[buf][i]); pin(Bh[buf][i]); }
#pragma unroll
    for (int i = 0; i < 8; ++i) B[i] = cat(Bl[buf][i], Bh[buf][i]);
    pin_a(W[slot][0]); pin_a(W[slot][1]);
    if constexpr (!GRAM) pin_a(W[slot][2]);
    static_for<26>([&](auto M) {
      constexpr int m = M, i = m / 3, u = m % 3;
      if constexpr (m >= 24) mfma_vv<ks == 0>(rsum[m - 24], ones, B[m - 24]);
      else if constexpr (u < 2 || !GRAM) mfma_av<ks == 0>(acc[u][i], W[slot][u], B[i]);
      else mfma_vv<ks == 0>(acc[2][i], B[i], B[i]);
      if constexpr (m == 1 && ks + 1 < KS1) { wait_vm<p2_younger(ks + 1, ks, OPS)>(); barrier(); }
      if constexpr (m >= 2 && m < 18 && ks + 1 < KS1) rdB(std::integral_constant<int, ks + 1>{}, std::integral_constant<int, m - 2>{});
      if constexpr (ks >= 9 && ks < 13 && m >= 2 && m < 14) dpp_stage<ks - 9>(cv[m - 2]);
      constexpr int v = m - (26 - nmain - next);
      if constexpr (v >= 0 && v < nmain) issue(std::integral_constant<int, p2_upto(ks - 1) + 1 + v / 5>{}, std::integral_constant<int, v % 5>{});
      if constexpr (v >= nmain) extra(std::integral_constant<int, p2_extra_first(ks) + v - nmain>{});
    });
  });
  mfma_fence(acc[0][0], acc[1][0], rsum[0], rsum[1]);
#pragma unroll
  for (int i = 0; i < 8; ++i) after_fence(acc[0][i], acc[1][i], acc[2][i], rsum[i & 1]);
#pragma unroll
  for (int u = 0; u < 3; ++u) csp[u] = f32x4{cv[4 * u], cv[4 * u + 1], cv[4 * u + 2], cv[4 * u + 3]};
}

__global__ __launch_bounds__(256) void kb_mlp(char* __restrict__ ws, u16* __restrict__ out) {
  extern __shared__ __attribute__((aligned(16))) char smem[];
  const int t = threadIdx.x, lane = t & 63;
  const int w = __builtin_amdgcn_readfirstlane(t >> 6);
  const int b = blockIdx.x, xcd = b & 7, bi = b >> 3;
#define KB_MAP 1
  const int rt = KB_MAP == 0 ? 2 * xcd + (bi & 1) : (xcd >> 1) * 4 + (bi & 3);
  const int j = KB_MAP == 0 ? bi >> 1 : (xcd & 1) * 8 + (bi >> 2);
  const int half = KB_MAP == 0 ? j & 1 : j >> 3, dt = KB_MAP == 0 ? j >> 1 : j & 7;
  const int U0 = slice_u0(j), nu = slice_nu(j);
  const int rem = nu - 8;
  const int nw = w < rem ? 3 : 2;
  const int uw = U0 + (w < rem ? 3 * w : 3 * rem + 2 * (w - rem));
  const int u2 = uw + (nw == 3 ? 2 : 0);
  const int n = lane & 15, g = lane >> 4;
  const int s0 = 128 * rt;
  char* ws_stamp = ws; (void)ws_stamp;
  STAMP(2, 0);
  auto sf = [&](int i) { return ((w + (i >> 1)) & 3) + 4 * (half ^ (i & 1)); };

  KbCtx c;
  c.sb = (u32)(size_t)(LDSP char*)smem;
  c.lv = lane * 16;
  c.gv = (u32)(g * 4096 + (((n >> 1) ^ (g | ((w & 1) << 2))) * 32 + (n & 1) * 16));
  c.hb0 = ws + WS_HT + (size_t)s0 * 2 + (size_t)(8 * w) * 4096;
  c.hb1 = c.hb0 + 4 * 4096;
  c.lb0 = c.sb + 8 * w * 256;
  c.w0 = ws + WS_W1 + (size_t)uw * KS1 * 1024;
  c.w1 = c.w0 + KS1 * 1024;
  c.w2 = ws + WS_W1 + (size_t)u2 * KS1 * 1024;
  c.cs0 = ws + WS_CS + (size_t)uw * 64;
  c.cs1 = c.cs0 + 64;
  c.cs2 = ws + WS_CS + (size_t)u2 * 64;
  c.csv = n * (NU * 64) + g * 16;
  c.wdr = ws + WS_WD + (size_t)dt * KS2 * 4096 + (size_t)KB_NL * 4096;
  c.wd = ws + WS_WD + (size_t)dt * KS2 * 4096 + (size_t)w * 1024;
  c.lwd = c.sb + 1024 * w;
  u32 ra[8], hoff[6];
  auto setup = [&]() {
    const int qd = n >> 2, p = n & 3;
    const int frow = qd | ((g & 1) << 2);
#pragma unroll
    for (int i = 0; i < 8; ++i) { ra[i] = c.sb + (8 * g + qd) * 256 + ((sf(i) ^ frow) * 32) + 8 * p; asm volatile("" : "+v"(ra[i])); }
#pragma unroll
    for (int tt = 0; tt < 6; ++tt) {
      const int q = 4 * tt + g, i = (q * 11) >> 5, u = q - 3 * i, U = uw + u;
      const u32 o = (u32)(((8 * rt + sf(i)) * KS2 + (U >> 2)) * 1024 + ((U & 3) * 16 + n) * 16);
      hoff[tt] = u < nw ? o : 0xfffffff0u;
      asm volatile("" : "+v"(hoff[tt]));
    }
  };
  f32x4 acc[3][8], cs[3], rsum[2];
  u32x4 Wr[4][4];
  if (w == 3) kb_phase2<1>(c, acc, rsum, cs, Wr, ra, setup, ws_stamp);
  else kb_phase2<0>(c, acc, rsum, cs, Wr, ra, setup, ws_stamp);
  STAMP(2, 2);

  u32x2 res[4];
  {
    const u32 rb = ra[0] + dt * 16384;
    ds_tr<0>(res[0], rb); ds_tr<1024>(res[1], rb); ds_tr<8192>(res[2], rb); ds_tr<9216>(res[3], rb);
  }
  float* stl = (float*)(smem + KB_ST);
  if (g == 0) {
#pragma unroll
    for (int i = 0; i < 2; ++i) stl[2 * (16 * sf(i) + n)] = rsum[i][0];
  }
  if (w == 3 && g == (n >> 2)) {
#pragma unroll
    for (int i = 0; i < 8; ++i) {
      f32x4 a = acc[2][i];
      float dg = (n & 2) ? ((n & 1) ? a[3] : a[2]) : ((n & 1) ? a[1] : a[0]);
      stl[2 * (16 * sf(i) + n) + 1] = dg;
    }
  }
  asm volatile("s_waitcnt lgkmcnt(0)\n\ts_barrier" : "+v"(res[0]), "+v"(res[1]), "+v"(res[2]), "+v"(res[3]) :: "memory");
  STAMP(2, 3);
  const __amdgpu_buffer_rsrc_t hrs = __builtin_amdgcn_make_buffer_rsrc(ws + WS_H, 0, (S / 16) * KS2 * 1024, 0x00020000);
  f32x2 sv[8];
#pragma unroll
  for (int i = 0; i < 8; ++i) sv[i] = *(const f32x2*)(stl + 2 * (16 * sf(i) + n));
  u32 hv[24];
  static_for<8>([&](auto I) {
    constexpr int i = I;
    const float mean = sv[i][0] * (1.f / D);
    const float var = fmaxf(sv[i][1] * (1.f / D) - mean * mean, 0.f) + EPS;
    const float rstd = __builtin_amdgcn_rsqf(var);
    const float rinv = var * rstd;
    const float ce = -1.4426950408889634f * rstd;
    const f32x2 nm = {-mean, -mean}, ce2 = {ce, ce}, ri2 = {rinv, rinv};
    static_for<3>([&](auto U_) {
      constexpr int u = U_;
      pin4(acc[u][i]);
      constexpr int slot = 3 * i + u;
      constexpr int wk = slot * EPI_FILL / 24;
      if constexpr (EPI_FILL > 0 && (slot + 1) * EPI_FILL / 24 > wk) glds16<wk * 4096>(c.lv, c.wd + (size_t)wk * 4096, c.lwd);
      const f32x4 a = acc[u][i];
      const f32x2 tg = (f32x2{a[0], a[1]} + nm * f32x2{cs[u][0], cs[u][1]}) * ce2;
      const f32x2 tu = f32x2{a[2], a[3]} + nm * f32x2{cs[u][2], cs[u][3]};
      const f32x2 e = {__builtin_amdgcn_exp2f(tg[0]), __builtin_amdgcn_exp2f(tg[1])};
      const f32x2 den = e * ri2 + ri2;
      const f32x2 o = tu * f32x2{__builtin_amdgcn_rcpf(den[0]), __builtin_amdgcn_rcpf(den[1])};
      hv[3 * i + u] = pk2(o[0], o[1]);
    });
    static_for<6>([&](auto T) {
      constexpr int tt = T;
      if constexpr ((4 * tt + 3) / 3 == i) {
        auto s02 = __builtin_amdgcn_permlane32_swap(hv[4 * tt], hv[4 * tt + 2], false, false);
        auto s13 = __builtin_amdgcn_permlane32_swap(hv[4 * tt + 1], hv[4 * tt + 3], false, false);
        auto t01 = __builtin_amdgcn_permlane16_swap(s02[0], s13[0], false, false);
        auto t23 = __builtin_amdgcn_permlane16_swap(s02[1], s13[1], false, false);
        __builtin_amdgcn_raw_buffer_store_b128(u32x4{t01[0], t01[1], t23[0], t23[1]}, hrs, hoff[tt], 0, 16);
      }
    });
  });
  STAMP(2, 4);
  u32* cnt = (u32*)(ws + WS_CNT + rt * 128);
  asm volatile("s_waitcnt vmcnt(0)\n\ts_barrier" ::: "memory");
  STAMP(2, 5);
  if (w == 0) {
    if (lane == 0) __hip_atomic_fetch_add(cnt, 1u, __ATOMIC_RELAXED, __HIP_MEMORY_SCOPE_AGENT);
    for (int spins = 0; spins < SPIN_MAX; ++spins) {
      const u32 v = __builtin_amdgcn_readfirstlane(__hip_atomic_load(cnt, __ATOMIC_RELAXED, __HIP_MEMORY_SCOPE_AGENT));
      if (v >= 16u) break;
      if (KB_POLL_SLEEP > 0) __builtin_amdgcn_s_sleep(KB_POLL_SLEEP);
    }
    wait_vm<0>();
  } else {
    const char* wsrc = c.wd - (size_t)w * 1024;
    for (int e = EPI_FILL + w - 1; e < 32; e += 3) {
      const char* src = wsrc + (size_t)e * 4096;
      const u32 dst = c.sb + e * 4096;
      glds16<0>(c.lv, src, dst); glds16<1024>(c.lv, src + 1024, dst); glds16<2048>(c.lv, src + 2048, dst); glds16<3072>(c.lv, src + 3072, dst);
    }
  }
  barrier();
  STAMP(2, 6);

  const char* hb = ws + WS_H + (size_t)(8 * rt + 4 * half + w) * KS2 * 1024;
  u32x4 Hf[KS2];
  constexpr int P3_AHEAD = P3_AHEAD_;
  auto ldH = [&](auto KS) { constexpr int ks = KS; if constexpr (ks < KS2) gld_a_sc1<(ks & 3) * 1024>(Hf[ks], c.lv, hb + (ks >> 2) * 4096); };
  static_for<P3_AHEAD>([&](auto KS) { ldH(KS); });
  STAMP(3, 0);
  f32x4 o4[4];
  u32x4 Af[3][4];
  const u32 la0 = c.sb + lane * 16, la1 = la0 + 65536, la2 = la0 + 131072;
  auto rdA = [&](auto KS, auto DF) {
    constexpr int ks = KS, df = DF;
    if constexpr (ks < KB_NL) ds_b128<(ks & 15) * 4096 + df * 1024>(Af[ks % 3][df], ks < 16 ? la0 : ks < 32 ? la1 : la2);
  };
  wait_vm<P3_AHEAD>();
  barrier();
  static_for<4>([&](auto DF) { rdA(std::integral_constant<int, 0>{}, DF); });
  static_for<4>([&](auto DF) { rdA(std::integral_constant<int, 1>{}, DF); });
  static_for<KS2>([&](auto KS) {
    constexpr int ks = KS;
    wait_vm<(ks + P3_AHEAD <= KS2 ? P3_AHEAD - 1 : KS2 - 1 - ks)>();
    if constexpr (ks + 1 < KB_NL) wait_lgkm<4>(); else wait_lgkm<0>();
    pin_a(Hf[ks]);
    static_for<4>([&](auto DF) {
      constexpr int df = DF;
      if constexpr (ks < KB_NL) { pin(Af[ks % 3][df]); mfma_va<ks == 0>(o4[df], Af[ks % 3][df], Hf[ks]); }
      else { pin_a(Wr[ks - KB_NL][df]); mfma_aa(o4[df], Wr[ks - KB_NL][df], Hf[ks]); }
      rdA(std::integral_constant<int, ks + 2>{}, DF);
      if constexpr (df == 1 && ks + P3_AHEAD < KS2) ldH(std::integral_constant<int, ks + P3_AHEAD>{});
    });
  });
  mfma_fence(o4[0], o4[1], o4[2], o4[3]);
  u32x4 o0, o1;
#pragma unroll
  for (int df = 0; df < 2; ++df) {
    o0[2 * df] = pk2(o4[df][0] + blo(res[df][0]), o4[df][1] + bhi(res[df][0]));
    o0[2 * df + 1] = pk2(o4[df][2] + blo(res[df][1]), o4[df][3] + bhi(res[df][1]));
    o1[2 * df] = pk2(o4[2 + df][0] + blo(res[2 + df][0]), o4[2 + df][1] + bhi(res[2 + df][0]));
    o1[2 * df + 1] = pk2(o4[2 + df][2] + blo(res[2 + df][1]), o4[2 + df][3] + bhi(res[2 + df][1]));
  }
  const __amdgpu_buffer_rsrc_t ors = __builtin_amdgcn_make_buffer_rsrc(out, 0, S * D * 2, 0x00020000);
  const u32 ooff = (u32)((s0 + 64 * half + 16 * w + n) * D + 64 * dt + 8 * g) * 2;
  __builtin_amdgcn_raw_buffer_store_b128(o0, ors, ooff, 0, 16);
  __builtin_amdgcn_raw_buffer_store_b128(o1, ors, ooff + 64, 0, 16);
  asm volatile("kb_code_end:");
  STAMP(2, 7);
}

extern "C" void kernel_launch(void* const* d_in, const int* in_sizes, int n_in, void* d_out, int out_size, void* d_ws,
                              size_t ws_size, hipStream_t stream) {
  const u16 *q = (const u16*)d_in[0], *k = (const u16*)d_in[1], *v = (const u16*)d_in[2], *shk = (const u16*)d_in[3];
  const u16 *ssmk = (const u16*)d_in[4], *dskip = (const u16*)d_in[5];
  const u16 *wg = (const u16*)d_in[7], *wu = (const u16*)d_in[8], *wd = (const u16*)d_in[9];
  char* ws = (char*)d_ws;
  (void)hipFuncSetAttribute((const void*)ka_conv, hipFuncAttributeMaxDynamicSharedMemorySize, KA_LDS);
  (void)hipFuncSetAttribute((const void*)kb_mlp, hipFuncAttributeMaxDynamicSharedMemorySize, KB_LDS);
  ka_conv<<<512, 256, KA_LDS, stream>>>(q, k, v, shk, ssmk, dskip, wg, wu, wd, ws);
  kb_mlp<<<256, 256, KB_LDS, stream>>>(ws, (u16*)d_out);
}
```

```cpp
#include <hip/hip_runtime.h>
#include <stdint.h>
#include <utility>

typedef unsigned short u16;
typedef unsigned u32;
typedef __attribute__((ext_vector_type(2))) u32 u32x2;
typedef __attribute__((ext_vector_type(4))) u32 u32x4;
typedef __attribute__((ext_vector_type(2))) float f32x2;
typedef __attribute__((ext_vector_type(4))) float f32x4;
typedef __attribute__((ext_vector_type(2))) __bf16 bf16x2;
typedef __attribute__((ext_vector_type(8))) __bf16 bf16x8;
typedef __attribute__((ext_vector_type(4))) short s16x4;
#define LDSP __attribute__((address_space(3)))
#define DI __device__ __forceinline__

constexpr int S = 2048, D = 512, F = 1376;
constexpr int NU = F / 8;
constexpr int KS1 = D / 32;
constexpr int KS2 = F / 32;
constexpr float EPS = 1e-5f;

constexpr size_t WS_XR = 0;
constexpr size_t WS_QT = WS_XR + (size_t)D * S * 2;
constexpr size_t WS_HT = WS_QT + (size_t)D * S * 2;
constexpr size_t WS_W1 = WS_HT + (size_t)D * S * 2;
constexpr size_t WS_WD = WS_W1 + (size_t)NU * KS1 * 1024;
constexpr size_t WS_H = WS_WD + (size_t)8 * KS2 * 4 * 1024;
constexpr size_t WS_CS = WS_H + (size_t)(S / 16) * KS2 * 1024;
constexpr size_t WS_SA = WS_CS + (size_t)KS1 * NU * 16 * 4;
constexpr size_t WS_SB = WS_SA + (size_t)S * 4;
constexpr size_t WS_CNT = WS_SB + (size_t)S * 4;
constexpr size_t WS_FLA = WS_CNT + 16 * 128;
constexpr size_t WS_FLG = WS_FLA + 256 * 8;
constexpr size_t WS_FLS = WS_FLG + 256 * 8;
constexpr size_t WS_FLZ = WS_FLS + 256 * 8;
constexpr size_t WS_HNT = WS_FLZ + 8 * 128;
constexpr size_t WS_EPO = WS_HNT + 256 * 8;

template <int... Is, class Fn> DI void static_for_(std::integer_sequence<int, Is...>, Fn&& f) { (f(std::integral_constant<int, Is>{}), ...); }
template <int N, class Fn> DI void static_for(Fn&& f) { static_for_(std::make_integer_sequence<int, N>{}, f); }

DI float blo(u32 u) { return __uint_as_float(u << 16); }
DI float bhi(u32 u) { return __uint_as_float(u & 0xffff0000u); }
DI u32 pk2(float a, float b) { f32x2 t = {a, b}; return __builtin_bit_cast(u32, __builtin_convertvector(t, bf16x2)); }
DI f32x4 mfma16(u32x4 a, u32x4 b, f32x4 c) {
  return __builtin_amdgcn_mfma_f32_16x16x32_bf16(__builtin_bit_cast(bf16x8, a), __builtin_bit_cast(bf16x8, b), c, 0, 0, 0);
}
DI u32x2 tr_read(u32 lds_byte) {
  return __builtin_bit_cast(u32x2, __builtin_amdgcn_ds_read_tr16_b64_v4i16((LDSP s16x4*)(size_t)lds_byte));
}
DI u32x4 tr_frag(u32 a0, u32 a1) { u32x2 x = tr_read(a0), y = tr_read(a1); return u32x4{x[0], x[1], y[0], y[1]}; }
DI u32x4 cat(u32x2 x, u32x2 y) { return __builtin_shufflevector(x, y, 0, 1, 2, 3); }
template <int CTRL> DI float dpp_add(float v) {
  return v + __builtin_bit_cast(float, __builtin_amdgcn_update_dpp(0, __builtin_bit_cast(int, v), CTRL, 0xf, 0xf, true));
}
DI float sum_rows(float v) {
  u32 u = __float_as_uint(v);
  auto a = __builtin_amdgcn_permlane16_swap(u, u, false, false);
  v = __uint_as_float(a[0]) + __uint_as_float(a[1]);
  u = __float_as_uint(v);
  auto b = __builtin_amdgcn_permlane32_swap(u, u, false, false);
  return __uint_as_float(b[0]) + __uint_as_float(b[1]);
}
DI float wave_sum(float v) {
  v = dpp_add<0xb1>(v);
  v = dpp_add<0x4e>(v);
  v = dpp_add<0x124>(v);
  v = dpp_add<0x128>(v);
  return sum_rows(v);
}
DI __amdgpu_buffer_rsrc_t ws_rsrc(char* ws) { return __builtin_amdgcn_make_buffer_rsrc(ws, 0, 0x04000000, 0x00020000); }
DI void st16_wt(__amdgpu_buffer_rsrc_t r, u32 off, u32x4 v) { __builtin_amdgcn_raw_buffer_store_b128(v, r, off, 0, 16); }
DI int slice_u0(int j) { return j < 12 ? 11 * j : 132 + 10 * (j - 12); }
DI int slice_nu(int j) { return j < 12 ? 11 : 10; }

template <bool FIRST = false> DI void mfma_vv(f32x4& acc, const u32x4& a, const u32x4& b) {
  if constexpr (FIRST) asm volatile("v_mfma_f32_16x16x32_bf16 %0, %1, %2, 0" : "=&v"(acc) : "v"(a), "v"(b));
  else asm volatile("v_mfma_f32_16x16x32_bf16 %0, %1, %2, %0" : "+v"(acc) : "v"(a), "v"(b));
}
template <bool FIRST = false> DI void mfma_av(f32x4& acc, const u32x4& a, const u32x4& b) {
  if constexpr (FIRST) asm volatile("v_mfma_f32_16x16x32_bf16 %0, %1, %2, 0" : "=&v"(acc) : "a"(a), "v"(b));
  else asm volatile("v_mfma_f32_16x16x32_bf16 %0, %1, %2, %0" : "+v"(acc) : "a"(a), "v"(b));
}
template <int OFF> DI void ds_b64(u32x2& d, u32 addr) { asm volatile("ds_read_b64 %0, %1 offset:%c2" : "=v"(d) : "v"(addr), "i"(OFF) : "memory"); }
template <int OFF> DI void ds_b128(u32x4& d, u32 addr) { asm volatile("ds_read_b128 %0, %1 offset:%c2" : "=v"(d) : "v"(addr), "i"(OFF) : "memory"); }
template <int OFF> DI void ds_tr(u32x2& d, u32 addr) { asm volatile("ds_read_b64_tr_b16 %0, %1 offset:%c2" : "=v"(d) : "v"(addr), "i"(OFF) : "memory"); }
template <int OFF> DI void gld_a(u32x4& d, u32 voff, const void* sbase) {
  asm volatile("global_load_dwordx4 %0, %1, %2 offset:%c3" : "=a"(d) : "v"(voff), "s"(sbase), "i"(OFF) : "memory");
}
template <int OFF> DI void gld_v(u32x4& d, u32 voff, const void* sbase) {
  asm volatile("global_load_dwordx4 %0, %1, %2 offset:%c3" : "=v"(d) : "v"(voff), "s"(sbase), "i"(OFF) : "memory");
}
template <int OFF> DI void gld2_v(u32x2& d, u32 voff, const void* sbase) {
  asm volatile("global_load_dwordx2 %0, %1, %2 offset:%c3" : "=v"(d) : "v"(voff), "s"(sbase), "i"(OFF) : "memory");
}
template <int LOFF> DI void glds16(u32 voff, const void* sbase, u32 ldsbase) {
  asm volatile("s_add_u32 m0, %2, %c3\n\ts_nop 0\n\tglobal_load_lds_dwordx4 %0, %1" ::"v"(voff), "s"(sbase), "s"(ldsbase), "i"(LOFF) : "memory", "scc");
}
template <int N> DI void wait_vm() { asm volatile("s_waitcnt vmcnt(%c0)" ::"i"(N) : "memory"); }
template <int N> DI void wait_lgkm() { asm volatile("s_waitcnt lgkmcnt(%c0)" ::"i"(N) : "memory"); }
template <int OFF> DI void gld_a_sc1(u32x4& d, u32 voff, const void* sbase) {
  asm volatile("global_load_dwordx4 %0, %1, %2 offset:%c3 sc1" : "=a"(d) : "v"(voff), "s"(sbase), "i"(OFF) : "memory");
}
DI void mfma_aa(f32x4& acc, const u32x4& a, const u32x4& b) {
  asm volatile("v_mfma_f32_16x16x32_bf16 %0, %1, %2, %0" : "+v"(acc) : "a"(a), "a"(b));
}
template <bool FIRST = false> DI void mfma_va(f32x4& acc, const u32x4& a, const u32x4& b) {
  if constexpr (FIRST) asm volatile("v_mfma_f32_16x16x32_bf16 %0, %1, %2, 0" : "=&v"(acc) : "v"(a), "a"(b));
  else asm volatile("v_mfma_f32_16x16x32_bf16 %0, %1, %2, %0" : "+v"(acc) : "v"(a), "a"(b));
}
DI void mfma_fence(f32x4& a, f32x4& b, f32x4& c, f32x4& d) { asm volatile("s_nop 7\n\ts_nop 1" : "+v"(a), "+v"(b), "+v"(c), "+v"(d)); }
DI void after_fence(f32x4& a, f32x4& b, f32x4& c, f32x4& d) { asm volatile("" : "+v"(a), "+v"(b), "+v"(c), "+v"(d)); }
DI void after_fence(f32x4& a, f32x4& b, f32x4& c) { asm volatile("" : "+v"(a), "+v"(b), "+v"(c)); }
template <int STAGE> DI void dpp_stage(float& v) {
  if constexpr (STAGE == 0) asm volatile("v_add_f32_dpp %0, %0, %0 quad_perm:[1,0,3,2] row_mask:0xf bank_mask:0xf" : "+v"(v));
  if constexpr (STAGE == 1) asm volatile("v_add_f32_dpp %0, %0, %0 quad_perm:[2,3,0,1] row_mask:0xf bank_mask:0xf" : "+v"(v));
  if constexpr (STAGE == 2) asm volatile("v_add_f32_dpp %0, %0, %0 row_ror:4 row_mask:0xf bank_mask:0xf" : "+v"(v));
  if constexpr (STAGE == 3) asm volatile("v_add_f32_dpp %0, %0, %0 row_ror:8 row_mask:0xf bank_mask:0xf" : "+v"(v));
}
DI void barrier() { asm volatile("s_barrier" ::: "memory"); }
DI void pin(u32x4& x) { asm volatile("" : "+v"(x)); }
DI void pin(u32x2& x) { asm volatile("" : "+v"(x)); }
DI void pin4(f32x4& x) { asm volatile("" : "+v"(x)); }
DI void pin_a(u32x4& x) { asm volatile("" : "+a"(x)); }

#define STAMP(kern, idx) do {} while (0)

template <class Mid, class Gate>
DI void w_job(int b, const u16* __restrict__ wg, const u16* __restrict__ wu, const u16* __restrict__ wd, char* __restrict__ ws, char* smem, Mid&& mid, Gate&& gate) {
  const int t = threadIdx.x, lane = t & 63, w = t >> 6;
  char* tile = smem;
  char* tile2 = smem + 32 * 368;
  const u32 tb = (u32)(size_t)(LDSP char*)smem;
  const int i16 = lane & 15, qd = i16 >> 2, p = i16 & 3, g = lane >> 4;
  const int ks = b & 15, j = b >> 4;
  const int U0 = slice_u0(j), nu = slice_nu(j);
  u32x4 g4[2], u4[2], d4[2];
#pragma unroll
  for (int rep = 0; rep < 2; ++rep) {
    int idx = t + 256 * rep, r = idx / 11, u = idx - 11 * r;
    bool ok = r < 32 && u < nu;
    size_t off = ok ? (size_t)(32 * ks + r) * F + 8 * (U0 + u) : 0;
    g4[rep] = *(const u32x4*)(wg + off); u4[rep] = *(const u32x4*)(wu + off);
  }
  const int ks2 = b >> 2, dp = b & 3;
  if (b < KS2 * 4) {
#pragma unroll
    for (int rep = 0; rep < 2; ++rep) {
      int idx = t + 256 * rep, r = idx >> 4, c = idx & 15;
      d4[rep] = *(const u32x4*)(wd + (size_t)(32 * ks2 + r) * D + 128 * dp + 8 * c);
    }
  }
  mid();
#pragma unroll
  for (int rep = 0; rep < 2; ++rep) {
    int idx = t + 256 * rep, r = idx / 11, u = idx - 11 * r;
    if (r < 32 && u < nu) {
      *(u32x4*)(tile + r * 368 + u * 32) = u32x4{g4[rep][0], u4[rep][0], g4[rep][1], u4[rep][1]};
      *(u32x4*)(tile + r * 368 + u * 32 + 16) = u32x4{g4[rep][2], u4[rep][2], g4[rep][3], u4[rep][3]};
    }
  }
  if (b < KS2 * 4) {
#pragma unroll
    for (int rep = 0; rep < 2; ++rep) { int idx = t + 256 * rep, r = idx >> 4, c = idx & 15; *(u32x4*)(tile2 + r * 272 + c * 16) = d4[rep]; }
  }
  __syncthreads();
  gate();
  const u32x4 ones = {0x3f803f80u, 0x3f803f80u, 0x3f803f80u, 0x3f803f80u};
  const __amdgpu_buffer_rsrc_t wr = ws_rsrc(ws);
  for (int u = w; u < nu; u += 4) {
    u32 a = tb + (8 * g + qd) * 368 + u * 32 + 8 * p;
    u32x4 fr = tr_frag(a, a + 4 * 368);
    st16_wt(wr, (u32)WS_W1 + (((U0 + u) * KS1 + ks) * 64 + lane) * 16, fr);
    f32x4 cs = mfma16(fr, ones, f32x4{0.f, 0.f, 0.f, 0.f});
    if (i16 == 0) st16_wt(wr, (u32)WS_CS + ((ks * NU + U0 + u) * 16 + 4 * g) * 4, __builtin_bit_cast(u32x4, cs));
  }
  if (b < KS2 * 4) {
#pragma unroll
    for (int rep = 0; rep < 2; ++rep) {
      int fi = w + 4 * rep, dtl = fi >> 2, df = fi & 3;
      u32 a = tb + 32 * 368 + (8 * g + qd) * 272 + (64 * dtl + 32 * (df >> 1) + 8 * p + 4 * (df & 1)) * 2;
      u32x4 fr = tr_frag(a, a + 4 * 272);
      st16_wt(wr, (u32)WS_WD + ((((2 * dp + dtl) * KS2 + ks2) * 4 + df) * 64 + lane) * 16, fr);
    }
  }
}

constexpr int KA_CS = 4160;
constexpr int KA_KC = 4 * KA_CS;
constexpr int KA_XR = 2 * KA_KC;
constexpr int KA_RED = KA_XR + 2 * 4608;
constexpr int KA_LDS = KA_RED + 4 * 4096;
constexpr int KA_TK = KA_XR, KA_TV = KA_TK + 72 * 128, KA_TQ = KA_TV + 64 * 128;
static_assert(KA_TQ + 64 * 128 == KA_LDS, "tile overlay");
constexpr int SPIN_MAX = 1 << 22;
#define HINT_STEP 0
#define KB_POLL_SLEEP 1
#define KA_POLL_SLEEP 1
#define P3_AHEAD_ 12
#define HELP_PRE 0
#define HELP_SLEEP 8
#define CHK 2
#define CHK_M 2
#define WEARLY 0
#define HINT_TICKS 800ull

template <int OFF> DI void gld_v_sc1(u32x4& d, u32 voff, const void* sbase) {
  asm volatile("global_load_dwordx4 %0, %1, %2 offset:%c3 sc1" : "=v"(d) : "v"(voff), "s"(sbase), "i"(OFF) : "memory");
}
template <int OFF> DI void gld2_v_sc1(u32x2& d, u32 voff, const void* sbase) {
  asm volatile("global_load_dwordx2 %0, %1, %2 offset:%c3 sc1" : "=v"(d) : "v"(voff), "s"(sbase), "i"(OFF) : "memory");
}
DI bool tags_ok(const u32x4& f, const u32x2& tag) { return f[0] == tag[0] && f[1] == tag[1] && f[2] == tag[0] && f[3] == tag[1]; }

__global__ __launch_bounds__(256, 2) void ka_conv(const u16* __restrict__ q, const u16* __restrict__ k, const u16* __restrict__ v,
                                                  const u16* __restrict__ shk, const u16* __restrict__ ssmk, const u16* __restrict__ dskip,
                                                  const u16* __restrict__ wg, const u16* __restrict__ wu, const u16* __restrict__ wd,
                                                  char* __restrict__ ws) {
  extern __shared__ __attribute__((aligned(16))) char smem[];
  const int t = threadIdx.x, lane = t & 63;
  const int w = __builtin_amdgcn_readfirstlane(t >> 6);
  const int b = blockIdx.x;
  char* ws_stamp = ws; (void)ws_stamp;
  STAMP(0, 0);
  if (b >= 256) {
    const int hb = b - 256, srow = 8 * hb + 2 * w;
    const __amdgpu_buffer_rsrc_t wr = ws_rsrc(ws);
    u32x2 epo; u32x4 qa, qb;
    gld2_v_sc1<0>(epo, 0, ws + WS_EPO);
    if (HELP_PRE > 0) __builtin_amdgcn_s_sleep(HELP_PRE);
    gld_v<0>(qa, lane * 16, (const char*)q + (size_t)srow * 1024);
    gld_v<1024>(qb, lane * 16, (const char*)q + (size_t)srow * 1024);
    if (b < 264 && w == 1) {
      const char* pc; u32 len;
      asm volatile("s_getpc_b64 %0\n1:\n\ts_mov_b32 %1, kb_code_end-1b" : "=s"(pc), "=s"(len));
      const u32 lb = (u32)(size_t)(LDSP char*)smem + 32768;
      for (u32 o0 = 0; o0 < len; o0 += 64 * 128) {
        u32 o = o0 + lane * 128; o = o < len ? o : 0;
        asm volatile("s_mov_b32 m0, %2\n\ts_nop 0\n\tglobal_load_lds_dword %0, %1" ::"v"(o), "s"(pc), "s"(lb) : "memory");
      }
    }
    unsigned long long want = 0;
    if (HELP_SLEEP > 0) __builtin_amdgcn_s_sleep(HELP_SLEEP);
    w_job(hb, wg, wu, wd, ws, smem, [&]() {
      if (hb < KS2 * 4) wait_vm<6>(); else wait_vm<4>();
      asm volatile("" : "+v"(qa), "+v"(qb), "+v"(epo));
      want = (((unsigned long long)epo[1] << 32) | epo[0]) + 1ull;
      {
        float sa = 0.f, sbb = 0.f;
  #pragma unroll
        for (int e = 0; e < 4; ++e) { sa += blo(qa[e]) + bhi(qa[e]); sbb += blo(qb[e]) + bhi(qb[e]); }
        const float ma = wave_sum(sa) * (1.f / D), mb = wave_sum(sbb) * (1.f / D);
        float va = 0.f, vb = 0.f;
  #pragma unroll
        for (int e = 0; e < 4; ++e) {
          float u0 = blo(qa[e]) - ma, u1 = bhi(qa[e]) - ma, u2 = blo(qb[e]) - mb, u3 = bhi(qb[e]) - mb;
          va += u0 * u0 + u1 * u1; vb += u2 * u2 + u3 * u3;
        }
        va = wave_sum(va) * (1.f / D); vb = wave_sum(vb) * (1.f / D);
        const float ra = __builtin_amdgcn_rsqf(va + EPS), rb = __builtin_amdgcn_rsqf(vb + EPS);
        const u32x2 val = lane == 0 ? u32x2{__float_as_uint(ra), __float_as_uint(rb)} : u32x2{__float_as_uint(-ma * ra), __float_as_uint(-mb * rb)};
        const u32 off = lane == 0 ? (u32)WS_SA + srow * 4 : lane == 1 ? (u32)WS_SB + srow * 4 : 0xfffffff0u;
        __builtin_amdgcn_raw_buffer_store_b64(val, wr, off, 0, 16);
      }
      asm volatile("s_waitcnt vmcnt(0)\n\ts_barrier" ::: "memory");
      if (t == 0) __builtin_amdgcn_raw_buffer_store_b64(u32x2{(u32)want, (u32)(want >> 32)}, wr, (u32)WS_FLS + hb * 8, 0, 16);
      STAMP(1, 6);
      if (hb == 1 && w == 1) {
        const unsigned long long* fs = (const unsigned long long*)(ws + WS_FLS) + lane;
        for (int spins = 0; spins < SPIN_MAX; ++spins) {
          bool ok = true;
  #pragma unroll
          for (int i = 0; i < 4; ++i) ok &= __hip_atomic_load(fs + 64 * i, __ATOMIC_RELAXED, __HIP_MEMORY_SCOPE_AGENT) == want;
          if (__all(ok)) break;
          __builtin_amdgcn_s_sleep(1);
        }
        __builtin_amdgcn_raw_buffer_store_b64(u32x2{(u32)want, (u32)(want >> 32)}, wr, lane < 8 ? (u32)WS_FLZ + lane * 128 : 0xfffffff0u, 0, 16);
      }
    }, [&]() {
      if (hb == 1 && w == 1) {
        const unsigned long long* fa = (const unsigned long long*)(ws + WS_FLA) + lane;
        const u32x2 tg = {(u32)want, (u32)(want >> 32)};
        for (int spins = 0; spins < SPIN_MAX; ++spins) {
          bool ok = true;
#pragma unroll
          for (int i = 0; i < 4; ++i) ok &= __hip_atomic_load(fa + 64 * i, __ATOMIC_RELAXED, __HIP_MEMORY_SCOPE_AGENT) == want;
          if (__all(ok)) break;
          __builtin_amdgcn_s_sleep(16);
        }
        if (lane == 0) __builtin_amdgcn_raw_buffer_store_b64(tg, wr, (u32)WS_EPO, 0, 16);
      }
      if (w == 0) {
        const unsigned long long* hp = (const unsigned long long*)(ws + WS_HNT) + hb;
        const unsigned long long t0 = __builtin_amdgcn_s_memrealtime();
        while (__hip_atomic_load(hp, __ATOMIC_RELAXED, __HIP_MEMORY_SCOPE_AGENT) != want && __builtin_amdgcn_s_memrealtime() - t0 < HINT_TICKS)
          __builtin_amdgcn_s_sleep(8);
      }
      __builtin_amdgcn_s_barrier();
      STAMP(1, 5);
    });
    STAMP(1, 4);
    return;
  }
  const int x = b & 7, bi = b >> 3, s0 = 64 * bi;
  const int ch0 = 64 * x + 2 * bi;
  const u32 sb = (u32)(size_t)(LDSP char*)smem;
  const int n = lane & 15, g = lane >> 4, qd = n >> 2, p = n & 3;
  const int d = 64 * x + 16 * w + n;
  const __amdgpu_buffer_rsrc_t wr = ws_rsrc(ws);

  u32x2 epo, sh; u32x4 kp[2], kc[2];
  gld2_v_sc1<0>(epo, 0, ws + WS_EPO);
  gld2_v<0>(sh, d * 8, shk);
  const int r8 = lane >> 3, c8 = lane & 7;
  auto voff = [&](int instr) { const int f = ((r8 >> 1) & 1) | ((instr & 1) << 1); return (u32)((((c8 >> 1) ^ f) * 2 + (c8 & 1)) * 16); };
  const size_t col = (size_t)x * 128;
  {
    int row = s0 - 8 + 8 * w + r8; row = row < 0 ? 0 : row;
    glds16<0>((u32)row * 1024 + voff(w), (const char*)k + col, sb + KA_TK + w * 1024);
    glds16<0>((u32)(s0 + 8 * w + r8) * 1024 + voff(w), (const char*)v + col, sb + KA_TV + w * 1024);
    glds16<0>((u32)(s0 - 8 + 8 * (w + 4) + r8) * 1024 + voff(w), (const char*)k + col, sb + KA_TK + (w + 4) * 1024);
    glds16<0>((u32)(s0 + 8 * (w + 4) + r8) * 1024 + voff(w), (const char*)v + col, sb + KA_TV + (w + 4) * 1024);
    if (w == 0) glds16<0>((u32)(s0 + 56 + r8) * 1024 + voff(8), (const char*)k + col, sb + KA_TK + 8 * 1024);
  }
#pragma unroll
  for (int cc = 0; cc < 2; ++cc) {
    const char* kr = (const char*)ssmk + (size_t)(ch0 + cc) * 4096;
    gld_v<0>(kp[cc], (u32)(t > 0 ? t - 1 : 0) * 16, kr);
    gld_v<0>(kc[cc], (u32)t * 16, kr);
  }
  glds16<0>((u32)(s0 + 8 * w + r8) * 1024 + voff(w), (const char*)q + col, sb + KA_TQ + w * 1024);
  glds16<0>((u32)(s0 + 8 * (w + 4) + r8) * 1024 + voff(w), (const char*)q + col, sb + KA_TQ + (w + 4) * 1024);
  const float dsk = blo(dskip[ch0 + (w >> 1)]);
  if (b == 0 && t < 16) *(u32*)(ws + WS_CNT + t * 128) = 0u;

  wait_vm<6>();
  barrier();
  asm volatile("" : "+v"(sh), "+v"(epo));
  STAMP(0, 1);
  u32x2 tag;
  { const unsigned long long e = (((unsigned long long)epo[1] << 32) | epo[0]) + 1ull; tag = u32x2{(u32)e, (u32)(e >> 32)}; }
  auto taddr = [&](int base, int tr) { const int f = ((tr >> 1) & 1) | ((tr >> 2) & 2); return sb + base + tr * 128 + ((w ^ f) * 32) + 8 * p; };
  {
    const float a0 = blo(sh[0]), a1 = bhi(sh[0]), a2 = blo(sh[1]), a3 = bhi(sh[1]);
#pragma unroll
    for (int e = 0; e < 2; ++e) {
      const int rg = g + 4 * e;
      u32x2 km = tr_read(taddr(KA_TK, 8 * rg + 4 + qd));
      const u32x2 k0 = tr_read(taddr(KA_TK, 8 * rg + 8 + qd)), k1 = tr_read(taddr(KA_TK, 8 * rg + 12 + qd));
      const u32x2 v0 = tr_read(taddr(KA_TV, 8 * rg + qd)), v1 = tr_read(taddr(KA_TV, 8 * rg + 4 + qd));
      if (s0 + rg == 0) km = u32x2{0, 0};
      const float kf[11] = {bhi(km[0]), blo(km[1]), bhi(km[1]), blo(k0[0]), bhi(k0[0]), blo(k0[1]), bhi(k0[1]), blo(k1[0]), bhi(k1[0]), blo(k1[1]), bhi(k1[1])};
      const float vf[8] = {blo(v0[0]), bhi(v0[0]), blo(v0[1]), bhi(v0[1]), blo(v1[0]), bhi(v1[0]), blo(v1[1]), bhi(v1[1])};
      float xo[8];
#pragma unroll
      for (int j = 0; j < 8; ++j) xo[j] = (kf[j + 3] * a0 + kf[j + 2] * a1 + kf[j + 1] * a2 + kf[j] * a3) * vf[j];
      st16_wt(wr, (u32)WS_XR + (d * S + (2040 - (s0 + 8 * rg))) * 2, u32x4{pk2(xo[7], xo[6]), pk2(xo[5], xo[4]), pk2(xo[3], xo[2]), pk2(xo[1], xo[0])});
    }
  }
  asm volatile("s_waitcnt vmcnt(0)\n\ts_barrier" : "+v"(kp[0]), "+v"(kc[0]), "+v"(kp[1]), "+v"(kc[1]) :: "memory");
  if (t == 0) __builtin_amdgcn_raw_buffer_store_b64(tag, wr, (u32)WS_FLA + (x * 32 + bi) * 8, 0, 16);
  STAMP(0, 2);
#pragma unroll
  for (int e = 0; e < 2; ++e) {
    const int rg = g + 4 * e;
    const u32x2 q0 = tr_read(taddr(KA_TQ, 8 * rg + qd)), q1 = tr_read(taddr(KA_TQ, 8 * rg + 4 + qd));
    st16_wt(wr, (u32)WS_QT + (d * S + s0 + 8 * rg) * 2, cat(q0, q1));
  }
#pragma unroll
  for (int cc = 0; cc < 2; ++cc) {
    const u32 m = t > 0 ? 0xffffffffu : 0u;
    u32 wv[6] = {kp[cc][0] & m, kp[cc][1] & m, kp[cc][2] & m, kp[cc][3] & m, kc[cc][0], kc[cc][1]};
    u32 o1[5];
#pragma unroll
    for (int i = 0; i < 5; ++i) o1[i] = __builtin_amdgcn_alignbit(wv[i + 1], wv[i], 16);
    char* base = smem + cc * KA_KC + (t + 1) * 16;
    *(u32x4*)(base + 0 * KA_CS) = u32x4{o1[0], o1[1], o1[2], o1[3]};
    *(u32x4*)(base + 1 * KA_CS) = u32x4{wv[1], wv[2], wv[3], wv[4]};
    *(u32x4*)(base + 2 * KA_CS) = u32x4{o1[1], o1[2], o1[3], o1[4]};
    *(u32x4*)(base + 3 * KA_CS) = u32x4{wv[2], wv[3], wv[4], wv[5]};
    if (t == 255) {
      const u32 z = 0;
      const u32 e0 = __builtin_amdgcn_alignbit(kc[cc][1], kc[cc][0], 16), e1 = __builtin_amdgcn_alignbit(kc[cc][2], kc[cc][1], 16);
      const u32 e2 = __builtin_amdgcn_alignbit(kc[cc][3], kc[cc][2], 16), e3 = __builtin_amdgcn_alignbit(z, kc[cc][3], 16);
      *(u32x4*)(base + 16 + 0 * KA_CS) = u32x4{e0, e1, e2, e3};
      *(u32x4*)(base + 16 + 1 * KA_CS) = u32x4{kc[cc][1], kc[cc][2], kc[cc][3], z};
      *(u32x4*)(base + 16 + 2 * KA_CS) = u32x4{e1, e2, e3, z};
      *(u32x4*)(base + 16 + 3 * KA_CS) = u32x4{kc[cc][2], kc[cc][3], z, z};
    }
  }
  if (t < 8) *(u32x4*)(smem + (t >> 2) * KA_KC + (t & 3) * KA_CS) = u32x4{0, 0, 0, 0};
  asm volatile("s_waitcnt vmcnt(0) lgkmcnt(0)\n\ts_barrier" ::: "memory");
  if (t == 0) {
    __builtin_amdgcn_raw_buffer_store_b64(tag, wr, (u32)WS_FLG + (x * 32 + bi) * 8, 0, 16);
  }
  if (t >= 64 && t < 64 + 60) { int i = t - 64; *(u32x4*)(smem + KA_XR + (i / 30) * 4608 + 4096 + (i % 30) * 16) = u32x4{0, 0, 0, 0}; }
  if (w == 0) {
    const unsigned long long* fp = (const unsigned long long*)(ws + WS_FLA) + x * 32 + (lane & 31);
    const unsigned long long want = ((unsigned long long)tag[1] << 32) | tag[0];
    for (int spins = 0; spins < SPIN_MAX; ++spins) {
      if (__all(__hip_atomic_load(fp, __ATOMIC_RELAXED, __HIP_MEMORY_SCOPE_AGENT) == want)) break;
      if (KA_POLL_SLEEP > 0) __builtin_amdgcn_s_sleep(KA_POLL_SLEEP);
    }
  }
  asm volatile("s_waitcnt vmcnt(0)\n\ts_barrier" ::: "memory");
  STAMP(0, 3);

  const int c = w >> 1, half = w & 1;
  u32x2 qv[4], fz; u32x4 sa[4], sbv[4], fq;
  const u32 eo = (u32)(1024 * half + 16 * n + 4 * g);
  const char* qTc = ws + WS_QT + (size_t)(ch0 + c) * 4096;
  {
    u32x4 x0, x1;
    gld_v_sc1<0>(x0, t * 16, ws + WS_XR + (size_t)ch0 * 4096);
    gld_v_sc1<0>(x1, t * 16, ws + WS_XR + (size_t)(ch0 + 1) * 4096);
    wait_vm<0>();
    asm volatile("" : "+v"(x0), "+v"(x1));
    *(u32x4*)(smem + KA_XR + t * 16) = x0;
    *(u32x4*)(smem + KA_XR + 4608 + t * 16) = x1;
  }
  asm volatile("s_waitcnt lgkmcnt(0)\n\ts_barrier" ::: "memory");
  if (HINT_STEP == 0 && t == 0) __builtin_amdgcn_raw_buffer_store_b64(tag, wr, (u32)WS_HNT + b * 8, 0, 16);
  STAMP(1, 1);

  const u32 aa = sb + c * KA_KC + (n & 3) * KA_CS + 16 * g + 2 * (n & 12) + half * 256;
  const u32 ba = sb + KA_XR + c * 4608 + 480 - 32 * n + 16 * g + half * 256;
  f32x4 acc[8];
  u32x2 Alo[2][8], Ahi[2][8];
  u32x4 B[2][8];
  auto rd = [&](auto C0, auto R) {
    constexpr int c0 = C0, r = R, buf = c0 & 1;
    if constexpr (r >= 2 && r < 10) {
      constexpr int u = r - 2;
      ds_b128<512 * (7 - u) + 64 * c0>(B[buf][u], ba);
    } else {
      constexpr int ar = r < 2 ? r : r - 8, i = ar >> 1;
      if constexpr ((ar & 1) == 0) ds_b64<512 * i + 64 * c0>(Alo[buf][i], aa);
      else ds_b64<512 * i + 64 * c0 + 8>(Ahi[buf][i], aa);
    }
  };
  auto ldS = [&](auto E) {
    constexpr int e = E, i = e / 3, kind = e % 3;
    if constexpr (kind == 0) gld_v_sc1<i * 1024>(sa[i], eo * 4, ws + WS_SA);
    if constexpr (kind == 1) gld_v_sc1<i * 1024>(sbv[i], eo * 4, ws + WS_SB);
    if constexpr (kind == 2) gld2_v_sc1<i * 512>(qv[i], eo * 2, qTc);
  };
  auto ldF = [&](auto E) {
    constexpr int e = E;
    if constexpr (e == 0) gld2_v_sc1<0>(fz, x * 128, ws + WS_FLZ);
    else gld_v_sc1<0>(fq, (x * 32 + 2 * (lane & 15)) * 8, ws + WS_FLG);
  };
  auto okF = [&]() { return fz[0] == tag[0] && fz[1] == tag[1] && tags_ok(fq, tag); };
  static_for<24>([&](auto R) { rd(std::integral_constant<int, 0>{}, R); });
  static_for<4>([&](auto C0) {
    constexpr int c0 = C0, buf = c0 & 1;
    if constexpr (c0 == CHK) {
      wait_vm<0>();
      asm volatile("" : "+v"(fz), "+v"(fq));
      if (!__all(okF())) {
        for (int spins = 0; spins < SPIN_MAX; ++spins) {
          ldF(std::integral_constant<int, 0>{}); ldF(std::integral_constant<int, 1>{});
          wait_vm<0>();
          asm volatile("" : "+v"(fz), "+v"(fq));
          if (__all(okF())) break;
          __builtin_amdgcn_s_sleep(1);
        }
      }
    }
    if constexpr (c0 > 0) { if (HINT_STEP == c0 && t == 0) __builtin_amdgcn_raw_buffer_store_b64(tag, wr, (u32)WS_HNT + b * 8, 0, 16); }
    u32x4 A[8];
    static_for<36>([&](auto M) {
      constexpr int m = M;
      constexpr int c1 = m < 8 ? 0 : m < 15 ? 1 : m < 21 ? 2 : m < 26 ? 3 : m < 30 ? 4 : m < 33 ? 5 : m < 35 ? 6 : 7;
      constexpr int first = c1 * 8 - c1 * (c1 - 1) / 2;
      constexpr int u = m - first;
      if constexpr (m == 0) {
        if constexpr (c0 == 0) wait_lgkm<14>(); else wait_lgkm<0>();
        pin(Alo[buf][0]); pin(Ahi[buf][0]);
#pragma unroll
        for (int i = 0; i < 8; ++i) pin(B[buf][i]);
        A[0] = cat(Alo[buf][0], Ahi[buf][0]);
      }
      if constexpr (m == 8) {
        if constexpr (c0 == 0) wait_lgkm<8>();
#pragma unroll
        for (int i = 1; i < 8; ++i) { pin(Alo[buf][i]); pin(Ahi[buf][i]); A[i] = cat(Alo[buf][i], Ahi[buf][i]); }
      }
      mfma_vv<(c0 == 0 && c1 == 0)>(acc[c1 + u], A[c1], B[buf][u]);
      if constexpr (c0 < 3 && m < 24) rd(std::integral_constant<int, c0 + 1>{}, M);
      if constexpr (c0 == CHK - 1 && (m == CHK_M || m == CHK_M + 2)) ldF(std::integral_constant<int, (m - CHK_M) / 2>{});
      if constexpr (c0 == CHK && m >= 1 && m < 25 && (m & 1) == 1) ldS(std::integral_constant<int, (m - 1) / 2>{});
    });
  });
  mfma_fence(acc[0], acc[1], acc[2], acc[3]);
  after_fence(acc[4], acc[5], acc[6], acc[7]);
  STAMP(1, 2);
  f32x4 fin[4];
  {
    f32x4* red = (f32x4*)(smem + KA_RED);
    if (half) {
#pragma unroll
      for (int i = 0; i < 4; ++i) { red[(w * 4 + i) * 64 + lane] = acc[i]; fin[i] = acc[4 + i]; }
      asm volatile("; half 1" ::: "memory");
    } else {
#pragma unroll
      for (int i = 0; i < 4; ++i) { red[(w * 4 + i) * 64 + lane] = acc[4 + i]; fin[i] = acc[i]; }
      asm volatile("; half 0" ::: "memory");
    }
    asm volatile("s_waitcnt lgkmcnt(0)\n\ts_barrier" ::: "memory");
#pragma unroll
    for (int i = 0; i < 4; ++i) fin[i] += red[((w ^ 1) * 4 + i) * 64 + lane];
  }
  wait_vm<0>();
#pragma unroll
  for (int i = 0; i < 4; ++i) asm volatile("" : "+v"(qv[i]), "+v"(sa[i]), "+v"(sbv[i]));
  const f32x2 dd = {dsk, dsk};
  u32x2 hv[4];
#pragma unroll
  for (int i = 0; i < 4; ++i) {
    const int s = 256 * (4 * half + i) + 16 * n + 4 * g;
    const u32x2 xq = *(LDSP const u32x2*)(size_t)(sb + KA_XR + c * 4608 + 2 * (2044 - s));
    const f32x2 x0 = {bhi(xq[1]), blo(xq[1])}, x1 = {bhi(xq[0]), blo(xq[0])};
    const f32x2 q0 = {blo(qv[i][0]), bhi(qv[i][0])}, q1 = {blo(qv[i][1]), bhi(qv[i][1])};
    const f32x2 z0 = x0 * dd + f32x2{fin[i][0], fin[i][1]}, z1 = x1 * dd + f32x2{fin[i][2], fin[i][3]};
    const f32x4 a4 = __builtin_bit_cast(f32x4, sa[i]), b4 = __builtin_bit_cast(f32x4, sbv[i]);
    const f32x2 t0 = q0 * f32x2{a4[0], a4[1]} + f32x2{b4[0], b4[1]};
    const f32x2 t1 = q1 * f32x2{a4[2], a4[3]} + f32x2{b4[2], b4[3]};
    const f32x2 h0 = t0 * z0 + q0, h1 = t1 * z1 + q1;
    hv[i] = u32x2{pk2(h0[0], h0[1]), pk2(h1[0], h1[1])};
  }
#pragma unroll
  for (int i = 0; i < 4; i += 2) {
    auto a = __builtin_amdgcn_permlane16_swap(hv[i][0], hv[i + 1][0], false, false);
    auto bb = __builtin_amdgcn_permlane16_swap(hv[i][1], hv[i + 1][1], false, false);
    const int s = 256 * (4 * half + i + (g & 1)) + 16 * n + 4 * (g & 2);
    st16_wt(wr, (u32)WS_HT + ((ch0 + c) * S + s) * 2, u32x4{a[0], bb[0], a[1], bb[1]});
  }
  STAMP(1, 3);
}

constexpr int KB_NL = 39;
constexpr int KB_ST = KB_NL * 4096;
constexpr int KB_LDS = KB_ST + 1024;
#define EPI_FILL 8
constexpr int KB_RING = 8;

#define P2_AHEAD 5
#define P2_PRO 3
constexpr int p2_upto(int ks) {
  const int ramp = P2_PRO - 1 + 2 * (ks + 1), steady = ks + P2_AHEAD;
  const int u = ks < 0 ? P2_PRO - 1 : (ramp < steady ? ramp : steady);
  return u > 15 ? 15 : u;
}
#define P2_XMAX 5
constexpr int P2_NEXTRA = 23;
constexpr int p2_extra(int ks) {
  const int tail0 = 16 - P2_AHEAD, rest = P2_NEXTRA - P2_AHEAD * P2_XMAX;
  if (ks >= tail0) return P2_XMAX;
  return (rest > 0 && ks >= tail0 - rest) ? 1 : 0;
}
constexpr int p2_extra_first(int ks) { int c = 0; for (int s = 0; s < ks; ++s) c += p2_extra(s); return c; }
constexpr int p2_extra_real(int ks) { int f = p2_extra_first(ks), n = p2_extra(ks); return f >= P2_NEXTRA ? 0 : (f + n > P2_NEXTRA ? P2_NEXTRA - f : n); }
static_assert(p2_extra_first(16) >= P2_NEXTRA, "all Wd operations are scheduled");
constexpr int p2_issue_step(int t) { for (int s = -1; s < KS1; ++s) if (p2_upto(s) >= t) return s; return KS1; }
constexpr int p2_younger(int t, int ks, int ops) {
  int c = (p2_upto(ks - 1) - t) * ops;
  for (int s = p2_issue_step(t); s < ks; ++s) if (s >= 0) c += p2_extra_real(s);
  return c;
}

struct KbCtx {
  u32 sb, gv, lv, lb0, lwd, csv;
  const char *hb0, *hb1, *w0, *w1, *w2, *cs0, *cs1, *cs2, *wd, *wdr;
};

template <int GRAM, class Setup>
DI void kb_phase2(const KbCtx& c, f32x4 (&acc)[3][8], f32x4 (&rsum)[2], f32x4 (&csp)[3], u32x4 (&Wr)[4][4], u32 (&ra)[8], Setup&& setup, char* ws_stamp) {
  constexpr int OPS = GRAM ? 4 : 5;
  u32x4 W[KB_RING][3];
  u32x2 Bl[2][8], Bh[2][8];
  auto issue = [&](auto KS, auto O) {
    constexpr int ks = KS, o = O;
    if constexpr (o == 0) glds16<ks * 8192>(c.gv, c.hb0 + (size_t)ks * 131072, c.lb0);
    if constexpr (o == 1) gld_a<(ks & 3) * 1024>(W[ks % KB_RING][0], c.lv, c.w0 + (ks >> 2) * 4096);
    if constexpr (o == 2) gld_a<(ks & 3) * 1024>(W[ks % KB_RING][1], c.lv, c.w1 + (ks >> 2) * 4096);
    if constexpr (o == 3 && !GRAM) gld_a<(ks & 3) * 1024>(W[ks % KB_RING][2], c.lv, c.w2 + (ks >> 2) * 4096);
    if constexpr (o == 4) glds16<ks * 8192 + 1024>(c.gv, c.hb1 + (size_t)ks * 131072, c.lb0);
  };
  auto extra = [&](auto E) {
    constexpr int e = E;
    if constexpr (e < 7) glds16<(32 + e) * 4096>(c.lv, c.wd + (size_t)(32 + e) * 4096, c.lwd);
    else if constexpr (e < P2_NEXTRA) gld_a<((e - 7) & 3) * 1024>(Wr[(e - 7) >> 2][(e - 7) & 3], c.lv, c.wdr + ((e - 7) >> 2) * 4096);
  };
  auto rdB = [&](auto KS, auto R) {
    constexpr int ks = KS, r = R, i = r >> 1;
    if constexpr ((r & 1) == 0) ds_tr<(ks & 7) * 8192>(Bl[ks & 1][i], ra[i] + (ks >= 8 ? 65536 : 0));
    else ds_tr<(ks & 7) * 8192 + 1024>(Bh[ks & 1][i], ra[i] + (ks >= 8 ? 65536 : 0));
  };
  u32x4 c0, c1, c2;
  static_for<5>([&](auto O) { issue(std::integral_constant<int, 0>{}, O); });
  gld_v<0>(c0, c.csv, c.cs0); gld_v<0>(c1, c.csv, c.cs1); gld_v<0>(c2, c.csv, c.cs2);
  static_for<p2_upto(-1)>([&](auto KS) { static_for<5>([&](auto O) { issue(std::integral_constant<int, KS + 1>{}, O); }); });
  setup();
  u32x4 ones = {0x3f803f80u, 0x3f803f80u, 0x3f803f80u, 0x3f803f80u};
  pin(ones);
  wait_vm<p2_upto(-1) * OPS>();
  barrier();
  static_for<16>([&](auto R) { rdB(std::integral_constant<int, 0>{}, R); });
  asm volatile("" : "+v"(c0), "+v"(c1), "+v"(c2));
  float cv[12];
#pragma unroll
  for (int e = 0; e < 4; ++e) { cv[e] = __uint_as_float(c0[e]); cv[4 + e] = __uint_as_float(c1[e]); cv[8 + e] = __uint_as_float(c2[e]); }
  static_for<KS1>([&](auto KS) {
    constexpr int ks = KS, buf = ks & 1, slot = ks % KB_RING;
    constexpr int nmain = (p2_upto(ks) - p2_upto(ks - 1)) * 5, next = p2_extra_real(ks);
    wait_lgkm<0>();
    u32x4 B[8];
#pragma unroll
    for (int i = 0; i < 8; ++i) { pin(Bl[buf][i]); pin(Bh[buf][i]); }
#pragma unroll
    for (int i = 0; i < 8; ++i) B[i] = cat(Bl[buf][i], Bh[buf][i]);
    pin_a(W[slot][0]); pin_a(W[slot][1]);
    if constexpr (!GRAM) pin_a(W[slot][2]);
    static_for<26>([&](auto M) {
      constexpr int m = M, i = m / 3, u = m % 3;
      if constexpr (m >= 24) mfma_vv<ks == 0>(rsum[m - 24], ones, B[m - 24]);
      else if constexpr (u < 2 || !GRAM) mfma_av<ks == 0>(acc[u][i], W[slot][u], B[i]);
      else mfma_vv<ks == 0>(acc[2][i], B[i], B[i]);
      if constexpr (m == 1 && ks + 1 < KS1) { wait_vm<p2_younger(ks + 1, ks, OPS)>(); barrier(); }
      if constexpr (m >= 2 && m < 18 && ks + 1 < KS1) rdB(std::integral_constant<int, ks + 1>{}, std::integral_constant<int, m - 2>{});
      if constexpr (ks >= 9 && ks < 13 && m >= 2 && m < 14) dpp_stage<ks - 9>(cv[m - 2]);
      constexpr int v = m - (26 - nmain - next);
      if constexpr (v >= 0 && v < nmain) issue(std::integral_constant<int, p2_upto(ks - 1) + 1 + v / 5>{}, std::integral_constant<int, v % 5>{});
      if constexpr (v >= nmain) extra(std::integral_constant<int, p2_extra_first(ks) + v - nmain>{});
    });
  });
  mfma_fence(acc[0][0], acc[1][0], rsum[0], rsum[1]);
#pragma unroll
  for (int i = 0; i < 8; ++i) after_fence(acc[0][i], acc[1][i], acc[2][i], rsum[i & 1]);
#pragma unroll
  for (int u = 0; u < 3; ++u) csp[u] = f32x4{cv[4 * u], cv[4 * u + 1], cv[4 * u + 2], cv[4 * u + 3]};
}

__global__ __launch_bounds__(256) void kb_mlp(char* __restrict__ ws, u16* __restrict__ out) {
  extern __shared__ __attribute__((aligned(16))) char smem[];
  const int t = threadIdx.x, lane = t & 63;
  const int w = __builtin_amdgcn_readfirstlane(t >> 6);
  const int b = blockIdx.x, xcd = b & 7, bi = b >> 3;
#define KB_MAP 1
  const int rt = KB_MAP == 0 ? 2 * xcd + (bi & 1) : (xcd >> 1) * 4 + (bi & 3);
  const int j = KB_MAP == 0 ? bi >> 1 : (xcd & 1) * 8 + (bi >> 2);
  const int half = KB_MAP == 0 ? j & 1 : j >> 3, dt = KB_MAP == 0 ? j >> 1 : j & 7;
  const int U0 = slice_u0(j), nu = slice_nu(j);
  const int rem = nu - 8;
  const int nw = w < rem ? 3 : 2;
  const int uw = U0 + (w < rem ? 3 * w : 3 * rem + 2 * (w - rem));
  const int u2 = uw + (nw == 3 ? 2 : 0);
  const int n = lane & 15, g = lane >> 4;
  const int s0 = 128 * rt;
  char* ws_stamp = ws; (void)ws_stamp;
  STAMP(2, 0);
  auto sf = [&](int i) { return ((w + (i >> 1)) & 3) + 4 * (half ^ (i & 1)); };

  KbCtx c;
  c.sb = (u32)(size_t)(LDSP char*)smem;
  c.lv = lane * 16;
  c.gv = (u32)(g * 4096 + (((n >> 1) ^ (g | ((w & 1) << 2))) * 32 + (n & 1) * 16));
  c.hb0 = ws + WS_HT + (size_t)s0 * 2 + (size_t)(8 * w) * 4096;
  c.hb1 = c.hb0 + 4 * 4096;
  c.lb0 = c.sb + 8 * w * 256;
  c.w0 = ws + WS_W1 + (size_t)uw * KS1 * 1024;
  c.w1 = c.w0 + KS1 * 1024;
  c.w2 = ws + WS_W1 + (size_t)u2 * KS1 * 1024;
  c.cs0 = ws + WS_CS + (size_t)uw * 64;
  c.cs1 = c.cs0 + 64;
  c.cs2 = ws + WS_CS + (size_t)u2 * 64;
  c.csv = n * (NU * 64) + g * 16;
  c.wdr = ws + WS_WD + (size_t)dt * KS2 * 4096 + (size_t)KB_NL * 4096;
  c.wd = ws + WS_WD + (size_t)dt * KS2 * 4096 + (size_t)w * 1024;
  c.lwd = c.sb + 1024 * w;
  u32 ra[8], hoff[6];
  auto setup = [&]() {
    const int qd = n >> 2, p = n & 3;
    const int frow = qd | ((g & 1) << 2);
#pragma unroll
    for (int i = 0; i < 8; ++i) { ra[i] = c.sb + (8 * g + qd) * 256 + ((sf(i) ^ frow) * 32) + 8 * p; asm volatile("" : "+v"(ra[i])); }
#pragma unroll
    for (int tt = 0; tt < 6; ++tt) {
      const int q = 4 * tt + g, i = (q * 11) >> 5, u = q - 3 * i, U = uw + u;
      const u32 o = (u32)(((8 * rt + sf(i)) * KS2 + (U >> 2)) * 1024 + ((U & 3) * 16 + n) * 16);
      hoff[tt] = u < nw ? o : 0xfffffff0u;
      asm volatile("" : "+v"(hoff[tt]));
    }
  };
  f32x4 acc[3][8], cs[3], rsum[2];
  u32x4 Wr[4][4];
  if (w == 3) kb_phase2<1>(c, acc, rsum, cs, Wr, ra, setup, ws_stamp);
  else kb_phase2<0>(c, acc, rsum, cs, Wr, ra, setup, ws_stamp);
  STAMP(2, 2);

  u32x2 res[4];
  {
    const u32 rb = ra[0] + dt * 16384;
    ds_tr<0>(res[0], rb); ds_tr<1024>(res[1], rb); ds_tr<8192>(res[2], rb); ds_tr<9216>(res[3], rb);
  }
  float* stl = (float*)(smem + KB_ST);
  if (g == 0) {
#pragma unroll
    for (int i = 0; i < 2; ++i) stl[2 * (16 * sf(i) + n)] = rsum[i][0];
  }
  if (w == 3 && g == (n >> 2)) {
#pragma unroll
    for (int i = 0; i < 8; ++i) {
      f32x4 a = acc[2][i];
      float dg = (n & 2) ? ((n & 1) ? a[3] : a[2]) : ((n & 1) ? a[1] : a[0]);
      stl[2 * (16 * sf(i) + n) + 1] = dg;
    }
  }
  asm volatile("s_waitcnt lgkmcnt(0)\n\ts_barrier" : "+v"(res[0]), "+v"(res[1]), "+v"(res[2]), "+v"(res[3]) :: "memory");
  STAMP(2, 3);
  const __amdgpu_buffer_rsrc_t hrs = __builtin_amdgcn_make_buffer_rsrc(ws + WS_H, 0, (S / 16) * KS2 * 1024, 0x00020000);
  f32x2 sv[8];
#pragma unroll
  for (int i = 0; i < 8; ++i) sv[i] = *(const f32x2*)(stl + 2 * (16 * sf(i) + n));
  u32 hv[24];
  static_for<8>([&](auto I) {
    constexpr int i = I;
    const float mean = sv[i][0] * (1.f / D);
    const float var = fmaxf(sv[i][1] * (1.f / D) - mean * mean, 0.f) + EPS;
    const float rstd = __builtin_amdgcn_rsqf(var);
    const float rinv = var * rstd;
    const float ce = -1.4426950408889634f * rstd;
    const f32x2 nm = {-mean, -mean}, ce2 = {ce, ce}, ri2 = {rinv, rinv};
    static_for<3>([&](auto U_) {
      constexpr int u = U_;
      pin4(acc[u][i]);
      constexpr int slot = 3 * i + u;
      constexpr int wk = slot * EPI_FILL / 24;
      if constexpr (EPI_FILL > 0 && (slot + 1) * EPI_FILL / 24 > wk) glds16<wk * 4096>(c.lv, c.wd + (size_t)wk * 4096, c.lwd);
      const f32x4 a = acc[u][i];
      const f32x2 tg = (f32x2{a[0], a[1]} + nm * f32x2{cs[u][0], cs[u][1]}) * ce2;
      const f32x2 tu = f32x2{a[2], a[3]} + nm * f32x2{cs[u][2], cs[u][3]};
      const f32x2 e = {__builtin_amdgcn_exp2f(tg[0]), __builtin_amdgcn_exp2f(tg[1])};
      const f32x2 den = e * ri2 + ri2;
      const f32x2 o = tu * f32x2{__builtin_amdgcn_rcpf(den[0]), __builtin_amdgcn_rcpf(den[1])};
      hv[3 * i + u] = pk2(o[0], o[1]);
    });
    static_for<6>([&](auto T) {
      constexpr int tt = T;
      if constexpr ((4 * tt + 3) / 3 == i) {
        auto s02 = __builtin_amdgcn_permlane32_swap(hv[4 * tt], hv[4 * tt + 2], false, false);
        auto s13 = __builtin_amdgcn_permlane32_swap(hv[4 * tt + 1], hv[4 * tt + 3], false, false);
        auto t01 = __builtin_amdgcn_permlane16_swap(s02[0], s13[0], false, false);
        auto t23 = __builtin_amdgcn_permlane16_swap(s02[1], s13[1], false, false);
        __builtin_amdgcn_raw_buffer_store_b128(u32x4{t01[0], t01[1], t23[0], t23[1]}, hrs, hoff[tt], 0, 16);
      }
    });
  });
  STAMP(2, 4);
  u32* cnt = (u32*)(ws + WS_CNT + rt * 128);
  asm volatile("s_waitcnt vmcnt(0)\n\ts_barrier" ::: "memory");
  STAMP(2, 5);
  if (w == 0) {
    if (lane == 0) __hip_atomic_fetch_add(cnt, 1u, __ATOMIC_RELAXED, __HIP_MEMORY_SCOPE_AGENT);
    for (int spins = 0; spins < SPIN_MAX; ++spins) {
      const u32 v = __builtin_amdgcn_readfirstlane(__hip_atomic_load(cnt, __ATOMIC_RELAXED, __HIP_MEMORY_SCOPE_AGENT));
      if (v >= 16u) break;
      if (KB_POLL_SLEEP > 0) __builtin_amdgcn_s_sleep(KB_POLL_SLEEP);
    }
    wait_vm<0>();
  } else {
    const char* wsrc = c.wd - (size_t)w * 1024;
    for (int e = EPI_FILL + w - 1; e < 32; e += 3) {
      const char* src = wsrc + (size_t)e * 4096;
      const u32 dst = c.sb + e * 4096;
      glds16<0>(c.lv, src, dst); glds16<1024>(c.lv, src + 1024, dst); glds16<2048>(c.lv, src + 2048, dst); glds16<3072>(c.lv, src + 3072, dst);
    }
  }
  barrier();
  STAMP(2, 6);

  const char* hb = ws + WS_H + (size_t)(8 * rt + 4 * half + w) * KS2 * 1024;
  u32x4 Hf[KS2];
  constexpr int P3_AHEAD = P3_AHEAD_;
  auto ldH = [&](auto KS) { constexpr int ks = KS; if constexpr (ks < KS2) gld_a_sc1<(ks & 3) * 1024>(Hf[ks], c.lv, hb + (ks >> 2) * 4096); };
  static_for<P3_AHEAD>([&](auto KS) { ldH(KS); });
  STAMP(3, 0);
  f32x4 o4[4];
  u32x4 Af[3][4];
  const u32 la0 = c.sb + lane * 16, la1 = la0 + 65536, la2 = la0 + 131072;
  auto rdA = [&](auto KS, auto DF) {
    constexpr int ks = KS, df = DF;
    if constexpr (ks < KB_NL) ds_b128<(ks & 15) * 4096 + df * 1024>(Af[ks % 3][df], ks < 16 ? la0 : ks < 32 ? la1 : la2);
  };
  wait_vm<P3_AHEAD>();
  barrier();
  static_for<4>([&](auto DF) { rdA(std::integral_constant<int, 0>{}, DF); });
  static_for<4>([&](auto DF) { rdA(std::integral_constant<int, 1>{}, DF); });
  static_for<KS2>([&](auto KS) {
    constexpr int ks = KS;
    wait_vm<(ks + P3_AHEAD <= KS2 ? P3_AHEAD - 1 : KS2 - 1 - ks)>();
    if constexpr (ks + 1 < KB_NL) wait_lgkm<4>(); else wait_lgkm<0>();
    pin_a(Hf[ks]);
    static_for<4>([&](auto DF) {
      constexpr int df = DF;
      if constexpr (ks < KB_NL) { pin(Af[ks % 3][df]); mfma_va<ks == 0>(o4[df], Af[ks % 3][df], Hf[ks]); }
      else { pin_a(Wr[ks - KB_NL][df]); mfma_aa(o4[df], Wr[ks - KB_NL][df], Hf[ks]); }
      rdA(std::integral_constant<int, ks + 2>{}, DF);
      if constexpr (df == 1 && ks + P3_AHEAD < KS2) ldH(std::integral_constant<int, ks + P3_AHEAD>{});
    });
  });
  mfma_fence(o4[0], o4[1], o4[2], o4[3]);
  u32x4 o0, o1;
#pragma unroll
  for (int df = 0; df < 2; ++df) {
    o0[2 * df] = pk2(o4[df][0] + blo(res[df][0]), o4[df][1] + bhi(res[df][0]));
    o0[2 * df + 1] = pk2(o4[df][2] + blo(res[df][1]), o4[df][3] + bhi(res[df][1]));
    o1[2 * df] = pk2(o4[2 + df][0] + blo(res[2 + df][0]), o4[2 + df][1] + bhi(res[2 + df][0]));
    o1[2 * df + 1] = pk2(o4[2 + df][2] + blo(res[2 + df][1]), o4[2 + df][3] + bhi(res[2 + df][1]));
  }
  const __amdgpu_buffer_rsrc_t ors = __builtin_amdgcn_make_buffer_rsrc(out, 0, S * D * 2, 0x00020000);
  const u32 ooff = (u32)((s0 + 64 * half + 16 * w + n) * D + 64 * dt + 8 * g) * 2;
  __builtin_amdgcn_raw_buffer_store_b128(o0, ors, ooff, 0, 16);
  __builtin_amdgcn_raw_buffer_store_b128(o1, ors, ooff + 64, 0, 16);
  asm volatile("kb_code_end:");
  STAMP(2, 7);
}

extern "C" void kernel_launch(void* const* d_in, const int* in_sizes, int n_in, void* d_out, int out_size, void* d_ws,
                              size_t ws_size, hipStream_t stream) {
  const u16 *q = (const u16*)d_in[0], *k = (const u16*)d_in[1], *v = (const u16*)d_in[2], *shk = (const u16*)d_in[3];
  const u16 *ssmk = (const u16*)d_in[4], *dskip = (const u16*)d_in[5];
  const u16 *wg = (const u16*)d_in[7], *wu = (const u16*)d_in[8], *wd = (const u16*)d_in[9];
  char* ws = (char*)d_ws;
  (void)hipFuncSetAttribute((const void*)ka_conv, hipFuncAttributeMaxDynamicSharedMemorySize, KA_LDS);
  (void)hipFuncSetAttribute((const void*)kb_mlp, hipFuncAttributeMaxDynamicSharedMemorySize, KB_LDS);
  ka_conv<<<512, 256, KA_LDS, stream>>>(q, k, v, shk, ssmk, dskip, wg, wu, wd, ws);
  kb_mlp<<<256, 256, KB_LDS, stream>>>(ws, (u16*)d_out);
}
```

```cpp
#include <hip/hip_runtime.h>
#include <stdint.h>
#include <utility>

typedef unsigned short u16;
typedef unsigned u32;
typedef __attribute__((ext_vector_type(2))) u32 u32x2;
typedef __attribute__((ext_vector_type(4))) u32 u32x4;
typedef __attribute__((ext_vector_type(2))) float f32x2;
typedef __attribute__((ext_vector_type(4))) float f32x4;
typedef __attribute__((ext_vector_type(2))) __bf16 bf16x2;
typedef __attribute__((ext_vector_type(8))) __bf16 bf16x8;
typedef __attribute__((ext_vector_type(4))) short s16x4;
#define LDSP __attribute__((address_space(3)))
#define DI __device__ __forceinline__

constexpr int S = 2048, D = 512, F = 1376;
constexpr int NU = F / 8;
constexpr int KS1 = D / 32;
constexpr int KS2 = F / 32;
constexpr float EPS = 1e-5f;

constexpr size_t WS_XR = 0;
constexpr size_t WS_QT = WS_XR + (size_t)D * S * 2;
constexpr size_t WS_HT = WS_QT + (size_t)D * S * 2;
constexpr size_t WS_W1 = WS_HT + (size_t)D * S * 2;
constexpr size_t WS_WD = WS_W1 + (size_t)NU * KS1 * 1024;
constexpr size_t WS_H = WS_WD + (size_t)8 * KS2 * 4 * 1024;
constexpr size_t WS_CS = WS_H + (size_t)(S / 16) * KS2 * 1024;
constexpr size_t WS_SA = WS_CS + (size_t)KS1 * NU * 16 * 4;
constexpr size_t WS_SB = WS_SA + (size_t)S * 4;
constexpr size_t WS_CNT = WS_SB + (size_t)S * 4;
constexpr size_t WS_FLA = WS_CNT + 48 * 128;
constexpr size_t WS_FLG = WS_FLA + 256 * 8;
constexpr size_t WS_FLS = WS_FLG + 256 * 8;
constexpr size_t WS_FLZ = WS_FLS + 256 * 8;
constexpr size_t WS_HNT = WS_FLZ + 8 * 128;
constexpr size_t WS_EPO = WS_HNT + 256 * 8;

template <int... Is, class Fn> DI void static_for_(std::integer_sequence<int, Is...>, Fn&& f) { (f(std::integral_constant<int, Is>{}), ...); }
template <int N, class Fn> DI void static_for(Fn&& f) { static_for_(std::make_integer_sequence<int, N>{}, f); }

DI float blo(u32 u) { return __uint_as_float(u << 16); }
DI float bhi(u32 u) { return __uint_as_float(u & 0xffff0000u); }
DI u32 pk2(float a, float b) { f32x2 t = {a, b}; return __builtin_bit_cast(u32, __builtin_convertvector(t, bf16x2)); }
DI f32x4 mfma16(u32x4 a, u32x4 b, f32x4 c) {
  return __builtin_amdgcn_mfma_f32_16x16x32_bf16(__builtin_bit_cast(bf16x8, a), __builtin_bit_cast(bf16x8, b), c, 0, 0, 0);
}
DI u32x2 tr_read(u32 lds_byte) {
  return __builtin_bit_cast(u32x2, __builtin_amdgcn_ds_read_tr16_b64_v4i16((LDSP s16x4*)(size_t)lds_byte));
}
DI u32x4 tr_frag(u32 a0, u32 a1) { u32x2 x = tr_read(a0), y = tr_read(a1); return u32x4{x[0], x[1], y[0], y[1]}; }
DI u32x4 cat(u32x2 x, u32x2 y) { return __builtin_shufflevector(x, y, 0, 1, 2, 3); }
template <int CTRL> DI float dpp_add(float v) {
  return v + __builtin_bit_cast(float, __builtin_amdgcn_update_dpp(0, __builtin_bit_cast(int, v), CTRL, 0xf, 0xf, true));
}
DI float sum_rows(float v) {
  u32 u = __float_as_uint(v);
  auto a = __builtin_amdgcn_permlane16_swap(u, u, false, false);
  v = __uint_as_float(a[0]) + __uint_as_float(a[1]);
  u = __float_as_uint(v);
  auto b = __builtin_amdgcn_permlane32_swap(u, u, false, false);
  return __uint_as_float(b[0]) + __uint_as_float(b[1]);
}
DI float wave_sum(float v) {
  v = dpp_add<0xb1>(v);
  v = dpp_add<0x4e>(v);
  v = dpp_add<0x124>(v);
  v = dpp_add<0x128>(v);
  return sum_rows(v);
}
DI __amdgpu_buffer_rsrc_t ws_rsrc(char* ws) { return __builtin_amdgcn_make_buffer_rsrc(ws, 0, 0x04000000, 0x00020000); }
DI void st16_wt(__amdgpu_buffer_rsrc_t r, u32 off, u32x4 v) { __builtin_amdgcn_raw_buffer_store_b128(v, r, off, 0, 16); }
DI int unit_pos(int j, int w, int u) { return u < 2 ? 64 * u + 4 * j + w : 128 + (j < 12 ? 3 * j + w : 36 + 2 * (j - 12) + w); }
DI int slice_u0(int j) { return j < 12 ? 11 * j : 132 + 10 * (j - 12); }
DI int slice_nu(int j) { return j < 12 ? 11 : 10; }
DI int wave_u0(int j, int w) { const int rem = slice_nu(j) - 8; return slice_u0(j) + (w < rem ? 3 * w : 3 * rem + 2 * (w - rem)); }
DI int pos_unit(int p) {
  if (p < 128) return wave_u0((p & 63) >> 2, p & 3) + (p >> 6);
  const int i = p - 128;
  return (i < 36 ? wave_u0(i / 3, i % 3) : wave_u0(12 + ((i - 36) >> 1), (i - 36) & 1)) + 2;
}

template <bool FIRST = false> DI void mfma_vv(f32x4& acc, const u32x4& a, const u32x4& b) {
  if constexpr (FIRST) asm volatile("v_mfma_f32_16x16x32_bf16 %0, %1, %2, 0" : "=&v"(acc) : "v"(a), "v"(b));
  else asm volatile("v_mfma_f32_16x16x32_bf16 %0, %1, %2, %0" : "+v"(acc) : "v"(a), "v"(b));
}
template <bool FIRST = false> DI void mfma_av(f32x4& acc, const u32x4& a, const u32x4& b) {
  if constexpr (FIRST) asm volatile("v_mfma_f32_16x16x32_bf16 %0, %1, %2, 0" : "=&v"(acc) : "a"(a), "v"(b));
  else asm volatile("v_mfma_f32_16x16x32_bf16 %0, %1, %2, %0" : "+v"(acc) : "a"(a), "v"(b));
}
template <int OFF> DI void ds_b64(u32x2& d, u32 addr) { asm volatile("ds_read_b64 %0, %1 offset:%c2" : "=v"(d) : "v"(addr), "i"(OFF) : "memory"); }
template <int OFF> DI void ds_b128(u32x4& d, u32 addr) { asm volatile("ds_read_b128 %0, %1 offset:%c2" : "=v"(d) : "v"(addr), "i"(OFF) : "memory"); }
template <int OFF> DI void ds_tr(u32x2& d, u32 addr) { asm volatile("ds_read_b64_tr_b16 %0, %1 offset:%c2" : "=v"(d) : "v"(addr), "i"(OFF) : "memory"); }
template <int OFF> DI void gld_a(u32x4& d, u32 voff, const void* sbase) {
  asm volatile("global_load_dwordx4 %0, %1, %2 offset:%c3" : "=a"(d) : "v"(voff), "s"(sbase), "i"(OFF) : "memory");
}
template <int OFF> DI void gld_v(u32x4& d, u32 voff, const void* sbase) {
  asm volatile("global_load_dwordx4 %0, %1, %2 offset:%c3" : "=v"(d) : "v"(voff), "s"(sbase), "i"(OFF) : "memory");
}
template <int OFF> DI void gld2_v(u32x2& d, u32 voff, const void* sbase) {
  asm volatile("global_load_dwordx2 %0, %1, %2 offset:%c3" : "=v"(d) : "v"(voff), "s"(sbase), "i"(OFF) : "memory");
}
template <int LOFF> DI void glds16(u32 voff, const void* sbase, u32 ldsbase) {
  asm volatile("s_add_u32 m0, %2, %c3\n\ts_nop 0\n\tglobal_load_lds_dwordx4 %0, %1" ::"v"(voff), "s"(sbase), "s"(ldsbase), "i"(LOFF) : "memory", "scc");
}
template <int N> DI void wait_vm() { asm volatile("s_waitcnt vmcnt(%c0)" ::"i"(N) : "memory"); }
template <int N> DI void wait_lgkm() { asm volatile("s_waitcnt lgkmcnt(%c0)" ::"i"(N) : "memory"); }
template <int OFF> DI void gld_a_sc1(u32x4& d, u32 voff, const void* sbase) {
  asm volatile("global_load_dwordx4 %0, %1, %2 offset:%c3 sc1" : "=a"(d) : "v"(voff), "s"(sbase), "i"(OFF) : "memory");
}
DI void mfma_aa(f32x4& acc, const u32x4& a, const u32x4& b) {
  asm volatile("v_mfma_f32_16x16x32_bf16 %0, %1, %2, %0" : "+v"(acc) : "a"(a), "a"(b));
}
template <bool FIRST = false> DI void mfma_va(f32x4& acc, const u32x4& a, const u32x4& b) {
  if constexpr (FIRST) asm volatile("v_mfma_f32_16x16x32_bf16 %0, %1, %2, 0" : "=&v"(acc) : "v"(a), "a"(b));
  else asm volatile("v_mfma_f32_16x16x32_bf16 %0, %1, %2, %0" : "+v"(acc) : "v"(a), "a"(b));
}
DI void mfma_fence(f32x4& a, f32x4& b, f32x4& c, f32x4& d) { asm volatile("s_nop 7\n\ts_nop 1" : "+v"(a), "+v"(b), "+v"(c), "+v"(d)); }
DI void after_fence(f32x4& a, f32x4& b, f32x4& c, f32x4& d) { asm volatile("" : "+v"(a), "+v"(b), "+v"(c), "+v"(d)); }
DI void after_fence(f32x4& a, f32x4& b, f32x4& c) { asm volatile("" : "+v"(a), "+v"(b), "+v"(c)); }
template <int STAGE> DI void dpp_stage(float& v) {
  if constexpr (STAGE == 0) asm volatile("v_add_f32_dpp %0, %0, %0 quad_perm:[1,0,3,2] row_mask:0xf bank_mask:0xf" : "+v"(v));
  if constexpr (STAGE == 1) asm volatile("v_add_f32_dpp %0, %0, %0 quad_perm:[2,3,0,1] row_mask:0xf bank_mask:0xf" : "+v"(v));
  if constexpr (STAGE == 2) asm volatile("v_add_f32_dpp %0, %0, %0 row_ror:4 row_mask:0xf bank_mask:0xf" : "+v"(v));
  if constexpr (STAGE == 3) asm volatile("v_add_f32_dpp %0, %0, %0 row_ror:8 row_mask:0xf bank_mask:0xf" : "+v"(v));
}
DI void barrier() { asm volatile("s_barrier" ::: "memory"); }
DI void pin(u32x4& x) { asm volatile("" : "+v"(x)); }
DI void pin(u32x2& x) { asm volatile("" : "+v"(x)); }
DI void pin4(f32x4& x) { asm volatile("" : "+v"(x)); }
DI void pin_a(u32x4& x) { asm volatile("" : "+a"(x)); }

#define STAMP(kern, idx) do {} while (0)

template <class Gate>
DI void w_job(int b, const u16* __restrict__ wg, const u16* __restrict__ wu, const u16* __restrict__ wd, char* __restrict__ ws, char* smem, Gate&& gate) {
  const int t = threadIdx.x, lane = t & 63, w = t >> 6;
  char* tile = smem;
  char* tile2 = smem + 32 * 368;
  const u32 tb = (u32)(size_t)(LDSP char*)smem;
  const int i16 = lane & 15, qd = i16 >> 2, p = i16 & 3, g = lane >> 4;
  const int ks = b & 15, j = b >> 4;
  const int U0 = slice_u0(j), nu = slice_nu(j);
  u32x4 g4[2], u4[2], d4[2];
#pragma unroll
  for (int rep = 0; rep < 2; ++rep) {
    int idx = t + 256 * rep, r = idx / 11, u = idx - 11 * r;
    bool ok = r < 32 && u < nu;
    size_t off = ok ? (size_t)(32 * ks + r) * F + 8 * (U0 + u) : 0;
    g4[rep] = *(const u32x4*)(wg + off); u4[rep] = *(const u32x4*)(wu + off);
  }
  const int ks2 = b >> 2, dp = b & 3;
  if (b < KS2 * 4) {
#pragma unroll
    for (int rep = 0; rep < 2; ++rep) {
      int idx = t + 256 * rep, r = idx >> 4, c = idx & 15;
      d4[rep] = *(const u32x4*)(wd + (size_t)(8 * pos_unit(4 * ks2 + (r >> 3)) + (r & 7)) * D + 128 * dp + 8 * c);
    }
  }
#pragma unroll
  for (int rep = 0; rep < 2; ++rep) {
    int idx = t + 256 * rep, r = idx / 11, u = idx - 11 * r;
    if (r < 32 && u < nu) {
      *(u32x4*)(tile + r * 368 + u * 32) = u32x4{g4[rep][0], u4[rep][0], g4[rep][1], u4[rep][1]};
      *(u32x4*)(tile + r * 368 + u * 32 + 16) = u32x4{g4[rep][2], u4[rep][2], g4[rep][3], u4[rep][3]};
    }
  }
  if (b < KS2 * 4) {
#pragma unroll
    for (int rep = 0; rep < 2; ++rep) { int idx = t + 256 * rep, r = idx >> 4, c = idx & 15; *(u32x4*)(tile2 + r * 272 + c * 16) = d4[rep]; }
  }
  __syncthreads();
  gate();
  const u32x4 ones = {0x3f803f80u, 0x3f803f80u, 0x3f803f80u, 0x3f803f80u};
  const __amdgpu_buffer_rsrc_t wr = ws_rsrc(ws);
  for (int u = w; u < nu; u += 4) {
    u32 a = tb + (8 * g + qd) * 368 + u * 32 + 8 * p;
    u32x4 fr = tr_frag(a, a + 4 * 368);
    st16_wt(wr, (u32)WS_W1 + (((U0 + u) * KS1 + ks) * 64 + lane) * 16, fr);
    f32x4 cs = mfma16(fr, ones, f32x4{0.f, 0.f, 0.f, 0.f});
    if (i16 == 0) st16_wt(wr, (u32)WS_CS + ((ks * NU + U0 + u) * 16 + 4 * g) * 4, __builtin_bit_cast(u32x4, cs));
  }
  if (b < KS2 * 4) {
#pragma unroll
    for (int rep = 0; rep < 2; ++rep) {
      int fi = w + 4 * rep, dtl = fi >> 2, df = fi & 3;
      u32 a = tb + 32 * 368 + (8 * g + qd) * 272 + (64 * dtl + 32 * (df >> 1) + 8 * p + 4 * (df & 1)) * 2;
      u32x4 fr = tr_frag(a, a + 4 * 272);
      st16_wt(wr, (u32)WS_WD + ((((2 * dp + dtl) * KS2 + ks2) * 4 + df) * 64 + lane) * 16, fr);
    }
  }
}

constexpr int KA_CS = 4160;
constexpr int KA_KC = 4 * KA_CS;
constexpr int KA_XR = 2 * KA_KC;
constexpr int KA_RED = KA_XR + 2 * 4608;
constexpr int KA_LDS = KA_RED + 4 * 4096;
constexpr int KA_TK = KA_XR, KA_TV = KA_TK + 72 * 128, KA_TQ = KA_TV + 64 * 128;
static_assert(KA_TQ + 64 * 128 == KA_LDS, "tile overlay");
constexpr int SPIN_MAX = 1 << 22;
#define HINT_STEP 0
#define KB_POLL_SLEEP 1
#define KA_POLL_SLEEP 1
#define P3_SAMPLE_ 5
#define P3_AHEAD_ 11
#define HELP_PRE 16
#define HELP_SLEEP 20
#define CHK 2
#define CHK_M 2
#define WEARLY 0
#define HINT_TICKS 800ull

template <int OFF> DI void gld_v_sc1(u32x4& d, u32 voff, const void* sbase) {
  asm volatile("global_load_dwordx4 %0, %1, %2 offset:%c3 sc1" : "=v"(d) : "v"(voff), "s"(sbase), "i"(OFF) : "memory");
}
template <int OFF> DI void gld2_v_sc1(u32x2& d, u32 voff, const void* sbase) {
  asm volatile("global_load_dwordx2 %0, %1, %2 offset:%c3 sc1" : "=v"(d) : "v"(voff), "s"(sbase), "i"(OFF) : "memory");
}
DI bool tags_ok(const u32x4& f, const u32x2& tag) { return f[0] == tag[0] && f[1] == tag[1] && f[2] == tag[0] && f[3] == tag[1]; }

__global__ __launch_bounds__(256, 2) void ka_conv(const u16* __restrict__ q, const u16* __restrict__ k, const u16* __restrict__ v,
                                                  const u16* __restrict__ shk, const u16* __restrict__ ssmk, const u16* __restrict__ dskip,
                                                  const u16* __restrict__ wg, const u16* __restrict__ wu, const u16* __restrict__ wd,
                                                  char* __restrict__ ws) {
  extern __shared__ __attribute__((aligned(16))) char smem[];
  const int t = threadIdx.x, lane = t & 63;
  const int w = __builtin_amdgcn_readfirstlane(t >> 6);
  const int b = blockIdx.x;
  char* ws_stamp = ws; (void)ws_stamp;
  STAMP(0, 0);
  if (b >= 256) {
    const int hb = b - 256, srow = 8 * hb + 2 * w;
    const __amdgpu_buffer_rsrc_t wr = ws_rsrc(ws);
    u32x2 epo; u32x4 qa, qb;
    gld2_v_sc1<0>(epo, 0, ws + WS_EPO);
    if (HELP_PRE > 0) __builtin_amdgcn_s_sleep(HELP_PRE);
    gld_v<0>(qa, lane * 16, (const char*)q + (size_t)srow * 1024);
    gld_v<1024>(qb, lane * 16, (const char*)q + (size_t)srow * 1024);
    if (b < 264 && w == 1) {
      const char* pc; u32 len;
      asm volatile("s_getpc_b64 %0\n1:\n\ts_mov_b32 %1, kb_code_end-1b" : "=s"(pc), "=s"(len));
      const u32 lb = (u32)(size_t)(LDSP char*)smem + 32768;
      for (u32 o0 = 0; o0 < len; o0 += 64 * 128) {
        u32 o = o0 + lane * 128; o = o < len ? o : 0;
        asm volatile("s_mov_b32 m0, %2\n\ts_nop 0\n\tglobal_load_lds_dword %0, %1" ::"v"(o), "s"(pc), "s"(lb) : "memory");
      }
    }
    wait_vm<0>();
    asm volatile("" : "+v"(qa), "+v"(qb), "+v"(epo));
    const unsigned long long want = (((unsigned long long)epo[1] << 32) | epo[0]) + 1ull;
    {
      float sa = 0.f, sbb = 0.f;
#pragma unroll
      for (int e = 0; e < 4; ++e) { sa += blo(qa[e]) + bhi(qa[e]); sbb += blo(qb[e]) + bhi(qb[e]); }
      const float ma = wave_sum(sa) * (1.f / D), mb = wave_sum(sbb) * (1.f / D);
      float va = 0.f, vb = 0.f;
#pragma unroll
      for (int e = 0; e < 4; ++e) {
        float u0 = blo(qa[e]) - ma, u1 = bhi(qa[e]) - ma, u2 = blo(qb[e]) - mb, u3 = bhi(qb[e]) - mb;
        va += u0 * u0 + u1 * u1; vb += u2 * u2 + u3 * u3;
      }
      va = wave_sum(va) * (1.f / D); vb = wave_sum(vb) * (1.f / D);
      const float ra = __builtin_amdgcn_rsqf(va + EPS), rb = __builtin_amdgcn_rsqf(vb + EPS);
      const u32x2 val = lane == 0 ? u32x2{__float_as_uint(ra), __float_as_uint(rb)} : u32x2{__float_as_uint(-ma * ra), __float_as_uint(-mb * rb)};
      const u32 off = lane == 0 ? (u32)WS_SA + srow * 4 : lane == 1 ? (u32)WS_SB + srow * 4 : 0xfffffff0u;
      __builtin_amdgcn_raw_buffer_store_b64(val, wr, off, 0, 16);
    }
    asm volatile("s_waitcnt vmcnt(0)\n\ts_barrier" ::: "memory");
    if (t == 0) __builtin_amdgcn_raw_buffer_store_b64(u32x2{(u32)want, (u32)(want >> 32)}, wr, (u32)WS_FLS + hb * 8, 0, 16);
    STAMP(1, 6);
    if (hb == 1 && w == 1) {
      const unsigned long long* fs = (const unsigned long long*)(ws + WS_FLS) + lane;
      for (int spins = 0; spins < SPIN_MAX; ++spins) {
        bool ok = true;
#pragma unroll
        for (int i = 0; i < 4; ++i) ok &= __hip_atomic_load(fs + 64 * i, __ATOMIC_RELAXED, __HIP_MEMORY_SCOPE_AGENT) == want;
        if (__all(ok)) break;
        __builtin_amdgcn_s_sleep(1);
      }
      __builtin_amdgcn_raw_buffer_store_b64(u32x2{(u32)want, (u32)(want >> 32)}, wr, lane < 8 ? (u32)WS_FLZ + lane * 128 : 0xfffffff0u, 0, 16);
    } else if (HELP_SLEEP > 0) __builtin_amdgcn_s_sleep(HELP_SLEEP);
    w_job(hb, wg, wu, wd, ws, smem, [&]() {
      if (hb == 1 && w == 1) {
        const unsigned long long* fa = (const unsigned long long*)(ws + WS_FLA) + lane;
        const u32x2 tg = {(u32)want, (u32)(want >> 32)};
        for (int spins = 0; spins < SPIN_MAX; ++spins) {
          bool ok = true;
#pragma unroll
          for (int i = 0; i < 4; ++i) ok &= __hip_atomic_load(fa + 64 * i, __ATOMIC_RELAXED, __HIP_MEMORY_SCOPE_AGENT) == want;
          if (__all(ok)) break;
          __builtin_amdgcn_s_sleep(16);
        }
        if (lane == 0) __builtin_amdgcn_raw_buffer_store_b64(tg, wr, (u32)WS_EPO, 0, 16);
      }
      if (w == 0) {
        const unsigned long long* hp = (const unsigned long long*)(ws + WS_HNT) + hb;
        const unsigned long long t0 = __builtin_amdgcn_s_memrealtime();
        while (__hip_atomic_load(hp, __ATOMIC_RELAXED, __HIP_MEMORY_SCOPE_AGENT) != want && __builtin_amdgcn_s_memrealtime() - t0 < HINT_TICKS)
          __builtin_amdgcn_s_sleep(8);
      }
      __builtin_amdgcn_s_barrier();
      STAMP(1, 5);
    });
    STAMP(1, 4);
    return;
  }
  const int x = b & 7, bi = b >> 3, s0 = 64 * bi;
  const int ch0 = 64 * x + 2 * bi;
  const u32 sb = (u32)(size_t)(LDSP char*)smem;
  const int n = lane & 15, g = lane >> 4, qd = n >> 2, p = n & 3;
  const int d = 64 * x + 16 * w + n;
  const __amdgpu_buffer_rsrc_t wr = ws_rsrc(ws);

  u32x2 epo, sh; u32x4 kp[2], kc[2];
  gld2_v_sc1<0>(epo, 0, ws + WS_EPO);
  gld2_v<0>(sh, d * 8, shk);
  const int r8 = lane >> 3, c8 = lane & 7;
  auto voff = [&](int instr) { const int f = ((r8 >> 1) & 1) | ((instr & 1) << 1); return (u32)((((c8 >> 1) ^ f) * 2 + (c8 & 1)) * 16); };
  const size_t col = (size_t)x * 128;
  {
    int row = s0 - 8 + 8 * w + r8; row = row < 0 ? 0 : row;
    glds16<0>((u32)row * 1024 + voff(w), (const char*)k + col, sb + KA_TK + w * 1024);
    glds16<0>((u32)(s0 + 8 * w + r8) * 1024 + voff(w), (const char*)v + col, sb + KA_TV + w * 1024);
    glds16<0>((u32)(s0 - 8 + 8 * (w + 4) + r8) * 1024 + voff(w), (const char*)k + col, sb + KA_TK + (w + 4) * 1024);
    glds16<0>((u32)(s0 + 8 * (w + 4) + r8) * 1024 + voff(w), (const char*)v + col, sb + KA_TV + (w + 4) * 1024);
    if (w == 0) glds16<0>((u32)(s0 + 56 + r8) * 1024 + voff(8), (const char*)k + col, sb + KA_TK + 8 * 1024);
  }
#pragma unroll
  for (int cc = 0; cc < 2; ++cc) {
    const char* kr = (const char*)ssmk + (size_t)(ch0 + cc) * 4096;
    gld_v<0>(kp[cc], (u32)(t > 0 ? t - 1 : 0) * 16, kr);
    gld_v<0>(kc[cc], (u32)t * 16, kr);
  }
  glds16<0>((u32)(s0 + 8 * w + r8) * 1024 + voff(w), (const char*)q + col, sb + KA_TQ + w * 1024);
  glds16<0>((u32)(s0 + 8 * (w + 4) + r8) * 1024 + voff(w), (const char*)q + col, sb + KA_TQ + (w + 4) * 1024);
  const float dsk = blo(dskip[ch0 + (w >> 1)]);
  if (b == 0 && t < 48) *(u32*)(ws + WS_CNT + t * 128) = 0u;

  wait_vm<6>();
  barrier();
  asm volatile("" : "+v"(sh), "+v"(epo));
  STAMP(0, 1);
  u32x2 tag;
  { const unsigned long long e = (((unsigned long long)epo[1] << 32) | epo[0]) + 1ull; tag = u32x2{(u32)e, (u32)(e >> 32)}; }
  auto taddr = [&](int base, int tr) { const int f = ((tr >> 1) & 1) | ((tr >> 2) & 2); return sb + base + tr * 128 + ((w ^ f) * 32) + 8 * p; };
  {
    const float a0 = blo(sh[0]), a1 = bhi(sh[0]), a2 = blo(sh[1]), a3 = bhi(sh[1]);
#pragma unroll
    for (int e = 0; e < 2; ++e) {
      const int rg = g + 4 * e;
      u32x2 km = tr_read(taddr(KA_TK, 8 * rg + 4 + qd));
      const u32x2 k0 = tr_read(taddr(KA_TK, 8 * rg + 8 + qd)), k1 = tr_read(taddr(KA_TK, 8 * rg + 12 + qd));
      const u32x2 v0 = tr_read(taddr(KA_TV, 8 * rg + qd)), v1 = tr_read(taddr(KA_TV, 8 * rg + 4 + qd));
      if (s0 + rg == 0) km = u32x2{0, 0};
      const float kf[11] = {bhi(km[0]), blo(km[1]), bhi(km[1]), blo(k0[0]), bhi(k0[0]), blo(k0[1]), bhi(k0[1]), blo(k1[0]), bhi(k1[0]), blo(k1[1]), bhi(k1[1])};
      const float vf[8] = {blo(v0[0]), bhi(v0[0]), blo(v0[1]), bhi(v0[1]), blo(v1[0]), bhi(v1[0]), blo(v1[1]), bhi(v1[1])};
      float xo[8];
#pragma unroll
      for (int j = 0; j < 8; ++j) xo[j] = (kf[j + 3] * a0 + kf[j + 2] * a1 + kf[j + 1] * a2 + kf[j] * a3) * vf[j];
      st16_wt(wr, (u32)WS_XR + (d * S + (2040 - (s0 + 8 * rg))) * 2, u32x4{pk2(xo[7], xo[6]), pk2(xo[5], xo[4]), pk2(xo[3], xo[2]), pk2(xo[1], xo[0])});
    }
  }
  asm volatile("s_waitcnt vmcnt(0)\n\ts_barrier" : "+v"(kp[0]), "+v"(kc[0]), "+v"(kp[1]), "+v"(kc[1]) :: "memory");
  if (t == 0) __builtin_amdgcn_raw_buffer_store_b64(tag, wr, (u32)WS_FLA + (x * 32 + bi) * 8, 0, 16);
  STAMP(0, 2);
#pragma unroll
  for (int e = 0; e < 2; ++e) {
    const int rg = g + 4 * e;
    const u32x2 q0 = tr_read(taddr(KA_TQ, 8 * rg + qd)), q1 = tr_read(taddr(KA_TQ, 8 * rg + 4 + qd));
    st16_wt(wr, (u32)WS_QT + (d * S + s0 + 8 * rg) * 2, cat(q0, q1));
  }
#pragma unroll
  for (int cc = 0; cc < 2; ++cc) {
    const u32 m = t > 0 ? 0xffffffffu : 0u;
    u32 wv[6] = {kp[cc][0] & m, kp[cc][1] & m, kp[cc][2] & m, kp[cc][3] & m, kc[cc][0], kc[cc][1]};
    u32 o1[5];
#pragma unroll
    for (int i = 0; i < 5; ++i) o1[i] = __builtin_amdgcn_alignbit(wv[i + 1], wv[i], 16);
    char* base = smem + cc * KA_KC + (t + 1) * 16;
    *(u32x4*)(base + 0 * KA_CS) = u32x4{o1[0], o1[1], o1[2], o1[3]};
    *(u32x4*)(base + 1 * KA_CS) = u32x4{wv[1], wv[2], wv[3], wv[4]};
    *(u32x4*)(base + 2 * KA_CS) = u32x4{o1[1], o1[2], o1[3], o1[4]};
    *(u32x4*)(base + 3 * KA_CS) = u32x4{wv[2], wv[3], wv[4], wv[5]};
    if (t == 255) {
      const u32 z = 0;
      const u32 e0 = __builtin_amdgcn_alignbit(kc[cc][1], kc[cc][0], 16), e1 = __builtin_amdgcn_alignbit(kc[cc][2], kc[cc][1], 16);
      const u32 e2 = __builtin_amdgcn_alignbit(kc[cc][3], kc[cc][2], 16), e3 = __builtin_amdgcn_alignbit(z, kc[cc][3], 16);
      *(u32x4*)(base + 16 + 0 * KA_CS) = u32x4{e0, e1, e2, e3};
      *(u32x4*)(base + 16 + 1 * KA_CS) = u32x4{kc[cc][1], kc[cc][2], kc[cc][3], z};
      *(u32x4*)(base + 16 + 2 * KA_CS) = u32x4{e1, e2, e3, z};
      *(u32x4*)(base + 16 + 3 * KA_CS) = u32x4{kc[cc][2], kc[cc][3], z, z};
    }
  }
  if (t < 8) *(u32x4*)(smem + (t >> 2) * KA_KC + (t & 3) * KA_CS) = u32x4{0, 0, 0, 0};
  asm volatile("s_waitcnt vmcnt(0) lgkmcnt(0)\n\ts_barrier" ::: "memory");
  if (t == 0) {
    __builtin_amdgcn_raw_buffer_store_b64(tag, wr, (u32)WS_FLG + (x * 32 + bi) * 8, 0, 16);
  }
  if (t >= 64 && t < 64 + 60) { int i = t - 64; *(u32x4*)(smem + KA_XR + (i / 30) * 4608 + 4096 + (i % 30) * 16) = u32x4{0, 0, 0, 0}; }
  if (w == 0) {
    const unsigned long long* fp = (const unsigned long long*)(ws + WS_FLA) + x * 32 + (lane & 31);
    const unsigned long long want = ((unsigned long long)tag[1] << 32) | tag[0];
    for (int spins = 0; spins < SPIN_MAX; ++spins) {
      if (__all(__hip_atomic_load(fp, __ATOMIC_RELAXED, __HIP_MEMORY_SCOPE_AGENT) == want)) break;
      if (KA_POLL_SLEEP > 0) __builtin_amdgcn_s_sleep(KA_POLL_SLEEP);
    }
  }
  asm volatile("s_waitcnt vmcnt(0)\n\ts_barrier" ::: "memory");
  STAMP(0, 3);

  const int c = w >> 1, half = w & 1;
  u32x2 qv[4], fz; u32x4 sa[4], sbv[4], fq;
  const u32 eo = (u32)(1024 * half + 16 * n + 4 * g);
  const char* qTc = ws + WS_QT + (size_t)(ch0 + c) * 4096;
  {
    u32x4 x0, x1;
    gld_v_sc1<0>(x0, t * 16, ws + WS_XR + (size_t)ch0 * 4096);
    gld_v_sc1<0>(x1, t * 16, ws + WS_XR + (size_t)(ch0 + 1) * 4096);
    wait_vm<0>();
    asm volatile("" : "+v"(x0), "+v"(x1));
    *(u32x4*)(smem + KA_XR + t * 16) = x0;
    *(u32x4*)(smem + KA_XR + 4608 + t * 16) = x1;
  }
  asm volatile("s_waitcnt lgkmcnt(0)\n\ts_barrier" ::: "memory");
  if (HINT_STEP == 0 && t == 0) __builtin_amdgcn_raw_buffer_store_b64(tag, wr, (u32)WS_HNT + b * 8, 0, 16);
  STAMP(1, 1);

  const u32 aa = sb + c * KA_KC + (n & 3) * KA_CS + 16 * g + 2 * (n & 12) + half * 256;
  const u32 ba = sb + KA_XR + c * 4608 + 480 - 32 * n + 16 * g + half * 256;
  f32x4 acc[8];
  u32x2 Alo[2][8], Ahi[2][8];
  u32x4 B[2][8];
  auto rd = [&](auto C0, auto R) {
    constexpr int c0 = C0, r = R, buf = c0 & 1;
    if constexpr (r >= 2 && r < 10) {
      constexpr int u = r - 2;
      ds_b128<512 * (7 - u) + 64 * c0>(B[buf][u], ba);
    } else {
      constexpr int ar = r < 2 ? r : r - 8, i = ar >> 1;
      if constexpr ((ar & 1) == 0) ds_b64<512 * i + 64 * c0>(Alo[buf][i], aa);
      else ds_b64<512 * i + 64 * c0 + 8>(Ahi[buf][i], aa);
    }
  };
  auto ldS = [&](auto E) {
    constexpr int e = E, i = e / 3, kind = e % 3;
    if constexpr (kind == 0) gld_v_sc1<i * 1024>(sa[i], eo * 4, ws + WS_SA);
    if constexpr (kind == 1) gld_v_sc1<i * 1024>(sbv[i], eo * 4, ws + WS_SB);
    if constexpr (kind == 2) gld2_v_sc1<i * 512>(qv[i], eo * 2, qTc);
  };
  auto ldF = [&](auto E) {
    constexpr int e = E;
    if constexpr (e == 0) gld2_v_sc1<0>(fz, x * 128, ws + WS_FLZ);
    else gld_v_sc1<0>(fq, (x * 32 + 2 * (lane & 15)) * 8, ws + WS_FLG);
  };
  auto okF = [&]() { return fz[0] == tag[0] && fz[1] == tag[1] && tags_ok(fq, tag); };
  static_for<24>([&](auto R) { rd(std::integral_constant<int, 0>{}, R); });
  static_for<4>([&](auto C0) {
    constexpr int c0 = C0, buf = c0 & 1;
    if constexpr (c0 == CHK) {
      wait_vm<0>();
      asm volatile("" : "+v"(fz), "+v"(fq));
      if (!__all(okF())) {
        for (int spins = 0; spins < SPIN_MAX; ++spins) {
          ldF(std::integral_constant<int, 0>{}); ldF(std::integral_constant<int, 1>{});
          wait_vm<0>();
          asm volatile("" : "+v"(fz), "+v"(fq));
          if (__all(okF())) break;
          __builtin_amdgcn_s_sleep(1);
        }
      }
    }
    if constexpr (c0 > 0) { if (HINT_STEP == c0 && t == 0) __builtin_amdgcn_raw_buffer_store_b64(tag, wr, (u32)WS_HNT + b * 8, 0, 16); }
    u32x4 A[8];
    static_for<36>([&](auto M) {
      constexpr int m = M;
      constexpr int c1 = m < 8 ? 0 : m < 15 ? 1 : m < 21 ? 2 : m < 26 ? 3 : m < 30 ? 4 : m < 33 ? 5 : m < 35 ? 6 : 7;
      constexpr int first = c1 * 8 - c1 * (c1 - 1) / 2;
      constexpr int u = m - first;
      if constexpr (m == 0) {
        if constexpr (c0 == 0) wait_lgkm<14>(); else wait_lgkm<0>();
        pin(Alo[buf][0]); pin(Ahi[buf][0]);
#pragma unroll
        for (int i = 0; i < 8; ++i) pin(B[buf][i]);
        A[0] = cat(Alo[buf][0], Ahi[buf][0]);
      }
      if constexpr (m == 8) {
        if constexpr (c0 == 0) wait_lgkm<8>();
#pragma unroll
        for (int i = 1; i < 8; ++i) { pin(Alo[buf][i]); pin(Ahi[buf][i]); A[i] = cat(Alo[buf][i], Ahi[buf][i]); }
      }
      mfma_vv<(c0 == 0 && c1 == 0)>(acc[c1 + u], A[c1], B[buf][u]);
      if constexpr (c0 < 3 && m < 24) rd(std::integral_constant<int, c0 + 1>{}, M);
      if constexpr (c0 == CHK - 1 && (m == CHK_M || m == CHK_M + 2)) ldF(std::integral_constant<int, (m - CHK_M) / 2>{});
      if constexpr (c0 == CHK && m >= 1 && m < 25 && (m & 1) == 1) ldS(std::integral_constant<int, (m - 1) / 2>{});
    });
  });
  mfma_fence(acc[0], acc[1], acc[2], acc[3]);
  after_fence(acc[4], acc[5], acc[6], acc[7]);
  STAMP(1, 2);
  f32x4 fin[4];
  {
    f32x4* red = (f32x4*)(smem + KA_RED);
    if (half) {
#pragma unroll
      for (int i = 0; i < 4; ++i) { red[(w * 4 + i) * 64 + lane] = acc[i]; fin[i] = acc[4 + i]; }
      asm volatile("; half 1" ::: "memory");
    } else {
#pragma unroll
      for (int i = 0; i < 4; ++i) { red[(w * 4 + i) * 64 + lane] = acc[4 + i]; fin[i] = acc[i]; }
      asm volatile("; half 0" ::: "memory");
    }
    asm volatile("s_waitcnt lgkmcnt(0)\n\ts_barrier" ::: "memory");
#pragma unroll
    for (int i = 0; i < 4; ++i) fin[i] += red[((w ^ 1) * 4 + i) * 64 + lane];
  }
  wait_vm<0>();
#pragma unroll
  for (int i = 0; i < 4; ++i) asm volatile("" : "+v"(qv[i]), "+v"(sa[i]), "+v"(sbv[i]));
  const f32x2 dd = {dsk, dsk};
  u32x2 hv[4];
#pragma unroll
  for (int i = 0; i < 4; ++i) {
    const int s = 256 * (4 * half + i) + 16 * n + 4 * g;
    const u32x2 xq = *(LDSP const u32x2*)(size_t)(sb + KA_XR + c * 4608 + 2 * (2044 - s));
    const f32x2 x0 = {bhi(xq[1]), blo(xq[1])}, x1 = {bhi(xq[0]), blo(xq[0])};
    const f32x2 q0 = {blo(qv[i][0]), bhi(qv[i][0])}, q1 = {blo(qv[i][1]), bhi(qv[i][1])};
    const f32x2 z0 = x0 * dd + f32x2{fin[i][0], fin[i][1]}, z1 = x1 * dd + f32x2{fin[i][2], fin[i][3]};
    const f32x4 a4 = __builtin_bit_cast(f32x4, sa[i]), b4 = __builtin_bit_cast(f32x4, sbv[i]);
    const f32x2 t0 = q0 * f32x2{a4[0], a4[1]} + f32x2{b4[0], b4[1]};
    const f32x2 t1 = q1 * f32x2{a4[2], a4[3]} + f32x2{b4[2], b4[3]};
    const f32x2 h0 = t0 * z0 + q0, h1 = t1 * z1 + q1;
    hv[i] = u32x2{pk2(h0[0], h0[1]), pk2(h1[0], h1[1])};
  }
#pragma unroll
  for (int i = 0; i < 4; i += 2) {
    auto a = __builtin_amdgcn_permlane16_swap(hv[i][0], hv[i + 1][0], false, false);
    auto bb = __builtin_amdgcn_permlane16_swap(hv[i][1], hv[i + 1][1], false, false);
    const int s = 256 * (4 * half + i + (g & 1)) + 16 * n + 4 * (g & 2);
    st16_wt(wr, (u32)WS_HT + ((ch0 + c) * S + s) * 2, u32x4{a[0], bb[0], a[1], bb[1]});
  }
  STAMP(1, 3);
}

constexpr int KB_NL = 39;
constexpr int KB_ST = KB_NL * 4096;
constexpr int KB_LDS = KB_ST + 1024;
#define EPI_F 6
constexpr int KB_RING = 8;

#define P2_AHEAD 5
#define P2_PRO 3
constexpr int p2_upto(int ks) {
  const int ramp = P2_PRO - 1 + 2 * (ks + 1), steady = ks + P2_AHEAD;
  const int u = ks < 0 ? P2_PRO - 1 : (ramp < steady ? ramp : steady);
  return u > 15 ? 15 : u;
}
#define P2_XMAX 5
constexpr int P2_NEXTRA = 23;
constexpr int p2_extra(int ks) {
  const int tail0 = 16 - P2_AHEAD, rest = P2_NEXTRA - P2_AHEAD * P2_XMAX;
  if (ks >= tail0) return P2_XMAX;
  return (rest > 0 && ks >= tail0 - rest) ? 1 : 0;
}
constexpr int p2_extra_first(int ks) { int c = 0; for (int s = 0; s < ks; ++s) c += p2_extra(s); return c; }
constexpr int p2_extra_real(int ks) { int f = p2_extra_first(ks), n = p2_extra(ks); return f >= P2_NEXTRA ? 0 : (f + n > P2_NEXTRA ? P2_NEXTRA - f : n); }
static_assert(p2_extra_first(16) >= P2_NEXTRA, "all Wd operations are scheduled");
constexpr int p2_issue_step(int t) { for (int s = -1; s < KS1; ++s) if (p2_upto(s) >= t) return s; return KS1; }
constexpr int p2_younger(int t, int ks, int ops) {
  int c = (p2_upto(ks - 1) - t) * ops;
  for (int s = p2_issue_step(t); s < ks; ++s) if (s >= 0) c += p2_extra_real(s);
  return c;
}

struct KbCtx {
  u32 sb, gv, lv, lb0, lwd, csv;
  const char *hb0, *hb1, *w0, *w1, *w2, *cs0, *cs1, *cs2, *wd, *wdr;
};

template <int GRAM, class Setup>
DI void kb_phase2(const KbCtx& c, f32x4 (&acc)[3][8], f32x4 (&rsum)[2], f32x4 (&csp)[3], u32x4 (&Wr)[4][4], u32 (&ra)[8], Setup&& setup, char* ws_stamp) {
  constexpr int OPS = GRAM ? 4 : 5;
  u32x4 W[KB_RING][3];
  u32x2 Bl[2][8], Bh[2][8];
  auto issue = [&](auto KS, auto O) {
    constexpr int ks = KS, o = O;
    if constexpr (o == 0) glds16<ks * 8192>(c.gv, c.hb0 + (size_t)ks * 131072, c.lb0);
    if constexpr (o == 1) gld_a<(ks & 3) * 1024>(W[ks % KB_RING][0], c.lv, c.w0 + (ks >> 2) * 4096);
    if constexpr (o == 2) gld_a<(ks & 3) * 1024>(W[ks % KB_RING][1], c.lv, c.w1 + (ks >> 2) * 4096);
    if constexpr (o == 3 && !GRAM) gld_a<(ks & 3) * 1024>(W[ks % KB_RING][2], c.lv, c.w2 + (ks >> 2) * 4096);
    if constexpr (o == 4) glds16<ks * 8192 + 1024>(c.gv, c.hb1 + (size_t)ks * 131072, c.lb0);
  };
  auto extra = [&](auto E) {
    constexpr int e = E;
    if constexpr (e < 7) glds16<(32 + e) * 4096>(c.lv, c.wd + (size_t)(32 + e) * 4096, c.lwd);
    else if constexpr (e < P2_NEXTRA) gld_a<((e - 7) & 3) * 1024>(Wr[(e - 7) >> 2][(e - 7) & 3], c.lv, c.wdr + ((e - 7) >> 2) * 4096);
  };
  auto rdB = [&](auto KS, auto R) {
    constexpr int ks = KS, r = R, i = r >> 1;
    if constexpr ((r & 1) == 0) ds_tr<(ks & 7) * 8192>(Bl[ks & 1][i], ra[i] + (ks >= 8 ? 65536 : 0));
    else ds_tr<(ks & 7) * 8192 + 1024>(Bh[ks & 1][i], ra[i] + (ks >= 8 ? 65536 : 0));
  };
  u32x4 c0, c1, c2;
  static_for<5>([&](auto O) { issue(std::integral_constant<int, 0>{}, O); });
  gld_v<0>(c0, c.csv, c.cs0); gld_v<0>(c1, c.csv, c.cs1); gld_v<0>(c2, c.csv, c.cs2);
  static_for<p2_upto(-1)>([&](auto KS) { static_for<5>([&](auto O) { issue(std::integral_constant<int, KS + 1>{}, O); }); });
  setup();
  u32x4 ones = {0x3f803f80u, 0x3f803f80u, 0x3f803f80u, 0x3f803f80u};
  pin(ones);
  wait_vm<p2_upto(-1) * OPS>();
  barrier();
  static_for<16>([&](auto R) { rdB(std::integral_constant<int, 0>{}, R); });
  asm volatile("" : "+v"(c0), "+v"(c1), "+v"(c2));
  float cv[12];
#pragma unroll
  for (int e = 0; e < 4; ++e) { cv[e] = __uint_as_float(c0[e]); cv[4 + e] = __uint_as_float(c1[e]); cv[8 + e] = __uint_as_float(c2[e]); }
  static_for<KS1>([&](auto KS) {
    constexpr int ks = KS, buf = ks & 1, slot = ks % KB_RING;
    constexpr int nmain = (p2_upto(ks) - p2_upto(ks - 1)) * 5, next = p2_extra_real(ks);
    wait_lgkm<0>();
    u32x4 B[8];
#pragma unroll
    for (int i = 0; i < 8; ++i) { pin(Bl[buf][i]); pin(Bh[buf][i]); }
#pragma unroll
    for (int i = 0; i < 8; ++i) B[i] = cat(Bl[buf][i], Bh[buf][i]);
    pin_a(W[slot][0]); pin_a(W[slot][1]);
    if constexpr (!GRAM) pin_a(W[slot][2]);
    static_for<26>([&](auto M) {
      constexpr int m = M, i = m / 3, u = m % 3;
      if constexpr (m >= 24) mfma_vv<ks == 0>(rsum[m - 24], ones, B[m - 24]);
      else if constexpr (u < 2 || !GRAM) mfma_av<ks == 0>(acc[u][i], W[slot][u], B[i]);
      else mfma_vv<ks == 0>(acc[2][i], B[i], B[i]);
      if constexpr (m == 1 && ks + 1 < KS1) { wait_vm<p2_younger(ks + 1, ks, OPS)>(); barrier(); }
      if constexpr (m >= 2 && m < 18 && ks + 1 < KS1) rdB(std::integral_constant<int, ks + 1>{}, std::integral_constant<int, m - 2>{});
      if constexpr (ks >= 9 && ks < 13 && m >= 2 && m < 14) dpp_stage<ks - 9>(cv[m - 2]);
      constexpr int v = m - (26 - nmain - next);
      if constexpr (v >= 0 && v < nmain) issue(std::integral_constant<int, p2_upto(ks - 1) + 1 + v / 5>{}, std::integral_constant<int, v % 5>{});
      if constexpr (v >= nmain) extra(std::integral_constant<int, p2_extra_first(ks) + v - nmain>{});
    });
  });
  mfma_fence(acc[0][0], acc[1][0], rsum[0], rsum[1]);
#pragma unroll
  for (int i = 0; i < 8; ++i) after_fence(acc[0][i], acc[1][i], acc[2][i], rsum[i & 1]);
#pragma unroll
  for (int u = 0; u < 3; ++u) csp[u] = f32x4{cv[4 * u], cv[4 * u + 1], cv[4 * u + 2], cv[4 * u + 3]};
}

__global__ __launch_bounds__(256) void kb_mlp(char* __restrict__ ws, u16* __restrict__ out) {
  extern __shared__ __attribute__((aligned(16))) char smem[];
  const int t = threadIdx.x, lane = t & 63;
  const int w = __builtin_amdgcn_readfirstlane(t >> 6);
  const int b = blockIdx.x, xcd = b & 7, bi = b >> 3;
#define KB_MAP 1
  const int rt = KB_MAP == 0 ? 2 * xcd + (bi & 1) : (xcd >> 1) * 4 + (bi & 3);
  const int j = KB_MAP == 0 ? bi >> 1 : (xcd & 1) * 8 + (bi >> 2);
  const int half = KB_MAP == 0 ? j & 1 : j >> 3, dt = KB_MAP == 0 ? j >> 1 : j & 7;
  const int U0 = slice_u0(j), nu = slice_nu(j);
  const int rem = nu - 8;
  const int nw = w < rem ? 3 : 2;
  const int uw = U0 + (w < rem ? 3 * w : 3 * rem + 2 * (w - rem));
  const int u2 = uw + (nw == 3 ? 2 : 0);
  const int n = lane & 15, g = lane >> 4;
  const int s0 = 128 * rt;
  char* ws_stamp = ws; (void)ws_stamp;
  STAMP(2, 0);
  auto sf = [&](int i) { return ((w + (i >> 1)) & 3) + 4 * (half ^ (i & 1)); };

  KbCtx c;
  c.sb = (u32)(size_t)(LDSP char*)smem;
  c.lv = lane * 16;
  c.gv = (u32)(g * 4096 + (((n >> 1) ^ (g | ((w & 1) << 2))) * 32 + (n & 1) * 16));
  c.hb0 = ws + WS_HT + (size_t)s0 * 2 + (size_t)(8 * w) * 4096;
  c.hb1 = c.hb0 + 4 * 4096;
  c.lb0 = c.sb + 8 * w * 256;
  c.w0 = ws + WS_W1 + (size_t)uw * KS1 * 1024;
  c.w1 = c.w0 + KS1 * 1024;
  c.w2 = ws + WS_W1 + (size_t)u2 * KS1 * 1024;
  c.cs0 = ws + WS_CS + (size_t)uw * 64;
  c.cs1 = c.cs0 + 64;
  c.cs2 = ws + WS_CS + (size_t)u2 * 64;
  c.csv = n * (NU * 64) + g * 16;
  c.wdr = ws + WS_WD + (size_t)dt * KS2 * 4096 + (size_t)KB_NL * 4096;
  c.wd = ws + WS_WD + (size_t)dt * KS2 * 4096 + (size_t)w * 1024;
  c.lwd = c.sb + 1024 * w;
  u32 ra[8], hoff[6];
  auto setup = [&]() {
    const int qd = n >> 2, p = n & 3;
    const int frow = qd | ((g & 1) << 2);
#pragma unroll
    for (int i = 0; i < 8; ++i) { ra[i] = c.sb + (8 * g + qd) * 256 + ((sf(i) ^ frow) * 32) + 8 * p; asm volatile("" : "+v"(ra[i])); }
#pragma unroll
    for (int tt = 0; tt < 6; ++tt) {
      const int u = tt >> 1, P = unit_pos(j, w, u);
      const u32 o = (u32)(((8 * rt + sf(4 * (tt & 1) + g)) * KS2 + (P >> 2)) * 1024 + ((P & 3) * 16 + n) * 16);
      hoff[tt] = u < nw ? o : 0xfffffff0u;
      asm volatile("" : "+v"(hoff[tt]));
    }
  };
  f32x4 acc[3][8], cs[3], rsum[2];
  u32x4 Wr[4][4];
  if (w == 3) kb_phase2<1>(c, acc, rsum, cs, Wr, ra, setup, ws_stamp);
  else kb_phase2<0>(c, acc, rsum, cs, Wr, ra, setup, ws_stamp);
  STAMP(2, 2);

  u32x2 res[4];
  {
    const u32 rb = ra[0] + dt * 16384;
    ds_tr<0>(res[0], rb); ds_tr<1024>(res[1], rb); ds_tr<8192>(res[2], rb); ds_tr<9216>(res[3], rb);
  }
  float* stl = (float*)(smem + KB_ST);
  if (g == 0) {
#pragma unroll
    for (int i = 0; i < 2; ++i) stl[2 * (16 * sf(i) + n)] = rsum[i][0];
  }
  if (w == 3 && g == (n >> 2)) {
#pragma unroll
    for (int i = 0; i < 8; ++i) {
      f32x4 a = acc[2][i];
      float dg = (n & 2) ? ((n & 1) ? a[3] : a[2]) : ((n & 1) ? a[1] : a[0]);
      stl[2 * (16 * sf(i) + n) + 1] = dg;
    }
  }
  asm volatile("s_waitcnt lgkmcnt(0)\n\ts_barrier" : "+v"(res[0]), "+v"(res[1]), "+v"(res[2]), "+v"(res[3]) :: "memory");
  STAMP(2, 3);
  const __amdgpu_buffer_rsrc_t hrs = __builtin_amdgcn_make_buffer_rsrc(ws + WS_H, 0, (S / 16) * KS2 * 1024, 0x00020000);
  f32x2 sv[8];
#pragma unroll
  for (int i = 0; i < 8; ++i) sv[i] = *(const f32x2*)(stl + 2 * (16 * sf(i) + n));
  float nm[8], ce[8], ri[8];
#pragma unroll
  for (int i = 0; i < 8; ++i) {
    const float mean = sv[i][0] * (1.f / D);
    const float var = fmaxf(sv[i][1] * (1.f / D) - mean * mean, 0.f) + EPS;
    const float rstd = __builtin_amdgcn_rsqf(var);
    nm[i] = -mean; ri[i] = var * rstd; ce[i] = -1.4426950408889634f * rstd;
  }
  u32* cnt = (u32*)(ws + WS_CNT + rt * 384);
  static_for<3>([&](auto U_) {
    constexpr int u = U_;
    u32 hv[8];
    static_for<8>([&](auto I) {
      constexpr int i = I;
      pin4(acc[u][i]);
      constexpr int fk = u * EPI_F + i * EPI_F / 8;
      if constexpr ((i + 1) * EPI_F / 8 > i * EPI_F / 8) glds16<fk * 4096>(c.lv, c.wd + (size_t)fk * 4096, c.lwd);
      const f32x4 a = acc[u][i];
      const f32x2 nm2 = {nm[i], nm[i]}, ce2 = {ce[i], ce[i]}, ri2 = {ri[i], ri[i]};
      const f32x2 tg = (f32x2{a[0], a[1]} + nm2 * f32x2{cs[u][0], cs[u][1]}) * ce2;
      const f32x2 tu = f32x2{a[2], a[3]} + nm2 * f32x2{cs[u][2], cs[u][3]};
      const f32x2 e = {__builtin_amdgcn_exp2f(tg[0]), __builtin_amdgcn_exp2f(tg[1])};
      const f32x2 den = e * ri2 + ri2;
      const f32x2 o = tu * f32x2{__builtin_amdgcn_rcpf(den[0]), __builtin_amdgcn_rcpf(den[1])};
      hv[i] = pk2(o[0], o[1]);
      if constexpr ((i & 3) == 3) {
        constexpr int tt = i >> 2;
        auto s02 = __builtin_amdgcn_permlane32_swap(hv[4 * tt], hv[4 * tt + 2], false, false);
        auto s13 = __builtin_amdgcn_permlane32_swap(hv[4 * tt + 1], hv[4 * tt + 3], false, false);
        auto t01 = __builtin_amdgcn_permlane16_swap(s02[0], s13[0], false, false);
        auto t23 = __builtin_amdgcn_permlane16_swap(s02[1], s13[1], false, false);
        __builtin_amdgcn_raw_buffer_store_b128(u32x4{t01[0], t01[1], t23[0], t23[1]}, hrs, hoff[2 * u + tt], 0, 16);
        asm volatile("" ::: "memory");
      }
    });
    if constexpr (u > 0) {
      wait_vm<EPI_F + 2>();
      barrier();
      if (t == 0) __hip_atomic_fetch_add(cnt + 32 * (u - 1), 1u, __ATOMIC_RELAXED, __HIP_MEMORY_SCOPE_AGENT);
      STAMP(2, 3 + u);
    }
  });
  if (w == 0) {
    for (int spins = 0; spins < SPIN_MAX; ++spins) {
      const u32 v = __builtin_amdgcn_readfirstlane(__hip_atomic_load(cnt, __ATOMIC_RELAXED, __HIP_MEMORY_SCOPE_AGENT));
      if (v >= 16u) break;
      if (KB_POLL_SLEEP > 0) __builtin_amdgcn_s_sleep(KB_POLL_SLEEP);
    }
    wait_vm<0>();
  } else {
    const char* wsrc = c.wd - (size_t)w * 1024;
    for (int e = 3 * EPI_F + w - 1; e < 32; e += 3) {
      const char* src = wsrc + (size_t)e * 4096;
      const u32 dst = c.sb + e * 4096;
      glds16<0>(c.lv, src, dst); glds16<1024>(c.lv, src + 1024, dst); glds16<2048>(c.lv, src + 2048, dst); glds16<3072>(c.lv, src + 3072, dst);
    }
  }
  barrier();
  STAMP(2, 6);

  const char* hb = ws + WS_H + (size_t)(8 * rt + 4 * half + w) * KS2 * 1024;
  u32x4 Hf[KS2];
  constexpr int P3_AHEAD = P3_AHEAD_;
  constexpr int P3_SAMPLE = P3_SAMPLE_;
  constexpr int CHK1 = 16 - P3_AHEAD, CHK2 = 32 - P3_AHEAD, SMP1 = CHK1 - P3_SAMPLE, SMP2 = CHK2 - P3_SAMPLE;
  static_assert(SMP1 >= 0 && P3_SAMPLE < P3_AHEAD, "sampling schedule");
  u32 smp1, smp2;
  auto ldH = [&](auto KS) { constexpr int ks = KS; if constexpr (ks < KS2) gld_a_sc1<(ks & 3) * 1024>(Hf[ks], c.lv, hb + (ks >> 2) * 4096); };
  auto sample = [&](u32& dst, const u32* p) { asm volatile("global_load_dword %0, %1, %2 sc1" : "=v"(dst) : "v"(0u), "s"(p) : "memory"); };
  auto confirm = [&](u32& v, const u32* p) {
    asm volatile("" : "+v"(v));
    if (__builtin_amdgcn_readfirstlane(v) < 16u) {
      for (int spins = 0; spins < SPIN_MAX; ++spins) {
        sample(v, p);
        wait_vm<0>();
        asm volatile("" : "+v"(v));
        if (__builtin_amdgcn_readfirstlane(v) >= 16u) break;
        __builtin_amdgcn_s_sleep(1);
      }
    }
  };
  static_for<P3_AHEAD>([&](auto KS) { ldH(KS); });
  STAMP(3, 0);
  f32x4 o4[4];
  u32x4 Af[3][4];
  const u32 la0 = c.sb + lane * 16, la1 = la0 + 65536, la2 = la0 + 131072;
  auto rdA = [&](auto KS, auto DF) {
    constexpr int ks = KS, df = DF;
    if constexpr (ks < KB_NL) ds_b128<(ks & 15) * 4096 + df * 1024>(Af[ks % 3][df], ks < 16 ? la0 : ks < 32 ? la1 : la2);
  };
  wait_vm<P3_AHEAD>();
  barrier();
  if (t == 0) __hip_atomic_fetch_add(cnt + 64, 1u, __ATOMIC_RELAXED, __HIP_MEMORY_SCOPE_AGENT);
  static_for<4>([&](auto DF) { rdA(std::integral_constant<int, 0>{}, DF); });
  static_for<4>([&](auto DF) { rdA(std::integral_constant<int, 1>{}, DF); });
  static_for<KS2>([&](auto KS) {
    constexpr int ks = KS;
    constexpr int nl = (ks + P3_AHEAD <= KS2 ? P3_AHEAD - 1 : KS2 - 1 - ks);
    constexpr int ns = ((SMP1 < ks && ks <= SMP1 + P3_AHEAD) ? 1 : 0) + ((SMP2 < ks && ks <= SMP2 + P3_AHEAD) ? 1 : 0);
    wait_vm<nl + ns>();
    if constexpr (ks + 1 < KB_NL) wait_lgkm<4>(); else wait_lgkm<0>();
    pin_a(Hf[ks]);
    if constexpr (ks == CHK1 || ks == CHK2) {
      wait_vm<P3_SAMPLE - 1>();
      if constexpr (ks == CHK1) confirm(smp1, cnt + 32); else confirm(smp2, cnt + 64);
    }
    static_for<4>([&](auto DF) {
      constexpr int df = DF;
      if constexpr (ks < KB_NL) { pin(Af[ks % 3][df]); mfma_va<ks == 0>(o4[df], Af[ks % 3][df], Hf[ks]); }
      else { pin_a(Wr[ks - KB_NL][df]); mfma_aa(o4[df], Wr[ks - KB_NL][df], Hf[ks]); }
      rdA(std::integral_constant<int, ks + 2>{}, DF);
      if constexpr (df == 1 && ks + P3_AHEAD < KS2) ldH(std::integral_constant<int, ks + P3_AHEAD>{});
      if constexpr (df == 2 && ks == SMP1) sample(smp1, cnt + 32);
      if constexpr (df == 2 && ks == SMP2) sample(smp2, cnt + 64);
    });
  });
  mfma_fence(o4[0], o4[1], o4[2], o4[3]);
  u32x4 o0, o1;
#pragma unroll
  for (int df = 0; df < 2; ++df) {
    o0[2 * df] = pk2(o4[df][0] + blo(res[df][0]), o4[df][1] + bhi(res[df][0]));
    o0[2 * df + 1] = pk2(o4[df][2] + blo(res[df][1]), o4[df][3] + bhi(res[df][1]));
    o1[2 * df] = pk2(o4[2 + df][0] + blo(res[2 + df][0]), o4[2 + df][1] + bhi(res[2 + df][0]));
    o1[2 * df + 1] = pk2(o4[2 + df][2] + blo(res[2 + df][1]), o4[2 + df][3] + bhi(res[2 + df][1]));
  }
  const __amdgpu_buffer_rsrc_t ors = __builtin_amdgcn_make_buffer_rsrc(out, 0, S * D * 2, 0x00020000);
  const u32 ooff = (u32)((s0 + 64 * half + 16 * w + n) * D + 64 * dt + 8 * g) * 2;
  __builtin_amdgcn_raw_buffer_store_b128(o0, ors, ooff, 0, 16);
  __builtin_amdgcn_raw_buffer_store_b128(o1, ors, ooff + 64, 0, 16);
  asm volatile("kb_code_end:");
  STAMP(2, 7);
}

extern "C" void kernel_launch(void* const* d_in, const int* in_sizes, int n_in, void* d_out, int out_size, void* d_ws,
                              size_t ws_size, hipStream_t stream) {
  const u16 *q = (const u16*)d_in[0], *k = (const u16*)d_in[1], *v = (const u16*)d_in[2], *shk = (const u16*)d_in[3];
  const u16 *ssmk = (const u16*)d_in[4], *dskip = (const u16*)d_in[5];
  const u16 *wg = (const u16*)d_in[7], *wu = (const u16*)d_in[8], *wd = (const u16*)d_in[9];
  char* ws = (char*)d_ws;
  (void)hipFuncSetAttribute((const void*)ka_conv, hipFuncAttributeMaxDynamicSharedMemorySize, KA_LDS);
  (void)hipFuncSetAttribute((const void*)kb_mlp, hipFuncAttributeMaxDynamicSharedMemorySize, KB_LDS);
  ka_conv<<<512, 256, KA_LDS, stream>>>(q, k, v, shk, ssmk, dskip, wg, wu, wd, ws);
  kb_mlp<<<256, 256, KB_LDS, stream>>>(ws, (u16*)d_out);
}
```

```cpp
#include <hip/hip_runtime.h>
#include <stdint.h>
#include <utility>

typedef unsigned short u16;
typedef unsigned u32;
typedef __attribute__((ext_vector_type(2))) u32 u32x2;
typedef __attribute__((ext_vector_type(4))) u32 u32x4;
typedef __attribute__((ext_vector_type(2))) float f32x2;
typedef __attribute__((ext_vector_type(4))) float f32x4;
typedef __attribute__((ext_vector_type(2))) __bf16 bf16x2;
typedef __attribute__((ext_vector_type(8))) __bf16 bf16x8;
typedef __attribute__((ext_vector_type(4))) short s16x4;
#define LDSP __attribute__((address_space(3)))
#define DI __device__ __forceinline__

constexpr int S = 2048, D = 512, F = 1376;
constexpr int NU = F / 8;
constexpr int KS1 = D / 32;
constexpr int KS2 = F / 32;
constexpr float EPS = 1e-5f;

constexpr size_t WS_XR = 0;
constexpr size_t WS_QT = WS_XR + (size_t)D * S * 2;
constexpr size_t WS_HT = WS_QT + (size_t)D * S * 2;
constexpr size_t WS_W1 = WS_HT + (size_t)D * S * 2;
constexpr size_t WS_WD = WS_W1 + (size_t)NU * KS1 * 1024;
constexpr size_t WS_H = WS_WD + (size_t)8 * KS2 * 4 * 1024;
constexpr size_t WS_CS = WS_H + (size_t)(S / 16) * KS2 * 1024;
constexpr size_t WS_SA = WS_CS + (size_t)KS1 * NU * 16 * 4;
constexpr size_t WS_SB = WS_SA + (size_t)S * 4;
constexpr size_t WS_CNT = WS_SB + (size_t)S * 4;
constexpr size_t WS_FLA = WS_CNT + 16 * 128;
constexpr size_t WS_FLG = WS_FLA + 256 * 8;
constexpr size_t WS_FLS = WS_FLG + 256 * 8;
constexpr size_t WS_FLZ = WS_FLS + 256 * 8;
constexpr size_t WS_HNT = WS_FLZ + 8 * 128;
constexpr size_t WS_EPO = WS_HNT + 256 * 8;

template <int... Is, class Fn> DI void static_for_(std::integer_sequence<int, Is...>, Fn&& f) { (f(std::integral_constant<int, Is>{}), ...); }
template <int N, class Fn> DI void static_for(Fn&& f) { static_for_(std::make_integer_sequence<int, N>{}, f); }

DI float blo(u32 u) { return __uint_as_float(u << 16); }
DI float bhi(u32 u) { return __uint_as_float(u & 0xffff0000u); }
DI u32 pk2(float a, float b) { f32x2 t = {a, b}; return __builtin_bit_cast(u32, __builtin_convertvector(t, bf16x2)); }
DI f32x4 mfma16(u32x4 a, u32x4 b, f32x4 c) {
  return __builtin_amdgcn_mfma_f32_16x16x32_bf16(__builtin_bit_cast(bf16x8, a), __builtin_bit_cast(bf16x8, b), c, 0, 0, 0);
}
DI u32x2 tr_read(u32 lds_byte) {
  return __builtin_bit_cast(u32x2, __builtin_amdgcn_ds_read_tr16_b64_v4i16((LDSP s16x4*)(size_t)lds_byte));
}
DI u32x4 tr_frag(u32 a0, u32 a1) { u32x2 x = tr_read(a0), y = tr_read(a1); return u32x4{x[0], x[1], y[0], y[1]}; }
DI u32x4 cat(u32x2 x, u32x2 y) { return __builtin_shufflevector(x, y, 0, 1, 2, 3); }
template <int CTRL> DI float dpp_add(float v) {
  return v + __builtin_bit_cast(float, __builtin_amdgcn_update_dpp(0, __builtin_bit_cast(int, v), CTRL, 0xf, 0xf, true));
}
DI float sum_rows(float v) {
  u32 u = __float_as_uint(v);
  auto a = __builtin_amdgcn_permlane16_swap(u, u, false, false);
  v = __uint_as_float(a[0]) + __uint_as_float(a[1]);
  u = __float_as_uint(v);
  auto b = __builtin_amdgcn_permlane32_swap(u, u, false, false);
  return __uint_as_float(b[0]) + __uint_as_float(b[1]);
}
DI float wave_sum(float v) {
  v = dpp_add<0xb1>(v);
  v = dpp_add<0x4e>(v);
  v = dpp_add<0x124>(v);
  v = dpp_add<0x128>(v);
  return sum_rows(v);
}
DI __amdgpu_buffer_rsrc_t ws_rsrc(char* ws) { return __builtin_amdgcn_make_buffer_rsrc(ws, 0, 0x04000000, 0x00020000); }
DI void st16_wt(__amdgpu_buffer_rsrc_t r, u32 off, u32x4 v) { __builtin_amdgcn_raw_buffer_store_b128(v, r, off, 0, 16); }
DI int slice_u0(int j) { return j < 12 ? 11 * j : 132 + 10 * (j - 12); }
DI int slice_nu(int j) { return j < 12 ? 11 : 10; }

template <bool FIRST = false> DI void mfma_vv(f32x4& acc, const u32x4& a, const u32x4& b) {
  if constexpr (FIRST) asm volatile("v_mfma_f32_16x16x32_bf16 %0, %1, %2, 0" : "=&v"(acc) : "v"(a), "v"(b));
  else asm volatile("v_mfma_f32_16x16x32_bf16 %0, %1, %2, %0" : "+v"(acc) : "v"(a), "v"(b));
}
template <bool FIRST = false> DI void mfma_av(f32x4& acc, const u32x4& a, const u32x4& b) {
  if constexpr (FIRST) asm volatile("v_mfma_f32_16x16x32_bf16 %0, %1, %2, 0" : "=&v"(acc) : "a"(a), "v"(b));
  else asm volatile("v_mfma_f32_16x16x32_bf16 %0, %1, %2, %0" : "+v"(acc) : "a"(a), "v"(b));
}
template <int OFF> DI void ds_b64(u32x2& d, u32 addr) { asm volatile("ds_read_b64 %0, %1 offset:%c2" : "=v"(d) : "v"(addr), "i"(OFF) : "memory"); }
template <int OFF> DI void ds_b128(u32x4& d, u32 addr) { asm volatile("ds_read_b128 %0, %1 offset:%c2" : "=v"(d) : "v"(addr), "i"(OFF) : "memory"); }
template <int OFF> DI void ds_tr(u32x2& d, u32 addr) { asm volatile("ds_read_b64_tr_b16 %0, %1 offset:%c2" : "=v"(d) : "v"(addr), "i"(OFF) : "memory"); }
template <int OFF> DI void gld_a(u32x4& d, u32 voff, const void* sbase) {
  asm volatile("global_load_dwordx4 %0, %1, %2 offset:%c3" : "=a"(d) : "v"(voff), "s"(sbase), "i"(OFF) : "memory");
}
template <int OFF> DI void gld_v(u32x4& d, u32 voff, const void* sbase) {
  asm volatile("global_load_dwordx4 %0, %1, %2 offset:%c3" : "=v"(d) : "v"(voff), "s"(sbase), "i"(OFF) : "memory");
}
template <int OFF> DI void gld2_v(u32x2& d, u32 voff, const void* sbase) {
  asm volatile("global_load_dwordx2 %0, %1, %2 offset:%c3" : "=v"(d) : "v"(voff), "s"(sbase), "i"(OFF) : "memory");
}
template <int LOFF> DI void glds16(u32 voff, const void* sbase, u32 ldsbase) {
  asm volatile("s_add_u32 m0, %2, %c3\n\ts_nop 0\n\tglobal_load_lds_dwordx4 %0, %1" ::"v"(voff), "s"(sbase), "s"(ldsbase), "i"(LOFF) : "memory", "scc");
}
template <int N> DI void wait_vm() { asm volatile("s_waitcnt vmcnt(%c0)" ::"i"(N) : "memory"); }
template <int N> DI void wait_lgkm() { asm volatile("s_waitcnt lgkmcnt(%c0)" ::"i"(N) : "memory"); }
template <int OFF> DI void gld_a_sc1(u32x4& d, u32 voff, const void* sbase) {
  asm volatile("global_load_dwordx4 %0, %1, %2 offset:%c3 sc1" : "=a"(d) : "v"(voff), "s"(sbase), "i"(OFF) : "memory");
}
DI void mfma_aa(f32x4& acc, const u32x4& a, const u32x4& b) {
  asm volatile("v_mfma_f32_16x16x32_bf16 %0, %1, %2, %0" : "+v"(acc) : "a"(a), "a"(b));
}
template <bool FIRST = false> DI void mfma_va(f32x4& acc, const u32x4& a, const u32x4& b) {
  if constexpr (FIRST) asm volatile("v_mfma_f32_16x16x32_bf16 %0, %1, %2, 0" : "=&v"(acc) : "v"(a), "a"(b));
  else asm volatile("v_mfma_f32_16x16x32_bf16 %0, %1, %2, %0" : "+v"(acc) : "v"(a), "a"(b));
}
DI void mfma_fence(f32x4& a, f32x4& b, f32x4& c, f32x4& d) { asm volatile("s_nop 7\n\ts_nop 1" : "+v"(a), "+v"(b), "+v"(c), "+v"(d)); }
DI void after_fence(f32x4& a, f32x4& b, f32x4& c, f32x4& d) { asm volatile("" : "+v"(a), "+v"(b), "+v"(c), "+v"(d)); }
DI void after_fence(f32x4& a, f32x4& b, f32x4& c) { asm volatile("" : "+v"(a), "+v"(b), "+v"(c)); }
template <int STAGE> DI void dpp_stage(float& v) {
  if constexpr (STAGE == 0) asm volatile("v_add_f32_dpp %0, %0, %0 quad_perm:[1,0,3,2] row_mask:0xf bank_mask:0xf" : "+v"(v));
  if constexpr (STAGE == 1) asm volatile("v_add_f32_dpp %0, %0, %0 quad_perm:[2,3,0,1] row_mask:0xf bank_mask:0xf" : "+v"(v));
  if constexpr (STAGE == 2) asm volatile("v_add_f32_dpp %0, %0, %0 row_ror:4 row_mask:0xf bank_mask:0xf" : "+v"(v));
  if constexpr (STAGE == 3) asm volatile("v_add_f32_dpp %0, %0, %0 row_ror:8 row_mask:0xf bank_mask:0xf" : "+v"(v));
}
DI void barrier() { asm volatile("s_barrier" ::: "memory"); }
DI void pin(u32x4& x) { asm volatile("" : "+v"(x)); }
DI void pin(u32x2& x) { asm volatile("" : "+v"(x)); }
DI void pin4(f32x4& x) { asm volatile("" : "+v"(x)); }
DI void pin_a(u32x4& x) { asm volatile("" : "+a"(x)); }

#define STAMP(kern, idx) do {} while (0)

template <class Gate>
DI void w_job(int b, const u16* __restrict__ wg, const u16* __restrict__ wu, const u16* __restrict__ wd, char* __restrict__ ws, char* smem, Gate&& gate) {
  const int t = threadIdx.x, lane = t & 63, w = t >> 6;
  char* tile = smem;
  char* tile2 = smem + 32 * 368;
  const u32 tb = (u32)(size_t)(LDSP char*)smem;
  const int i16 = lane & 15, qd = i16 >> 2, p = i16 & 3, g = lane >> 4;
  const int ks = b & 15, j = b >> 4;
  const int U0 = slice_u0(j), nu = slice_nu(j);
  u32x4 g4[2], u4[2], d4[2];
#pragma unroll
  for (int rep = 0; rep < 2; ++rep) {
    int idx = t + 256 * rep, r = idx / 11, u = idx - 11 * r;
    bool ok = r < 32 && u < nu;
    size_t off = ok ? (size_t)(32 * ks + r) * F + 8 * (U0 + u) : 0;
    g4[rep] = *(const u32x4*)(wg + off); u4[rep] = *(const u32x4*)(wu + off);
  }
  const int ks2 = b >> 2, dp = b & 3;
  if (b < KS2 * 4) {
#pragma unroll
    for (int rep = 0; rep < 2; ++rep) {
      int idx = t + 256 * rep, r = idx >> 4, c = idx & 15;
      d4[rep] = *(const u32x4*)(wd + (size_t)(32 * ks2 + r) * D + 128 * dp + 8 * c);
    }
  }
#pragma unroll
  for (int rep = 0; rep < 2; ++rep) {
    int idx = t + 256 * rep, r = idx / 11, u = idx - 11 * r;
    if (r < 32 && u < nu) {
      *(u32x4*)(tile + r * 368 + u * 32) = u32x4{g4[rep][0], u4[rep][0], g4[rep][1], u4[rep][1]};
      *(u32x4*)(tile + r * 368 + u * 32 + 16) = u32x4{g4[rep][2], u4[rep][2], g4[rep][3], u4[rep][3]};
    }
  }
  if (b < KS2 * 4) {
#pragma unroll
    for (int rep = 0; rep < 2; ++rep) { int idx = t + 256 * rep, r = idx >> 4, c = idx & 15; *(u32x4*)(tile2 + r * 272 + c * 16) = d4[rep]; }
  }
  __syncthreads();
  gate();
  const u32x4 ones = {0x3f803f80u, 0x3f803f80u, 0x3f803f80u, 0x3f803f80u};
  const __amdgpu_buffer_rsrc_t wr = ws_rsrc(ws);
  for (int u = w; u < nu; u += 4) {
    u32 a = tb + (8 * g + qd) * 368 + u * 32 + 8 * p;
    u32x4 fr = tr_frag(a, a + 4 * 368);
    st16_wt(wr, (u32)WS_W1 + (((U0 + u) * KS1 + ks) * 64 + lane) * 16, fr);
    f32x4 cs = mfma16(fr, ones, f32x4{0.f, 0.f, 0.f, 0.f});
    if (i16 == 0) st16_wt(wr, (u32)WS_CS + ((ks * NU + U0 + u) * 16 + 4 * g) * 4, __builtin_bit_cast(u32x4, cs));
  }
  if (b < KS2 * 4) {
#pragma unroll
    for (int rep = 0; rep < 2; ++rep) {
      int fi = w + 4 * rep, dtl = fi >> 2, df = fi & 3;
      u32 a = tb + 32 * 368 + (8 * g + qd) * 272 + (64 * dtl + 32 * (df >> 1) + 8 * p + 4 * (df & 1)) * 2;
      u32x4 fr = tr_frag(a, a + 4 * 272);
      st16_wt(wr, (u32)WS_WD + ((((2 * dp + dtl) * KS2 + ks2) * 4 + df) * 64 + lane) * 16, fr);
    }
  }
}

constexpr int KA_CS = 4160;
constexpr int KA_KC = 4 * KA_CS;
constexpr int KA_XR = 2 * KA_KC;
constexpr int KA_RED = KA_XR + 2 * 4608;
constexpr int KA_LDS = KA_RED + 4 * 4096;
constexpr int KA_TK = KA_XR, KA_TV = KA_TK + 72 * 128, KA_TQ = KA_TV + 64 * 128;
static_assert(KA_TQ + 64 * 128 == KA_LDS, "tile overlay");
constexpr int SPIN_MAX = 1 << 22;
#define HINT_STEP 0
#define KB_POLL_SLEEP 1
#define KA_POLL_SLEEP 1
#define P3_AHEAD_ 12
#define HELP_PRE 16
#define HELP_SLEEP 20
#define CHK 2
#define CHK_M 2
#define WEARLY 0
#define HINT_TICKS 800ull

template <int OFF> DI void gld_v_sc1(u32x4& d, u32 voff, const void* sbase) {
  asm volatile("global_load_dwordx4 %0, %1, %2 offset:%c3 sc1" : "=v"(d) : "v"(voff), "s"(sbase), "i"(OFF) : "memory");
}
template <int OFF> DI void gld2_v_sc1(u32x2& d, u32 voff, const void* sbase) {
  asm volatile("global_load_dwordx2 %0, %1, %2 offset:%c3 sc1" : "=v"(d) : "v"(voff), "s"(sbase), "i"(OFF) : "memory");
}
DI bool tags_ok(const u32x4& f, const u32x2& tag) { return f[0] == tag[0] && f[1] == tag[1] && f[2] == tag[0] && f[3] == tag[1]; }

__global__ __launch_bounds__(256, 2) void ka_conv(const u16* __restrict__ q, const u16* __restrict__ k, const u16* __restrict__ v,
                                                  const u16* __restrict__ shk, const u16* __restrict__ ssmk, const u16* __restrict__ dskip,
                                                  const u16* __restrict__ wg, const u16* __restrict__ wu, const u16* __restrict__ wd,
                                                  char* __restrict__ ws) {
  extern __shared__ __attribute__((aligned(16))) char smem[];
  const int t = threadIdx.x, lane = t & 63;
  const int w = __builtin_amdgcn_readfirstlane(t >> 6);
  const int b = blockIdx.x;
  char* ws_stamp = ws; (void)ws_stamp;
  STAMP(0, 0);
  if (b >= 256) {
    const int hb = b - 256, srow = 8 * hb + 2 * w;
    const __amdgpu_buffer_rsrc_t wr = ws_rsrc(ws);
    u32x2 epo; u32x4 qa, qb;
    gld2_v_sc1<0>(epo, 0, ws + WS_EPO);
    if (HELP_PRE > 0) __builtin_amdgcn_s_sleep(HELP_PRE);
    gld_v<0>(qa, lane * 16, (const char*)q + (size_t)srow * 1024);
    gld_v<1024>(qb, lane * 16, (const char*)q + (size_t)srow * 1024);
    if (b < 264 && w == 1) {
      const char* pc; u32 len;
      asm volatile("s_getpc_b64 %0\n1:\n\ts_mov_b32 %1, kb_code_end-1b" : "=s"(pc), "=s"(len));
      const u32 lb = (u32)(size_t)(LDSP char*)smem + 32768;
      for (u32 o0 = 0; o0 < len; o0 += 64 * 128) {
        u32 o = o0 + lane * 128; o = o < len ? o : 0;
        asm volatile("s_mov_b32 m0, %2\n\ts_nop 0\n\tglobal_load_lds_dword %0, %1" ::"v"(o), "s"(pc), "s"(lb) : "memory");
      }
    }
    wait_vm<0>();
    asm volatile("" : "+v"(qa), "+v"(qb), "+v"(epo));
    const unsigned long long want = (((unsigned long long)epo[1] << 32) | epo[0]) + 1ull;
    {
      float sa = 0.f, sbb = 0.f;
#pragma unroll
      for (int e = 0; e < 4; ++e) { sa += blo(qa[e]) + bhi(qa[e]); sbb += blo(qb[e]) + bhi(qb[e]); }
      const float ma = wave_sum(sa) * (1.f / D), mb = wave_sum(sbb) * (1.f / D);
      float va = 0.f, vb = 0.f;
#pragma unroll
      for (int e = 0; e < 4; ++e) {
        float u0 = blo(qa[e]) - ma, u1 = bhi(qa[e]) - ma, u2 = blo(qb[e]) - mb, u3 = bhi(qb[e]) - mb;
        va += u0 * u0 + u1 * u1; vb += u2 * u2 + u3 * u3;
      }
      va = wave_sum(va) * (1.f / D); vb = wave_sum(vb) * (1.f / D);
      const float ra = __builtin_amdgcn_rsqf(va + EPS), rb = __builtin_amdgcn_rsqf(vb + EPS);
      const u32x2 val = lane == 0 ? u32x2{__float_as_uint(ra), __float_as_uint(rb)} : u32x2{__float_as_uint(-ma * ra), __float_as_uint(-mb * rb)};
      const u32 off = lane == 0 ? (u32)WS_SA + srow * 4 : lane == 1 ? (u32)WS_SB + srow * 4 : 0xfffffff0u;
      __builtin_amdgcn_raw_buffer_store_b64(val, wr, off, 0, 16);
    }
    asm volatile("s_waitcnt vmcnt(0)\n\ts_barrier" ::: "memory");
    if (t == 0) __builtin_amdgcn_raw_buffer_store_b64(u32x2{(u32)want, (u32)(want >> 32)}, wr, (u32)WS_FLS + hb * 8, 0, 16);
    STAMP(1, 6);
    if (hb == 1 && w == 1) {
      const unsigned long long* fs = (const unsigned long long*)(ws + WS_FLS) + lane;
      for (int spins = 0; spins < SPIN_MAX; ++spins) {
        bool ok = true;
#pragma unroll
        for (int i = 0; i < 4; ++i) ok &= __hip_atomic_load(fs + 64 * i, __ATOMIC_RELAXED, __HIP_MEMORY_SCOPE_AGENT) == want;
        if (__all(ok)) break;
        __builtin_amdgcn_s_sleep(1);
      }
      __builtin_amdgcn_raw_buffer_store_b64(u32x2{(u32)want, (u32)(want >> 32)}, wr, lane < 8 ? (u32)WS_FLZ + lane * 128 : 0xfffffff0u, 0, 16);
    } else if (HELP_SLEEP > 0) __builtin_amdgcn_s_sleep(HELP_SLEEP);
    w_job(hb, wg, wu, wd, ws, smem, [&]() {
      if (hb == 1 && w == 1) {
        const unsigned long long* fa = (const unsigned long long*)(ws + WS_FLA) + lane;
        const u32x2 tg = {(u32)want, (u32)(want >> 32)};
        for (int spins = 0; spins < SPIN_MAX; ++spins) {
          bool ok = true;
#pragma unroll
          for (int i = 0; i < 4; ++i) ok &= __hip_atomic_load(fa + 64 * i, __ATOMIC_RELAXED, __HIP_MEMORY_SCOPE_AGENT) == want;
          if (__all(ok)) break;
          __builtin_amdgcn_s_sleep(16);
        }
        if (lane == 0) __builtin_amdgcn_raw_buffer_store_b64(tg, wr, (u32)WS_EPO, 0, 16);
      }
      if (w == 0) {
        const unsigned long long* hp = (const unsigned long long*)(ws + WS_HNT) + hb;
        const unsigned long long t0 = __builtin_amdgcn_s_memrealtime();
        while (__hip_atomic_load(hp, __ATOMIC_RELAXED, __HIP_MEMORY_SCOPE_AGENT) != want && __builtin_amdgcn_s_memrealtime() - t0 < HINT_TICKS)
          __builtin_amdgcn_s_sleep(8);
      }
      __builtin_amdgcn_s_barrier();
      STAMP(1, 5);
    });
    STAMP(1, 4);
    return;
  }
  const int x = b & 7, bi = b >> 3, s0 = 64 * bi;
  const int ch0 = 64 * x + 2 * bi;
  const u32 sb = (u32)(size_t)(LDSP char*)smem;
  const int n = lane & 15, g = lane >> 4, qd = n >> 2, p = n & 3;
  const int d = 64 * x + 16 * w + n;
  const __amdgpu_buffer_rsrc_t wr = ws_rsrc(ws);

  u32x2 epo, sh; u32x4 kp[2], kc[2];
  gld2_v_sc1<0>(epo, 0, ws + WS_EPO);
  gld2_v<0>(sh, d * 8, shk);
  const int r8 = lane >> 3, c8 = lane & 7;
  auto voff = [&](int instr) { const int f = ((r8 >> 1) & 1) | ((instr & 1) << 1); return (u32)((((c8 >> 1) ^ f) * 2 + (c8 & 1)) * 16); };
  const size_t col = (size_t)x * 128;
  {
    int row = s0 - 8 + 8 * w + r8; row = row < 0 ? 0 : row;
    glds16<0>((u32)row * 1024 + voff(w), (const char*)k + col, sb + KA_TK + w * 1024);
    glds16<0>((u32)(s0 + 8 * w + r8) * 1024 + voff(w), (const char*)v + col, sb + KA_TV + w * 1024);
    glds16<0>((u32)(s0 - 8 + 8 * (w + 4) + r8) * 1024 + voff(w), (const char*)k + col, sb + KA_TK + (w + 4) * 1024);
    glds16<0>((u32)(s0 + 8 * (w + 4) + r8) * 1024 + voff(w), (const char*)v + col, sb + KA_TV + (w + 4) * 1024);
    if (w == 0) glds16<0>((u32)(s0 + 56 + r8) * 1024 + voff(8), (const char*)k + col, sb + KA_TK + 8 * 1024);
  }
#pragma unroll
  for (int cc = 0; cc < 2; ++cc) {
    const char* kr = (const char*)ssmk + (size_t)(ch0 + cc) * 4096;
    gld_v<0>(kp[cc], (u32)(t > 0 ? t - 1 : 0) * 16, kr);
    gld_v<0>(kc[cc], (u32)t * 16, kr);
  }
  glds16<0>((u32)(s0 + 8 * w + r8) * 1024 + voff(w), (const char*)q + col, sb + KA_TQ + w * 1024);
  glds16<0>((u32)(s0 + 8 * (w + 4) + r8) * 1024 + voff(w), (const char*)q + col, sb + KA_TQ + (w + 4) * 1024);
  const float dsk = blo(dskip[ch0 + (w >> 1)]);
  if (b == 0 && t < 16) *(u32*)(ws + WS_CNT + t * 128) = 0u;

  wait_vm<6>();
  barrier();
  asm volatile("" : "+v"(sh), "+v"(epo));
  STAMP(0, 1);
  u32x2 tag;
  { const unsigned long long e = (((unsigned long long)epo[1] << 32) | epo[0]) + 1ull; tag = u32x2{(u32)e, (u32)(e >> 32)}; }
  auto taddr = [&](int base, int tr) { const int f = ((tr >> 1) & 1) | ((tr >> 2) & 2); return sb + base + tr * 128 + ((w ^ f) * 32) + 8 * p; };
  {
    const float a0 = blo(sh[0]), a1 = bhi(sh[0]), a2 = blo(sh[1]), a3 = bhi(sh[1]);
#pragma unroll
    for (int e = 0; e < 2; ++e) {
      const int rg = g + 4 * e;
      u32x2 km = tr_read(taddr(KA_TK, 8 * rg + 4 + qd));
      const u32x2 k0 = tr_read(taddr(KA_TK, 8 * rg + 8 + qd)), k1 = tr_read(taddr(KA_TK, 8 * rg + 12 + qd));
      const u32x2 v0 = tr_read(taddr(KA_TV, 8 * rg + qd)), v1 = tr_read(taddr(KA_TV, 8 * rg + 4 + qd));
      if (s0 + rg == 0) km = u32x2{0, 0};
      const float kf[11] = {bhi(km[0]), blo(km[1]), bhi(km[1]), blo(k0[0]), bhi(k0[0]), blo(k0[1]), bhi(k0[1]), blo(k1[0]), bhi(k1[0]), blo(k1[1]), bhi(k1[1])};
      const float vf[8] = {blo(v0[0]), bhi(v0[0]), blo(v0[1]), bhi(v0[1]), blo(v1[0]), bhi(v1[0]), blo(v1[1]), bhi(v1[1])};
      float xo[8];
#pragma unroll
      for (int j = 0; j < 8; ++j) xo[j] = (kf[j + 3] * a0 + kf[j + 2] * a1 + kf[j + 1] * a2 + kf[j] * a3) * vf[j];
      const int ir = 2040 - (s0 + 8 * rg);
      st16_wt(wr, (u32)WS_XR + (d >> 1) * 8192 + (ir >> 5) * 128 + (d & 1) * 64 + (ir & 31) * 2, u32x4{pk2(xo[7], xo[6]), pk2(xo[5], xo[4]), pk2(xo[3], xo[2]), pk2(xo[1], xo[0])});
    }
  }
  asm volatile("s_waitcnt vmcnt(0)\n\ts_barrier" : "+v"(kp[0]), "+v"(kc[0]), "+v"(kp[1]), "+v"(kc[1]) :: "memory");
  if (t == 0) __builtin_amdgcn_raw_buffer_store_b64(tag, wr, (u32)WS_FLA + (x * 32 + bi) * 8, 0, 16);
  STAMP(0, 2);
#pragma unroll
  for (int e = 0; e < 2; ++e) {
    const int rg = g + 4 * e;
    const u32x2 q0 = tr_read(taddr(KA_TQ, 8 * rg + qd)), q1 = tr_read(taddr(KA_TQ, 8 * rg + 4 + qd));
    const int r0 = s0 + 8 * rg;
    st16_wt(wr, (u32)WS_QT + (d >> 1) * 8192 + (r0 >> 5) * 128 + (d & 1) * 64 + (r0 & 31) * 2, cat(q0, q1));
  }
#pragma unroll
  for (int cc = 0; cc < 2; ++cc) {
    const u32 m = t > 0 ? 0xffffffffu : 0u;
    u32 wv[6] = {kp[cc][0] & m, kp[cc][1] & m, kp[cc][2] & m, kp[cc][3] & m, kc[cc][0], kc[cc][1]};
    u32 o1[5];
#pragma unroll
    for (int i = 0; i < 5; ++i) o1[i] = __builtin_amdgcn_alignbit(wv[i + 1], wv[i], 16);
    char* base = smem + cc * KA_KC + (t + 1) * 16;
    *(u32x4*)(base + 0 * KA_CS) = u32x4{o1[0], o1[1], o1[2], o1[3]};
    *(u32x4*)(base + 1 * KA_CS) = u32x4{wv[1], wv[2], wv[3], wv[4]};
    *(u32x4*)(base + 2 * KA_CS) = u32x4{o1[1], o1[2], o1[3], o1[4]};
    *(u32x4*)(base + 3 * KA_CS) = u32x4{wv[2], wv[3], wv[4], wv[5]};
    if (t == 255) {
      const u32 z = 0;
      const u32 e0 = __builtin_amdgcn_alignbit(kc[cc][1], kc[cc][0], 16), e1 = __builtin_amdgcn_alignbit(kc[cc][2], kc[cc][1], 16);
      const u32 e2 = __builtin_amdgcn_alignbit(kc[cc][3], kc[cc][2], 16), e3 = __builtin_amdgcn_alignbit(z, kc[cc][3], 16);
      *(u32x4*)(base + 16 + 0 * KA_CS) = u32x4{e0, e1, e2, e3};
      *(u32x4*)(base + 16 + 1 * KA_CS) = u32x4{kc[cc][1], kc[cc][2], kc[cc][3], z};
      *(u32x4*)(base + 16 + 2 * KA_CS) = u32x4{e1, e2, e3, z};
      *(u32x4*)(base + 16 + 3 * KA_CS) = u32x4{kc[cc][2], kc[cc][3], z, z};
    }
  }
  if (t < 8) *(u32x4*)(smem + (t >> 2) * KA_KC + (t & 3) * KA_CS) = u32x4{0, 0, 0, 0};
  asm volatile("s_waitcnt vmcnt(0) lgkmcnt(0)\n\ts_barrier" ::: "memory");
  if (t == 0) {
    __builtin_amdgcn_raw_buffer_store_b64(tag, wr, (u32)WS_FLG + (x * 32 + bi) * 8, 0, 16);
  }
  if (t >= 64 && t < 64 + 60) { int i = t - 64; *(u32x4*)(smem + KA_XR + (i / 30) * 4608 + 4096 + (i % 30) * 16) = u32x4{0, 0, 0, 0}; }
  if (w == 0) {
    const unsigned long long* fp = (const unsigned long long*)(ws + WS_FLA) + x * 32 + (lane & 31);
    const unsigned long long want = ((unsigned long long)tag[1] << 32) | tag[0];
    for (int spins = 0; spins < SPIN_MAX; ++spins) {
      if (__all(__hip_atomic_load(fp, __ATOMIC_RELAXED, __HIP_MEMORY_SCOPE_AGENT) == want)) break;
      if (KA_POLL_SLEEP > 0) __builtin_amdgcn_s_sleep(KA_POLL_SLEEP);
    }
  }
  asm volatile("s_waitcnt vmcnt(0)\n\ts_barrier" ::: "memory");
  STAMP(0, 3);

  const int c = w >> 1, half = w & 1;
  u32x2 qv[4], fz; u32x4 sa[4], sbv[4], fq;
  const u32 eo = (u32)(1024 * half + 16 * n + 4 * g);
  const char* qTc = ws + WS_QT + (size_t)ch0 * 4096 + c * 64;
  const u32 qo = (u32)(4096 * half + (n >> 1) * 128 + (n & 1) * 32 + 8 * g);
  {
    u32x4 x0, x1;
    gld_v_sc1<0>(x0, t * 16, ws + WS_XR + (size_t)ch0 * 4096);
    gld_v_sc1<0>(x1, t * 16 + 4096, ws + WS_XR + (size_t)ch0 * 4096);
    wait_vm<0>();
    asm volatile("" : "+v"(x0), "+v"(x1));
    char* dst = smem + KA_XR + ((t >> 2) & 1) * 4608 + (t >> 3) * 64 + (t & 3) * 16;
    *(u32x4*)dst = x0;
    *(u32x4*)(dst + 2048) = x1;
  }
  asm volatile("s_waitcnt lgkmcnt(0)\n\ts_barrier" ::: "memory");
  if (HINT_STEP == 0 && t == 0) __builtin_amdgcn_raw_buffer_store_b64(tag, wr, (u32)WS_HNT + b * 8, 0, 16);
  STAMP(1, 1);

  const u32 aa = sb + c * KA_KC + (n & 3) * KA_CS + 16 * g + 2 * (n & 12) + half * 256;
  const u32 ba = sb + KA_XR + c * 4608 + 480 - 32 * n + 16 * g + half * 256;
  f32x4 acc[8];
  u32x2 Alo[2][8], Ahi[2][8];
  u32x4 B[2][8];
  auto rd = [&](auto C0, auto R) {
    constexpr int c0 = C0, r = R, buf = c0 & 1;
    if constexpr (r >= 2 && r < 10) {
      constexpr int u = r - 2;
      ds_b128<512 * (7 - u) + 64 * c0>(B[buf][u], ba);
    } else {
      constexpr int ar = r < 2 ? r : r - 8, i = ar >> 1;
      if constexpr ((ar & 1) == 0) ds_b64<512 * i + 64 * c0>(Alo[buf][i], aa);
      else ds_b64<512 * i + 64 * c0 + 8>(Ahi[buf][i], aa);
    }
  };
  auto ldS = [&](auto E) {
    constexpr int e = E, i = e / 3, kind = e % 3;
    if constexpr (kind == 0) gld_v_sc1<i * 1024>(sa[i], eo * 4, ws + WS_SA);
    if constexpr (kind == 1) gld_v_sc1<i * 1024>(sbv[i], eo * 4, ws + WS_SB);
    if constexpr (kind == 2) gld2_v_sc1<i * 1024>(qv[i], qo, qTc);
  };
  auto ldF = [&](auto E) {
    constexpr int e = E;
    if constexpr (e == 0) gld2_v_sc1<0>(fz, x * 128, ws + WS_FLZ);
    else gld_v_sc1<0>(fq, (x * 32 + 2 * (lane & 15)) * 8, ws + WS_FLG);
  };
  auto okF = [&]() { return fz[0] == tag[0] && fz[1] == tag[1] && tags_ok(fq, tag); };
  static_for<24>([&](auto R) { rd(std::integral_constant<int, 0>{}, R); });
  static_for<4>([&](auto C0) {
    constexpr int c0 = C0, buf = c0 & 1;
    if constexpr (c0 == CHK) {
      wait_vm<0>();
      asm volatile("" : "+v"(fz), "+v"(fq));
      if (!__all(okF())) {
        for (int spins = 0; spins < SPIN_MAX; ++spins) {
          ldF(std::integral_constant<int, 0>{}); ldF(std::integral_constant<int, 1>{});
          wait_vm<0>();
          asm volatile("" : "+v"(fz), "+v"(fq));
          if (__all(okF())) break;
          __builtin_amdgcn_s_sleep(1);
        }
      }
    }
    if constexpr (c0 > 0) { if (HINT_STEP == c0 && t == 0) __builtin_amdgcn_raw_buffer_store_b64(tag, wr, (u32)WS_HNT + b * 8, 0, 16); }
    u32x4 A[8];
    static_for<36>([&](auto M) {
      constexpr int m = M;
      constexpr int c1 = m < 8 ? 0 : m < 15 ? 1 : m < 21 ? 2 : m < 26 ? 3 : m < 30 ? 4 : m < 33 ? 5 : m < 35 ? 6 : 7;
      constexpr int first = c1 * 8 - c1 * (c1 - 1) / 2;
      constexpr int u = m - first;
      if constexpr (m == 0) {
        if constexpr (c0 == 0) wait_lgkm<14>(); else wait_lgkm<0>();
        pin(Alo[buf][0]); pin(Ahi[buf][0]);
#pragma unroll
        for (int i = 0; i < 8; ++i) pin(B[buf][i]);
        A[0] = cat(Alo[buf][0], Ahi[buf][0]);
      }
      if constexpr (m == 8) {
        if constexpr (c0 == 0) wait_lgkm<8>();
#pragma unroll
        for (int i = 1; i < 8; ++i) { pin(Alo[buf][i]); pin(Ahi[buf][i]); A[i] = cat(Alo[buf][i], Ahi[buf][i]); }
      }
      mfma_vv<(c0 == 0 && c1 == 0)>(acc[c1 + u], A[c1], B[buf][u]);
      if constexpr (c0 < 3 && m < 24) rd(std::integral_constant<int, c0 + 1>{}, M);
      if constexpr (c0 == CHK - 1 && (m == CHK_M || m == CHK_M + 2)) ldF(std::integral_constant<int, (m - CHK_M) / 2>{});
      if constexpr (c0 == CHK && m >= 1 && m < 25 && (m & 1) == 1) ldS(std::integral_constant<int, (m - 1) / 2>{});
    });
  });
  mfma_fence(acc[0], acc[1], acc[2], acc[3]);
  after_fence(acc[4], acc[5], acc[6], acc[7]);
  STAMP(1, 2);
  f32x4 fin[4];
  {
    f32x4* red = (f32x4*)(smem + KA_RED);
    if (half) {
#pragma unroll
      for (int i = 0; i < 4; ++i) { red[(w * 4 + i) * 64 + lane] = acc[i]; fin[i] = acc[4 + i]; }
      asm volatile("; half 1" ::: "memory");
    } else {
#pragma unroll
      for (int i = 0; i < 4; ++i) { red[(w * 4 + i) * 64 + lane] = acc[4 + i]; fin[i] = acc[i]; }
      asm volatile("; half 0" ::: "memory");
    }
    asm volatile("s_waitcnt lgkmcnt(0)\n\ts_barrier" ::: "memory");
#pragma unroll
    for (int i = 0; i < 4; ++i) fin[i] += red[((w ^ 1) * 4 + i) * 64 + lane];
  }
  wait_vm<0>();
#pragma unroll
  for (int i = 0; i < 4; ++i) asm volatile("" : "+v"(qv[i]), "+v"(sa[i]), "+v"(sbv[i]));
  const f32x2 dd = {dsk, dsk};
  u32x2 hv[4];
#pragma unroll
  for (int i = 0; i < 4; ++i) {
    const int s = 256 * (4 * half + i) + 16 * n + 4 * g;
    const u32x2 xq = *(LDSP const u32x2*)(size_t)(sb + KA_XR + c * 4608 + 2 * (2044 - s));
    const f32x2 x0 = {bhi(xq[1]), blo(xq[1])}, x1 = {bhi(xq[0]), blo(xq[0])};
    const f32x2 q0 = {blo(qv[i][0]), bhi(qv[i][0])}, q1 = {blo(qv[i][1]), bhi(qv[i][1])};
    const f32x2 z0 = x0 * dd + f32x2{fin[i][0], fin[i][1]}, z1 = x1 * dd + f32x2{fin[i][2], fin[i][3]};
    const f32x4 a4 = __builtin_bit_cast(f32x4, sa[i]), b4 = __builtin_bit_cast(f32x4, sbv[i]);
    const f32x2 t0 = q0 * f32x2{a4[0], a4[1]} + f32x2{b4[0], b4[1]};
    const f32x2 t1 = q1 * f32x2{a4[2], a4[3]} + f32x2{b4[2], b4[3]};
    const f32x2 h0 = t0 * z0 + q0, h1 = t1 * z1 + q1;
    hv[i] = u32x2{pk2(h0[0], h0[1]), pk2(h1[0], h1[1])};
  }
#pragma unroll
  for (int i = 0; i < 4; i += 2) {
    auto a = __builtin_amdgcn_permlane16_swap(hv[i][0], hv[i + 1][0], false, false);
    auto bb = __builtin_amdgcn_permlane16_swap(hv[i][1], hv[i + 1][1], false, false);
    const int s = 256 * (4 * half + i + (g & 1)) + 16 * n + 4 * (g & 2);
    st16_wt(wr, (u32)WS_HT + ((ch0 + c) * S + s) * 2, u32x4{a[0], bb[0], a[1], bb[1]});
  }
  STAMP(1, 3);
}

constexpr int KB_NL = 39;
constexpr int KB_ST = KB_NL * 4096;
constexpr int KB_LDS = KB_ST + 1024;
#define EPI_FILL 8
constexpr int KB_RING = 8;

#define P2_AHEAD 5
#define P2_PRO 3
constexpr int p2_upto(int ks) {
  const int ramp = P2_PRO - 1 + 2 * (ks + 1), steady = ks + P2_AHEAD;
  const int u = ks < 0 ? P2_PRO - 1 : (ramp < steady ? ramp : steady);
  return u > 15 ? 15 : u;
}
#define P2_XMAX 5
constexpr int P2_NEXTRA = 23;
constexpr int p2_extra(int ks) {
  const int tail0 = 16 - P2_AHEAD, rest = P2_NEXTRA - P2_AHEAD * P2_XMAX;
  if (ks >= tail0) return P2_XMAX;
  return (rest > 0 && ks >= tail0 - rest) ? 1 : 0;
}
constexpr int p2_extra_first(int ks) { int c = 0; for (int s = 0; s < ks; ++s) c += p2_extra(s); return c; }
constexpr int p2_extra_real(int ks) { int f = p2_extra_first(ks), n = p2_extra(ks); return f >= P2_NEXTRA ? 0 : (f + n > P2_NEXTRA ? P2_NEXTRA - f : n); }
static_assert(p2_extra_first(16) >= P2_NEXTRA, "all Wd operations are scheduled");
constexpr int p2_issue_step(int t) { for (int s = -1; s < KS1; ++s) if (p2_upto(s) >= t) return s; return KS1; }
constexpr int p2_younger(int t, int ks, int ops) {
  int c = (p2_upto(ks - 1) - t) * ops;
  for (int s = p2_issue_step(t); s < ks; ++s) if (s >= 0) c += p2_extra_real(s);
  return c;
}

struct KbCtx {
  u32 sb, gv, lv, lb0, lwd, csv;
  const char *hb0, *hb1, *w0, *w1, *w2, *cs0, *cs1, *cs2, *wd, *wdr;
};

template <int GRAM, class Setup>
DI void kb_phase2(const KbCtx& c, f32x4 (&acc)[3][8], f32x4 (&rsum)[2], f32x4 (&csp)[3], u32x4 (&Wr)[4][4], u32 (&ra)[8], Setup&& setup, char* ws_stamp) {
  constexpr int OPS = GRAM ? 4 : 5;
  u32x4 W[KB_RING][3];
  u32x2 Bl[2][8], Bh[2][8];
  auto issue = [&](auto KS, auto O) {
    constexpr int ks = KS, o = O;
    if constexpr (o == 0) glds16<ks * 8192>(c.gv, c.hb0 + (size_t)ks * 131072, c.lb0);
    if constexpr (o == 1) gld_a<(ks & 3) * 1024>(W[ks % KB_RING][0], c.lv, c.w0 + (ks >> 2) * 4096);
    if constexpr (o == 2) gld_a<(ks & 3) * 1024>(W[ks % KB_RING][1], c.lv, c.w1 + (ks >> 2) * 4096);
    if constexpr (o == 3 && !GRAM) gld_a<(ks & 3) * 1024>(W[ks % KB_RING][2], c.lv, c.w2 + (ks >> 2) * 4096);
    if constexpr (o == 4) glds16<ks * 8192 + 1024>(c.gv, c.hb1 + (size_t)ks * 131072, c.lb0);
  };
  auto extra = [&](auto E) {
    constexpr int e = E;
    if constexpr (e < 7) glds16<(32 + e) * 4096>(c.lv, c.wd + (size_t)(32 + e) * 4096, c.lwd);
    else if constexpr (e < P2_NEXTRA) gld_a<((e - 7) & 3) * 1024>(Wr[(e - 7) >> 2][(e - 7) & 3], c.lv, c.wdr + ((e - 7) >> 2) * 4096);
  };
  auto rdB = [&](auto KS, auto R) {
    constexpr int ks = KS, r = R, i = r >> 1;
    if constexpr ((r & 1) == 0) ds_tr<(ks & 7) * 8192>(Bl[ks & 1][i], ra[i] + (ks >= 8 ? 65536 : 0));
    else ds_tr<(ks & 7) * 8192 + 1024>(Bh[ks & 1][i], ra[i] + (ks >= 8 ? 65536 : 0));
  };
  u32x4 c0, c1, c2;
  static_for<5>([&](auto O) { issue(std::integral_constant<int, 0>{}, O); });
  gld_v<0>(c0, c.csv, c.cs0); gld_v<0>(c1, c.csv, c.cs1); gld_v<0>(c2, c.csv, c.cs2);
  static_for<p2_upto(-1)>([&](auto KS) { static_for<5>([&](auto O) { issue(std::integral_constant<int, KS + 1>{}, O); }); });
  setup();
  u32x4 ones = {0x3f803f80u, 0x3f803f80u, 0x3f803f80u, 0x3f803f80u};
  pin(ones);
  wait_vm<p2_upto(-1) * OPS>();
  barrier();
  static_for<16>([&](auto R) { rdB(std::integral_constant<int, 0>{}, R); });
  asm volatile("" : "+v"(c0), "+v"(c1), "+v"(c2));
  float cv[12];
#pragma unroll
  for (int e = 0; e < 4; ++e) { cv[e] = __uint_as_float(c0[e]); cv[4 + e] = __uint_as_float(c1[e]); cv[8 + e] = __uint_as_float(c2[e]); }
  static_for<KS1>([&](auto KS) {
    constexpr int ks = KS, buf = ks & 1, slot = ks % KB_RING;
    constexpr int nmain = (p2_upto(ks) - p2_upto(ks - 1)) * 5, next = p2_extra_real(ks);
    wait_lgkm<0>();
    u32x4 B[8];
#pragma unroll
    for (int i = 0; i < 8; ++i) { pin(Bl[buf][i]); pin(Bh[buf][i]); }
#pragma unroll
    for (int i = 0; i < 8; ++i) B[i] = cat(Bl[buf][i], Bh[buf][i]);
    pin_a(W[slot][0]); pin_a(W[slot][1]);
    if constexpr (!GRAM) pin_a(W[slot][2]);
    static_for<26>([&](auto M) {
      constexpr int m = M, i = m / 3, u = m % 3;
      if constexpr (m >= 24) mfma_vv<ks == 0>(rsum[m - 24], ones, B[m - 24]);
      else if constexpr (u < 2 || !GRAM) mfma_av<ks == 0>(acc[u][i], W[slot][u], B[i]);
      else mfma_vv<ks == 0>(acc[2][i], B[i], B[i]);
      if constexpr (m == 1 && ks + 1 < KS1) { wait_vm<p2_younger(ks + 1, ks, OPS)>(); barrier(); }
      if constexpr (m >= 2 && m < 18 && ks + 1 < KS1) rdB(std::integral_constant<int, ks + 1>{}, std::integral_constant<int, m - 2>{});
      if constexpr (ks >= 9 && ks < 13 && m >= 2 && m < 14) dpp_stage<ks - 9>(cv[m - 2]);
      constexpr int v = m - (26 - nmain - next);
      if constexpr (v >= 0 && v < nmain) issue(std::integral_constant<int, p2_upto(ks - 1) + 1 + v / 5>{}, std::integral_constant<int, v % 5>{});
      if constexpr (v >= nmain) extra(std::integral_constant<int, p2_extra_first(ks) + v - nmain>{});
    });
  });
  mfma_fence(acc[0][0], acc[1][0], rsum[0], rsum[1]);
#pragma unroll
  for (int i = 0; i < 8; ++i) after_fence(acc[0][i], acc[1][i], acc[2][i], rsum[i & 1]);
#pragma unroll
  for (int u = 0; u < 3; ++u) csp[u] = f32x4{cv[4 * u], cv[4 * u + 1], cv[4 * u + 2], cv[4 * u + 3]};
}

__global__ __launch_bounds__(256) void kb_mlp(char* __restrict__ ws, u16* __restrict__ out) {
  extern __shared__ __attribute__((aligned(16))) char smem[];
  const int t = threadIdx.x, lane = t & 63;
  const int w = __builtin_amdgcn_readfirstlane(t >> 6);
  const int b = blockIdx.x, xcd = b & 7, bi = b >> 3;
#define KB_MAP 1
  const int rt = KB_MAP == 0 ? 2 * xcd + (bi & 1) : (xcd >> 1) * 4 + (bi & 3);
  const int j = KB_MAP == 0 ? bi >> 1 : (xcd & 1) * 8 + (bi >> 2);
  const int half = KB_MAP == 0 ? j & 1 : j >> 3, dt = KB_MAP == 0 ? j >> 1 : j & 7;
  const int U0 = slice_u0(j), nu = slice_nu(j);
  const int rem = nu - 8;
  const int nw = w < rem ? 3 : 2;
  const int uw = U0 + (w < rem ? 3 * w : 3 * rem + 2 * (w - rem));
  const int u2 = uw + (nw == 3 ? 2 : 0);
  const int n = lane & 15, g = lane >> 4;
  const int s0 = 128 * rt;
  char* ws_stamp = ws; (void)ws_stamp;
  STAMP(2, 0);
  auto sf = [&](int i) { return ((w + (i >> 1)) & 3) + 4 * (half ^ (i & 1)); };

  KbCtx c;
  c.sb = (u32)(size_t)(LDSP char*)smem;
  c.lv = lane * 16;
  c.gv = (u32)(g * 4096 + (((n >> 1) ^ (g | ((w & 1) << 2))) * 32 + (n & 1) * 16));
  c.hb0 = ws + WS_HT + (size_t)s0 * 2 + (size_t)(8 * w) * 4096;
  c.hb1 = c.hb0 + 4 * 4096;
  c.lb0 = c.sb + 8 * w * 256;
  c.w0 = ws + WS_W1 + (size_t)uw * KS1 * 1024;
  c.w1 = c.w0 + KS1 * 1024;
  c.w2 = ws + WS_W1 + (size_t)u2 * KS1 * 1024;
  c.cs0 = ws + WS_CS + (size_t)uw * 64;
  c.cs1 = c.cs0 + 64;
  c.cs2 = ws + WS_CS + (size_t)u2 * 64;
  c.csv = n * (NU * 64) + g * 16;
  c.wdr = ws + WS_WD + (size_t)dt * KS2 * 4096 + (size_t)KB_NL * 4096;
  c.wd = ws + WS_WD + (size_t)dt * KS2 * 4096 + (size_t)w * 1024;
  c.lwd = c.sb + 1024 * w;
  u32 ra[8], hoff[6];
  auto setup = [&]() {
    const int qd = n >> 2, p = n & 3;
    const int frow = qd | ((g & 1) << 2);
#pragma unroll
    for (int i = 0; i < 8; ++i) { ra[i] = c.sb + (8 * g + qd) * 256 + ((sf(i) ^ frow) * 32) + 8 * p; asm volatile("" : "+v"(ra[i])); }
#pragma unroll
    for (int tt = 0; tt < 6; ++tt) {
      const int q = 4 * tt + g, i = (q * 11) >> 5, u = q - 3 * i, U = uw + u;
      const u32 o = (u32)(((8 * rt + sf(i)) * KS2 + (U >> 2)) * 1024 + ((U & 3) * 16 + n) * 16);
      hoff[tt] = u < nw ? o : 0xfffffff0u;
      asm volatile("" : "+v"(hoff[tt]));
    }
  };
  f32x4 acc[3][8], cs[3], rsum[2];
  u32x4 Wr[4][4];
  if (w == 3) kb_phase2<1>(c, acc, rsum, cs, Wr, ra, setup, ws_stamp);
  else kb_phase2<0>(c, acc, rsum, cs, Wr, ra, setup, ws_stamp);
  STAMP(2, 2);

  u32x2 res[4];
  {
    const u32 rb = ra[0] + dt * 16384;
    ds_tr<0>(res[0], rb); ds_tr<1024>(res[1], rb); ds_tr<8192>(res[2], rb); ds_tr<9216>(res[3], rb);
  }
  float* stl = (float*)(smem + KB_ST);
  if (g == 0) {
#pragma unroll
    for (int i = 0; i < 2; ++i) stl[2 * (16 * sf(i) + n)] = rsum[i][0];
  }
  if (w == 3 && g == (n >> 2)) {
#pragma unroll
    for (int i = 0; i < 8; ++i) {
      f32x4 a = acc[2][i];
      float dg = (n & 2) ? ((n & 1) ? a[3] : a[2]) : ((n & 1) ? a[1] : a[0]);
      stl[2 * (16 * sf(i) + n) + 1] = dg;
    }
  }
  asm volatile("s_waitcnt lgkmcnt(0)\n\ts_barrier" : "+v"(res[0]), "+v"(res[1]), "+v"(res[2]), "+v"(res[3]) :: "memory");
  STAMP(2, 3);
  const __amdgpu_buffer_rsrc_t hrs = __builtin_amdgcn_make_buffer_rsrc(ws + WS_H, 0, (S / 16) * KS2 * 1024, 0x00020000);
  f32x2 sv[8];
#pragma unroll
  for (int i = 0; i < 8; ++i) sv[i] = *(const f32x2*)(stl + 2 * (16 * sf(i) + n));
  u32 hv[24];
  static_for<8>([&](auto I) {
    constexpr int i = I;
    const float mean = sv[i][0] * (1.f / D);
    const float var = fmaxf(sv[i][1] * (1.f / D) - mean * mean, 0.f) + EPS;
    const float rstd = __builtin_amdgcn_rsqf(var);
    const float rinv = var * rstd;
    const float ce = -1.4426950408889634f * rstd;
    const f32x2 nm = {-mean, -mean}, ce2 = {ce, ce}, ri2 = {rinv, rinv};
    static_for<3>([&](auto U_) {
      constexpr int u = U_;
      pin4(acc[u][i]);
      constexpr int slot = 3 * i + u;
      constexpr int wk = slot * EPI_FILL / 24;
      if constexpr (EPI_FILL > 0 && (slot + 1) * EPI_FILL / 24 > wk) glds16<wk * 4096>(c.lv, c.wd + (size_t)wk * 4096, c.lwd);
      const f32x4 a = acc[u][i];
      const f32x2 tg = (f32x2{a[0], a[1]} + nm * f32x2{cs[u][0], cs[u][1]}) * ce2;
      const f32x2 tu = f32x2{a[2], a[3]} + nm * f32x2{cs[u][2], cs[u][3]};
      const f32x2 e = {__builtin_amdgcn_exp2f(tg[0]), __builtin_amdgcn_exp2f(tg[1])};
      const f32x2 den = e * ri2 + ri2;
      const f32x2 o = tu * f32x2{__builtin_amdgcn_rcpf(den[0]), __builtin_amdgcn_rcpf(den[1])};
      hv[3 * i + u] = pk2(o[0], o[1]);
    });
    static_for<6>([&](auto T) {
      constexpr int tt = T;
      if constexpr ((4 * tt + 3) / 3 == i) {
        auto s02 = __builtin_amdgcn_permlane32_swap(hv[4 * tt], hv[4 * tt + 2], false, false);
        auto s13 = __builtin_amdgcn_permlane32_swap(hv[4 * tt + 1], hv[4 * tt + 3], false, false);
        auto t01 = __builtin_amdgcn_permlane16_swap(s02[0], s13[0], false, false);
        auto t23 = __builtin_amdgcn_permlane16_swap(s02[1], s13[1], false, false);
        __builtin_amdgcn_raw_buffer_store_b128(u32x4{t01[0], t01[1], t23[0], t23[1]}, hrs, hoff[tt], 0, 16);
      }
    });
  });
  STAMP(2, 4);
  u32* cnt = (u32*)(ws + WS_CNT + rt * 128);
  asm volatile("s_waitcnt vmcnt(0)\n\ts_barrier" ::: "memory");
  STAMP(2, 5);
  if (w == 0) {
    if (lane == 0) __hip_atomic_fetch_add(cnt, 1u, __ATOMIC_RELAXED, __HIP_MEMORY_SCOPE_AGENT);
    for (int spins = 0; spins < SPIN_MAX; ++spins) {
      const u32 v = __builtin_amdgcn_readfirstlane(__hip_atomic_load(cnt, __ATOMIC_RELAXED, __HIP_MEMORY_SCOPE_AGENT));
      if (v >= 16u) break;
      if (KB_POLL_SLEEP > 0) __builtin_amdgcn_s_sleep(KB_POLL_SLEEP);
    }
    wait_vm<0>();
  } else {
    const char* wsrc = c.wd - (size_t)w * 1024;
    for (int e = EPI_FILL + w - 1; e < 32; e += 3) {
      const char* src = wsrc + (size_t)e * 4096;
      const u32 dst = c.sb + e * 4096;
      glds16<0>(c.lv, src, dst); glds16<1024>(c.lv, src + 1024, dst); glds16<2048>(c.lv, src + 2048, dst); glds16<3072>(c.lv, src + 3072, dst);
    }
  }
  barrier();
  STAMP(2, 6);

  const char* hb = ws + WS_H + (size_t)(8 * rt + 4 * half + w) * KS2 * 1024;
  u32x4 Hf[KS2];
  constexpr int P3_AHEAD = P3_AHEAD_;
  auto ldH = [&](auto KS) { constexpr int ks = KS; if constexpr (ks < KS2) gld_a_sc1<(ks & 3) * 1024>(Hf[ks], c.lv, hb + (ks >> 2) * 4096); };
  static_for<P3_AHEAD>([&](auto KS) { ldH(KS); });
  STAMP(3, 0);
  f32x4 o4[4];
  u32x4 Af[3][4];
  const u32 la0 = c.sb + lane * 16, la1 = la0 + 65536, la2 = la0 + 131072;
  auto rdA = [&](auto KS, auto DF) {
    constexpr int ks = KS, df = DF;
    if constexpr (ks < KB_NL) ds_b128<(ks & 15) * 4096 + df * 1024>(Af[ks % 3][df], ks < 16 ? la0 : ks < 32 ? la1 : la2);
  };
  wait_vm<P3_AHEAD>();
  barrier();
  static_for<4>([&](auto DF) { rdA(std::integral_constant<int, 0>{}, DF); });
  static_for<4>([&](auto DF) { rdA(std::integral_constant<int, 1>{}, DF); });
  static_for<KS2>([&](auto KS) {
    constexpr int ks = KS;
    wait_vm<(ks + P3_AHEAD <= KS2 ? P3_AHEAD - 1 : KS2 - 1 - ks)>();
    if constexpr (ks + 1 < KB_NL) wait_lgkm<4>(); else wait_lgkm<0>();
    pin_a(Hf[ks]);
    static_for<4>([&](auto DF) {
      constexpr int df = DF;
      if constexpr (ks < KB_NL) { pin(Af[ks % 3][df]); mfma_va<ks == 0>(o4[df], Af[ks % 3][df], Hf[ks]); }
      else { pin_a(Wr[ks - KB_NL][df]); mfma_aa(o4[df], Wr[ks - KB_NL][df], Hf[ks]); }
      rdA(std::integral_constant<int, ks + 2>{}, DF);
      if constexpr (df == 1 && ks + P3_AHEAD < KS2) ldH(std::integral_constant<int, ks + P3_AHEAD>{});
    });
  });
  mfma_fence(o4[0], o4[1], o4[2], o4[3]);
  u32x4 o0, o1;
#pragma unroll
  for (int df = 0; df < 2; ++df) {
    o0[2 * df] = pk2(o4[df][0] + blo(res[df][0]), o4[df][1] + bhi(res[df][0]));
    o0[2 * df + 1] = pk2(o4[df][2] + blo(res[df][1]), o4[df][3] + bhi(res[df][1]));
    o1[2 * df] = pk2(o4[2 + df][0] + blo(res[2 + df][0]), o4[2 + df][1] + bhi(res[2 + df][0]));
    o1[2 * df + 1] = pk2(o4[2 + df][2] + blo(res[2 + df][1]), o4[2 + df][3] + bhi(res[2 + df][1]));
  }
  const __amdgpu_buffer_rsrc_t ors = __builtin_amdgcn_make_buffer_rsrc(out, 0, S * D * 2, 0x00020000);
  const u32 ooff = (u32)((s0 + 64 * half + 16 * w + n) * D + 64 * dt + 8 * g) * 2;
  __builtin_amdgcn_raw_buffer_store_b128(o0, ors, ooff, 0, 16);
  __builtin_amdgcn_raw_buffer_store_b128(o1, ors, ooff + 64, 0, 16);
  asm volatile("kb_code_end:");
  STAMP(2, 7);
}

extern "C" void kernel_launch(void* const* d_in, const int* in_sizes, int n_in, void* d_out, int out_size, void* d_ws,
                              size_t ws_size, hipStream_t stream) {
  const u16 *q = (const u16*)d_in[0], *k = (const u16*)d_in[1], *v = (const u16*)d_in[2], *shk = (const u16*)d_in[3];
  const u16 *ssmk = (const u16*)d_in[4], *dskip = (const u16*)d_in[5];
  const u16 *wg = (const u16*)d_in[7], *wu = (const u16*)d_in[8], *wd = (const u16*)d_in[9];
  char* ws = (char*)d_ws;
  (void)hipFuncSetAttribute((const void*)ka_conv, hipFuncAttributeMaxDynamicSharedMemorySize, KA_LDS);
  (void)hipFuncSetAttribute((const void*)kb_mlp, hipFuncAttributeMaxDynamicSharedMemorySize, KB_LDS);
  ka_conv<<<512, 256, KA_LDS, stream>>>(q, k, v, shk, ssmk, dskip, wg, wu, wd, ws);
  kb_mlp<<<256, 256, KB_LDS, stream>>>(ws, (u16*)d_out);
}
```

```cpp
#include <hip/hip_runtime.h>
#include <stdint.h>
#include <utility>

typedef unsigned short u16;
typedef unsigned u32;
typedef __attribute__((ext_vector_type(2))) u32 u32x2;
typedef __attribute__((ext_vector_type(4))) u32 u32x4;
typedef __attribute__((ext_vector_type(2))) float f32x2;
typedef __attribute__((ext_vector_type(4))) float f32x4;
typedef __attribute__((ext_vector_type(2))) __bf16 bf16x2;
typedef __attribute__((ext_vector_type(8))) __bf16 bf16x8;
typedef __attribute__((ext_vector_type(4))) short s16x4;
#define LDSP __attribute__((address_space(3)))
#define DI __device__ __forceinline__

constexpr int S = 2048, D = 512, F = 1376;
constexpr int NU = F / 8;
constexpr int KS1 = D / 32;
constexpr int KS2 = F / 32;
constexpr float EPS = 1e-5f;

constexpr size_t WS_XR = 0;
constexpr size_t WS_QT = WS_XR + (size_t)D * S * 2;
constexpr size_t WS_HT = WS_QT + (size_t)D * S * 2;
constexpr size_t WS_W1 = WS_HT + (size_t)D * S * 2;
constexpr size_t WS_WD = WS_W1 + (size_t)NU * KS1 * 1024;
constexpr size_t WS_H = WS_WD + (size_t)8 * KS2 * 4 * 1024;
constexpr size_t WS_CS = WS_H + (size_t)(S / 16) * KS2 * 1024;
constexpr size_t WS_SA = WS_CS + (size_t)KS1 * NU * 16 * 4;
constexpr size_t WS_SB = WS_SA + (size_t)S * 4;
constexpr size_t WS_CNT = WS_SB + (size_t)S * 4;
constexpr size_t WS_FLA = WS_CNT + 16 * 128;
constexpr size_t WS_FLG = WS_FLA + 256 * 8;
constexpr size_t WS_FLS = WS_FLG + 256 * 8;
constexpr size_t WS_FLZ = WS_FLS + 256 * 8;
constexpr size_t WS_HNT = WS_FLZ + 8 * 128;
constexpr size_t WS_EPO = WS_HNT + 256 * 8;

template <int... Is, class Fn> DI void static_for_(std::integer_sequence<int, Is...>, Fn&& f) { (f(std::integral_constant<int, Is>{}), ...); }
template <int N, class Fn> DI void static_for(Fn&& f) { static_for_(std::make_integer_sequence<int, N>{}, f); }

DI float blo(u32 u) { return __uint_as_float(u << 16); }
DI float bhi(u32 u) { return __uint_as_float(u & 0xffff0000u); }
DI u32 pk2(float a, float b) { f32x2 t = {a, b}; return __builtin_bit_cast(u32, __builtin_convertvector(t, bf16x2)); }
DI f32x4 mfma16(u32x4 a, u32x4 b, f32x4 c) {
  return __builtin_amdgcn_mfma_f32_16x16x32_bf16(__builtin_bit_cast(bf16x8, a), __builtin_bit_cast(bf16x8, b), c, 0, 0, 0);
}
DI u32x2 tr_read(u32 lds_byte) {
  return __builtin_bit_cast(u32x2, __builtin_amdgcn_ds_read_tr16_b64_v4i16((LDSP s16x4*)(size_t)lds_byte));
}
DI u32x4 tr_frag(u32 a0, u32 a1) { u32x2 x = tr_read(a0), y = tr_read(a1); return u32x4{x[0], x[1], y[0], y[1]}; }
DI u32x4 cat(u32x2 x, u32x2 y) { return __builtin_shufflevector(x, y, 0, 1, 2, 3); }
template <int CTRL> DI float dpp_add(float v) {
  return v + __builtin_bit_cast(float, __builtin_amdgcn_update_dpp(0, __builtin_bit_cast(int, v), CTRL, 0xf, 0xf, true));
}
DI float sum_rows(float v) {
  u32 u = __float_as_uint(v);
  auto a = __builtin_amdgcn_permlane16_swap(u, u, false, false);
  v = __uint_as_float(a[0]) + __uint_as_float(a[1]);
  u = __float_as_uint(v);
  auto b = __builtin_amdgcn_permlane32_swap(u, u, false, false);
  return __uint_as_float(b[0]) + __uint_as_float(b[1]);
}
DI float wave_sum(float v) {
  v = dpp_add<0xb1>(v);
  v = dpp_add<0x4e>(v);
  v = dpp_add<0x124>(v);
  v = dpp_add<0x128>(v);
  return sum_rows(v);
}
DI __amdgpu_buffer_rsrc_t ws_rsrc(char* ws) { return __builtin_amdgcn_make_buffer_rsrc(ws, 0, 0x04000000, 0x00020000); }
DI void st16_wt(__amdgpu_buffer_rsrc_t r, u32 off, u32x4 v) { __builtin_amdgcn_raw_buffer_store_b128(v, r, off, 0, 16); }
DI int slice_u0(int j) { return j < 12 ? 11 * j : 132 + 10 * (j - 12); }
DI int slice_nu(int j) { return j < 12 ? 11 : 10; }

template <bool FIRST = false> DI void mfma_vv(f32x4& acc, const u32x4& a, const u32x4& b) {
  if constexpr (FIRST) asm volatile("v_mfma_f32_16x16x32_bf16 %0, %1, %2, 0" : "=&v"(acc) : "v"(a), "v"(b));
  else asm volatile("v_mfma_f32_16x16x32_bf16 %0, %1, %2, %0" : "+v"(acc) : "v"(a), "v"(b));
}
template <bool FIRST = false> DI void mfma_av(f32x4& acc, const u32x4& a, const u32x4& b) {
  if constexpr (FIRST) asm volatile("v_mfma_f32_16x16x32_bf16 %0, %1, %2, 0" : "=&v"(acc) : "a"(a), "v"(b));
  else asm volatile("v_mfma_f32_16x16x32_bf16 %0, %1, %2, %0" : "+v"(acc) : "a"(a), "v"(b));
}
template <int OFF> DI void ds_b64(u32x2& d, u32 addr) { asm volatile("ds_read_b64 %0, %1 offset:%c2" : "=v"(d) : "v"(addr), "i"(OFF) : "memory"); }
template <int OFF> DI void ds_b128(u32x4& d, u32 addr) { asm volatile("ds_read_b128 %0, %1 offset:%c2" : "=v"(d) : "v"(addr), "i"(OFF) : "memory"); }
template <int OFF> DI void ds_tr(u32x2& d, u32 addr) { asm volatile("ds_read_b64_tr_b16 %0, %1 offset:%c2" : "=v"(d) : "v"(addr), "i"(OFF) : "memory"); }
template <int OFF> DI void gld_a(u32x4& d, u32 voff, const void* sbase) {
  asm volatile("global_load_dwordx4 %0, %1, %2 offset:%c3" : "=a"(d) : "v"(voff), "s"(sbase), "i"(OFF) : "memory");
}
template <int OFF> DI void gld_v(u32x4& d, u32 voff, const void* sbase) {
  asm volatile("global_load_dwordx4 %0, %1, %2 offset:%c3" : "=v"(d) : "v"(voff), "s"(sbase), "i"(OFF) : "memory");
}
template <int OFF> DI void gld2_v(u32x2& d, u32 voff, const void* sbase) {
  asm volatile("global_load_dwordx2 %0, %1, %2 offset:%c3" : "=v"(d) : "v"(voff), "s"(sbase), "i"(OFF) : "memory");
}
template <int LOFF> DI void glds16(u32 voff, const void* sbase, u32 ldsbase) {
  asm volatile("s_add_u32 m0, %2, %c3\n\ts_nop 0\n\tglobal_load_lds_dwordx4 %0, %1" ::"v"(voff), "s"(sbase), "s"(ldsbase), "i"(LOFF) : "memory", "scc");
}
template <int N> DI void wait_vm() { asm volatile("s_waitcnt vmcnt(%c0)" ::"i"(N) : "memory"); }
template <int N> DI void wait_lgkm() { asm volatile("s_waitcnt lgkmcnt(%c0)" ::"i"(N) : "memory"); }
template <int OFF> DI void gld_a_sc1(u32x4& d, u32 voff, const void* sbase) {
  asm volatile("global_load_dwordx4 %0, %1, %2 offset:%c3 sc1" : "=a"(d) : "v"(voff), "s"(sbase), "i"(OFF) : "memory");
}
DI void mfma_aa(f32x4& acc, const u32x4& a, const u32x4& b) {
  asm volatile("v_mfma_f32_16x16x32_bf16 %0, %1, %2, %0" : "+v"(acc) : "a"(a), "a"(b));
}
template <bool FIRST = false> DI void mfma_va(f32x4& acc, const u32x4& a, const u32x4& b) {
  if constexpr (FIRST) asm volatile("v_mfma_f32_16x16x32_bf16 %0, %1, %2, 0" : "=&v"(acc) : "v"(a), "a"(b));
  else asm volatile("v_mfma_f32_16x16x32_bf16 %0, %1, %2, %0" : "+v"(acc) : "v"(a), "a"(b));
}
DI void mfma_fence(f32x4& a, f32x4& b, f32x4& c, f32x4& d) { asm volatile("s_nop 7\n\ts_nop 1" : "+v"(a), "+v"(b), "+v"(c), "+v"(d)); }
DI void after_fence(f32x4& a, f32x4& b, f32x4& c, f32x4& d) { asm volatile("" : "+v"(a), "+v"(b), "+v"(c), "+v"(d)); }
template <int STAGE> DI void dpp_stage(float& v) {
  if constexpr (STAGE == 0) asm volatile("v_add_f32_dpp %0, %0, %0 quad_perm:[1,0,3,2] row_mask:0xf bank_mask:0xf" : "+v"(v));
  if constexpr (STAGE == 1) asm volatile("v_add_f32_dpp %0, %0, %0 quad_perm:[2,3,0,1] row_mask:0xf bank_mask:0xf" : "+v"(v));
  if constexpr (STAGE == 2) asm volatile("v_add_f32_dpp %0, %0, %0 row_ror:4 row_mask:0xf bank_mask:0xf" : "+v"(v));
  if constexpr (STAGE == 3) asm volatile("v_add_f32_dpp %0, %0, %0 row_ror:8 row_mask:0xf bank_mask:0xf" : "+v"(v));
}
DI void barrier() { asm volatile("s_barrier" ::: "memory"); }
DI void pin(u32x4& x) { asm volatile("" : "+v"(x)); }
DI void pin(u32x2& x) { asm volatile("" : "+v"(x)); }
DI void pin4(f32x4& x) { asm volatile("" : "+v"(x)); }
DI void pin_a(u32x4& x) { asm volatile("" : "+a"(x)); }

template <class Gate>
DI void w_job(int b, const u16* __restrict__ wg, const u16* __restrict__ wu, const u16* __restrict__ wd, char* __restrict__ ws, char* smem, Gate&& gate) {
  const int t = threadIdx.x, lane = t & 63, w = t >> 6;
  char* tile = smem;
  char* tile2 = smem + 32 * 368;
  const u32 tb = (u32)(size_t)(LDSP char*)smem;
  const int i16 = lane & 15, qd = i16 >> 2, p = i16 & 3, g = lane >> 4;
  const int ks = b & 15, j = b >> 4;
  const int U0 = slice_u0(j), nu = slice_nu(j);
  u32x4 g4[2], u4[2], d4[2];
#pragma unroll
  for (int rep = 0; rep < 2; ++rep) {
    int idx = t + 256 * rep, r = idx / 11, u = idx - 11 * r;
    bool ok = r < 32 && u < nu;
    size_t off = ok ? (size_t)(32 * ks + r) * F + 8 * (U0 + u) : 0;
    g4[rep] = *(const u32x4*)(wg + off); u4[rep] = *(const u32x4*)(wu + off);
  }
  const int ks2 = b >> 2, dp = b & 3;
  if (b < KS2 * 4) {
#pragma unroll
    for (int rep = 0; rep < 2; ++rep) {
      int idx = t + 256 * rep, r = idx >> 4, c = idx & 15;
      d4[rep] = *(const u32x4*)(wd + (size_t)(32 * ks2 + r) * D + 128 * dp + 8 * c);
    }
  }
#pragma unroll
  for (int rep = 0; rep < 2; ++rep) {
    int idx = t + 256 * rep, r = idx / 11, u = idx - 11 * r;
    if (r < 32 && u < nu) {
      *(u32x4*)(tile + r * 368 + u * 32) = u32x4{g4[rep][0], u4[rep][0], g4[rep][1], u4[rep][1]};
      *(u32x4*)(tile + r * 368 + u * 32 + 16) = u32x4{g4[rep][2], u4[rep][2], g4[rep][3], u4[rep][3]};
    }
  }
  if (b < KS2 * 4) {
#pragma unroll
    for (int rep = 0; rep < 2; ++rep) { int idx = t + 256 * rep, r = idx >> 4, c = idx & 15; *(u32x4*)(tile2 + r * 272 + c * 16) = d4[rep]; }
  }
  __syncthreads();
  gate();
  const u32x4 ones = {0x3f803f80u, 0x3f803f80u, 0x3f803f80u, 0x3f803f80u};
  const __amdgpu_buffer_rsrc_t wr = ws_rsrc(ws);
  for (int u = w; u < nu; u += 4) {
    u32 a = tb + (8 * g + qd) * 368 + u * 32 + 8 * p;
    u32x4 fr = tr_frag(a, a + 4 * 368);
    st16_wt(wr, (u32)WS_W1 + (((U0 + u) * KS1 + ks) * 64 + lane) * 16, fr);
    f32x4 cs = mfma16(fr, ones, f32x4{0.f, 0.f, 0.f, 0.f});
    if (i16 == 0) st16_wt(wr, (u32)WS_CS + ((ks * NU + U0 + u) * 16 + 4 * g) * 4, __builtin_bit_cast(u32x4, cs));
  }
  if (b < KS2 * 4) {
#pragma unroll
    for (int rep = 0; rep < 2; ++rep) {
      int fi = w + 4 * rep, dtl = fi >> 2, df = fi & 3;
      u32 a = tb + 32 * 368 + (8 * g + qd) * 272 + (64 * dtl + 32 * (df >> 1) + 8 * p + 4 * (df & 1)) * 2;
      u32x4 fr = tr_frag(a, a + 4 * 272);
      st16_wt(wr, (u32)WS_WD + ((((2 * dp + dtl) * KS2 + ks2) * 4 + df) * 64 + lane) * 16, fr);
    }
  }
}

constexpr int KA_CS = 4160;
constexpr int KA_KC = 4 * KA_CS;
constexpr int KA_XR = 2 * KA_KC;
constexpr int KA_RED = KA_XR + 2 * 4608;
constexpr int KA_LDS = KA_RED + 4 * 4096;
constexpr int KA_TK = KA_XR, KA_TV = KA_TK + 72 * 128, KA_TQ = KA_TV + 64 * 128;
static_assert(KA_TQ + 64 * 128 == KA_LDS, "tile overlay");
constexpr int SPIN_MAX = 1 << 22;
constexpr int HELP_PRE = 16;
constexpr int HELP_SLEEP = 20;
constexpr unsigned long long HINT_TICKS = 800;

template <int OFF> DI void gld_v_sc1(u32x4& d, u32 voff, const void* sbase) {
  asm volatile("global_load_dwordx4 %0, %1, %2 offset:%c3 sc1" : "=v"(d) : "v"(voff), "s"(sbase), "i"(OFF) : "memory");
}
template <int OFF> DI void gld2_v_sc1(u32x2& d, u32 voff, const void* sbase) {
  asm volatile("global_load_dwordx2 %0, %1, %2 offset:%c3 sc1" : "=v"(d) : "v"(voff), "s"(sbase), "i"(OFF) : "memory");
}
DI bool tags_ok(const u32x4& f, const u32x2& tag) { return f[0] == tag[0] && f[1] == tag[1] && f[2] == tag[0] && f[3] == tag[1]; }

__global__ __launch_bounds__(256, 2) void ka_conv(const u16* __restrict__ q, const u16* __restrict__ k, const u16* __restrict__ v,
                                                  const u16* __restrict__ shk, const u16* __restrict__ ssmk, const u16* __restrict__ dskip,
                                                  const u16* __restrict__ wg, const u16* __restrict__ wu, const u16* __restrict__ wd,
                                                  char* __restrict__ ws) {
  extern __shared__ __attribute__((aligned(16))) char smem[];
  const int t = threadIdx.x, lane = t & 63;
  const int w = __builtin_amdgcn_readfirstlane(t >> 6);
  const int b = blockIdx.x;
  if (b >= 256) {
    const int hb = b - 256, srow = 8 * hb + 2 * w;
    const __amdgpu_buffer_rsrc_t wr = ws_rsrc(ws);
    u32x2 epo; u32x4 qa, qb;
    gld2_v_sc1<0>(epo, 0, ws + WS_EPO);
    __builtin_amdgcn_s_sleep(HELP_PRE);
    gld_v<0>(qa, lane * 16, (const char*)q + (size_t)srow * 1024);
    gld_v<1024>(qb, lane * 16, (const char*)q + (size_t)srow * 1024);
    if (b < 264 && w == 1) {
      const char* pc; u32 len;
      asm volatile("s_getpc_b64 %0\n1:\n\ts_mov_b32 %1, kb_code_end-1b" : "=s"(pc), "=s"(len));
      const u32 lb = (u32)(size_t)(LDSP char*)smem + 32768;
      for (u32 o0 = 0; o0 < len; o0 += 64 * 128) {
        u32 o = o0 + lane * 128; o = o < len ? o : 0;
        asm volatile("s_mov_b32 m0, %2\n\ts_nop 0\n\tglobal_load_lds_dword %0, %1" ::"v"(o), "s"(pc), "s"(lb) : "memory");
      }
    }
    wait_vm<0>();
    asm volatile("" : "+v"(qa), "+v"(qb), "+v"(epo));
    const unsigned long long want = (((unsigned long long)epo[1] << 32) | epo[0]) + 1ull;
    {
      float sa = 0.f, sbb = 0.f;
#pragma unroll
      for (int e = 0; e < 4; ++e) { sa += blo(qa[e]) + bhi(qa[e]); sbb += blo(qb[e]) + bhi(qb[e]); }
      const float ma = wave_sum(sa) * (1.f / D), mb = wave_sum(sbb) * (1.f / D);
      float va = 0.f, vb = 0.f;
#pragma unroll
      for (int e = 0; e < 4; ++e) {
        float u0 = blo(qa[e]) - ma, u1 = bhi(qa[e]) - ma, u2 = blo(qb[e]) - mb, u3 = bhi(qb[e]) - mb;
        va += u0 * u0 + u1 * u1; vb += u2 * u2 + u3 * u3;
      }
      va = wave_sum(va) * (1.f / D); vb = wave_sum(vb) * (1.f / D);
      const float ra = __builtin_amdgcn_rsqf(va + EPS), rb = __builtin_amdgcn_rsqf(vb + EPS);
      const u32x2 val = lane == 0 ? u32x2{__float_as_uint(ra), __float_as_uint(rb)} : u32x2{__float_as_uint(-ma * ra), __float_as_uint(-mb * rb)};
      const u32 off = lane == 0 ? (u32)WS_SA + srow * 4 : lane == 1 ? (u32)WS_SB + srow * 4 : 0xfffffff0u;
      __builtin_amdgcn_raw_buffer_store_b64(val, wr, off, 0, 16);
    }
    asm volatile("s_waitcnt vmcnt(0)\n\ts_barrier" ::: "memory");
    if (t == 0) __builtin_amdgcn_raw_buffer_store_b64(u32x2{(u32)want, (u32)(want >> 32)}, wr, (u32)WS_FLS + hb * 8, 0, 16);
    if (hb == 1 && w == 1) {
      const unsigned long long* fs = (const unsigned long long*)(ws + WS_FLS) + lane;
      for (int spins = 0; spins < SPIN_MAX; ++spins) {
        bool ok = true;
#pragma unroll
        for (int i = 0; i < 4; ++i) ok &= __hip_atomic_load(fs + 64 * i, __ATOMIC_RELAXED, __HIP_MEMORY_SCOPE_AGENT) == want;
        if (__all(ok)) break;
        __builtin_amdgcn_s_sleep(1);
      }
      __builtin_amdgcn_raw_buffer_store_b64(u32x2{(u32)want, (u32)(want >> 32)}, wr, lane < 8 ? (u32)WS_FLZ + lane * 128 : 0xfffffff0u, 0, 16);
    } else __builtin_amdgcn_s_sleep(HELP_SLEEP);
    w_job(hb, wg, wu, wd, ws, smem, [&]() {
      if (hb == 1 && w == 1) {
        const unsigned long long* fa = (const unsigned long long*)(ws + WS_FLA) + lane;
        const u32x2 tg = {(u32)want, (u32)(want >> 32)};
        for (int spins = 0; spins < SPIN_MAX; ++spins) {
          bool ok = true;
#pragma unroll
          for (int i = 0; i < 4; ++i) ok &= __hip_atomic_load(fa + 64 * i, __ATOMIC_RELAXED, __HIP_MEMORY_SCOPE_AGENT) == want;
          if (__all(ok)) break;
          __builtin_amdgcn_s_sleep(16);
        }
        if (lane == 0) __builtin_amdgcn_raw_buffer_store_b64(tg, wr, (u32)WS_EPO, 0, 16);
      }
      if (w == 0) {
        const unsigned long long* hp = (const unsigned long long*)(ws + WS_HNT) + hb;
        const unsigned long long t0 = __builtin_amdgcn_s_memrealtime();
        while (__hip_atomic_load(hp, __ATOMIC_RELAXED, __HIP_MEMORY_SCOPE_AGENT) != want && __builtin_amdgcn_s_memrealtime() - t0 < HINT_TICKS)
          __builtin_amdgcn_s_sleep(8);
      }
      __builtin_amdgcn_s_barrier();
    });
    return;
  }
  const int x = b & 7, bi = b >> 3, s0 = 64 * bi;
  const int ch0 = 64 * x + 2 * bi;
  const u32 sb = (u32)(size_t)(LDSP char*)smem;
  const int n = lane & 15, g = lane >> 4, qd = n >> 2, p = n & 3;
  const int d = 64 * x + 16 * w + n;
  const __amdgpu_buffer_rsrc_t wr = ws_rsrc(ws);

  u32x2 epo, sh; u32x4 kp[2], kc[2];
  gld2_v_sc1<0>(epo, 0, ws + WS_EPO);
  gld2_v<0>(sh, d * 8, shk);
  const int r8 = lane >> 3, c8 = lane & 7;
  auto voff = [&](int instr) { const int f = ((r8 >> 1) & 1) | ((instr & 1) << 1); return (u32)((((c8 >> 1) ^ f) * 2 + (c8 & 1)) * 16); };
  const size_t col = (size_t)x * 128;
  {
    int row = s0 - 8 + 8 * w + r8; row = row < 0 ? 0 : row;
    glds16<0>((u32)row * 1024 + voff(w), (const char*)k + col, sb + KA_TK + w * 1024);
    glds16<0>((u32)(s0 + 8 * w + r8) * 1024 + voff(w), (const char*)v + col, sb + KA_TV + w * 1024);
    glds16<0>((u32)(s0 - 8 + 8 * (w + 4) + r8) * 1024 + voff(w), (const char*)k + col, sb + KA_TK + (w + 4) * 1024);
    glds16<0>((u32)(s0 + 8 * (w + 4) + r8) * 1024 + voff(w), (const char*)v + col, sb + KA_TV + (w + 4) * 1024);
    if (w == 0) glds16<0>((u32)(s0 + 56 + r8) * 1024 + voff(8), (const char*)k + col, sb + KA_TK + 8 * 1024);
  }
#pragma unroll
  for (int cc = 0; cc < 2; ++cc) {
    const char* kr = (const char*)ssmk + (size_t)(ch0 + cc) * 4096;
    gld_v<0>(kp[cc], (u32)(t > 0 ? t - 1 : 0) * 16, kr);
    gld_v<0>(kc[cc], (u32)t * 16, kr);
  }
  glds16<0>((u32)(s0 + 8 * w + r8) * 1024 + voff(w), (const char*)q + col, sb + KA_TQ + w * 1024);
  glds16<0>((u32)(s0 + 8 * (w + 4) + r8) * 1024 + voff(w), (const char*)q + col, sb + KA_TQ + (w + 4) * 1024);
  const float dsk = blo(dskip[ch0 + (w >> 1)]);
  if (b == 0 && t < 16) *(u32*)(ws + WS_CNT + t * 128) = 0u;

  wait_vm<6>();
  barrier();
  asm volatile("" : "+v"(sh), "+v"(epo));
  u32x2 tag;
  { const unsigned long long e = (((unsigned long long)epo[1] << 32) | epo[0]) + 1ull; tag = u32x2{(u32)e, (u32)(e >> 32)}; }
  auto taddr = [&](int base, int tr) { const int f = ((tr >> 1) & 1) | ((tr >> 2) & 2); return sb + base + tr * 128 + ((w ^ f) * 32) + 8 * p; };
  {
    const float a0 = blo(sh[0]), a1 = bhi(sh[0]), a2 = blo(sh[1]), a3 = bhi(sh[1]);
#pragma unroll
    for (int e = 0; e < 2; ++e) {
      const int rg = g + 4 * e;
      u32x2 km = tr_read(taddr(KA_TK, 8 * rg + 4 + qd));
      const u32x2 k0 = tr_read(taddr(KA_TK, 8 * rg + 8 + qd)), k1 = tr_read(taddr(KA_TK, 8 * rg + 12 + qd));
      const u32x2 v0 = tr_read(taddr(KA_TV, 8 * rg + qd)), v1 = tr_read(taddr(KA_TV, 8 * rg + 4 + qd));
      if (s0 + rg == 0) km = u32x2{0, 0};
      const float kf[11] = {bhi(km[0]), blo(km[1]), bhi(km[1]), blo(k0[0]), bhi(k0[0]), blo(k0[1]), bhi(k0[1]), blo(k1[0]), bhi(k1[0]), blo(k1[1]), bhi(k1[1])};
      const float vf[8] = {blo(v0[0]), bhi(v0[0]), blo(v0[1]), bhi(v0[1]), blo(v1[0]), bhi(v1[0]), blo(v1[1]), bhi(v1[1])};
      float xo[8];
#pragma unroll
      for (int j = 0; j < 8; ++j) xo[j] = (kf[j + 3] * a0 + kf[j + 2] * a1 + kf[j + 1] * a2 + kf[j] * a3) * vf[j];
      const int ir = 2040 - (s0 + 8 * rg);
      st16_wt(wr, (u32)WS_XR + (d >> 1) * 8192 + (ir >> 5) * 128 + (d & 1) * 64 + (ir & 31) * 2, u32x4{pk2(xo[7], xo[6]), pk2(xo[5], xo[4]), pk2(xo[3], xo[2]), pk2(xo[1], xo[0])});
    }
  }
  asm volatile("s_waitcnt vmcnt(0)\n\ts_barrier" : "+v"(kp[0]), "+v"(kc[0]), "+v"(kp[1]), "+v"(kc[1]) :: "memory");
  if (t == 0) __builtin_amdgcn_raw_buffer_store_b64(tag, wr, (u32)WS_FLA + (x * 32 + bi) * 8, 0, 16);
#pragma unroll
  for (int e = 0; e < 2; ++e) {
    const int rg = g + 4 * e;
    const u32x2 q0 = tr_read(taddr(KA_TQ, 8 * rg + qd)), q1 = tr_read(taddr(KA_TQ, 8 * rg + 4 + qd));
    const int r0 = s0 + 8 * rg;
    st16_wt(wr, (u32)WS_QT + (d >> 1) * 8192 + (r0 >> 5) * 128 + (d & 1) * 64 + (r0 & 31) * 2, cat(q0, q1));
  }
#pragma unroll
  for (int cc = 0; cc < 2; ++cc) {
    const u32 m = t > 0 ? 0xffffffffu : 0u;
    u32 wv[6] = {kp[cc][0] & m, kp[cc][1] & m, kp[cc][2] & m, kp[cc][3] & m, kc[cc][0], kc[cc][1]};
    u32 o1[5];
#pragma unroll
    for (int i = 0; i < 5; ++i) o1[i] = __builtin_amdgcn_alignbit(wv[i + 1], wv[i], 16);
    char* base = smem + cc * KA_KC + (t + 1) * 16;
    *(u32x4*)(base + 0 * KA_CS) = u32x4{o1[0], o1[1], o1[2], o1[3]};
    *(u32x4*)(base + 1 * KA_CS) = u32x4{wv[1], wv[2], wv[3], wv[4]};
    *(u32x4*)(base + 2 * KA_CS) = u32x4{o1[1], o1[2], o1[3], o1[4]};
    *(u32x4*)(base + 3 * KA_CS) = u32x4{wv[2], wv[3], wv[4], wv[5]};
    if (t == 255) {
      const u32 z = 0;
      const u32 e0 = __builtin_amdgcn_alignbit(kc[cc][1], kc[cc][0], 16), e1 = __builtin_amdgcn_alignbit(kc[cc][2], kc[cc][1], 16);
      const u32 e2 = __builtin_amdgcn_alignbit(kc[cc][3], kc[cc][2], 16), e3 = __builtin_amdgcn_alignbit(z, kc[cc][3], 16);
      *(u32x4*)(base + 16 + 0 * KA_CS) = u32x4{e0, e1, e2, e3};
      *(u32x4*)(base + 16 + 1 * KA_CS) = u32x4{kc[cc][1], kc[cc][2], kc[cc][3], z};
      *(u32x4*)(base + 16 + 2 * KA_CS) = u32x4{e1, e2, e3, z};
      *(u32x4*)(base + 16 + 3 * KA_CS) = u32x4{kc[cc][2], kc[cc][3], z, z};
    }
  }
  if (t < 8) *(u32x4*)(smem + (t >> 2) * KA_KC + (t & 3) * KA_CS) = u32x4{0, 0, 0, 0};
  asm volatile("s_waitcnt vmcnt(0) lgkmcnt(0)\n\ts_barrier" ::: "memory");
  if (t == 0) __builtin_amdgcn_raw_buffer_store_b64(tag, wr, (u32)WS_FLG + (x * 32 + bi) * 8, 0, 16);
  if (t >= 64 && t < 64 + 60) { int i = t - 64; *(u32x4*)(smem + KA_XR + (i / 30) * 4608 + 4096 + (i % 30) * 16) = u32x4{0, 0, 0, 0}; }
  if (w == 0) {
    const unsigned long long* fp = (const unsigned long long*)(ws + WS_FLA) + x * 32 + (lane & 31);
    const unsigned long long want = ((unsigned long long)tag[1] << 32) | tag[0];
    for (int spins = 0; spins < SPIN_MAX; ++spins) {
      if (__all(__hip_atomic_load(fp, __ATOMIC_RELAXED, __HIP_MEMORY_SCOPE_AGENT) == want)) break;
      __builtin_amdgcn_s_sleep(1);
    }
  }
  asm volatile("s_waitcnt vmcnt(0)\n\ts_barrier" ::: "memory");

  const int c = w >> 1, half = w & 1;
  u32x2 qv[4], fz; u32x4 sa[4], sbv[4], fq;
  const u32 eo = (u32)(1024 * half + 16 * n + 4 * g);
  const char* qTc = ws + WS_QT + (size_t)ch0 * 4096 + c * 64;
  const u32 qo = (u32)(4096 * half + (n >> 1) * 128 + (n & 1) * 32 + 8 * g);
  {
    u32x4 x0, x1;
    gld_v_sc1<0>(x0, t * 16, ws + WS_XR + (size_t)ch0 * 4096);
    gld_v_sc1<0>(x1, t * 16 + 4096, ws + WS_XR + (size_t)ch0 * 4096);
    wait_vm<0>();
    asm volatile("" : "+v"(x0), "+v"(x1));
    char* dst = smem + KA_XR + ((t >> 2) & 1) * 4608 + (t >> 3) * 64 + (t & 3) * 16;
    *(u32x4*)dst = x0;
    *(u32x4*)(dst + 2048) = x1;
  }
  asm volatile("s_waitcnt lgkmcnt(0)\n\ts_barrier" ::: "memory");
  if (t == 0) __builtin_amdgcn_raw_buffer_store_b64(tag, wr, (u32)WS_HNT + b * 8, 0, 16);

  const u32 aa = sb + c * KA_KC + (n & 3) * KA_CS + 16 * g + 2 * (n & 12) + half * 256;
  const u32 ba = sb + KA_XR + c * 4608 + 480 - 32 * n + 16 * g + half * 256;
  f32x4 acc[8];
  u32x2 Alo[2][8], Ahi[2][8];
  u32x4 B[2][8];
  auto rd = [&](auto C0, auto R) {
    constexpr int c0 = C0, r = R, buf = c0 & 1;
    if constexpr (r >= 2 && r < 10) {
      constexpr int u = r - 2;
      ds_b128<512 * (7 - u) + 64 * c0>(B[buf][u], ba);
    } else {
      constexpr int ar = r < 2 ? r : r - 8, i = ar >> 1;
      if constexpr ((ar & 1) == 0) ds_b64<512 * i + 64 * c0>(Alo[buf][i], aa);
      else ds_b64<512 * i + 64 * c0 + 8>(Ahi[buf][i], aa);
    }
  };
  auto ldS = [&](auto E) {
    constexpr int e = E, i = e / 3, kind = e % 3;
    if constexpr (kind == 0) gld_v_sc1<i * 1024>(sa[i], eo * 4, ws + WS_SA);
    if constexpr (kind == 1) gld_v_sc1<i * 1024>(sbv[i], eo * 4, ws + WS_SB);
    if constexpr (kind == 2) gld2_v_sc1<i * 1024>(qv[i], qo, qTc);
  };
  auto ldF = [&](auto E) {
    constexpr int e = E;
    if constexpr (e == 0) gld2_v_sc1<0>(fz, x * 128, ws + WS_FLZ);
    else gld_v_sc1<0>(fq, (x * 32 + 2 * (lane & 15)) * 8, ws + WS_FLG);
  };
  auto okF = [&]() { return fz[0] == tag[0] && fz[1] == tag[1] && tags_ok(fq, tag); };
  static_for<24>([&](auto R) { rd(std::integral_constant<int, 0>{}, R); });
  static_for<4>([&](auto C0) {
    constexpr int c0 = C0, buf = c0 & 1;
    if constexpr (c0 == 2) {
      wait_vm<0>();
      asm volatile("" : "+v"(fz), "+v"(fq));
      if (!__all(okF())) {
        for (int spins = 0; spins < SPIN_MAX; ++spins) {
          ldF(std::integral_constant<int, 0>{}); ldF(std::integral_constant<int, 1>{});
          wait_vm<0>();
          asm volatile("" : "+v"(fz), "+v"(fq));
          if (__all(okF())) break;
          __builtin_amdgcn_s_sleep(1);
        }
      }
    }
    u32x4 A[8];
    static_for<36>([&](auto M) {
      constexpr int m = M;
      constexpr int c1 = m < 8 ? 0 : m < 15 ? 1 : m < 21 ? 2 : m < 26 ? 3 : m < 30 ? 4 : m < 33 ? 5 : m < 35 ? 6 : 7;
      constexpr int first = c1 * 8 - c1 * (c1 - 1) / 2;
      constexpr int u = m - first;
      if constexpr (m == 0) {
        if constexpr (c0 == 0) wait_lgkm<14>(); else wait_lgkm<0>();
        pin(Alo[buf][0]); pin(Ahi[buf][0]);
#pragma unroll
        for (int i = 0; i < 8; ++i) pin(B[buf][i]);
        A[0] = cat(Alo[buf][0], Ahi[buf][0]);
      }
      if constexpr (m == 8) {
        if constexpr (c0 == 0) wait_lgkm<8>();
#pragma unroll
        for (int i = 1; i < 8; ++i) { pin(Alo[buf][i]); pin(Ahi[buf][i]); A[i] = cat(Alo[buf][i], Ahi[buf][i]); }
      }
      mfma_vv<(c0 == 0 && c1 == 0)>(acc[c1 + u], A[c1], B[buf][u]);
      if constexpr (c0 < 3 && m < 24) rd(std::integral_constant<int, c0 + 1>{}, M);
      if constexpr (c0 == 1 && (m == 2 || m == 4)) ldF(std::integral_constant<int, (m - 2) / 2>{});
      if constexpr (c0 == 2 && m >= 1 && m < 25 && (m & 1) == 1) ldS(std::integral_constant<int, (m - 1) / 2>{});
    });
  });
  mfma_fence(acc[0], acc[1], acc[2], acc[3]);
  after_fence(acc[4], acc[5], acc[6], acc[7]);
  f32x4 fin[4];
  {
    f32x4* red = (f32x4*)(smem + KA_RED);
    if (half) {
#pragma unroll
      for (int i = 0; i < 4; ++i) { red[(w * 4 + i) * 64 + lane] = acc[i]; fin[i] = acc[4 + i]; }
      asm volatile("; half 1" ::: "memory");
    } else {
#pragma unroll
      for (int i = 0; i < 4; ++i) { red[(w * 4 + i) * 64 + lane] = acc[4 + i]; fin[i] = acc[i]; }
      asm volatile("; half 0" ::: "memory");
    }
    asm volatile("s_waitcnt lgkmcnt(0)\n\ts_barrier" ::: "memory");
#pragma unroll
    for (int i = 0; i < 4; ++i) fin[i] += red[((w ^ 1) * 4 + i) * 64 + lane];
  }
  wait_vm<0>();
#pragma unroll
  for (int i = 0; i < 4; ++i) asm volatile("" : "+v"(qv[i]), "+v"(sa[i]), "+v"(sbv[i]));
  const f32x2 dd = {dsk, dsk};
  u32x2 hv[4];
#pragma unroll
  for (int i = 0; i < 4; ++i) {
    const int s = 256 * (4 * half + i) + 16 * n + 4 * g;
    const u32x2 xq = *(LDSP const u32x2*)(size_t)(sb + KA_XR + c * 4608 + 2 * (2044 - s));
    const f32x2 x0 = {bhi(xq[1]), blo(xq[1])}, x1 = {bhi(xq[0]), blo(xq[0])};
    const f32x2 q0 = {blo(qv[i][0]), bhi(qv[i][0])}, q1 = {blo(qv[i][1]), bhi(qv[i][1])};
    const f32x2 z0 = x0 * dd + f32x2{fin[i][0], fin[i][1]}, z1 = x1 * dd + f32x2{fin[i][2], fin[i][3]};
    const f32x4 a4 = __builtin_bit_cast(f32x4, sa[i]), b4 = __builtin_bit_cast(f32x4, sbv[i]);
    const f32x2 t0 = q0 * f32x2{a4[0], a4[1]} + f32x2{b4[0], b4[1]};
    const f32x2 t1 = q1 * f32x2{a4[2], a4[3]} + f32x2{b4[2], b4[3]};
    const f32x2 h0 = t0 * z0 + q0, h1 = t1 * z1 + q1;
    hv[i] = u32x2{pk2(h0[0], h0[1]), pk2(h1[0], h1[1])};
  }
#pragma unroll
  for (int i = 0; i < 4; i += 2) {
    auto a = __builtin_amdgcn_permlane16_swap(hv[i][0], hv[i + 1][0], false, false);
    auto bb = __builtin_amdgcn_permlane16_swap(hv[i][1], hv[i + 1][1], false, false);
    const int s = 256 * (4 * half + i + (g & 1)) + 16 * n + 4 * (g & 2);
    st16_wt(wr, (u32)WS_HT + ((ch0 + c) * S + s) * 2, u32x4{a[0], bb[0], a[1], bb[1]});
  }
}

constexpr int KB_NL = 39;
constexpr int KB_ST = KB_NL * 4096;
constexpr int KB_LDS = KB_ST + 1024;
constexpr int EPI_FILL = 8;
constexpr int KB_RING = 8;

constexpr int P2_AHEAD = 5;
constexpr int P2_PRO = 3;
constexpr int p2_upto(int ks) {
  const int ramp = P2_PRO - 1 + 2 * (ks + 1), steady = ks + P2_AHEAD;
  const int u = ks < 0 ? P2_PRO - 1 : (ramp < steady ? ramp : steady);
  return u > 15 ? 15 : u;
}
constexpr int P2_XMAX = 5;
constexpr int P2_NEXTRA = 23;
constexpr int p2_extra(int ks) {
  const int tail0 = 16 - P2_AHEAD, rest = P2_NEXTRA - P2_AHEAD * P2_XMAX;
  if (ks >= tail0) return P2_XMAX;
  return (rest > 0 && ks >= tail0 - rest) ? 1 : 0;
}
constexpr int p2_extra_first(int ks) { int c = 0; for (int s = 0; s < ks; ++s) c += p2_extra(s); return c; }
constexpr int p2_extra_real(int ks) { int f = p2_extra_first(ks), n = p2_extra(ks); return f >= P2_NEXTRA ? 0 : (f + n > P2_NEXTRA ? P2_NEXTRA - f : n); }
static_assert(p2_extra_first(16) >= P2_NEXTRA, "all Wd operations are scheduled");
constexpr int p2_issue_step(int t) { for (int s = -1; s < KS1; ++s) if (p2_upto(s) >= t) return s; return KS1; }
constexpr int p2_younger(int t, int ks, int ops) {
  int c = (p2_upto(ks - 1) - t) * ops;
  for (int s = p2_issue_step(t); s < ks; ++s) if (s >= 0) c += p2_extra_real(s);
  return c;
}

struct KbCtx {
  u32 sb, gv, lv, lb0, lwd, csv;
  const char *hb0, *hb1, *w0, *w1, *w2, *cs0, *cs1, *cs2, *wd, *wdr;
};

template <int GRAM, class Setup>
DI void kb_phase2(const KbCtx& c, f32x4 (&acc)[3][8], f32x4 (&rsum)[2], f32x4 (&csp)[3], u32x4 (&Wr)[4][4], u32 (&ra)[8], Setup&& setup) {
  constexpr int OPS = GRAM ? 4 : 5;
  u32x4 W[KB_RING][3];
  u32x2 Bl[2][8], Bh[2][8];
  auto issue = [&](auto KS, auto O) {
    constexpr int ks = KS, o = O;
    if constexpr (o == 0) glds16<ks * 8192>(c.gv, c.hb0 + (size_t)ks * 131072, c.lb0);
    if constexpr (o == 1) gld_a<(ks & 3) * 1024>(W[ks % KB_RING][0], c.lv, c.w0 + (ks >> 2) * 4096);
    if constexpr (o == 2) gld_a<(ks & 3) * 1024>(W[ks % KB_RING][1], c.lv, c.w1 + (ks >> 2) * 4096);
    if constexpr (o == 3 && !GRAM) gld_a<(ks & 3) * 1024>(W[ks % KB_RING][2], c.lv, c.w2 + (ks >> 2) * 4096);
    if constexpr (o == 4) glds16<ks * 8192 + 1024>(c.gv, c.hb1 + (size_t)ks * 131072, c.lb0);
  };
  auto extra = [&](auto E) {
    constexpr int e = E;
    if constexpr (e < 7) glds16<(32 + e) * 4096>(c.lv, c.wd + (size_t)(32 + e) * 4096, c.lwd);
    else if constexpr (e < P2_NEXTRA) gld_a<((e - 7) & 3) * 1024>(Wr[(e - 7) >> 2][(e - 7) & 3], c.lv, c.wdr + ((e - 7) >> 2) * 4096);
  };
  auto rdB = [&](auto KS, auto R) {
    constexpr int ks = KS, r = R, i = r >> 1;
    if constexpr ((r & 1) == 0) ds_tr<(ks & 7) * 8192>(Bl[ks & 1][i], ra[i] + (ks >= 8 ? 65536 : 0));
    else ds_tr<(ks & 7) * 8192 + 1024>(Bh[ks & 1][i], ra[i] + (ks >= 8 ? 65536 : 0));
  };
  u32x4 c0, c1, c2;
  static_for<5>([&](auto O) { issue(std::integral_constant<int, 0>{}, O); });
  gld_v<0>(c0, c.csv, c.cs0); gld_v<0>(c1, c.csv, c.cs1); gld_v<0>(c2, c.csv, c.cs2);
  static_for<p2_upto(-1)>([&](auto KS) { static_for<5>([&](auto O) { issue(std::integral_constant<int, KS + 1>{}, O); }); });
  setup();
  u32x4 ones = {0x3f803f80u, 0x3f803f80u, 0x3f803f80u, 0x3f803f80u};
  pin(ones);
  wait_vm<p2_upto(-1) * OPS>();
  barrier();
  static_for<16>([&](auto R) { rdB(std::integral_constant<int, 0>{}, R); });
  asm volatile("" : "+v"(c0), "+v"(c1), "+v"(c2));
  float cv[12];
#pragma unroll
  for (int e = 0; e < 4; ++e) { cv[e] = __uint_as_float(c0[e]); cv[4 + e] = __uint_as_float(c1[e]); cv[8 + e] = __uint_as_float(c2[e]); }
  static_for<KS1>([&](auto KS) {
    constexpr int ks = KS, buf = ks & 1, slot = ks % KB_RING;
    constexpr int nmain = (p2_upto(ks) - p2_upto(ks - 1)) * 5, next = p2_extra_real(ks);
    wait_lgkm<0>();
    u32x4 B[8];
#pragma unroll
    for (int i = 0; i < 8; ++i) { pin(Bl[buf][i]); pin(Bh[buf][i]); }
#pragma unroll
    for (int i = 0; i < 8; ++i) B[i] = cat(Bl[buf][i], Bh[buf][i]);
    pin_a(W[slot][0]); pin_a(W[slot][1]);
    if constexpr (!GRAM) pin_a(W[slot][2]);
    static_for<26>([&](auto M) {
      constexpr int m = M, i = m / 3, u = m % 3;
      if constexpr (m >= 24) mfma_vv<ks == 0>(rsum[m - 24], ones, B[m - 24]);
      else if constexpr (u < 2 || !GRAM) mfma_av<ks == 0>(acc[u][i], W[slot][u], B[i]);
      else mfma_vv<ks == 0>(acc[2][i], B[i], B[i]);
      if constexpr (m == 1 && ks + 1 < KS1) { wait_vm<p2_younger(ks + 1, ks, OPS)>(); barrier(); }
      if constexpr (m >= 2 && m < 18 && ks + 1 < KS1) rdB(std::integral_constant<int, ks + 1>{}, std::integral_constant<int, m - 2>{});
      if constexpr (ks >= 9 && ks < 13 && m >= 2 && m < 14) dpp_stage<ks - 9>(cv[m - 2]);
      constexpr int v = m - (26 - nmain - next);
      if constexpr (v >= 0 && v < nmain) issue(std::integral_constant<int, p2_upto(ks - 1) + 1 + v / 5>{}, std::integral_constant<int, v % 5>{});
      if constexpr (v >= nmain) extra(std::integral_constant<int, p2_extra_first(ks) + v - nmain>{});
    });
  });
  mfma_fence(acc[0][0], acc[1][0], rsum[0], rsum[1]);
#pragma unroll
  for (int i = 0; i < 8; ++i) after_fence(acc[0][i], acc[1][i], acc[2][i], rsum[i & 1]);
#pragma unroll
  for (int u = 0; u < 3; ++u) csp[u] = f32x4{cv[4 * u], cv[4 * u + 1], cv[4 * u + 2], cv[4 * u + 3]};
}

__global__ __launch_bounds__(256) void kb_mlp(char* __restrict__ ws, u16* __restrict__ out) {
  extern __shared__ __attribute__((aligned(16))) char smem[];
  const int t = threadIdx.x, lane = t & 63;
  const int w = __builtin_amdgcn_readfirstlane(t >> 6);
  const int b = blockIdx.x, xcd = b & 7, bi = b >> 3;
  const int rt = (xcd >> 1) * 4 + (bi & 3);
  const int j = (xcd & 1) * 8 + (bi >> 2);
  const int half = j >> 3, dt = j & 7;
  const int U0 = slice_u0(j), nu = slice_nu(j);
  const int rem = nu - 8;
  const int nw = w < rem ? 3 : 2;
  const int uw = U0 + (w < rem ? 3 * w : 3 * rem + 2 * (w - rem));
  const int u2 = uw + (nw == 3 ? 2 : 0);
  const int n = lane & 15, g = lane >> 4;
  const int s0 = 128 * rt;
  auto sf = [&](int i) { return ((w + (i >> 1)) & 3) + 4 * (half ^ (i & 1)); };

  KbCtx c;
  c.sb = (u32)(size_t)(LDSP char*)smem;
  c.lv = lane * 16;
  c.gv = (u32)(g * 4096 + (((n >> 1) ^ (g | ((w & 1) << 2))) * 32 + (n & 1) * 16));
  c.hb0 = ws + WS_HT + (size_t)s0 * 2 + (size_t)(8 * w) * 4096;
  c.hb1 = c.hb0 + 4 * 4096;
  c.lb0 = c.sb + 8 * w * 256;
  c.w0 = ws + WS_W1 + (size_t)uw * KS1 * 1024;
  c.w1 = c.w0 + KS1 * 1024;
  c.w2 = ws + WS_W1 + (size_t)u2 * KS1 * 1024;
  c.cs0 = ws + WS_CS + (size_t)uw * 64;
  c.cs1 = c.cs0 + 64;
  c.cs2 = ws + WS_CS + (size_t)u2 * 64;
  c.csv = n * (NU * 64) + g * 16;
  c.wdr = ws + WS_WD + (size_t)dt * KS2 * 4096 + (size_t)KB_NL * 4096;
  c.wd = ws + WS_WD + (size_t)dt * KS2 * 4096 + (size_t)w * 1024;
  c.lwd = c.sb + 1024 * w;
  u32 ra[8], hoff[6];
  auto setup = [&]() {
    const int qd = n >> 2, p = n & 3;
    const int frow = qd | ((g & 1) << 2);
#pragma unroll
    for (int i = 0; i < 8; ++i) { ra[i] = c.sb + (8 * g + qd) * 256 + ((sf(i) ^ frow) * 32) + 8 * p; asm volatile("" : "+v"(ra[i])); }
#pragma unroll
    for (int tt = 0; tt < 6; ++tt) {
      const int q = 4 * tt + g, i = (q * 11) >> 5, u = q - 3 * i, U = uw + u;
      const u32 o = (u32)(((8 * rt + sf(i)) * KS2 + (U >> 2)) * 1024 + ((U & 3) * 16 + n) * 16);
      hoff[tt] = u < nw ? o : 0xfffffff0u;
      asm volatile("" : "+v"(hoff[tt]));
    }
  };
  f32x4 acc[3][8], cs[3], rsum[2];
  u32x4 Wr[4][4];
  if (w == 3) kb_phase2<1>(c, acc, rsum, cs, Wr, ra, setup);
  else kb_phase2<0>(c, acc, rsum, cs, Wr, ra, setup);

  u32x2 res[4];
  {
    const u32 rb = ra[0] + dt * 16384;
    ds_tr<0>(res[0], rb); ds_tr<1024>(res[1], rb); ds_tr<8192>(res[2], rb); ds_tr<9216>(res[3], rb);
  }
  float* stl = (float*)(smem + KB_ST);
  if (g == 0) {
#pragma unroll
    for (int i = 0; i < 2; ++i) stl[2 * (16 * sf(i) + n)] = rsum[i][0];
  }
  if (w == 3 && g == (n >> 2)) {
#pragma unroll
    for (int i = 0; i < 8; ++i) {
      f32x4 a = acc[2][i];
      float dg = (n & 2) ? ((n & 1) ? a[3] : a[2]) : ((n & 1) ? a[1] : a[0]);
      stl[2 * (16 * sf(i) + n) + 1] = dg;
    }
  }
  asm volatile("s_waitcnt lgkmcnt(0)\n\ts_barrier" : "+v"(res[0]), "+v"(res[1]), "+v"(res[2]), "+v"(res[3]) :: "memory");
  const __amdgpu_buffer_rsrc_t hrs = __builtin_amdgcn_make_buffer_rsrc(ws + WS_H, 0, (S / 16) * KS2 * 1024, 0x00020000);
  f32x2 sv[8];
#pragma unroll
  for (int i = 0; i < 8; ++i) sv[i] = *(const f32x2*)(stl + 2 * (16 * sf(i) + n));
  u32 hv[24];
  static_for<8>([&](auto I) {
    constexpr int i = I;
    const float mean = sv[i][0] * (1.f / D);
    const float var = fmaxf(sv[i][1] * (1.f / D) - mean * mean, 0.f) + EPS;
    const float rstd = __builtin_amdgcn_rsqf(var);
    const float rinv = var * rstd;
    const float ce = -1.4426950408889634f * rstd;
    const f32x2 nm = {-mean, -mean}, ce2 = {ce, ce}, ri2 = {rinv, rinv};
    static_for<3>([&](auto U_) {
      constexpr int u = U_;
      pin4(acc[u][i]);
      constexpr int slot = 3 * i + u;
      constexpr int wk = slot * EPI_FILL / 24;
      if constexpr (EPI_FILL > 0 && (slot + 1) * EPI_FILL / 24 > wk) glds16<wk * 4096>(c.lv, c.wd + (size_t)wk * 4096, c.lwd);
      const f32x4 a = acc[u][i];
      const f32x2 tg = (f32x2{a[0], a[1]} + nm * f32x2{cs[u][0], cs[u][1]}) * ce2;
      const f32x2 tu = f32x2{a[2], a[3]} + nm * f32x2{cs[u][2], cs[u][3]};
      const f32x2 e = {__builtin_amdgcn_exp2f(tg[0]), __builtin_amdgcn_exp2f(tg[1])};
      const f32x2 den = e * ri2 + ri2;
      const f32x2 o = tu * f32x2{__builtin_amdgcn_rcpf(den[0]), __builtin_amdgcn_rcpf(den[1])};
      hv[3 * i + u] = pk2(o[0], o[1]);
    });
    static_for<6>([&](auto T) {
      constexpr int tt = T;
      if constexpr ((4 * tt + 3) / 3 == i) {
        auto s02 = __builtin_amdgcn_permlane32_swap(hv[4 * tt], hv[4 * tt + 2], false, false);
        auto s13 = __builtin_amdgcn_permlane32_swap(hv[4 * tt + 1], hv[4 * tt + 3], false, false);
        auto t01 = __builtin_amdgcn_permlane16_swap(s02[0], s13[0], false, false);
        auto t23 = __builtin_amdgcn_permlane16_swap(s02[1], s13[1], false, false);
        __builtin_amdgcn_raw_buffer_store_b128(u32x4{t01[0], t01[1], t23[0], t23[1]}, hrs, hoff[tt], 0, 16);
      }
    });
  });
  u32* cnt = (u32*)(ws + WS_CNT + rt * 128);
  asm volatile("s_waitcnt vmcnt(0)\n\ts_barrier" ::: "memory");
  if (w == 0) {
    if (lane == 0) __hip_atomic_fetch_add(cnt, 1u, __ATOMIC_RELAXED, __HIP_MEMORY_SCOPE_AGENT);
    for (int spins = 0; spins < SPIN_MAX; ++spins) {
      const u32 v = __builtin_amdgcn_readfirstlane(__hip_atomic_load(cnt, __ATOMIC_RELAXED, __HIP_MEMORY_SCOPE_AGENT));
      if (v >= 16u) break;
      __builtin_amdgcn_s_sleep(1);
    }
    wait_vm<0>();
  } else {
    const char* wsrc = c.wd - (size_t)w * 1024;
    for (int e = EPI_FILL + w - 1; e < 32; e += 3) {
      const char* src = wsrc + (size_t)e * 4096;
      const u32 dst = c.sb + e * 4096;
      glds16<0>(c.lv, src, dst); glds16<1024>(c.lv, src + 1024, dst); glds16<2048>(c.lv, src + 2048, dst); glds16<3072>(c.lv, src + 3072, dst);
    }
  }
  barrier();

  const char* hb = ws + WS_H + (size_t)(8 * rt + 4 * half + w) * KS2 * 1024;
  u32x4 Hf[KS2];
  constexpr int P3_AHEAD = 12;
  auto ldH = [&](auto KS) { constexpr int ks = KS; if constexpr (ks < KS2) gld_a_sc1<(ks & 3) * 1024>(Hf[ks], c.lv, hb + (ks >> 2) * 4096); };
  static_for<P3_AHEAD>([&](auto KS) { ldH(KS); });
  f32x4 o4[4];
  u32x4 Af[3][4];
  const u32 la0 = c.sb + lane * 16, la1 = la0 + 65536, la2 = la0 + 131072;
  auto rdA = [&](auto KS, auto DF) {
    constexpr int ks = KS, df = DF;
    if constexpr (ks < KB_NL) ds_b128<(ks & 15) * 4096 + df * 1024>(Af[ks % 3][df], ks < 16 ? la0 : ks < 32 ? la1 : la2);
  };
  wait_vm<P3_AHEAD>();
  barrier();
  static_for<4>([&](auto DF) { rdA(std::integral_constant<int, 0>{}, DF); });
  static_for<4>([&](auto DF) { rdA(std::integral_constant<int, 1>{}, DF); });
  static_for<KS2>([&](auto KS) {
    constexpr int ks = KS;
    wait_vm<(ks + P3_AHEAD <= KS2 ? P3_AHEAD - 1 : KS2 - 1 - ks)>();
    if constexpr (ks + 1 < KB_NL) wait_lgkm<4>(); else wait_lgkm<0>();
    pin_a(Hf[ks]);
    static_for<4>([&](auto DF) {
      constexpr int df = DF;
      if constexpr (ks < KB_NL) { pin(Af[ks % 3][df]); mfma_va<ks == 0>(o4[df], Af[ks % 3][df], Hf[ks]); }
      else { pin_a(Wr[ks - KB_NL][df]); mfma_aa(o4[df], Wr[ks - KB_NL][df], Hf[ks]); }
      rdA(std::integral_constant<int, ks + 2>{}, DF);
      if constexpr (df == 1 && ks + P3_AHEAD < KS2) ldH(std::integral_constant<int, ks + P3_AHEAD>{});
    });
  });
  mfma_fence(o4[0], o4[1], o4[2], o4[3]);
  u32x4 o0, o1;
#pragma unroll
  for (int df = 0; df < 2; ++df) {
    o0[2 * df] = pk2(o4[df][0] + blo(res[df][0]), o4[df][1] + bhi(res[df][0]));
    o0[2 * df + 1] = pk2(o4[df][2] + blo(res[df][1]), o4[df][3] + bhi(res[df][1]));
    o1[2 * df] = pk2(o4[2 + df][0] + blo(res[2 + df][0]), o4[2 + df][1] + bhi(res[2 + df][0]));
    o1[2 * df + 1] = pk2(o4[2 + df][2] + blo(res[2 + df][1]), o4[2 + df][3] + bhi(res[2 + df][1]));
  }
  const __amdgpu_buffer_rsrc_t ors = __builtin_amdgcn_make_buffer_rsrc(out, 0, S * D * 2, 0x00020000);
  u32x4 oa, ob;
#pragma unroll
  for (int e = 0; e < 4; ++e) {
    const u32 x = (u32)__builtin_amdgcn_update_dpp(0, (int)o1[e], 0x128, 0xf, 0xf, true);
    oa[e] = n < 8 ? o0[e] : x;
    ob[e] = n < 8 ? x : o0[e];
  }
  const u32 ocol = (u32)(64 * dt + 8 * g + (n < 8 ? 0 : 32)), orow = (u32)(s0 + 64 * half + 16 * w + (n & 7));
  __builtin_amdgcn_raw_buffer_store_b128(oa, ors, (orow * D + ocol) * 2, 0, 16);
  __builtin_amdgcn_raw_buffer_store_b128(ob, ors, ((orow + 8) * D + (ocol ^ 32)) * 2, 0, 16);
  asm volatile("kb_code_end:");
}

extern "C" void kernel_launch(void* const* d_in, const int* in_sizes, int n_in, void* d_out, int out_size, void* d_ws,
                              size_t ws_size, hipStream_t stream) {
  const u16 *q = (const u16*)d_in[0], *k = (const u16*)d_in[1], *v = (const u16*)d_in[2], *shk = (const u16*)d_in[3];
  const u16 *ssmk = (const u16*)d_in[4], *dskip = (const u16*)d_in[5];
  const u16 *wg = (const u16*)d_in[7], *wu = (const u16*)d_in[8], *wd = (const u16*)d_in[9];
  char* ws = (char*)d_ws;
  (void)hipFuncSetAttribute((const void*)ka_conv, hipFuncAttributeMaxDynamicSharedMemorySize, KA_LDS);
  (void)hipFuncSetAttribute((const void*)kb_mlp, hipFuncAttributeMaxDynamicSharedMemorySize, KB_LDS);
  ka_conv<<<512, 256, KA_LDS, stream>>>(q, k, v, shk, ssmk, dskip, wg, wu, wd, ws);
  kb_mlp<<<256, 256, KB_LDS, stream>>>(ws, (u16*)d_out);
}
```

```cpp
#include <hip/hip_runtime.h>
#include <stdint.h>
#include <utility>

typedef unsigned short u16;
typedef unsigned u32;
typedef __attribute__((ext_vector_type(2))) u32 u32x2;
typedef __attribute__((ext_vector_type(4))) u32 u32x4;
typedef __attribute__((ext_vector_type(2))) float f32x2;
typedef __attribute__((ext_vector_type(4))) float f32x4;
typedef __attribute__((ext_vector_type(2))) __bf16 bf16x2;
typedef __attribute__((ext_vector_type(8))) __bf16 bf16x8;
typedef __attribute__((ext_vector_type(4))) short s16x4;
#define LDSP __attribute__((address_space(3)))
#define DI __device__ __forceinline__

constexpr int S = 2048, D = 512, F = 1376;
constexpr int NU = F / 8;
constexpr int KS1 = D / 32;
constexpr int KS2 = F / 32;
constexpr float EPS = 1e-5f;

constexpr size_t WS_XR = 0;
constexpr size_t WS_QT = WS_XR + (size_t)D * S * 2;
constexpr size_t WS_HT = WS_QT + (size_t)D * S * 2;
constexpr size_t WS_W1 = WS_HT + (size_t)D * S * 2;
constexpr size_t WS_WD = WS_W1 + (size_t)NU * KS1 * 1024;
constexpr size_t WS_H = WS_WD + (size_t)8 * KS2 * 4 * 1024;
constexpr size_t WS_CS = WS_H + (size_t)(S / 16) * KS2 * 1024;
constexpr size_t WS_SA = WS_CS + (size_t)KS1 * NU * 16 * 4;
constexpr size_t WS_SB = WS_SA + (size_t)S * 4;
constexpr size_t WS_CNT = WS_SB + (size_t)S * 4;
constexpr size_t WS_FLA = WS_CNT + 16 * 128;
constexpr size_t WS_FLG = WS_FLA + 256 * 8;
constexpr size_t WS_FLS = WS_FLG + 256 * 8;
constexpr size_t WS_FLZ = WS_FLS + 256 * 8;
constexpr size_t WS_HNT = WS_FLZ + 8 * 128;
constexpr size_t WS_EPO = WS_HNT + 256 * 8;

template <int... Is, class Fn> DI void static_for_(std::integer_sequence<int, Is...>, Fn&& f) { (f(std::integral_constant<int, Is>{}), ...); }
template <int N, class Fn> DI void static_for(Fn&& f) { static_for_(std::make_integer_sequence<int, N>{}, f); }

DI float blo(u32 u) { return __uint_as_float(u << 16); }
DI float bhi(u32 u) { return __uint_as_float(u & 0xffff0000u); }
DI u32 pk2(float a, float b) { f32x2 t = {a, b}; return __builtin_bit_cast(u32, __builtin_convertvector(t, bf16x2)); }
DI f32x4 mfma16(u32x4 a, u32x4 b, f32x4 c) {
  return __builtin_amdgcn_mfma_f32_16x16x32_bf16(__builtin_bit_cast(bf16x8, a), __builtin_bit_cast(bf16x8, b), c, 0, 0, 0);
}
DI u32x2 tr_read(u32 lds_byte) {
  return __builtin_bit_cast(u32x2, __builtin_amdgcn_ds_read_tr16_b64_v4i16((LDSP s16x4*)(size_t)lds_byte));
}
DI u32x4 tr_frag(u32 a0, u32 a1) { u32x2 x = tr_read(a0), y = tr_read(a1); return u32x4{x[0], x[1], y[0], y[1]}; }
DI u32x4 cat(u32x2 x, u32x2 y) { return __builtin_shufflevector(x, y, 0, 1, 2, 3); }
template <int CTRL> DI float dpp_add(float v) {
  return v + __builtin_bit_cast(float, __builtin_amdgcn_update_dpp(0, __builtin_bit_cast(int, v), CTRL, 0xf, 0xf, true));
}
DI float sum_rows(float v) {
  u32 u = __float_as_uint(v);
  auto a = __builtin_amdgcn_permlane16_swap(u, u, false, false);
  v = __uint_as_float(a[0]) + __uint_as_float(a[1]);
  u = __float_as_uint(v);
  auto b = __builtin_amdgcn_permlane32_swap(u, u, false, false);
  return __uint_as_float(b[0]) + __uint_as_float(b[1]);
}
DI float wave_sum(float v) {
  v = dpp_add<0xb1>(v);
  v = dpp_add<0x4e>(v);
  v = dpp_add<0x124>(v);
  v = dpp_add<0x128>(v);
  return sum_rows(v);
}
DI __amdgpu_buffer_rsrc_t ws_rsrc(char* ws) { return __builtin_amdgcn_make_buffer_rsrc(ws, 0, 0x04000000, 0x00020000); }
DI void st16_wt(__amdgpu_buffer_rsrc_t r, u32 off, u32x4 v) { __builtin_amdgcn_raw_buffer_store_b128(v, r, off, 0, 16); }
DI int slice_u0(int j) { return j < 12 ? 11 * j : 132 + 10 * (j - 12); }
DI int slice_nu(int j) { return j < 12 ? 11 : 10; }

template <bool FIRST = false> DI void mfma_vv(f32x4& acc, const u32x4& a, const u32x4& b) {
  if constexpr (FIRST) asm volatile("v_mfma_f32_16x16x32_bf16 %0, %1, %2, 0" : "=&v"(acc) : "v"(a), "v"(b));
  else asm volatile("v_mfma_f32_16x16x32_bf16 %0, %1, %2, %0" : "+v"(acc) : "v"(a), "v"(b));
}
template <bool FIRST = false> DI void mfma_av(f32x4& acc, const u32x4& a, const u32x4& b) {
  if constexpr (FIRST) asm volatile("v_mfma_f32_16x16x32_bf16 %0, %1, %2, 0" : "=&v"(acc) : "a"(a), "v"(b));
  else asm volatile("v_mfma_f32_16x16x32_bf16 %0, %1, %2, %0" : "+v"(acc) : "a"(a), "v"(b));
}
template <int OFF> DI void ds_b64(u32x2& d, u32 addr) { asm volatile("ds_read_b64 %0, %1 offset:%c2" : "=v"(d) : "v"(addr), "i"(OFF) : "memory"); }
template <int OFF> DI void ds_b128(u32x4& d, u32 addr) { asm volatile("ds_read_b128 %0, %1 offset:%c2" : "=v"(d) : "v"(addr), "i"(OFF) : "memory"); }
template <int OFF> DI void ds_tr(u32x2& d, u32 addr) { asm volatile("ds_read_b64_tr_b16 %0, %1 offset:%c2" : "=v"(d) : "v"(addr), "i"(OFF) : "memory"); }
template <int OFF> DI void gld_a(u32x4& d, u32 voff, const void* sbase) {
  asm volatile("global_load_dwordx4 %0, %1, %2 offset:%c3" : "=a"(d) : "v"(voff), "s"(sbase), "i"(OFF) : "memory");
}
template <int OFF> DI void gld_v(u32x4& d, u32 voff, const void* sbase) {
  asm volatile("global_load_dwordx4 %0, %1, %2 offset:%c3" : "=v"(d) : "v"(voff), "s"(sbase), "i"(OFF) : "memory");
}
template <int OFF> DI void gld2_v(u32x2& d, u32 voff, const void* sbase) {
  asm volatile("global_load_dwordx2 %0, %1, %2 offset:%c3" : "=v"(d) : "v"(voff), "s"(sbase), "i"(OFF) : "memory");
}
template <int LOFF> DI void glds16(u32 voff, const void* sbase, u32 ldsbase) {
  asm volatile("s_add_u32 m0, %2, %c3\n\ts_nop 0\n\tglobal_load_lds_dwordx4 %0, %1" ::"v"(voff), "s"(sbase), "s"(ldsbase), "i"(LOFF) : "memory", "scc");
}
template <int N> DI void wait_vm() { asm volatile("s_waitcnt vmcnt(%c0)" ::"i"(N) : "memory"); }
template <int N> DI void wait_lgkm() { asm volatile("s_waitcnt lgkmcnt(%c0)" ::"i"(N) : "memory"); }
template <int OFF> DI void gld_a_sc1(u32x4& d, u32 voff, const void* sbase) {
  asm volatile("global_load_dwordx4 %0, %1, %2 offset:%c3 sc1" : "=a"(d) : "v"(voff), "s"(sbase), "i"(OFF) : "memory");
}
DI void mfma_aa(f32x4& acc, const u32x4& a, const u32x4& b) {
  asm volatile("v_mfma_f32_16x16x32_bf16 %0, %1, %2, %0" : "+v"(acc) : "a"(a), "a"(b));
}
template <bool FIRST = false> DI void mfma_va(f32x4& acc, const u32x4& a, const u32x4& b) {
  if constexpr (FIRST) asm volatile("v_mfma_f32_16x16x32_bf16 %0, %1, %2, 0" : "=&v"(acc) : "v"(a), "a"(b));
  else asm volatile("v_mfma_f32_16x16x32_bf16 %0, %1, %2, %0" : "+v"(acc) : "v"(a), "a"(b));
}
DI void mfma_fence(f32x4& a, f32x4& b, f32x4& c, f32x4& d) { asm volatile("s_nop 7\n\ts_nop 1" : "+v"(a), "+v"(b), "+v"(c), "+v"(d)); }
DI void after_fence(f32x4& a, f32x4& b, f32x4& c, f32x4& d) { asm volatile("" : "+v"(a), "+v"(b), "+v"(c), "+v"(d)); }
template <int STAGE> DI void dpp_stage(float& v) {
  if constexpr (STAGE == 0) asm volatile("v_add_f32_dpp %0, %0, %0 quad_perm:[1,0,3,2] row_mask:0xf bank_mask:0xf" : "+v"(v));
  if constexpr (STAGE == 1) asm volatile("v_add_f32_dpp %0, %0, %0 quad_perm:[2,3,0,1] row_mask:0xf bank_mask:0xf" : "+v"(v));
  if constexpr (STAGE == 2) asm volatile("v_add_f32_dpp %0, %0, %0 row_ror:4 row_mask:0xf bank_mask:0xf" : "+v"(v));
  if constexpr (STAGE == 3) asm volatile("v_add_f32_dpp %0, %0, %0 row_ror:8 row_mask:0xf bank_mask:0xf" : "+v"(v));
}
DI void barrier() { asm volatile("s_barrier" ::: "memory"); }
DI void pin(u32x4& x) { asm volatile("" : "+v"(x)); }
DI void pin(u32x2& x) { asm volatile("" : "+v"(x)); }
DI void pin4(f32x4& x) { asm volatile("" : "+v"(x)); }
DI void pin_a(u32x4& x) { asm volatile("" : "+a"(x)); }

template <class Gate>
DI void w_job(int b, const u16* __restrict__ wg, const u16* __restrict__ wu, const u16* __restrict__ wd, char* __restrict__ ws, char* smem, Gate&& gate) {
  const int t = threadIdx.x, lane = t & 63, w = t >> 6;
  char* tile = smem;
  char* tile2 = smem + 32 * 368;
  const u32 tb = (u32)(size_t)(LDSP char*)smem;
  const int i16 = lane & 15, qd = i16 >> 2, p = i16 & 3, g = lane >> 4;
  const int ks = b & 15, j = b >> 4;
  const int U0 = slice_u0(j), nu = slice_nu(j);
  u32x4 g4[2], u4[2], d4[2];
#pragma unroll
  for (int rep = 0; rep < 2; ++rep) {
    int idx = t + 256 * rep, r = idx / 11, u = idx - 11 * r;
    bool ok = r < 32 && u < nu;
    size_t off = ok ? (size_t)(32 * ks + r) * F + 8 * (U0 + u) : 0;
    g4[rep] = *(const u32x4*)(wg + off); u4[rep] = *(const u32x4*)(wu + off);
  }
  const int ks2 = b >> 2, dp = b & 3;
  if (b < KS2 * 4) {
#pragma unroll
    for (int rep = 0; rep < 2; ++rep) {
      int idx = t + 256 * rep, r = idx >> 4, c = idx & 15;
      d4[rep] = *(const u32x4*)(wd + (size_t)(32 * ks2 + r) * D + 128 * dp + 8 * c);
    }
  }
#pragma unroll
  for (int rep = 0; rep < 2; ++rep) {
    int idx = t + 256 * rep, r = idx / 11, u = idx - 11 * r;
    if (r < 32 && u < nu) {
      *(u32x4*)(tile + r * 368 + u * 32) = u32x4{g4[rep][0], u4[rep][0], g4[rep][1], u4[rep][1]};
      *(u32x4*)(tile + r * 368 + u * 32 + 16) = u32x4{g4[rep][2], u4[rep][2], g4[rep][3], u4[rep][3]};
    }
  }
  if (b < KS2 * 4) {
#pragma unroll
    for (int rep = 0; rep < 2; ++rep) { int idx = t + 256 * rep, r = idx >> 4, c = idx & 15; *(u32x4*)(tile2 + r * 272 + c * 16) = d4[rep]; }
  }
  __syncthreads();
  gate();
  const u32x4 ones = {0x3f803f80u, 0x3f803f80u, 0x3f803f80u, 0x3f803f80u};
  const __amdgpu_buffer_rsrc_t wr = ws_rsrc(ws);
  for (int u = w; u < nu; u += 4) {
    u32 a = tb + (8 * g + qd) * 368 + u * 32 + 8 * p;
    u32x4 fr = tr_frag(a, a + 4 * 368);
    st16_wt(wr, (u32)WS_W1 + (((U0 + u) * KS1 + ks) * 64 + lane) * 16, fr);
    f32x4 cs = mfma16(fr, ones, f32x4{0.f, 0.f, 0.f, 0.f});
    if (i16 == 0) st16_wt(wr, (u32)WS_CS + ((ks * NU + U0 + u) * 16 + 4 * g) * 4, __builtin_bit_cast(u32x4, cs));
  }
  if (b < KS2 * 4) {
#pragma unroll
    for (int rep = 0; rep < 2; ++rep) {
      int fi = w + 4 * rep, dtl = fi >> 2, df = fi & 3;
      u32 a = tb + 32 * 368 + (8 * g + qd) * 272 + (64 * dtl + 32 * (df >> 1) + 8 * p + 4 * (df & 1)) * 2;
      u32x4 fr = tr_frag(a, a + 4 * 272);
      st16_wt(wr, (u32)WS_WD + ((((2 * dp + dtl) * KS2 + ks2) * 4 + df) * 64 + lane) * 16, fr);
    }
  }
}

constexpr int KA_CS = 4160;
constexpr int KA_KC = 4 * KA_CS;
constexpr int KA_XR = 2 * KA_KC;
constexpr int KA_RED = KA_XR + 2 * 4608;
constexpr int KA_LDS = KA_RED + 4 * 4096;
constexpr int KA_TK = KA_XR, KA_TV = KA_TK + 72 * 128, KA_TQ = KA_TV + 64 * 128;
static_assert(KA_TQ + 64 * 128 == KA_LDS, "tile overlay");
constexpr int SPIN_MAX = 1 << 22;
constexpr int HELP_PRE = 16;
constexpr int HELP_SLEEP = 20;
constexpr int TOUCHERS = 1;
constexpr unsigned long long HINT_TICKS = 800;

template <int OFF> DI void gld_v_sc1(u32x4& d, u32 voff, const void* sbase) {
  asm volatile("global_load_dwordx4 %0, %1, %2 offset:%c3 sc1" : "=v"(d) : "v"(voff), "s"(sbase), "i"(OFF) : "memory");
}
template <int OFF> DI void gld2_v_sc1(u32x2& d, u32 voff, const void* sbase) {
  asm volatile("global_load_dwordx2 %0, %1, %2 offset:%c3 sc1" : "=v"(d) : "v"(voff), "s"(sbase), "i"(OFF) : "memory");
}
DI bool tags_ok(const u32x4& f, const u32x2& tag) { return f[0] == tag[0] && f[1] == tag[1] && f[2] == tag[0] && f[3] == tag[1]; }

__global__ __launch_bounds__(256, 2) void ka_conv(const u16* __restrict__ q, const u16* __restrict__ k, const u16* __restrict__ v,
                                                  const u16* __restrict__ shk, const u16* __restrict__ ssmk, const u16* __restrict__ dskip,
                                                  const u16* __restrict__ wg, const u16* __restrict__ wu, const u16* __restrict__ wd,
                                                  char* __restrict__ ws) {
  extern __shared__ __attribute__((aligned(16))) char smem[];
  const int t = threadIdx.x, lane = t & 63;
  const int w = __builtin_amdgcn_readfirstlane(t >> 6);
  const int b = blockIdx.x;
  if (b >= 256) {
    const int hb = b - 256, srow = 8 * hb + 2 * w;
    const __amdgpu_buffer_rsrc_t wr = ws_rsrc(ws);
    u32x2 epo; u32x4 qa, qb;
    gld2_v_sc1<0>(epo, 0, ws + WS_EPO);
    if (hb < TOUCHERS && w == 3) {
      const u32 lb = (u32)(size_t)(LDSP char*)smem + 32768 + 256;
      asm volatile("s_mov_b32 m0, %2\n\ts_nop 0\n\tglobal_load_lds_dword %0, %1" ::"v"((u32)lane * 128), "s"(ws + WS_FLA), "s"(lb) : "memory");
      asm volatile("s_mov_b32 m0, %2\n\ts_nop 0\n\tglobal_load_lds_dword %0, %1" ::"v"((u32)(lane < 9 ? 64 + lane : 64) * 128), "s"(ws + WS_FLA), "s"(lb) : "memory");
    }
    __builtin_amdgcn_s_sleep(HELP_PRE);
    gld_v<0>(qa, lane * 16, (const char*)q + (size_t)srow * 1024);
    gld_v<1024>(qb, lane * 16, (const char*)q + (size_t)srow * 1024);
    if (b < 264 && w == 1) {
      const char* pc; u32 len;
      asm volatile("s_getpc_b64 %0\n1:\n\ts_mov_b32 %1, kb_code_end-1b" : "=s"(pc), "=s"(len));
      const u32 lb = (u32)(size_t)(LDSP char*)smem + 32768;
      for (u32 o0 = 0; o0 < len; o0 += 64 * 128) {
        u32 o = o0 + lane * 128; o = o < len ? o : 0;
        asm volatile("s_mov_b32 m0, %2\n\ts_nop 0\n\tglobal_load_lds_dword %0, %1" ::"v"(o), "s"(pc), "s"(lb) : "memory");
      }
    }
    wait_vm<0>();
    asm volatile("" : "+v"(qa), "+v"(qb), "+v"(epo));
    const unsigned long long want = (((unsigned long long)epo[1] << 32) | epo[0]) + 1ull;
    {
      float sa = 0.f, sbb = 0.f;
#pragma unroll
      for (int e = 0; e < 4; ++e) { sa += blo(qa[e]) + bhi(qa[e]); sbb += blo(qb[e]) + bhi(qb[e]); }
      const float ma = wave_sum(sa) * (1.f / D), mb = wave_sum(sbb) * (1.f / D);
      float va = 0.f, vb = 0.f;
#pragma unroll
      for (int e = 0; e < 4; ++e) {
        float u0 = blo(qa[e]) - ma, u1 = bhi(qa[e]) - ma, u2 = blo(qb[e]) - mb, u3 = bhi(qb[e]) - mb;
        va += u0 * u0 + u1 * u1; vb += u2 * u2 + u3 * u3;
      }
      va = wave_sum(va) * (1.f / D); vb = wave_sum(vb) * (1.f / D);
      const float ra = __builtin_amdgcn_rsqf(va + EPS), rb = __builtin_amdgcn_rsqf(vb + EPS);
      const u32x2 val = lane == 0 ? u32x2{__float_as_uint(ra), __float_as_uint(rb)} : u32x2{__float_as_uint(-ma * ra), __float_as_uint(-mb * rb)};
      const u32 off = lane == 0 ? (u32)WS_SA + srow * 4 : lane == 1 ? (u32)WS_SB + srow * 4 : 0xfffffff0u;
      __builtin_amdgcn_raw_buffer_store_b64(val, wr, off, 0, 16);
    }
    asm volatile("s_waitcnt vmcnt(0)\n\ts_barrier" ::: "memory");
    if (t == 0) __builtin_amdgcn_raw_buffer_store_b64(u32x2{(u32)want, (u32)(want >> 32)}, wr, (u32)WS_FLS + hb * 8, 0, 16);
    if (hb == 1 && w == 1) {
      const unsigned long long* fs = (const unsigned long long*)(ws + WS_FLS) + lane;
      for (int spins = 0; spins < SPIN_MAX; ++spins) {
        bool ok = true;
#pragma unroll
        for (int i = 0; i < 4; ++i) ok &= __hip_atomic_load(fs + 64 * i, __ATOMIC_RELAXED, __HIP_MEMORY_SCOPE_AGENT) == want;
        if (__all(ok)) break;
        __builtin_amdgcn_s_sleep(1);
      }
      __builtin_amdgcn_raw_buffer_store_b64(u32x2{(u32)want, (u32)(want >> 32)}, wr, lane < 8 ? (u32)WS_FLZ + lane * 128 : 0xfffffff0u, 0, 16);
    } else __builtin_amdgcn_s_sleep(HELP_SLEEP);
    w_job(hb, wg, wu, wd, ws, smem, [&]() {
      if (hb == 1 && w == 1) {
        const unsigned long long* fa = (const unsigned long long*)(ws + WS_FLA) + lane;
        const u32x2 tg = {(u32)want, (u32)(want >> 32)};
        for (int spins = 0; spins < SPIN_MAX; ++spins) {
          bool ok = true;
#pragma unroll
          for (int i = 0; i < 4; ++i) ok &= __hip_atomic_load(fa + 64 * i, __ATOMIC_RELAXED, __HIP_MEMORY_SCOPE_AGENT) == want;
          if (__all(ok)) break;
          __builtin_amdgcn_s_sleep(16);
        }
        if (lane == 0) __builtin_amdgcn_raw_buffer_store_b64(tg, wr, (u32)WS_EPO, 0, 16);
      }
      if (w == 0) {
        const unsigned long long* hp = (const unsigned long long*)(ws + WS_HNT) + hb;
        const unsigned long long t0 = __builtin_amdgcn_s_memrealtime();
        while (__hip_atomic_load(hp, __ATOMIC_RELAXED, __HIP_MEMORY_SCOPE_AGENT) != want && __builtin_amdgcn_s_memrealtime() - t0 < HINT_TICKS)
          __builtin_amdgcn_s_sleep(8);
      }
      __builtin_amdgcn_s_barrier();
    });
    return;
  }
  const int x = b & 7, bi = b >> 3, s0 = 64 * bi;
  const int ch0 = 64 * x + 2 * bi;
  const u32 sb = (u32)(size_t)(LDSP char*)smem;
  const int n = lane & 15, g = lane >> 4, qd = n >> 2, p = n & 3;
  const int d = 64 * x + 16 * w + n;
  const __amdgpu_buffer_rsrc_t wr = ws_rsrc(ws);

  u32x2 epo, sh; u32x4 kp[2], kc[2];
  gld2_v_sc1<0>(epo, 0, ws + WS_EPO);
  gld2_v<0>(sh, d * 8, shk);
  const int r8 = lane >> 3, c8 = lane & 7;
  auto voff = [&](int instr) { const int f = ((r8 >> 1) & 1) | ((instr & 1) << 1); return (u32)((((c8 >> 1) ^ f) * 2 + (c8 & 1)) * 16); };
  const size_t col = (size_t)x * 128;
  {
    int row = s0 - 8 + 8 * w + r8; row = row < 0 ? 0 : row;
    glds16<0>((u32)row * 1024 + voff(w), (const char*)k + col, sb + KA_TK + w * 1024);
    glds16<0>((u32)(s0 + 8 * w + r8) * 1024 + voff(w), (const char*)v + col, sb + KA_TV + w * 1024);
    glds16<0>((u32)(s0 - 8 + 8 * (w + 4) + r8) * 1024 + voff(w), (const char*)k + col, sb + KA_TK + (w + 4) * 1024);
    glds16<0>((u32)(s0 + 8 * (w + 4) + r8) * 1024 + voff(w), (const char*)v + col, sb + KA_TV + (w + 4) * 1024);
    if (w == 0) glds16<0>((u32)(s0 + 56 + r8) * 1024 + voff(8), (const char*)k + col, sb + KA_TK + 8 * 1024);
  }
#pragma unroll
  for (int cc = 0; cc < 2; ++cc) {
    const char* kr = (const char*)ssmk + (size_t)(ch0 + cc) * 4096;
    gld_v<0>(kp[cc], (u32)(t > 0 ? t - 1 : 0) * 16, kr);
    gld_v<0>(kc[cc], (u32)t * 16, kr);
  }
  glds16<0>((u32)(s0 + 8 * w + r8) * 1024 + voff(w), (const char*)q + col, sb + KA_TQ + w * 1024);
  glds16<0>((u32)(s0 + 8 * (w + 4) + r8) * 1024 + voff(w), (const char*)q + col, sb + KA_TQ + (w + 4) * 1024);
  const float dsk = blo(dskip[ch0 + (w >> 1)]);
  if (b == 0 && t < 16) *(u32*)(ws + WS_CNT + t * 128) = 0u;

  wait_vm<6>();
  barrier();
  asm volatile("" : "+v"(sh), "+v"(epo));
  u32x2 tag;
  { const unsigned long long e = (((unsigned long long)epo[1] << 32) | epo[0]) + 1ull; tag = u32x2{(u32)e, (u32)(e >> 32)}; }
  auto taddr = [&](int base, int tr) { const int f = ((tr >> 1) & 1) | ((tr >> 2) & 2); return sb + base + tr * 128 + ((w ^ f) * 32) + 8 * p; };
  {
    const float a0 = blo(sh[0]), a1 = bhi(sh[0]), a2 = blo(sh[1]), a3 = bhi(sh[1]);
#pragma unroll
    for (int e = 0; e < 2; ++e) {
      const int rg = g + 4 * e;
      u32x2 km = tr_read(taddr(KA_TK, 8 * rg + 4 + qd));
      const u32x2 k0 = tr_read(taddr(KA_TK, 8 * rg + 8 + qd)), k1 = tr_read(taddr(KA_TK, 8 * rg + 12 + qd));
      const u32x2 v0 = tr_read(taddr(KA_TV, 8 * rg + qd)), v1 = tr_read(taddr(KA_TV, 8 * rg + 4 + qd));
      if (s0 + rg == 0) km = u32x2{0, 0};
      const float kf[11] = {bhi(km[0]), blo(km[1]), bhi(km[1]), blo(k0[0]), bhi(k0[0]), blo(k0[1]), bhi(k0[1]), blo(k1[0]), bhi(k1[0]), blo(k1[1]), bhi(k1[1])};
      const float vf[8] = {blo(v0[0]), bhi(v0[0]), blo(v0[1]), bhi(v0[1]), blo(v1[0]), bhi(v1[0]), blo(v1[1]), bhi(v1[1])};
      float xo[8];
#pragma unroll
      for (int j = 0; j < 8; ++j) xo[j] = (kf[j + 3] * a0 + kf[j + 2] * a1 + kf[j + 1] * a2 + kf[j] * a3) * vf[j];
      const int ir = 2040 - (s0 + 8 * rg);
      st16_wt(wr, (u32)WS_XR + (d >> 1) * 8192 + (ir >> 5) * 128 + (d & 1) * 64 + (ir & 31) * 2, u32x4{pk2(xo[7], xo[6]), pk2(xo[5], xo[4]), pk2(xo[3], xo[2]), pk2(xo[1], xo[0])});
    }
  }
  asm volatile("s_waitcnt vmcnt(0)\n\ts_barrier" : "+v"(kp[0]), "+v"(kc[0]), "+v"(kp[1]), "+v"(kc[1]) :: "memory");
  if (t == 0) __builtin_amdgcn_raw_buffer_store_b64(tag, wr, (u32)WS_FLA + (x * 32 + bi) * 8, 0, 16);
#pragma unroll
  for (int e = 0; e < 2; ++e) {
    const int rg = g + 4 * e;
    const u32x2 q0 = tr_read(taddr(KA_TQ, 8 * rg + qd)), q1 = tr_read(taddr(KA_TQ, 8 * rg + 4 + qd));
    const int r0 = s0 + 8 * rg;
    st16_wt(wr, (u32)WS_QT + (d >> 1) * 8192 + (r0 >> 5) * 128 + (d & 1) * 64 + (r0 & 31) * 2, cat(q0, q1));
  }
#pragma unroll
  for (int cc = 0; cc < 2; ++cc) {
    const u32 m = t > 0 ? 0xffffffffu : 0u;
    u32 wv[6] = {kp[cc][0] & m, kp[cc][1] & m, kp[cc][2] & m, kp[cc][3] & m, kc[cc][0], kc[cc][1]};
    u32 o1[5];
#pragma unroll
    for (int i = 0; i < 5; ++i) o1[i] = __builtin_amdgcn_alignbit(wv[i + 1], wv[i], 16);
    char* base = smem + cc * KA_KC + (t + 1) * 16;
    *(u32x4*)(base + 0 * KA_CS) = u32x4{o1[0], o1[1], o1[2], o1[3]};
    *(u32x4*)(base + 1 * KA_CS) = u32x4{wv[1], wv[2], wv[3], wv[4]};
    *(u32x4*)(base + 2 * KA_CS) = u32x4{o1[1], o1[2], o1[3], o1[4]};
    *(u32x4*)(base + 3 * KA_CS) = u32x4{wv[2], wv[3], wv[4], wv[5]};
    if (t == 255) {
      const u32 z = 0;
      const u32 e0 = __builtin_amdgcn_alignbit(kc[cc][1], kc[cc][0], 16), e1 = __builtin_amdgcn_alignbit(kc[cc][2], kc[cc][1], 16);
      const u32 e2 = __builtin_amdgcn_alignbit(kc[cc][3], kc[cc][2], 16), e3 = __builtin_amdgcn_alignbit(z, kc[cc][3], 16);
      *(u32x4*)(base + 16 + 0 * KA_CS) = u32x4{e0, e1, e2, e3};
      *(u32x4*)(base + 16 + 1 * KA_CS) = u32x4{kc[cc][1], kc[cc][2], kc[cc][3], z};
      *(u32x4*)(base + 16 + 2 * KA_CS) = u32x4{e1, e2, e3, z};
      *(u32x4*)(base + 16 + 3 * KA_CS) = u32x4{kc[cc][2], kc[cc][3], z, z};
    }
  }
  if (t < 8) *(u32x4*)(smem + (t >> 2) * KA_KC + (t & 3) * KA_CS) = u32x4{0, 0, 0, 0};
  asm volatile("s_waitcnt vmcnt(0) lgkmcnt(0)\n\ts_barrier" ::: "memory");
  if (t == 0) __builtin_amdgcn_raw_buffer_store_b64(tag, wr, (u32)WS_FLG + (x * 32 + bi) * 8, 0, 16);
  if (t >= 64 && t < 64 + 60) { int i = t - 64; *(u32x4*)(smem + KA_XR + (i / 30) * 4608 + 4096 + (i % 30) * 16) = u32x4{0, 0, 0, 0}; }
  if (w == 0) {
    const unsigned long long* fp = (const unsigned long long*)(ws + WS_FLA) + x * 32 + (lane & 31);
    const unsigned long long want = ((unsigned long long)tag[1] << 32) | tag[0];
    for (int spins = 0; spins < SPIN_MAX; ++spins) {
      if (__all(__hip_atomic_load(fp, __ATOMIC_RELAXED, __HIP_MEMORY_SCOPE_AGENT) == want)) break;
      __builtin_amdgcn_s_sleep(1);
    }
  }
  asm volatile("s_waitcnt vmcnt(0)\n\ts_barrier" ::: "memory");

  const int c = w >> 1, half = w & 1;
  u32x2 qv[4], fz; u32x4 sa[4], sbv[4], fq;
  const u32 eo = (u32)(1024 * half + 16 * n + 4 * g);
  const char* qTc = ws + WS_QT + (size_t)ch0 * 4096 + c * 64;
  const u32 qo = (u32)(4096 * half + (n >> 1) * 128 + (n & 1) * 32 + 8 * g);
  {
    u32x4 x0, x1;
    gld_v_sc1<0>(x0, t * 16, ws + WS_XR + (size_t)ch0 * 4096);
    gld_v_sc1<0>(x1, t * 16 + 4096, ws + WS_XR + (size_t)ch0 * 4096);
    wait_vm<0>();
    asm volatile("" : "+v"(x0), "+v"(x1));
    char* dst = smem + KA_XR + ((t >> 2) & 1) * 4608 + (t >> 3) * 64 + (t & 3) * 16;
    *(u32x4*)dst = x0;
    *(u32x4*)(dst + 2048) = x1;
  }
  asm volatile("s_waitcnt lgkmcnt(0)\n\ts_barrier" ::: "memory");
  if (t == 0) __builtin_amdgcn_raw_buffer_store_b64(tag, wr, (u32)WS_HNT + b * 8, 0, 16);

  const u32 aa = sb + c * KA_KC + (n & 3) * KA_CS + 16 * g + 2 * (n & 12) + half * 256;
  const u32 ba = sb + KA_XR + c * 4608 + 480 - 32 * n + 16 * g + half * 256;
  f32x4 acc[8];
  u32x2 Alo[2][8], Ahi[2][8];
  u32x4 B[2][8];
  auto rd = [&](auto C0, auto R) {
    constexpr int c0 = C0, r = R, buf = c0 & 1;
    if constexpr (r >= 2 && r < 10) {
      constexpr int u = r - 2;
      ds_b128<512 * (7 - u) + 64 * c0>(B[buf][u], ba);
    } else {
      constexpr int ar = r < 2 ? r : r - 8, i = ar >> 1;
      if constexpr ((ar & 1) == 0) ds_b64<512 * i + 64 * c0>(Alo[buf][i], aa);
      else ds_b64<512 * i + 64 * c0 + 8>(Ahi[buf][i], aa);
    }
  };
  auto ldS = [&](auto E) {
    constexpr int e = E, i = e / 3, kind = e % 3;
    if constexpr (kind == 0) gld_v_sc1<i * 1024>(sa[i], eo * 4, ws + WS_SA);
    if constexpr (kind == 1) gld_v_sc1<i * 1024>(sbv[i], eo * 4, ws + WS_SB);
    if constexpr (kind == 2) gld2_v_sc1<i * 1024>(qv[i], qo, qTc);
  };
  auto ldF = [&](auto E) {
    constexpr int e = E;
    if constexpr (e == 0) gld2_v_sc1<0>(fz, x * 128, ws + WS_FLZ);
    else gld_v_sc1<0>(fq, (x * 32 + 2 * (lane & 15)) * 8, ws + WS_FLG);
  };
  auto okF = [&]() { return fz[0] == tag[0] && fz[1] == tag[1] && tags_ok(fq, tag); };
  static_for<24>([&](auto R) { rd(std::integral_constant<int, 0>{}, R); });
  static_for<4>([&](auto C0) {
    constexpr int c0 = C0, buf = c0 & 1;
    if constexpr (c0 == 2) {
      wait_vm<0>();
      asm volatile("" : "+v"(fz), "+v"(fq));
      if (!__all(okF())) {
        for (int spins = 0; spins < SPIN_MAX; ++spins) {
          ldF(std::integral_constant<int, 0>{}); ldF(std::integral_constant<int, 1>{});
          wait_vm<0>();
          asm volatile("" : "+v"(fz), "+v"(fq));
          if (__all(okF())) break;
          __builtin_amdgcn_s_sleep(1);
        }
      }
    }
    u32x4 A[8];
    static_for<36>([&](auto M) {
      constexpr int m = M;
      constexpr int c1 = m < 8 ? 0 : m < 15 ? 1 : m < 21 ? 2 : m < 26 ? 3 : m < 30 ? 4 : m < 33 ? 5 : m < 35 ? 6 : 7;
      constexpr int first = c1 * 8 - c1 * (c1 - 1) / 2;
      constexpr int u = m - first;
      if constexpr (m == 0) {
        if constexpr (c0 == 0) wait_lgkm<14>(); else wait_lgkm<0>();
        pin(Alo[buf][0]); pin(Ahi[buf][0]);
#pragma unroll
        for (int i = 0; i < 8; ++i) pin(B[buf][i]);
        A[0] = cat(Alo[buf][0], Ahi[buf][0]);
      }
      if constexpr (m == 8) {
        if constexpr (c0 == 0) wait_lgkm<8>();
#pragma unroll
        for (int i = 1; i < 8; ++i) { pin(Alo[buf][i]); pin(Ahi[buf][i]); A[i] = cat(Alo[buf][i], Ahi[buf][i]); }
      }
      mfma_vv<(c0 == 0 && c1 == 0)>(acc[c1 + u], A[c1], B[buf][u]);
      if constexpr (c0 < 3 && m < 24) rd(std::integral_constant<int, c0 + 1>{}, M);
      if constexpr (c0 == 1 && (m == 2 || m == 4)) ldF(std::integral_constant<int, (m - 2) / 2>{});
      if constexpr (c0 == 2 && m >= 1 && m < 25 && (m & 1) == 1) ldS(std::integral_constant<int, (m - 1) / 2>{});
    });
  });
  mfma_fence(acc[0], acc[1], acc[2], acc[3]);
  after_fence(acc[4], acc[5], acc[6], acc[7]);
  f32x4 fin[4];
  {
    f32x4* red = (f32x4*)(smem + KA_RED);
    if (half) {
#pragma unroll
      for (int i = 0; i < 4; ++i) { red[(w * 4 + i) * 64 + lane] = acc[i]; fin[i] = acc[4 + i]; }
      asm volatile("; half 1" ::: "memory");
    } else {
#pragma unroll
      for (int i = 0; i < 4; ++i) { red[(w * 4 + i) * 64 + lane] = acc[4 + i]; fin[i] = acc[i]; }
      asm volatile("; half 0" ::: "memory");
    }
    asm volatile("s_waitcnt lgkmcnt(0)\n\ts_barrier" ::: "memory");
#pragma unroll
    for (int i = 0; i < 4; ++i) fin[i] += red[((w ^ 1) * 4 + i) * 64 + lane];
  }
  wait_vm<0>();
#pragma unroll
  for (int i = 0; i < 4; ++i) asm volatile("" : "+v"(qv[i]), "+v"(sa[i]), "+v"(sbv[i]));
  const f32x2 dd = {dsk, dsk};
  u32x2 hv[4];
#pragma unroll
  for (int i = 0; i < 4; ++i) {
    const int s = 256 * (4 * half + i) + 16 * n + 4 * g;
    const u32x2 xq = *(LDSP const u32x2*)(size_t)(sb + KA_XR + c * 4608 + 2 * (2044 - s));
    const f32x2 x0 = {bhi(xq[1]), blo(xq[1])}, x1 = {bhi(xq[0]), blo(xq[0])};
    const f32x2 q0 = {blo(qv[i][0]), bhi(qv[i][0])}, q1 = {blo(qv[i][1]), bhi(qv[i][1])};
    const f32x2 z0 = x0 * dd + f32x2{fin[i][0], fin[i][1]}, z1 = x1 * dd + f32x2{fin[i][2], fin[i][3]};
    const f32x4 a4 = __builtin_bit_cast(f32x4, sa[i]), b4 = __builtin_bit_cast(f32x4, sbv[i]);
    const f32x2 t0 = q0 * f32x2{a4[0], a4[1]} + f32x2{b4[0], b4[1]};
    const f32x2 t1 = q1 * f32x2{a4[2], a4[3]} + f32x2{b4[2], b4[3]};
    const f32x2 h0 = t0 * z0 + q0, h1 = t1 * z1 + q1;
    hv[i] = u32x2{pk2(h0[0], h0[1]), pk2(h1[0], h1[1])};
  }
#pragma unroll
  for (int i = 0; i < 4; i += 2) {
    auto a = __builtin_amdgcn_permlane16_swap(hv[i][0], hv[i + 1][0], false, false);
    auto bb = __builtin_amdgcn_permlane16_swap(hv[i][1], hv[i + 1][1], false, false);
    const int s = 256 * (4 * half + i + (g & 1)) + 16 * n + 4 * (g & 2);
    st16_wt(wr, (u32)WS_HT + ((ch0 + c) * S + s) * 2, u32x4{a[0], bb[0], a[1], bb[1]});
  }
}

constexpr int KB_NL = 39;
constexpr int KB_ST = KB_NL * 4096;
constexpr int KB_LDS = KB_ST + 1024;
constexpr int EPI_FILL = 8;
constexpr int KB_RING = 8;

constexpr int P2_AHEAD = 5;
constexpr int P2_PRO = 3;
constexpr int p2_upto(int ks) {
  const int ramp = P2_PRO - 1 + 2 * (ks + 1), steady = ks + P2_AHEAD;
  const int u = ks < 0 ? P2_PRO - 1 : (ramp < steady ? ramp : steady);
  return u > 15 ? 15 : u;
}
constexpr int P2_XMAX = 5;
constexpr int P2_NEXTRA = 23;
constexpr int p2_extra(int ks) {
  const int tail0 = 16 - P2_AHEAD, rest = P2_NEXTRA - P2_AHEAD * P2_XMAX;
  if (ks >= tail0) return P2_XMAX;
  return (rest > 0 && ks >= tail0 - rest) ? 1 : 0;
}
constexpr int p2_extra_first(int ks) { int c = 0; for (int s = 0; s < ks; ++s) c += p2_extra(s); return c; }
constexpr int p2_extra_real(int ks) { int f = p2_extra_first(ks), n = p2_extra(ks); return f >= P2_NEXTRA ? 0 : (f + n > P2_NEXTRA ? P2_NEXTRA - f : n); }
static_assert(p2_extra_first(16) >= P2_NEXTRA, "all Wd operations are scheduled");
constexpr int p2_issue_step(int t) { for (int s = -1; s < KS1; ++s) if (p2_upto(s) >= t) return s; return KS1; }
constexpr int p2_younger(int t, int ks, int ops) {
  int c = (p2_upto(ks - 1) - t) * ops;
  for (int s = p2_issue_step(t); s < ks; ++s) if (s >= 0) c += p2_extra_real(s);
  return c;
}

struct KbCtx {
  u32 sb, gv, lv, lb0, lwd, csv;
  const char *hb0, *hb1, *w0, *w1, *w2, *cs0, *cs1, *cs2, *wd, *wdr;
};

template <int GRAM, class Setup>
DI void kb_phase2(const KbCtx& c, f32x4 (&acc)[3][8], f32x4 (&rsum)[2], f32x4 (&csp)[3], u32x4 (&Wr)[4][4], u32 (&ra)[8], Setup&& setup) {
  constexpr int OPS = GRAM ? 4 : 5;
  u32x4 W[KB_RING][3];
  u32x2 Bl[2][8], Bh[2][8];
  auto issue = [&](auto KS, auto O) {
    constexpr int ks = KS, o = O;
    if constexpr (o == 0) glds16<ks * 8192>(c.gv, c.hb0 + (size_t)ks * 131072, c.lb0);
    if constexpr (o == 1) gld_a<(ks & 3) * 1024>(W[ks % KB_RING][0], c.lv, c.w0 + (ks >> 2) * 4096);
    if constexpr (o == 2) gld_a<(ks & 3) * 1024>(W[ks % KB_RING][1], c.lv, c.w1 + (ks >> 2) * 4096);
    if constexpr (o == 3 && !GRAM) gld_a<(ks & 3) * 1024>(W[ks % KB_RING][2], c.lv, c.w2 + (ks >> 2) * 4096);
    if constexpr (o == 4) glds16<ks * 8192 + 1024>(c.gv, c.hb1 + (size_t)ks * 131072, c.lb0);
  };
  auto extra = [&](auto E) {
    constexpr int e = E;
    if constexpr (e < 7) glds16<(32 + e) * 4096>(c.lv, c.wd + (size_t)(32 + e) * 4096, c.lwd);
    else if constexpr (e < P2_NEXTRA) gld_a<((e - 7) & 3) * 1024>(Wr[(e - 7) >> 2][(e - 7) & 3], c.lv, c.wdr + ((e - 7) >> 2) * 4096);
  };
  auto rdB = [&](auto KS, auto R) {
    constexpr int ks = KS, r = R, i = r >> 1;
    if constexpr ((r & 1) == 0) ds_tr<(ks & 7) * 8192>(Bl[ks & 1][i], ra[i] + (ks >= 8 ? 65536 : 0));
    else ds_tr<(ks & 7) * 8192 + 1024>(Bh[ks & 1][i], ra[i] + (ks >= 8 ? 65536 : 0));
  };
  u32x4 c0, c1, c2;
  static_for<5>([&](auto O) { issue(std::integral_constant<int, 0>{}, O); });
  gld_v<0>(c0, c.csv, c.cs0); gld_v<0>(c1, c.csv, c.cs1); gld_v<0>(c2, c.csv, c.cs2);
  static_for<p2_upto(-1)>([&](auto KS) { static_for<5>([&](auto O) { issue(std::integral_constant<int, KS + 1>{}, O); }); });
  setup();
  u32x4 ones = {0x3f803f80u, 0x3f803f80u, 0x3f803f80u, 0x3f803f80u};
  pin(ones);
  wait_vm<p2_upto(-1) * OPS>();
  barrier();
  static_for<16>([&](auto R) { rdB(std::integral_constant<int, 0>{}, R); });
  asm volatile("" : "+v"(c0), "+v"(c1), "+v"(c2));
  float cv[12];
#pragma unroll
  for (int e = 0; e < 4; ++e) { cv[e] = __uint_as_float(c0[e]); cv[4 + e] = __uint_as_float(c1[e]); cv[8 + e] = __uint_as_float(c2[e]); }
  static_for<KS1>([&](auto KS) {
    constexpr int ks = KS, buf = ks & 1, slot = ks % KB_RING;
    constexpr int nmain = (p2_upto(ks) - p2_upto(ks - 1)) * 5, next = p2_extra_real(ks);
    wait_lgkm<0>();
    u32x4 B[8];
#pragma unroll
    for (int i = 0; i < 8; ++i) { pin(Bl[buf][i]); pin(Bh[buf][i]); }
#pragma unroll
    for (int i = 0; i < 8; ++i) B[i] = cat(Bl[buf][i], Bh[buf][i]);
    pin_a(W[slot][0]); pin_a(W[slot][1]);
    if constexpr (!GRAM) pin_a(W[slot][2]);
    static_for<26>([&](auto M) {
      constexpr int m = M, i = m / 3, u = m % 3;
      if constexpr (m >= 24) mfma_vv<ks == 0>(rsum[m - 24], ones, B[m - 24]);
      else if constexpr (u < 2 || !GRAM) mfma_av<ks == 0>(acc[u][i], W[slot][u], B[i]);
      else mfma_vv<ks == 0>(acc[2][i], B[i], B[i]);
      if constexpr (m == 1 && ks + 1 < KS1) { wait_vm<p2_younger(ks + 1, ks, OPS)>(); barrier(); }
      if constexpr (m >= 2 && m < 18 && ks + 1 < KS1) rdB(std::integral_constant<int, ks + 1>{}, std::integral_constant<int, m - 2>{});
      if constexpr (ks >= 9 && ks < 13 && m >= 2 && m < 14) dpp_stage<ks - 9>(cv[m - 2]);
      constexpr int v = m - (26 - nmain - next);
      if constexpr (v >= 0 && v < nmain) issue(std::integral_constant<int, p2_upto(ks - 1) + 1 + v / 5>{}, std::integral_constant<int, v % 5>{});
      if constexpr (v >= nmain) extra(std::integral_constant<int, p2_extra_first(ks) + v - nmain>{});
    });
  });
  mfma_fence(acc[0][0], acc[1][0], rsum[0], rsum[1]);
#pragma unroll
  for (int i = 0; i < 8; ++i) after_fence(acc[0][i], acc[1][i], acc[2][i], rsum[i & 1]);
#pragma unroll
  for (int u = 0; u < 3; ++u) csp[u] = f32x4{cv[4 * u], cv[4 * u + 1], cv[4 * u + 2], cv[4 * u + 3]};
}

__global__ __launch_bounds__(256) void kb_mlp(char* __restrict__ ws, u16* __restrict__ out) {
  extern __shared__ __attribute__((aligned(16))) char smem[];
  const int t = threadIdx.x, lane = t & 63;
  const int w = __builtin_amdgcn_readfirstlane(t >> 6);
  const int b = blockIdx.x, xcd = b & 7, bi = b >> 3;
  const int rt = (xcd >> 1) * 4 + (bi & 3);
  const int j = (xcd & 1) * 8 + (bi >> 2);
  const int half = j >> 3, dt = j & 7;
  const int U0 = slice_u0(j), nu = slice_nu(j);
  const int rem = nu - 8;
  const int nw = w < rem ? 3 : 2;
  const int uw = U0 + (w < rem ? 3 * w : 3 * rem + 2 * (w - rem));
  const int u2 = uw + (nw == 3 ? 2 : 0);
  const int n = lane & 15, g = lane >> 4;
  const int s0 = 128 * rt;
  auto sf = [&](int i) { return ((w + (i >> 1)) & 3) + 4 * (half ^ (i & 1)); };

  KbCtx c;
  c.sb = (u32)(size_t)(LDSP char*)smem;
  c.lv = lane * 16;
  c.gv = (u32)(g * 4096 + (((n >> 1) ^ (g | ((w & 1) << 2))) * 32 + (n & 1) * 16));
  c.hb0 = ws + WS_HT + (size_t)s0 * 2 + (size_t)(8 * w) * 4096;
  c.hb1 = c.hb0 + 4 * 4096;
  c.lb0 = c.sb + 8 * w * 256;
  c.w0 = ws + WS_W1 + (size_t)uw * KS1 * 1024;
  c.w1 = c.w0 + KS1 * 1024;
  c.w2 = ws + WS_W1 + (size_t)u2 * KS1 * 1024;
  c.cs0 = ws + WS_CS + (size_t)uw * 64;
  c.cs1 = c.cs0 + 64;
  c.cs2 = ws + WS_CS + (size_t)u2 * 64;
  c.csv = n * (NU * 64) + g * 16;
  c.wdr = ws + WS_WD + (size_t)dt * KS2 * 4096 + (size_t)KB_NL * 4096;
  c.wd = ws + WS_WD + (size_t)dt * KS2 * 4096 + (size_t)w * 1024;
  c.lwd = c.sb + 1024 * w;
  u32 ra[8], hoff[6];
  auto setup = [&]() {
    const int qd = n >> 2, p = n & 3;
    const int frow = qd | ((g & 1) << 2);
#pragma unroll
    for (int i = 0; i < 8; ++i) { ra[i] = c.sb + (8 * g + qd) * 256 + ((sf(i) ^ frow) * 32) + 8 * p; asm volatile("" : "+v"(ra[i])); }
#pragma unroll
    for (int tt = 0; tt < 6; ++tt) {
      const int q = 4 * tt + g, i = (q * 11) >> 5, u = q - 3 * i, U = uw + u;
      const u32 o = (u32)(((8 * rt + sf(i)) * KS2 + (U >> 2)) * 1024 + ((U & 3) * 16 + n) * 16);
      hoff[tt] = u < nw ? o : 0xfffffff0u;
      asm volatile("" : "+v"(hoff[tt]));
    }
  };
  f32x4 acc[3][8], cs[3], rsum[2];
  u32x4 Wr[4][4];
  if (w == 3) kb_phase2<1>(c, acc, rsum, cs, Wr, ra, setup);
  else kb_phase2<0>(c, acc, rsum, cs, Wr, ra, setup);

  u32x2 res[4];
  {
    const u32 rb = ra[0] + dt * 16384;
    ds_tr<0>(res[0], rb); ds_tr<1024>(res[1], rb); ds_tr<8192>(res[2], rb); ds_tr<9216>(res[3], rb);
  }
  float* stl = (float*)(smem + KB_ST);
  if (g == 0) {
#pragma unroll
    for (int i = 0; i < 2; ++i) stl[2 * (16 * sf(i) + n)] = rsum[i][0];
  }
  if (w == 3 && g == (n >> 2)) {
#pragma unroll
    for (int i = 0; i < 8; ++i) {
      f32x4 a = acc[2][i];
      float dg = (n & 2) ? ((n & 1) ? a[3] : a[2]) : ((n & 1) ? a[1] : a[0]);
      stl[2 * (16 * sf(i) + n) + 1] = dg;
    }
  }
  asm volatile("s_waitcnt lgkmcnt(0)\n\ts_barrier" : "+v"(res[0]), "+v"(res[1]), "+v"(res[2]), "+v"(res[3]) :: "memory");
  const __amdgpu_buffer_rsrc_t hrs = __builtin_amdgcn_make_buffer_rsrc(ws + WS_H, 0, (S / 16) * KS2 * 1024, 0x00020000);
  f32x2 sv[8];
#pragma unroll
  for (int i = 0; i < 8; ++i) sv[i] = *(const f32x2*)(stl + 2 * (16 * sf(i) + n));
  u32 hv[24];
  static_for<8>([&](auto I) {
    constexpr int i = I;
    const float mean = sv[i][0] * (1.f / D);
    const float var = fmaxf(sv[i][1] * (1.f / D) - mean * mean, 0.f) + EPS;
    const float rstd = __builtin_amdgcn_rsqf(var);
    const float rinv = var * rstd;
    const float ce = -1.4426950408889634f * rstd;
    const f32x2 nm = {-mean, -mean}, ce2 = {ce, ce}, ri2 = {rinv, rinv};
    static_for<3>([&](auto U_) {
      constexpr int u = U_;
      pin4(acc[u][i]);
      constexpr int slot = 3 * i + u;
      constexpr int wk = slot * EPI_FILL / 24;
      if constexpr (EPI_FILL > 0 && (slot + 1) * EPI_FILL / 24 > wk) glds16<wk * 4096>(c.lv, c.wd + (size_t)wk * 4096, c.lwd);
      const f32x4 a = acc[u][i];
      const f32x2 tg = (f32x2{a[0], a[1]} + nm * f32x2{cs[u][0], cs[u][1]}) * ce2;
      const f32x2 tu = f32x2{a[2], a[3]} + nm * f32x2{cs[u][2], cs[u][3]};
      const f32x2 e = {__builtin_amdgcn_exp2f(tg[0]), __builtin_amdgcn_exp2f(tg[1])};
      const f32x2 den = e * ri2 + ri2;
      const f32x2 o = tu * f32x2{__builtin_amdgcn_rcpf(den[0]), __builtin_amdgcn_rcpf(den[1])};
      hv[3 * i + u] = pk2(o[0], o[1]);
    });
    static_for<6>([&](auto T) {
      constexpr int tt = T;
      if constexpr ((4 * tt + 3) / 3 == i) {
        auto s02 = __builtin_amdgcn_permlane32_swap(hv[4 * tt], hv[4 * tt + 2], false, false);
        auto s13 = __builtin_amdgcn_permlane32_swap(hv[4 * tt + 1], hv[4 * tt + 3], false, false);
        auto t01 = __builtin_amdgcn_permlane16_swap(s02[0], s13[0], false, false);
        auto t23 = __builtin_amdgcn_permlane16_swap(s02[1], s13[1], false, false);
        __builtin_amdgcn_raw_buffer_store_b128(u32x4{t01[0], t01[1], t23[0], t23[1]}, hrs, hoff[tt], 0, 16);
      }
    });
  });
  u32* cnt = (u32*)(ws + WS_CNT + rt * 128);
  asm volatile("s_waitcnt vmcnt(0)\n\ts_barrier" ::: "memory");
  if (w == 0) {
    if (lane == 0) __hip_atomic_fetch_add(cnt, 1u, __ATOMIC_RELAXED, __HIP_MEMORY_SCOPE_AGENT);
    for (int spins = 0; spins < SPIN_MAX; ++spins) {
      const u32 v = __builtin_amdgcn_readfirstlane(__hip_atomic_load(cnt, __ATOMIC_RELAXED, __HIP_MEMORY_SCOPE_AGENT));
      if (v >= 16u) break;
      __builtin_amdgcn_s_sleep(1);
    }
    wait_vm<0>();
  } else {
    const char* wsrc = c.wd - (size_t)w * 1024;
    for (int e = EPI_FILL + w - 1; e < 32; e += 3) {
      const char* src = wsrc + (size_t)e * 4096;
      const u32 dst = c.sb + e * 4096;
      glds16<0>(c.lv, src, dst); glds16<1024>(c.lv, src + 1024, dst); glds16<2048>(c.lv, src + 2048, dst); glds16<3072>(c.lv, src + 3072, dst);
    }
  }
  barrier();

  const char* hb = ws + WS_H + (size_t)(8 * rt + 4 * half + w) * KS2 * 1024;
  u32x4 Hf[KS2];
  constexpr int P3_AHEAD = 12;
  auto ldH = [&](auto KS) { constexpr int ks = KS; if constexpr (ks < KS2) gld_a_sc1<(ks & 3) * 1024>(Hf[ks], c.lv, hb + (ks >> 2) * 4096); };
  static_for<P3_AHEAD>([&](auto KS) { ldH(KS); });
  f32x4 o4[4];
  u32x4 Af[3][4];
  const u32 la0 = c.sb + lane * 16, la1 = la0 + 65536, la2 = la0 + 131072;
  auto rdA = [&](auto KS, auto DF) {
    constexpr int ks = KS, df = DF;
    if constexpr (ks < KB_NL) ds_b128<(ks & 15) * 4096 + df * 1024>(Af[ks % 3][df], ks < 16 ? la0 : ks < 32 ? la1 : la2);
  };
  wait_vm<P3_AHEAD>();
  barrier();
  static_for<4>([&](auto DF) { rdA(std::integral_constant<int, 0>{}, DF); });
  static_for<4>([&](auto DF) { rdA(std::integral_constant<int, 1>{}, DF); });
  static_for<KS2>([&](auto KS) {
    constexpr int ks = KS;
    wait_vm<(ks + P3_AHEAD <= KS2 ? P3_AHEAD - 1 : KS2 - 1 - ks)>();
    if constexpr (ks + 1 < KB_NL) wait_lgkm<4>(); else wait_lgkm<0>();
    pin_a(Hf[ks]);
    static_for<4>([&](auto DF) {
      constexpr int df = DF;
      if constexpr (ks < KB_NL) { pin(Af[ks % 3][df]); mfma_va<ks == 0>(o4[df], Af[ks % 3][df], Hf[ks]); }
      else { pin_a(Wr[ks - KB_NL][df]); mfma_aa(o4[df], Wr[ks - KB_NL][df], Hf[ks]); }
      rdA(std::integral_constant<int, ks + 2>{}, DF);
      if constexpr (df == 1 && ks + P3_AHEAD < KS2) ldH(std::integral_constant<int, ks + P3_AHEAD>{});
    });
  });
  mfma_fence(o4[0], o4[1], o4[2], o4[3]);
  u32x4 o0, o1;
#pragma unroll
  for (int df = 0; df < 2; ++df) {
    o0[2 * df] = pk2(o4[df][0] + blo(res[df][0]), o4[df][1] + bhi(res[df][0]));
    o0[2 * df + 1] = pk2(o4[df][2] + blo(res[df][1]), o4[df][3] + bhi(res[df][1]));
    o1[2 * df] = pk2(o4[2 + df][0] + blo(res[2 + df][0]), o4[2 + df][1] + bhi(res[2 + df][0]));
    o1[2 * df + 1] = pk2(o4[2 + df][2] + blo(res[2 + df][1]), o4[2 + df][3] + bhi(res[2 + df][1]));
  }
  const __amdgpu_buffer_rsrc_t ors = __builtin_amdgcn_make_buffer_rsrc(out, 0, S * D * 2, 0x00020000);
  u32x4 oa, ob;
#pragma unroll
  for (int e = 0; e < 4; ++e) {
    const u32 x = (u32)__builtin_amdgcn_update_dpp(0, (int)o1[e], 0x128, 0xf, 0xf, true);
    oa[e] = n < 8 ? o0[e] : x;
    ob[e] = n < 8 ? x : o0[e];
  }
  const u32 ocol = (u32)(64 * dt + 8 * g + (n < 8 ? 0 : 32)), orow = (u32)(s0 + 64 * half + 16 * w + (n & 7));
  __builtin_amdgcn_raw_buffer_store_b128(oa, ors, (orow * D + ocol) * 2, 0, 16);
  __builtin_amdgcn_raw_buffer_store_b128(ob, ors, ((orow + 8) * D + (ocol ^ 32)) * 2, 0, 16);
  asm volatile("kb_code_end:");
}

extern "C" void kernel_launch(void* const* d_in, const int* in_sizes, int n_in, void* d_out, int out_size, void* d_ws,
                              size_t ws_size, hipStream_t stream) {
  const u16 *q = (const u16*)d_in[0], *k = (const u16*)d_in[1], *v = (const u16*)d_in[2], *shk = (const u16*)d_in[3];
  const u16 *ssmk = (const u16*)d_in[4], *dskip = (const u16*)d_in[5];
  const u16 *wg = (const u16*)d_in[7], *wu = (const u16*)d_in[8], *wd = (const u16*)d_in[9];
  char* ws = (char*)d_ws;
  (void)hipFuncSetAttribute((const void*)ka_conv, hipFuncAttributeMaxDynamicSharedMemorySize, KA_LDS);
  (void)hipFuncSetAttribute((const void*)kb_mlp, hipFuncAttributeMaxDynamicSharedMemorySize, KB_LDS);
  ka_conv<<<512, 256, KA_LDS, stream>>>(q, k, v, shk, ssmk, dskip, wg, wu, wd, ws);
  kb_mlp<<<256, 256, KB_LDS, stream>>>(ws, (u16*)d_out);
}
```

```cpp
#include <hip/hip_runtime.h>
#include <stdint.h>
#include <utility>

typedef unsigned short u16;
typedef unsigned u32;
typedef __attribute__((ext_vector_type(2))) u32 u32x2;
typedef __attribute__((ext_vector_type(4))) u32 u32x4;
typedef __attribute__((ext_vector_type(2))) float f32x2;
typedef __attribute__((ext_vector_type(4))) float f32x4;
typedef __attribute__((ext_vector_type(2))) __bf16 bf16x2;
typedef __attribute__((ext_vector_type(8))) __bf16 bf16x8;
typedef __attribute__((ext_vector_type(4))) short s16x4;
#define LDSP __attribute__((address_space(3)))
#define DI __device__ __forceinline__

constexpr int S = 2048, D = 512, F = 1376;
constexpr int NU = F / 8;
constexpr int KS1 = D / 32;
constexpr int KS2 = F / 32;
constexpr float EPS = 1e-5f;

constexpr size_t WS_XR = 0;
constexpr size_t WS_QT = WS_XR + (size_t)D * S * 2;
constexpr size_t WS_HT = WS_QT + (size_t)D * S * 2;
constexpr size_t WS_W1 = WS_HT + (size_t)D * S * 2;
constexpr size_t WS_WD = WS_W1 + (size_t)NU * KS1 * 1024;
constexpr size_t WS_H = WS_WD + (size_t)8 * KS2 * 4 * 1024;
constexpr size_t WS_CS = WS_H + (size_t)(S / 16) * KS2 * 1024;
constexpr size_t WS_SA = WS_CS + (size_t)KS1 * 16 * 768;
constexpr size_t WS_SB = WS_SA + (size_t)S * 4;
constexpr size_t WS_CNT = WS_SB + (size_t)S * 4;
constexpr size_t WS_FLA = WS_CNT + 16 * 128;
constexpr size_t WS_FLG = WS_FLA + 256 * 8;
constexpr size_t WS_FLS = WS_FLG + 256 * 8;
constexpr size_t WS_FLZ = WS_FLS + 256 * 8;
constexpr size_t WS_HNT = WS_FLZ + 8 * 128;
constexpr size_t WS_EPO = WS_HNT + 256 * 8;

template <int... Is, class Fn> DI void static_for_(std::integer_sequence<int, Is...>, Fn&& f) { (f(std::integral_constant<int, Is>{}), ...); }
template <int N, class Fn> DI void static_for(Fn&& f) { static_for_(std::make_integer_sequence<int, N>{}, f); }

DI float blo(u32 u) { return __uint_as_float(u << 16); }
DI float bhi(u32 u) { return __uint_as_float(u & 0xffff0000u); }
DI u32 pk2(float a, float b) { f32x2 t = {a, b}; return __builtin_bit_cast(u32, __builtin_convertvector(t, bf16x2)); }
DI f32x4 mfma16(u32x4 a, u32x4 b, f32x4 c) {
  return __builtin_amdgcn_mfma_f32_16x16x32_bf16(__builtin_bit_cast(bf16x8, a), __builtin_bit_cast(bf16x8, b), c, 0, 0, 0);
}
DI u32x2 tr_read(u32 lds_byte) {
  return __builtin_bit_cast(u32x2, __builtin_amdgcn_ds_read_tr16_b64_v4i16((LDSP s16x4*)(size_t)lds_byte));
}
DI u32x4 tr_frag(u32 a0, u32 a1) { u32x2 x = tr_read(a0), y = tr_read(a1); return u32x4{x[0], x[1], y[0], y[1]}; }
DI u32x4 cat(u32x2 x, u32x2 y) { return __builtin_shufflevector(x, y, 0, 1, 2, 3); }
template <int CTRL> DI float dpp_add(float v) {
  return v + __builtin_bit_cast(float, __builtin_amdgcn_update_dpp(0, __builtin_bit_cast(int, v), CTRL, 0xf, 0xf, true));
}
DI float sum_rows(float v) {
  u32 u = __float_as_uint(v);
  auto a = __builtin_amdgcn_permlane16_swap(u, u, false, false);
  v = __uint_as_float(a[0]) + __uint_as_float(a[1]);
  u = __float_as_uint(v);
  auto b = __builtin_amdgcn_permlane32_swap(u, u, false, false);
  return __uint_as_float(b[0]) + __uint_as_float(b[1]);
}
DI float wave_sum(float v) {
  v = dpp_add<0xb1>(v);
  v = dpp_add<0x4e>(v);
  v = dpp_add<0x124>(v);
  v = dpp_add<0x128>(v);
  return sum_rows(v);
}
DI __amdgpu_buffer_rsrc_t ws_rsrc(char* ws) { return __builtin_amdgcn_make_buffer_rsrc(ws, 0, 0x04000000, 0x00020000); }
DI void st16_wt(__amdgpu_buffer_rsrc_t r, u32 off, u32x4 v) { __builtin_amdgcn_raw_buffer_store_b128(v, r, off, 0, 16); }
DI int slice_u0(int j) { return j < 12 ? 11 * j : 132 + 10 * (j - 12); }
DI int slice_nu(int j) { return j < 12 ? 11 : 10; }

template <bool FIRST = false> DI void mfma_vv(f32x4& acc, const u32x4& a, const u32x4& b) {
  if constexpr (FIRST) asm volatile("v_mfma_f32_16x16x32_bf16 %0, %1, %2, 0" : "=&v"(acc) : "v"(a), "v"(b));
  else asm volatile("v_mfma_f32_16x16x32_bf16 %0, %1, %2, %0" : "+v"(acc) : "v"(a), "v"(b));
}
template <bool FIRST = false> DI void mfma_av(f32x4& acc, const u32x4& a, const u32x4& b) {
  if constexpr (FIRST) asm volatile("v_mfma_f32_16x16x32_bf16 %0, %1, %2, 0" : "=&v"(acc) : "a"(a), "v"(b));
  else asm volatile("v_mfma_f32_16x16x32_bf16 %0, %1, %2, %0" : "+v"(acc) : "a"(a), "v"(b));
}
template <int OFF> DI void ds_b64(u32x2& d, u32 addr) { asm volatile("ds_read_b64 %0, %1 offset:%c2" : "=v"(d) : "v"(addr), "i"(OFF) : "memory"); }
template <int OFF> DI void ds_b128(u32x4& d, u32 addr) { asm volatile("ds_read_b128 %0, %1 offset:%c2" : "=v"(d) : "v"(addr), "i"(OFF) : "memory"); }
template <int OFF> DI void ds_tr(u32x2& d, u32 addr) { asm volatile("ds_read_b64_tr_b16 %0, %1 offset:%c2" : "=v"(d) : "v"(addr), "i"(OFF) : "memory"); }
template <int OFF> DI void gld_a(u32x4& d, u32 voff, const void* sbase) {
  asm volatile("global_load_dwordx4 %0, %1, %2 offset:%c3" : "=a"(d) : "v"(voff), "s"(sbase), "i"(OFF) : "memory");
}
template <int OFF> DI void gld_v(u32x4& d, u32 voff, const void* sbase) {
  asm volatile("global_load_dwordx4 %0, %1, %2 offset:%c3" : "=v"(d) : "v"(voff), "s"(sbase), "i"(OFF) : "memory");
}
template <int OFF> DI void gld2_v(u32x2& d, u32 voff, const void* sbase) {
  asm volatile("global_load_dwordx2 %0, %1, %2 offset:%c3" : "=v"(d) : "v"(voff), "s"(sbase), "i"(OFF) : "memory");
}
template <int LOFF> DI void glds16(u32 voff, const void* sbase, u32 ldsbase) {
  asm volatile("s_add_u32 m0, %2, %c3\n\ts_nop 0\n\tglobal_load_lds_dwordx4 %0, %1" ::"v"(voff), "s"(sbase), "s"(ldsbase), "i"(LOFF) : "memory", "scc");
}
template <int N> DI void wait_vm() { asm volatile("s_waitcnt vmcnt(%c0)" ::"i"(N) : "memory"); }
template <int N> DI void wait_lgkm() { asm volatile("s_waitcnt lgkmcnt(%c0)" ::"i"(N) : "memory"); }
template <int OFF> DI void gld_a_sc1(u32x4& d, u32 voff, const void* sbase) {
  asm volatile("global_load_dwordx4 %0, %1, %2 offset:%c3 sc1" : "=a"(d) : "v"(voff), "s"(sbase), "i"(OFF) : "memory");
}
DI void mfma_aa(f32x4& acc, const u32x4& a, const u32x4& b) {
  asm volatile("v_mfma_f32_16x16x32_bf16 %0, %1, %2, %0" : "+v"(acc) : "a"(a), "a"(b));
}
template <bool FIRST = false> DI void mfma_va(f32x4& acc, const u32x4& a, const u32x4& b) {
  if constexpr (FIRST) asm volatile("v_mfma_f32_16x16x32_bf16 %0, %1, %2, 0" : "=&v"(acc) : "v"(a), "a"(b));
  else asm volatile("v_mfma_f32_16x16x32_bf16 %0, %1, %2, %0" : "+v"(acc) : "v"(a), "a"(b));
}
DI void mfma_fence(f32x4& a, f32x4& b, f32x4& c, f32x4& d) { asm volatile("s_nop 7\n\ts_nop 1" : "+v"(a), "+v"(b), "+v"(c), "+v"(d)); }
DI void after_fence(f32x4& a, f32x4& b, f32x4& c, f32x4& d) { asm volatile("" : "+v"(a), "+v"(b), "+v"(c), "+v"(d)); }
template <int STAGE> DI void dpp_stage(float& v) {
  if constexpr (STAGE == 0) asm volatile("v_add_f32_dpp %0, %0, %0 quad_perm:[1,0,3,2] row_mask:0xf bank_mask:0xf" : "+v"(v));
  if constexpr (STAGE == 1) asm volatile("v_add_f32_dpp %0, %0, %0 quad_perm:[2,3,0,1] row_mask:0xf bank_mask:0xf" : "+v"(v));
  if constexpr (STAGE == 2) asm volatile("v_add_f32_dpp %0, %0, %0 row_ror:4 row_mask:0xf bank_mask:0xf" : "+v"(v));
  if constexpr (STAGE == 3) asm volatile("v_add_f32_dpp %0, %0, %0 row_ror:8 row_mask:0xf bank_mask:0xf" : "+v"(v));
}
DI void barrier() { asm volatile("s_barrier" ::: "memory"); }
DI void pin(u32x4& x) { asm volatile("" : "+v"(x)); }
DI void pin(u32x2& x) { asm volatile("" : "+v"(x)); }
DI void pin4(f32x4& x) { asm volatile("" : "+v"(x)); }
DI void pin_a(u32x4& x) { asm volatile("" : "+a"(x)); }

template <class Gate>
DI void w_job(int b, const u16* __restrict__ wg, const u16* __restrict__ wu, const u16* __restrict__ wd, char* __restrict__ ws, char* smem, Gate&& gate) {
  const int t = threadIdx.x, lane = t & 63, w = t >> 6;
  char* tile = smem;
  char* tile2 = smem + 32 * 368;
  const u32 tb = (u32)(size_t)(LDSP char*)smem;
  const int i16 = lane & 15, qd = i16 >> 2, p = i16 & 3, g = lane >> 4;
  const int ks = b & 15, j = b >> 4;
  const int U0 = slice_u0(j), nu = slice_nu(j);
  u32x4 g4[2], u4[2], d4[2];
#pragma unroll
  for (int rep = 0; rep < 2; ++rep) {
    int idx = t + 256 * rep, r = idx / 11, u = idx - 11 * r;
    bool ok = r < 32 && u < nu;
    size_t off = ok ? (size_t)(32 * ks + r) * F + 8 * (U0 + u) : 0;
    g4[rep] = *(const u32x4*)(wg + off); u4[rep] = *(const u32x4*)(wu + off);
  }
  const int ks2 = b >> 2, dp = b & 3;
  if (b < KS2 * 4) {
#pragma unroll
    for (int rep = 0; rep < 2; ++rep) {
      int idx = t + 256 * rep, r = idx >> 4, c = idx & 15;
      d4[rep] = *(const u32x4*)(wd + (size_t)(32 * ks2 + r) * D + 128 * dp + 8 * c);
    }
  }
#pragma unroll
  for (int rep = 0; rep < 2; ++rep) {
    int idx = t + 256 * rep, r = idx / 11, u = idx - 11 * r;
    if (r < 32 && u < nu) {
      *(u32x4*)(tile + r * 368 + u * 32) = u32x4{g4[rep][0], u4[rep][0], g4[rep][1], u4[rep][1]};
      *(u32x4*)(tile + r * 368 + u * 32 + 16) = u32x4{g4[rep][2], u4[rep][2], g4[rep][3], u4[rep][3]};
    }
  }
  if (b < KS2 * 4) {
#pragma unroll
    for (int rep = 0; rep < 2; ++rep) { int idx = t + 256 * rep, r = idx >> 4, c = idx & 15; *(u32x4*)(tile2 + r * 272 + c * 16) = d4[rep]; }
  }
  f32x4* csl = (f32x4*)(smem + 32 * 368 + 32 * 272);
  if (t < 48) csl[t] = f32x4{0.f, 0.f, 0.f, 0.f};
  __syncthreads();
  gate();
  const u32x4 ones = {0x3f803f80u, 0x3f803f80u, 0x3f803f80u, 0x3f803f80u};
  const __amdgpu_buffer_rsrc_t wr = ws_rsrc(ws);
  for (int u = w; u < nu; u += 4) {
    u32 a = tb + (8 * g + qd) * 368 + u * 32 + 8 * p;
    u32x4 fr = tr_frag(a, a + 4 * 368);
    st16_wt(wr, (u32)WS_W1 + (((U0 + u) * KS1 + ks) * 64 + lane) * 16, fr);
    f32x4 cs = mfma16(fr, ones, f32x4{0.f, 0.f, 0.f, 0.f});
    if (i16 == 0) csl[4 * u + g] = cs;
  }
  if (b < KS2 * 4) {
#pragma unroll
    for (int rep = 0; rep < 2; ++rep) {
      int fi = w + 4 * rep, dtl = fi >> 2, df = fi & 3;
      u32 a = tb + 32 * 368 + (8 * g + qd) * 272 + (64 * dtl + 32 * (df >> 1) + 8 * p + 4 * (df & 1)) * 2;
      u32x4 fr = tr_frag(a, a + 4 * 272);
      st16_wt(wr, (u32)WS_WD + ((((2 * dp + dtl) * KS2 + ks2) * 4 + df) * 64 + lane) * 16, fr);
    }
  }
  asm volatile("s_waitcnt lgkmcnt(0)\n\ts_barrier" ::: "memory");
  if (t < 48) st16_wt(wr, (u32)WS_CS + (ks * 16 + j) * 768 + t * 16, __builtin_bit_cast(u32x4, csl[t]));
}

constexpr int KA_CS = 4160;
constexpr int KA_KC = 4 * KA_CS;
constexpr int KA_XR = 2 * KA_KC;
constexpr int KA_RED = KA_XR + 2 * 4608;
constexpr int KA_LDS = KA_RED + 4 * 4096;
constexpr int KA_TK = KA_XR, KA_TV = KA_TK + 72 * 128, KA_TQ = KA_TV + 64 * 128;
static_assert(KA_TQ + 64 * 128 == KA_LDS, "tile overlay");
constexpr int SPIN_MAX = 1 << 22;
constexpr int HELP_PRE = 16;
constexpr int HELP_SLEEP = 20;
constexpr unsigned long long HINT_TICKS = 800;

template <int OFF> DI void gld_v_sc1(u32x4& d, u32 voff, const void* sbase) {
  asm volatile("global_load_dwordx4 %0, %1, %2 offset:%c3 sc1" : "=v"(d) : "v"(voff), "s"(sbase), "i"(OFF) : "memory");
}
template <int OFF> DI void gld2_v_sc1(u32x2& d, u32 voff, const void* sbase) {
  asm volatile("global_load_dwordx2 %0, %1, %2 offset:%c3 sc1" : "=v"(d) : "v"(voff), "s"(sbase), "i"(OFF) : "memory");
}
DI bool tags_ok(const u32x4& f, const u32x2& tag) { return f[0] == tag[0] && f[1] == tag[1] && f[2] == tag[0] && f[3] == tag[1]; }

__global__ __launch_bounds__(256, 2) void ka_conv(const u16* __restrict__ q, const u16* __restrict__ k, const u16* __restrict__ v,
                                                  const u16* __restrict__ shk, const u16* __restrict__ ssmk, const u16* __restrict__ dskip,
                                                  const u16* __restrict__ wg, const u16* __restrict__ wu, const u16* __restrict__ wd,
                                                  char* __restrict__ ws) {
  extern __shared__ __attribute__((aligned(16))) char smem[];
  const int t = threadIdx.x, lane = t & 63;
  const int w = __builtin_amdgcn_readfirstlane(t >> 6);
  const int b = blockIdx.x;
  if (b >= 256) {
    const int hb = b - 256, srow = 8 * hb + 2 * w;
    const __amdgpu_buffer_rsrc_t wr = ws_rsrc(ws);
    u32x2 epo; u32x4 qa, qb;
    gld2_v_sc1<0>(epo, 0, ws + WS_EPO);
    if (hb == 0 && w == 3) {
      const u32 lb = (u32)(size_t)(LDSP char*)smem + 32768 + 256;
      asm volatile("s_mov_b32 m0, %2\n\ts_nop 0\n\tglobal_load_lds_dword %0, %1" ::"v"((u32)lane * 128), "s"(ws + WS_FLA), "s"(lb) : "memory");
      asm volatile("s_mov_b32 m0, %2\n\ts_nop 0\n\tglobal_load_lds_dword %0, %1" ::"v"((u32)(lane < 9 ? 64 + lane : 64) * 128), "s"(ws + WS_FLA), "s"(lb) : "memory");
    }
    __builtin_amdgcn_s_sleep(HELP_PRE);
    gld_v<0>(qa, lane * 16, (const char*)q + (size_t)srow * 1024);
    gld_v<1024>(qb, lane * 16, (const char*)q + (size_t)srow * 1024);
    if (b < 264 && w == 1) {
      const char* pc; u32 len;
      asm volatile("s_getpc_b64 %0\n1:\n\ts_mov_b32 %1, kb_code_end-1b" : "=s"(pc), "=s"(len));
      const u32 lb = (u32)(size_t)(LDSP char*)smem + 32768;
      for (u32 o0 = 0; o0 < len; o0 += 64 * 128) {
        u32 o = o0 + lane * 128; o = o < len ? o : 0;
        asm volatile("s_mov_b32 m0, %2\n\ts_nop 0\n\tglobal_load_lds_dword %0, %1" ::"v"(o), "s"(pc), "s"(lb) : "memory");
      }
    }
    wait_vm<0>();
    asm volatile("" : "+v"(qa), "+v"(qb), "+v"(epo));
    const unsigned long long want = (((unsigned long long)epo[1] << 32) | epo[0]) + 1ull;
    {
      float sa = 0.f, sbb = 0.f;
#pragma unroll
      for (int e = 0; e < 4; ++e) { sa += blo(qa[e]) + bhi(qa[e]); sbb += blo(qb[e]) + bhi(qb[e]); }
      const float ma = wave_sum(sa) * (1.f / D), mb = wave_sum(sbb) * (1.f / D);
      float va = 0.f, vb = 0.f;
#pragma unroll
      for (int e = 0; e < 4; ++e) {
        float u0 = blo(qa[e]) - ma, u1 = bhi(qa[e]) - ma, u2 = blo(qb[e]) - mb, u3 = bhi(qb[e]) - mb;
        va += u0 * u0 + u1 * u1; vb += u2 * u2 + u3 * u3;
      }
      va = wave_sum(va) * (1.f / D); vb = wave_sum(vb) * (1.f / D);
      const float ra = __builtin_amdgcn_rsqf(va + EPS), rb = __builtin_amdgcn_rsqf(vb + EPS);
      const u32x2 val = lane == 0 ? u32x2{__float_as_uint(ra), __float_as_uint(rb)} : u32x2{__float_as_uint(-ma * ra), __float_as_uint(-mb * rb)};
      const u32 off = lane == 0 ? (u32)WS_SA + srow * 4 : lane == 1 ? (u32)WS_SB + srow * 4 : 0xfffffff0u;
      __builtin_amdgcn_raw_buffer_store_b64(val, wr, off, 0, 16);
    }
    asm volatile("s_waitcnt vmcnt(0)\n\ts_barrier" ::: "memory");
    if (t == 0) __builtin_amdgcn_raw_buffer_store_b64(u32x2{(u32)want, (u32)(want >> 32)}, wr, (u32)WS_FLS + hb * 8, 0, 16);
    if (hb == 1 && w == 1) {
      const unsigned long long* fs = (const unsigned long long*)(ws + WS_FLS) + lane;
      for (int spins = 0; spins < SPIN_MAX; ++spins) {
        bool ok = true;
#pragma unroll
        for (int i = 0; i < 4; ++i) ok &= __hip_atomic_load(fs + 64 * i, __ATOMIC_RELAXED, __HIP_MEMORY_SCOPE_AGENT) == want;
        if (__all(ok)) break;
        __builtin_amdgcn_s_sleep(1);
      }
      __builtin_amdgcn_raw_buffer_store_b64(u32x2{(u32)want, (u32)(want >> 32)}, wr, lane < 8 ? (u32)WS_FLZ + lane * 128 : 0xfffffff0u, 0, 16);
    } else __builtin_amdgcn_s_sleep(HELP_SLEEP);
    w_job(hb, wg, wu, wd, ws, smem, [&]() {
      if (hb == 1 && w == 1) {
        const unsigned long long* fa = (const unsigned long long*)(ws + WS_FLA) + lane;
        const u32x2 tg = {(u32)want, (u32)(want >> 32)};
        for (int spins = 0; spins < SPIN_MAX; ++spins) {
          bool ok = true;
#pragma unroll
          for (int i = 0; i < 4; ++i) ok &= __hip_atomic_load(fa + 64 * i, __ATOMIC_RELAXED, __HIP_MEMORY_SCOPE_AGENT) == want;
          if (__all(ok)) break;
          __builtin_amdgcn_s_sleep(16);
        }
        if (lane == 0) __builtin_amdgcn_raw_buffer_store_b64(tg, wr, (u32)WS_EPO, 0, 16);
      }
      if (w == 0) {
        const unsigned long long* hp = (const unsigned long long*)(ws + WS_HNT) + hb;
        const unsigned long long t0 = __builtin_amdgcn_s_memrealtime();
        while (__hip_atomic_load(hp, __ATOMIC_RELAXED, __HIP_MEMORY_SCOPE_AGENT) != want && __builtin_amdgcn_s_memrealtime() - t0 < HINT_TICKS)
          __builtin_amdgcn_s_sleep(8);
      }
      __builtin_amdgcn_s_barrier();
    });
    return;
  }
  const int x = b & 7, bi = b >> 3, s0 = 64 * bi;
  const int ch0 = 64 * x + 2 * bi;
  const u32 sb = (u32)(size_t)(LDSP char*)smem;
  const int n = lane & 15, g = lane >> 4, qd = n >> 2, p = n & 3;
  const int d = 64 * x + 16 * w + n;
  const __amdgpu_buffer_rsrc_t wr = ws_rsrc(ws);

  u32x2 epo, sh; u32x4 kp[2], kc[2];
  gld2_v_sc1<0>(epo, 0, ws + WS_EPO);
  gld2_v<0>(sh, d * 8, shk);
  const int r8 = lane >> 3, c8 = lane & 7;
  auto voff = [&](int instr) { const int f = ((r8 >> 1) & 1) | ((instr & 1) << 1); return (u32)((((c8 >> 1) ^ f) * 2 + (c8 & 1)) * 16); };
  const size_t col = (size_t)x * 128;
  {
    int row = s0 - 8 + 8 * w + r8; row = row < 0 ? 0 : row;
    glds16<0>((u32)row * 1024 + voff(w), (const char*)k + col, sb + KA_TK + w * 1024);
    glds16<0>((u32)(s0 + 8 * w + r8) * 1024 + voff(w), (const char*)v + col, sb + KA_TV + w * 1024);
    glds16<0>((u32)(s0 - 8 + 8 * (w + 4) + r8) * 1024 + voff(w), (const char*)k + col, sb + KA_TK + (w + 4) * 1024);
    glds16<0>((u32)(s0 + 8 * (w + 4) + r8) * 1024 + voff(w), (const char*)v + col, sb + KA_TV + (w + 4) * 1024);
    if (w == 0) glds16<0>((u32)(s0 + 56 + r8) * 1024 + voff(8), (const char*)k + col, sb + KA_TK + 8 * 1024);
  }
#pragma unroll
  for (int cc = 0; cc < 2; ++cc) {
    const char* kr = (const char*)ssmk + (size_t)(ch0 + cc) * 4096;
    gld_v<0>(kp[cc], (u32)(t > 0 ? t - 1 : 0) * 16, kr);
    gld_v<0>(kc[cc], (u32)t * 16, kr);
  }
  glds16<0>((u32)(s0 + 8 * w + r8) * 1024 + voff(w), (const char*)q + col, sb + KA_TQ + w * 1024);
  glds16<0>((u32)(s0 + 8 * (w + 4) + r8) * 1024 + voff(w), (const char*)q + col, sb + KA_TQ + (w + 4) * 1024);
  const float dsk = blo(dskip[ch0 + (w >> 1)]);
  if (b == 0 && t < 16) *(u32*)(ws + WS_CNT + t * 128) = 0u;

  wait_vm<6>();
  barrier();
  asm volatile("" : "+v"(sh), "+v"(epo));
  u32x2 tag;
  { const unsigned long long e = (((unsigned long long)epo[1] << 32) | epo[0]) + 1ull; tag = u32x2{(u32)e, (u32)(e >> 32)}; }
  auto taddr = [&](int base, int tr) { const int f = ((tr >> 1) & 1) | ((tr >> 2) & 2); return sb + base + tr * 128 + ((w ^ f) * 32) + 8 * p; };
  {
    const float a0 = blo(sh[0]), a1 = bhi(sh[0]), a2 = blo(sh[1]), a3 = bhi(sh[1]);
#pragma unroll
    for (int e = 0; e < 2; ++e) {
      const int rg = g + 4 * e;
      u32x2 km = tr_read(taddr(KA_TK, 8 * rg + 4 + qd));
      const u32x2 k0 = tr_read(taddr(KA_TK, 8 * rg + 8 + qd)), k1 = tr_read(taddr(KA_TK, 8 * rg + 12 + qd));
      const u32x2 v0 = tr_read(taddr(KA_TV, 8 * rg + qd)), v1 = tr_read(taddr(KA_TV, 8 * rg + 4 + qd));
      if (s0 + rg == 0) km = u32x2{0, 0};
      const float kf[11] = {bhi(km[0]), blo(km[1]), bhi(km[1]), blo(k0[0]), bhi(k0[0]), blo(k0[1]), bhi(k0[1]), blo(k1[0]), bhi(k1[0]), blo(k1[1]), bhi(k1[1])};
      const float vf[8] = {blo(v0[0]), bhi(v0[0]), blo(v0[1]), bhi(v0[1]), blo(v1[0]), bhi(v1[0]), blo(v1[1]), bhi(v1[1])};
      float xo[8];
#pragma unroll
      for (int j = 0; j < 8; ++j) xo[j] = (kf[j + 3] * a0 + kf[j + 2] * a1 + kf[j + 1] * a2 + kf[j] * a3) * vf[j];
      const int ir = 2040 - (s0 + 8 * rg);
      st16_wt(wr, (u32)WS_XR + (d >> 1) * 8192 + (ir >> 5) * 128 + (d & 1) * 64 + (ir & 31) * 2, u32x4{pk2(xo[7], xo[6]), pk2(xo[5], xo[4]), pk2(xo[3], xo[2]), pk2(xo[1], xo[0])});
    }
  }
  asm volatile("s_waitcnt vmcnt(0)\n\ts_barrier" : "+v"(kp[0]), "+v"(kc[0]), "+v"(kp[1]), "+v"(kc[1]) :: "memory");
  if (t == 0) __builtin_amdgcn_raw_buffer_store_b64(tag, wr, (u32)WS_FLA + (x * 32 + bi) * 8, 0, 16);
#pragma unroll
  for (int e = 0; e < 2; ++e) {
    const int rg = g + 4 * e;
    const u32x2 q0 = tr_read(taddr(KA_TQ, 8 * rg + qd)), q1 = tr_read(taddr(KA_TQ, 8 * rg + 4 + qd));
    const int r0 = s0 + 8 * rg;
    st16_wt(wr, (u32)WS_QT + (d >> 1) * 8192 + (r0 >> 5) * 128 + (d & 1) * 64 + (r0 & 31) * 2, cat(q0, q1));
  }
#pragma unroll
  for (int cc = 0; cc < 2; ++cc) {
    const u32 m = t > 0 ? 0xffffffffu : 0u;
    u32 wv[6] = {kp[cc][0] & m, kp[cc][1] & m, kp[cc][2] & m, kp[cc][3] & m, kc[cc][0], kc[cc][1]};
    u32 o1[5];
#pragma unroll
    for (int i = 0; i < 5; ++i) o1[i] = __builtin_amdgcn_alignbit(wv[i + 1], wv[i], 16);
    char* base = smem + cc * KA_KC + (t + 1) * 16;
    *(u32x4*)(base + 0 * KA_CS) = u32x4{o1[0], o1[1], o1[2], o1[3]};
    *(u32x4*)(base + 1 * KA_CS) = u32x4{wv[1], wv[2], wv[3], wv[4]};
    *(u32x4*)(base + 2 * KA_CS) = u32x4{o1[1], o1[2], o1[3], o1[4]};
    *(u32x4*)(base + 3 * KA_CS) = u32x4{wv[2], wv[3], wv[4], wv[5]};
    if (t == 255) {
      const u32 z = 0;
      const u32 e0 = __builtin_amdgcn_alignbit(kc[cc][1], kc[cc][0], 16), e1 = __builtin_amdgcn_alignbit(kc[cc][2], kc[cc][1], 16);
      const u32 e2 = __builtin_amdgcn_alignbit(kc[cc][3], kc[cc][2], 16), e3 = __builtin_amdgcn_alignbit(z, kc[cc][3], 16);
      *(u32x4*)(base + 16 + 0 * KA_CS) = u32x4{e0, e1, e2, e3};
      *(u32x4*)(base + 16 + 1 * KA_CS) = u32x4{kc[cc][1], kc[cc][2], kc[cc][3], z};
      *(u32x4*)(base + 16 + 2 * KA_CS) = u32x4{e1, e2, e3, z};
      *(u32x4*)(base + 16 + 3 * KA_CS) = u32x4{kc[cc][2], kc[cc][3], z, z};
    }
  }
  if (t < 8) *(u32x4*)(smem + (t >> 2) * KA_KC + (t & 3) * KA_CS) = u32x4{0, 0, 0, 0};
  asm volatile("s_waitcnt vmcnt(0) lgkmcnt(0)\n\ts_barrier" ::: "memory");
  if (t == 0) __builtin_amdgcn_raw_buffer_store_b64(tag, wr, (u32)WS_FLG + (x * 32 + bi) * 8, 0, 16);
  if (t >= 64 && t < 64 + 60) { int i = t - 64; *(u32x4*)(smem + KA_XR + (i / 30) * 4608 + 4096 + (i % 30) * 16) = u32x4{0, 0, 0, 0}; }
  if (w == 0) {
    const unsigned long long* fp = (const unsigned long long*)(ws + WS_FLA) + x * 32 + (lane & 31);
    const unsigned long long want = ((unsigned long long)tag[1] << 32) | tag[0];
    for (int spins = 0; spins < SPIN_MAX; ++spins) {
      if (__all(__hip_atomic_load(fp, __ATOMIC_RELAXED, __HIP_MEMORY_SCOPE_AGENT) == want)) break;
      __builtin_amdgcn_s_sleep(1);
    }
  }
  asm volatile("s_waitcnt vmcnt(0)\n\ts_barrier" ::: "memory");

  const int c = w >> 1, half = w & 1;
  u32x2 qv[4], fz; u32x4 sa[4], sbv[4], fq;
  const u32 eo = (u32)(1024 * half + 16 * n + 4 * g);
  const char* qTc = ws + WS_QT + (size_t)ch0 * 4096 + c * 64;
  const u32 qo = (u32)(4096 * half + (n >> 1) * 128 + (n & 1) * 32 + 8 * g);
  {
    u32x4 x0, x1;
    gld_v_sc1<0>(x0, t * 16, ws + WS_XR + (size_t)ch0 * 4096);
    gld_v_sc1<0>(x1, t * 16 + 4096, ws + WS_XR + (size_t)ch0 * 4096);
    wait_vm<0>();
    asm volatile("" : "+v"(x0), "+v"(x1));
    char* dst = smem + KA_XR + ((t >> 2) & 1) * 4608 + (t >> 3) * 64 + (t & 3) * 16;
    *(u32x4*)dst = x0;
    *(u32x4*)(dst + 2048) = x1;
  }
  asm volatile("s_waitcnt lgkmcnt(0)\n\ts_barrier" ::: "memory");
  if (t == 0) __builtin_amdgcn_raw_buffer_store_b64(tag, wr, (u32)WS_HNT + b * 8, 0, 16);

  const u32 aa = sb + c * KA_KC + (n & 3) * KA_CS + 16 * g + 2 * (n & 12) + half * 256;
  const u32 ba = sb + KA_XR + c * 4608 + 480 - 32 * n + 16 * g + half * 256;
  f32x4 acc[8];
  u32x2 Alo[2][8], Ahi[2][8];
  u32x4 B[2][8];
  auto rd = [&](auto C0, auto R) {
    constexpr int c0 = C0, r = R, buf = c0 & 1;
    if constexpr (r >= 2 && r < 10) {
      constexpr int u = r - 2;
      ds_b128<512 * (7 - u) + 64 * c0>(B[buf][u], ba);
    } else {
      constexpr int ar = r < 2 ? r : r - 8, i = ar >> 1;
      if constexpr ((ar & 1) == 0) ds_b64<512 * i + 64 * c0>(Alo[buf][i], aa);
      else ds_b64<512 * i + 64 * c0 + 8>(Ahi[buf][i], aa);
    }
  };
  auto ldS = [&](auto E) {
    constexpr int e = E, i = e / 3, kind = e % 3;
    if constexpr (kind == 0) gld_v_sc1<i * 1024>(sa[i], eo * 4, ws + WS_SA);
    if constexpr (kind == 1) gld_v_sc1<i * 1024>(sbv[i], eo * 4, ws + WS_SB);
    if constexpr (kind == 2) gld2_v_sc1<i * 1024>(qv[i], qo, qTc);
  };
  auto ldF = [&](auto E) {
    constexpr int e = E;
    if constexpr (e == 0) gld2_v_sc1<0>(fz, x * 128, ws + WS_FLZ);
    else gld_v_sc1<0>(fq, (x * 32 + 2 * (lane & 15)) * 8, ws + WS_FLG);
  };
  auto okF = [&]() { return fz[0] == tag[0] && fz[1] == tag[1] && tags_ok(fq, tag); };
  static_for<24>([&](auto R) { rd(std::integral_constant<int, 0>{}, R); });
  static_for<4>([&](auto C0) {
    constexpr int c0 = C0, buf = c0 & 1;
    if constexpr (c0 == 2) {
      wait_vm<0>();
      asm volatile("" : "+v"(fz), "+v"(fq));
      if (!__all(okF())) {
        for (int spins = 0; spins < SPIN_MAX; ++spins) {
          ldF(std::integral_constant<int, 0>{}); ldF(std::integral_constant<int, 1>{});
          wait_vm<0>();
          asm volatile("" : "+v"(fz), "+v"(fq));
          if (__all(okF())) break;
          __builtin_amdgcn_s_sleep(1);
        }
      }
    }
    u32x4 A[8];
    static_for<36>([&](auto M) {
      constexpr int m = M;
      constexpr int c1 = m < 8 ? 0 : m < 15 ? 1 : m < 21 ? 2 : m < 26 ? 3 : m < 30 ? 4 : m < 33 ? 5 : m < 35 ? 6 : 7;
      constexpr int first = c1 * 8 - c1 * (c1 - 1) / 2;
      constexpr int u = m - first;
      if constexpr (m == 0) {
        if constexpr (c0 == 0) wait_lgkm<14>(); else wait_lgkm<0>();
        pin(Alo[buf][0]); pin(Ahi[buf][0]);
#pragma unroll
        for (int i = 0; i < 8; ++i) pin(B[buf][i]);
        A[0] = cat(Alo[buf][0], Ahi[buf][0]);
      }
      if constexpr (m == 8) {
        if constexpr (c0 == 0) wait_lgkm<8>();
#pragma unroll
        for (int i = 1; i < 8; ++i) { pin(Alo[buf][i]); pin(Ahi[buf][i]); A[i] = cat(Alo[buf][i], Ahi[buf][i]); }
      }
      mfma_vv<(c0 == 0 && c1 == 0)>(acc[c1 + u], A[c1], B[buf][u]);
      if constexpr (c0 < 3 && m < 24) rd(std::integral_constant<int, c0 + 1>{}, M);
      if constexpr (c0 == 1 && (m == 2 || m == 4)) ldF(std::integral_constant<int, (m - 2) / 2>{});
      if constexpr (c0 == 2 && m >= 1 && m < 25 && (m & 1) == 1) ldS(std::integral_constant<int, (m - 1) / 2>{});
    });
  });
  mfma_fence(acc[0], acc[1], acc[2], acc[3]);
  after_fence(acc[4], acc[5], acc[6], acc[7]);
  f32x4 fin[4];
  {
    f32x4* red = (f32x4*)(smem + KA_RED);
    if (half) {
#pragma unroll
      for (int i = 0; i < 4; ++i) { red[(w * 4 + i) * 64 + lane] = acc[i]; fin[i] = acc[4 + i]; }
      asm volatile("; half 1" ::: "memory");
    } else {
#pragma unroll
      for (int i = 0; i < 4; ++i) { red[(w * 4 + i) * 64 + lane] = acc[4 + i]; fin[i] = acc[i]; }
      asm volatile("; half 0" ::: "memory");
    }
    asm volatile("s_waitcnt lgkmcnt(0)\n\ts_barrier" ::: "memory");
#pragma unroll
    for (int i = 0; i < 4; ++i) fin[i] += red[((w ^ 1) * 4 + i) * 64 + lane];
  }
  wait_vm<0>();
#pragma unroll
  for (int i = 0; i < 4; ++i) asm volatile("" : "+v"(qv[i]), "+v"(sa[i]), "+v"(sbv[i]));
  const f32x2 dd = {dsk, dsk};
  u32x2 hv[4];
#pragma unroll
  for (int i = 0; i < 4; ++i) {
    const int s = 256 * (4 * half + i) + 16 * n + 4 * g;
    const u32x2 xq = *(LDSP const u32x2*)(size_t)(sb + KA_XR + c * 4608 + 2 * (2044 - s));
    const f32x2 x0 = {bhi(xq[1]), blo(xq[1])}, x1 = {bhi(xq[0]), blo(xq[0])};
    const f32x2 q0 = {blo(qv[i][0]), bhi(qv[i][0])}, q1 = {blo(qv[i][1]), bhi(qv[i][1])};
    const f32x2 z0 = x0 * dd + f32x2{fin[i][0], fin[i][1]}, z1 = x1 * dd + f32x2{fin[i][2], fin[i][3]};
    const f32x4 a4 = __builtin_bit_cast(f32x4, sa[i]), b4 = __builtin_bit_cast(f32x4, sbv[i]);
    const f32x2 t0 = q0 * f32x2{a4[0], a4[1]} + f32x2{b4[0], b4[1]};
    const f32x2 t1 = q1 * f32x2{a4[2], a4[3]} + f32x2{b4[2], b4[3]};
    const f32x2 h0 = t0 * z0 + q0, h1 = t1 * z1 + q1;
    hv[i] = u32x2{pk2(h0[0], h0[1]), pk2(h1[0], h1[1])};
  }
#pragma unroll
  for (int i = 0; i < 4; i += 2) {
    auto a = __builtin_amdgcn_permlane16_swap(hv[i][0], hv[i + 1][0], false, false);
    auto bb = __builtin_amdgcn_permlane16_swap(hv[i][1], hv[i + 1][1], false, false);
    const int s = 256 * (4 * half + i + (g & 1)) + 16 * n + 4 * (g & 2);
    st16_wt(wr, (u32)WS_HT + ((ch0 + c) * S + s) * 2, u32x4{a[0], bb[0], a[1], bb[1]});
  }
}

constexpr int KB_NL = 39;
constexpr int KB_ST = KB_NL * 4096;
constexpr int KB_LDS = KB_ST + 1024;
constexpr int EPI_FILL = 8;
constexpr int KB_RING = 8;

constexpr int P2_AHEAD = 5;
constexpr int P2_PRO = 3;
constexpr int p2_upto(int ks) {
  const int ramp = P2_PRO - 1 + 2 * (ks + 1), steady = ks + P2_AHEAD;
  const int u = ks < 0 ? P2_PRO - 1 : (ramp < steady ? ramp : steady);
  return u > 15 ? 15 : u;
}
constexpr int P2_XMAX = 5;
constexpr int P2_NEXTRA = 23;
constexpr int p2_extra(int ks) {
  const int tail0 = 16 - P2_AHEAD, rest = P2_NEXTRA - P2_AHEAD * P2_XMAX;
  if (ks >= tail0) return P2_XMAX;
  return (rest > 0 && ks >= tail0 - rest) ? 1 : 0;
}
constexpr int p2_extra_first(int ks) { int c = 0; for (int s = 0; s < ks; ++s) c += p2_extra(s); return c; }
constexpr int p2_extra_real(int ks) { int f = p2_extra_first(ks), n = p2_extra(ks); return f >= P2_NEXTRA ? 0 : (f + n > P2_NEXTRA ? P2_NEXTRA - f : n); }
static_assert(p2_extra_first(16) >= P2_NEXTRA, "all Wd operations are scheduled");
constexpr int p2_issue_step(int t) { for (int s = -1; s < KS1; ++s) if (p2_upto(s) >= t) return s; return KS1; }
constexpr int p2_younger(int t, int ks, int ops) {
  int c = (p2_upto(ks - 1) - t) * ops;
  for (int s = p2_issue_step(t); s < ks; ++s) if (s >= 0) c += p2_extra_real(s);
  return c;
}

struct KbCtx {
  u32 sb, gv, lv, lb0, lwd, csv;
  const char *hb0, *hb1, *w0, *w1, *w2, *cs0, *cs1, *cs2, *wd, *wdr;
};

template <int GRAM, class Setup>
DI void kb_phase2(const KbCtx& c, f32x4 (&acc)[3][8], f32x4 (&rsum)[2], f32x4 (&csp)[3], u32x4 (&Wr)[4][4], u32 (&ra)[8], Setup&& setup) {
  constexpr int OPS = GRAM ? 4 : 5;
  u32x4 W[KB_RING][3];
  u32x2 Bl[2][8], Bh[2][8];
  auto issue = [&](auto KS, auto O) {
    constexpr int ks = KS, o = O;
    if constexpr (o == 0) glds16<ks * 8192>(c.gv, c.hb0 + (size_t)ks * 131072, c.lb0);
    if constexpr (o == 1) gld_a<(ks & 3) * 1024>(W[ks % KB_RING][0], c.lv, c.w0 + (ks >> 2) * 4096);
    if constexpr (o == 2) gld_a<(ks & 3) * 1024>(W[ks % KB_RING][1], c.lv, c.w1 + (ks >> 2) * 4096);
    if constexpr (o == 3 && !GRAM) gld_a<(ks & 3) * 1024>(W[ks % KB_RING][2], c.lv, c.w2 + (ks >> 2) * 4096);
    if constexpr (o == 4) glds16<ks * 8192 + 1024>(c.gv, c.hb1 + (size_t)ks * 131072, c.lb0);
  };
  auto extra = [&](auto E) {
    constexpr int e = E;
    if constexpr (e < 7) glds16<(32 + e) * 4096>(c.lv, c.wd + (size_t)(32 + e) * 4096, c.lwd);
    else if constexpr (e < P2_NEXTRA) gld_a<((e - 7) & 3) * 1024>(Wr[(e - 7) >> 2][(e - 7) & 3], c.lv, c.wdr + ((e - 7) >> 2) * 4096);
  };
  auto rdB = [&](auto KS, auto R) {
    constexpr int ks = KS, r = R, i = r >> 1;
    if constexpr ((r & 1) == 0) ds_tr<(ks & 7) * 8192>(Bl[ks & 1][i], ra[i] + (ks >= 8 ? 65536 : 0));
    else ds_tr<(ks & 7) * 8192 + 1024>(Bh[ks & 1][i], ra[i] + (ks >= 8 ? 65536 : 0));
  };
  u32x4 c0, c1, c2;
  static_for<5>([&](auto O) { issue(std::integral_constant<int, 0>{}, O); });
  gld_v<0>(c0, c.csv, c.cs0); gld_v<0>(c1, c.csv, c.cs1); gld_v<0>(c2, c.csv, c.cs2);
  static_for<p2_upto(-1)>([&](auto KS) { static_for<5>([&](auto O) { issue(std::integral_constant<int, KS + 1>{}, O); }); });
  setup();
  u32x4 ones = {0x3f803f80u, 0x3f803f80u, 0x3f803f80u, 0x3f803f80u};
  pin(ones);
  wait_vm<p2_upto(-1) * OPS>();
  barrier();
  static_for<16>([&](auto R) { rdB(std::integral_constant<int, 0>{}, R); });
  asm volatile("" : "+v"(c0), "+v"(c1), "+v"(c2));
  float cv[12];
#pragma unroll
  for (int e = 0; e < 4; ++e) { cv[e] = __uint_as_float(c0[e]); cv[4 + e] = __uint_as_float(c1[e]); cv[8 + e] = __uint_as_float(c2[e]); }
  static_for<KS1>([&](auto KS) {
    constexpr int ks = KS, buf = ks & 1, slot = ks % KB_RING;
    constexpr int nmain = (p2_upto(ks) - p2_upto(ks - 1)) * 5, next = p2_extra_real(ks);
    wait_lgkm<0>();
    u32x4 B[8];
#pragma unroll
    for (int i = 0; i < 8; ++i) { pin(Bl[buf][i]); pin(Bh[buf][i]); }
#pragma unroll
    for (int i = 0; i < 8; ++i) B[i] = cat(Bl[buf][i], Bh[buf][i]);
    pin_a(W[slot][0]); pin_a(W[slot][1]);
    if constexpr (!GRAM) pin_a(W[slot][2]);
    static_for<26>([&](auto M) {
      constexpr int m = M, i = m / 3, u = m % 3;
      if constexpr (m >= 24) mfma_vv<ks == 0>(rsum[m - 24], ones, B[m - 24]);
      else if constexpr (u < 2 || !GRAM) mfma_av<ks == 0>(acc[u][i], W[slot][u], B[i]);
      else mfma_vv<ks == 0>(acc[2][i], B[i], B[i]);
      if constexpr (m == 1 && ks + 1 < KS1) { wait_vm<p2_younger(ks + 1, ks, OPS)>(); barrier(); }
      if constexpr (m >= 2 && m < 18 && ks + 1 < KS1) rdB(std::integral_constant<int, ks + 1>{}, std::integral_constant<int, m - 2>{});
      if constexpr (ks >= 9 && ks < 13 && m >= 2 && m < 14) dpp_stage<ks - 9>(cv[m - 2]);
      constexpr int v = m - (26 - nmain - next);
      if constexpr (v >= 0 && v < nmain) issue(std::integral_constant<int, p2_upto(ks - 1) + 1 + v / 5>{}, std::integral_constant<int, v % 5>{});
      if constexpr (v >= nmain) extra(std::integral_constant<int, p2_extra_first(ks) + v - nmain>{});
    });
  });
  mfma_fence(acc[0][0], acc[1][0], rsum[0], rsum[1]);
#pragma unroll
  for (int i = 0; i < 8; ++i) after_fence(acc[0][i], acc[1][i], acc[2][i], rsum[i & 1]);
#pragma unroll
  for (int u = 0; u < 3; ++u) csp[u] = f32x4{cv[4 * u], cv[4 * u + 1], cv[4 * u + 2], cv[4 * u + 3]};
}

__global__ __launch_bounds__(256) void kb_mlp(char* __restrict__ ws, u16* __restrict__ out) {
  extern __shared__ __attribute__((aligned(16))) char smem[];
  const int t = threadIdx.x, lane = t & 63;
  const int w = __builtin_amdgcn_readfirstlane(t >> 6);
  const int b = blockIdx.x, xcd = b & 7, bi = b >> 3;
  const int rt = (xcd >> 1) * 4 + (bi & 3);
  const int j = (xcd & 1) * 8 + (bi >> 2);
  const int half = j >> 3, dt = j & 7;
  const int U0 = slice_u0(j), nu = slice_nu(j);
  const int rem = nu - 8;
  const int nw = w < rem ? 3 : 2;
  const int uw = U0 + (w < rem ? 3 * w : 3 * rem + 2 * (w - rem));
  const int u2 = uw + (nw == 3 ? 2 : 0);
  const int n = lane & 15, g = lane >> 4;
  const int s0 = 128 * rt;
  auto sf = [&](int i) { return ((w + (i >> 1)) & 3) + 4 * (half ^ (i & 1)); };

  KbCtx c;
  c.sb = (u32)(size_t)(LDSP char*)smem;
  c.lv = lane * 16;
  c.gv = (u32)(g * 4096 + (((n >> 1) ^ (g | ((w & 1) << 2))) * 32 + (n & 1) * 16));
  c.hb0 = ws + WS_HT + (size_t)s0 * 2 + (size_t)(8 * w) * 4096;
  c.hb1 = c.hb0 + 4 * 4096;
  c.lb0 = c.sb + 8 * w * 256;
  c.w0 = ws + WS_W1 + (size_t)uw * KS1 * 1024;
  c.w1 = c.w0 + KS1 * 1024;
  c.w2 = ws + WS_W1 + (size_t)u2 * KS1 * 1024;
  c.cs0 = ws + WS_CS + (size_t)j * 768 + (size_t)(uw - U0) * 64;
  c.cs1 = c.cs0 + 64;
  c.cs2 = ws + WS_CS + (size_t)j * 768 + (size_t)(u2 - U0) * 64;
  c.csv = n * (16 * 768) + g * 16;
  c.wdr = ws + WS_WD + (size_t)dt * KS2 * 4096 + (size_t)KB_NL * 4096;
  c.wd = ws + WS_WD + (size_t)dt * KS2 * 4096 + (size_t)w * 1024;
  c.lwd = c.sb + 1024 * w;
  u32 ra[8], hoff[6];
  auto setup = [&]() {
    const int qd = n >> 2, p = n & 3;
    const int frow = qd | ((g & 1) << 2);
#pragma unroll
    for (int i = 0; i < 8; ++i) { ra[i] = c.sb + (8 * g + qd) * 256 + ((sf(i) ^ frow) * 32) + 8 * p; asm volatile("" : "+v"(ra[i])); }
#pragma unroll
    for (int tt = 0; tt < 6; ++tt) {
      const int q = 4 * tt + g, i = (q * 11) >> 5, u = q - 3 * i, U = uw + u;
      const u32 o = (u32)(((8 * rt + sf(i)) * KS2 + (U >> 2)) * 1024 + ((U & 3) * 16 + n) * 16);
      hoff[tt] = u < nw ? o : 0xfffffff0u;
      asm volatile("" : "+v"(hoff[tt]));
    }
  };
  f32x4 acc[3][8], cs[3], rsum[2];
  u32x4 Wr[4][4];
  if (w == 3) kb_phase2<1>(c, acc, rsum, cs, Wr, ra, setup);
  else kb_phase2<0>(c, acc, rsum, cs, Wr, ra, setup);

  u32x2 res[4];
  {
    const u32 rb = ra[0] + dt * 16384;
    ds_tr<0>(res[0], rb); ds_tr<1024>(res[1], rb); ds_tr<8192>(res[2], rb); ds_tr<9216>(res[3], rb);
  }
  float* stl = (float*)(smem + KB_ST);
  if (g == 0) {
#pragma unroll
    for (int i = 0; i < 2; ++i) stl[2 * (16 * sf(i) + n)] = rsum[i][0];
  }
  if (w == 3 && g == (n >> 2)) {
#pragma unroll
    for (int i = 0; i < 8; ++i) {
      f32x4 a = acc[2][i];
      float dg = (n & 2) ? ((n & 1) ? a[3] : a[2]) : ((n & 1) ? a[1] : a[0]);
      stl[2 * (16 * sf(i) + n) + 1] = dg;
    }
  }
  asm volatile("s_waitcnt lgkmcnt(0)\n\ts_barrier" : "+v"(res[0]), "+v"(res[1]), "+v"(res[2]), "+v"(res[3]) :: "memory");
  const __amdgpu_buffer_rsrc_t hrs = __builtin_amdgcn_make_buffer_rsrc(ws + WS_H, 0, (S / 16) * KS2 * 1024, 0x00020000);
  f32x2 sv[8];
#pragma unroll
  for (int i = 0; i < 8; ++i) sv[i] = *(const f32x2*)(stl + 2 * (16 * sf(i) + n));
  u32 hv[24];
  static_for<8>([&](auto I) {
    constexpr int i = I;
    const float mean = sv[i][0] * (1.f / D);
    const float var = fmaxf(sv[i][1] * (1.f / D) - mean * mean, 0.f) + EPS;
    const float rstd = __builtin_amdgcn_rsqf(var);
    const float rinv = var * rstd;
    const float ce = -1.4426950408889634f * rstd;
    const f32x2 nm = {-mean, -mean}, ce2 = {ce, ce}, ri2 = {rinv, rinv};
    static_for<3>([&](auto U_) {
      constexpr int u = U_;
      pin4(acc[u][i]);
      constexpr int slot = 3 * i + u;
      constexpr int wk = slot * EPI_FILL / 24;
      if constexpr (EPI_FILL > 0 && (slot + 1) * EPI_FILL / 24 > wk) glds16<wk * 4096>(c.lv, c.wd + (size_t)wk * 4096, c.lwd);
      const f32x4 a = acc[u][i];
      const f32x2 tg = (f32x2{a[0], a[1]} + nm * f32x2{cs[u][0], cs[u][1]}) * ce2;
      const f32x2 tu = f32x2{a[2], a[3]} + nm * f32x2{cs[u][2], cs[u][3]};
      const f32x2 e = {__builtin_amdgcn_exp2f(tg[0]), __builtin_amdgcn_exp2f(tg[1])};
      const f32x2 den = e * ri2 + ri2;
      const f32x2 o = tu * f32x2{__builtin_amdgcn_rcpf(den[0]), __builtin_amdgcn_rcpf(den[1])};
      hv[3 * i + u] = pk2(o[0], o[1]);
    });
    static_for<6>([&](auto T) {
      constexpr int tt = T;
      if constexpr ((4 * tt + 3) / 3 == i) {
        auto s02 = __builtin_amdgcn_permlane32_swap(hv[4 * tt], hv[4 * tt + 2], false, false);
        auto s13 = __builtin_amdgcn_permlane32_swap(hv[4 * tt + 1], hv[4 * tt + 3], false, false);
        auto t01 = __builtin_amdgcn_permlane16_swap(s02[0], s13[0], false, false);
        auto t23 = __builtin_amdgcn_permlane16_swap(s02[1], s13[1], false, false);
        __builtin_amdgcn_raw_buffer_store_b128(u32x4{t01[0], t01[1], t23[0], t23[1]}, hrs, hoff[tt], 0, 16);
      }
    });
  });
  u32* cnt = (u32*)(ws + WS_CNT + rt * 128);
  asm volatile("s_waitcnt vmcnt(0)\n\ts_barrier" ::: "memory");
  if (w == 0) {
    if (lane == 0) __hip_atomic_fetch_add(cnt, 1u, __ATOMIC_RELAXED, __HIP_MEMORY_SCOPE_AGENT);
    for (int spins = 0; spins < SPIN_MAX; ++spins) {
      const u32 v = __builtin_amdgcn_readfirstlane(__hip_atomic_load(cnt, __ATOMIC_RELAXED, __HIP_MEMORY_SCOPE_AGENT));
      if (v >= 16u) break;
      __builtin_amdgcn_s_sleep(1);
    }
    wait_vm<0>();
  } else {
    const char* wsrc = c.wd - (size_t)w * 1024;
    for (int e = EPI_FILL + w - 1; e < 32; e += 3) {
      const char* src = wsrc + (size_t)e * 4096;
      const u32 dst = c.sb + e * 4096;
      glds16<0>(c.lv, src, dst); glds16<1024>(c.lv, src + 1024, dst); glds16<2048>(c.lv, src + 2048, dst); glds16<3072>(c.lv, src + 3072, dst);
    }
  }
  barrier();

  const char* hb = ws + WS_H + (size_t)(8 * rt + 4 * half + w) * KS2 * 1024;
  u32x4 Hf[KS2];
  constexpr int P3_AHEAD = 12;
  auto ldH = [&](auto KS) { constexpr int ks = KS; if constexpr (ks < KS2) gld_a_sc1<(ks & 3) * 1024>(Hf[ks], c.lv, hb + (ks >> 2) * 4096); };
  static_for<P3_AHEAD>([&](auto KS) { ldH(KS); });
  f32x4 o4[4];
  u32x4 Af[3][4];
  const u32 la0 = c.sb + lane * 16, la1 = la0 + 65536, la2 = la0 + 131072;
  auto rdA = [&](auto KS, auto DF) {
    constexpr int ks = KS, df = DF;
    if constexpr (ks < KB_NL) ds_b128<(ks & 15) * 4096 + df * 1024>(Af[ks % 3][df], ks < 16 ? la0 : ks < 32 ? la1 : la2);
  };
  wait_vm<P3_AHEAD>();
  barrier();
  static_for<4>([&](auto DF) { rdA(std::integral_constant<int, 0>{}, DF); });
  static_for<4>([&](auto DF) { rdA(std::integral_constant<int, 1>{}, DF); });
  static_for<KS2>([&](auto KS) {
    constexpr int ks = KS;
    wait_vm<(ks + P3_AHEAD <= KS2 ? P3_AHEAD - 1 : KS2 - 1 - ks)>();
    if constexpr (ks + 1 < KB_NL) wait_lgkm<4>(); else wait_lgkm<0>();
    pin_a(Hf[ks]);
    static_for<4>([&](auto DF) {
      constexpr int df = DF;
      if constexpr (ks < KB_NL) { pin(Af[ks % 3][df]); mfma_va<ks == 0>(o4[df], Af[ks % 3][df], Hf[ks]); }
      else { pin_a(Wr[ks - KB_NL][df]); mfma_aa(o4[df], Wr[ks - KB_NL][df], Hf[ks]); }
      rdA(std::integral_constant<int, ks + 2>{}, DF);
      if constexpr (df == 1 && ks + P3_AHEAD < KS2) ldH(std::integral_constant<int, ks + P3_AHEAD>{});
    });
  });
  mfma_fence(o4[0], o4[1], o4[2], o4[3]);
  u32x4 o0, o1;
#pragma unroll
  for (int df = 0; df < 2; ++df) {
    o0[2 * df] = pk2(o4[df][0] + blo(res[df][0]), o4[df][1] + bhi(res[df][0]));
    o0[2 * df + 1] = pk2(o4[df][2] + blo(res[df][1]), o4[df][3] + bhi(res[df][1]));
    o1[2 * df] = pk2(o4[2 + df][0] + blo(res[2 + df][0]), o4[2 + df][1] + bhi(res[2 + df][0]));
    o1[2 * df + 1] = pk2(o4[2 + df][2] + blo(res[2 + df][1]), o4[2 + df][3] + bhi(res[2 + df][1]));
  }
  const __amdgpu_buffer_rsrc_t ors = __builtin_amdgcn_make_buffer_rsrc(out, 0, S * D * 2, 0x00020000);
  u32x4 oa, ob;
#pragma unroll
  for (int e = 0; e < 4; ++e) {
    const u32 x = (u32)__builtin_amdgcn_update_dpp(0, (int)o1[e], 0x128, 0xf, 0xf, true);
    oa[e] = n < 8 ? o0[e] : x;
    ob[e] = n < 8 ? x : o0[e];
  }
  const u32 ocol = (u32)(64 * dt + 8 * g + (n < 8 ? 0 : 32)), orow = (u32)(s0 + 64 * half + 16 * w + (n & 7));
  __builtin_amdgcn_raw_buffer_store_b128(oa, ors, (orow * D + ocol) * 2, 0, 16);
  __builtin_amdgcn_raw_buffer_store_b128(ob, ors, ((orow + 8) * D + (ocol ^ 32)) * 2, 0, 16);
  asm volatile("kb_code_end:");
}

extern "C" void kernel_launch(void* const* d_in, const int* in_sizes, int n_in, void* d_out, int out_size, void* d_ws,
                              size_t ws_size, hipStream_t stream) {
  const u16 *q = (const u16*)d_in[0], *k = (const u16*)d_in[1], *v = (const u16*)d_in[2], *shk = (const u16*)d_in[3];
  const u16 *ssmk = (const u16*)d_in[4], *dskip = (const u16*)d_in[5];
  const u16 *wg = (const u16*)d_in[7], *wu = (const u16*)d_in[8], *wd = (const u16*)d_in[9];
  char* ws = (char*)d_ws;
  (void)hipFuncSetAttribute((const void*)ka_conv, hipFuncAttributeMaxDynamicSharedMemorySize, KA_LDS);
  (void)hipFuncSetAttribute((const void*)kb_mlp, hipFuncAttributeMaxDynamicSharedMemorySize, KB_LDS);
  ka_conv<<<512, 256, KA_LDS, stream>>>(q, k, v, shk, ssmk, dskip, wg, wu, wd, ws);
  kb_mlp<<<256, 256, KB_LDS, stream>>>(ws, (u16*)d_out);
}
```
